```python
import torch

def _softmax(x, axis=-1):
    return torch.softmax(x, dim=axis)

def _gelu(x):
    return torch.nn.functional.gelu(x, approximate='tanh')

def _silu(x):
    return torch.nn.functional.silu(x)

def _sigmoid(x):
    return torch.sigmoid(x)

def _layernorm(x, axis=-1, eps=1e-05):
    m = x.mean(dim=axis, keepdim=True)
    v = x.var(dim=axis, keepdim=True, unbiased=False)
    return (x - m) / torch.sqrt(v + eps)

def _scatter_add(tgt, idx, src, axis=0):
    return tgt.clone().index_add_(axis, idx, src)

def _segment_sum(src, seg, n):
    out = torch.zeros((n,) + src.shape[1:], dtype=src.dtype)
    return out.index_add_(0, seg, src)

def _pad(x, pw):
    flat = tuple((v for pair in reversed(pw) for v in pair))
    return torch.nn.functional.pad(x, flat)
kernel_name = 'interleave_layers__spectral_gfnet__memory_aug_recurrent_mem_tr__prompt'

def setup_inputs():

    _w = lambda a: (lambda t: t.to(torch.bfloat16) if t.is_floating_point() else t)(torch.as_tensor(a))

    def _A_setup():
        B, S, D = (1, 2048, 640)
        Df = D // 2 + 1
        return {'x': _w((lambda *s: torch.randn(*s, dtype=torch.bfloat16) * 0.05)(B, S, D)), 'w_r': _w((lambda *s: torch.randn(*s, dtype=torch.bfloat16) * 0.4)(S, Df) * 0.02), 'w_i': _w((lambda *s: torch.randn(*s, dtype=torch.bfloat16) * 0.4)(S, Df) * 0.02)}
    _A_in = _A_setup()

    def _B_setup():
        B, S, Dm = (1, 2048, 640)
        H, Dh = (32, 128)
        M, seg = (8, 32)
        seg = min(seg, S)
        S = S // seg * seg
        return {'x': _w((lambda *s: torch.randn(*s, dtype=torch.bfloat16) * 0.05)(B, S, Dm)), 'mem0': _w((lambda *s: torch.randn(*s, dtype=torch.bfloat16) * 0.2)(B, M, Dm)), 'Wq': _w((lambda *s: torch.randn(*s, dtype=torch.bfloat16) * 1.6)(Dm, H, Dh) * Dm ** (-0.5)), 'Wk': _w((lambda *s: torch.randn(*s, dtype=torch.bfloat16) * 1.6)(Dm, H, Dh) * Dm ** (-0.5)), 'Wv': _w((lambda *s: torch.randn(*s, dtype=torch.bfloat16) * 0.2)(Dm, H, Dh) * Dm ** (-0.5)), 'Wo': _w((lambda *s: torch.randn(*s, dtype=torch.bfloat16) * 0.2)(H, Dh, Dm) * (H * Dh) ** (-0.5)), 'seg_len': seg}
    _B_in = _B_setup()
    return {'A_x': _A_in['x'], 'A_w_r': _A_in['w_r'], 'A_w_i': _A_in['w_i'], 'B_x': _B_in['x'], 'B_mem0': _B_in['mem0'], 'B_Wq': _B_in['Wq'], 'B_Wk': _B_in['Wk'], 'B_Wv': _B_in['Wv'], 'B_Wo': _B_in['Wo'], 'B_seg_len': _B_in['seg_len']}

def reference(A_x, A_w_r, A_w_i, B_x, B_mem0, B_Wq, B_Wk, B_Wv, B_Wo, B_seg_len):

    def _A_ref(x, w_r, w_i):
        B, S, D = x.shape
        X = (lambda x, axes=(-2, -1): torch.fft.rfft2(x.float(), dim=axes))(x, axes=(-2, -1))
        W = w_r + 1j * w_i
        X = X * W
        y = (lambda x, s, axes=(-2, -1): torch.fft.irfft2(x, s=s, dim=axes).to(torch.bfloat16))(X, (S, D), axes=(-2, -1))
        return y

    def _B_ref(x, mem0, Wq, Wk, Wv, Wo, seg_len):
        B, S, Dm = x.shape
        H, Dh = (Wq.shape[1], Wq.shape[2])
        M = mem0.shape[1]
        scale = Dh ** (-0.5)
        n_seg = S // seg_len
        L = M + seg_len
        idx = torch.arange(L)
        mask = (idx[:, None] < M) | (idx[:, None] >= idx[None, :])
        mem = mem0
        outs = []
        for s in range(n_seg):
            xs = x[:, s * seg_len:(s + 1) * seg_len, :].clone()
            h = torch.concatenate([mem, xs], axis=1)
            q = torch.einsum('btd,dhe->bhte', h, Wq)
            k = torch.einsum('btd,dhe->bhte', h, Wk)
            v = torch.einsum('btd,dhe->bhte', h, Wv)
            scores = torch.einsum('bhqe,bhke->bhqk', q, k) * scale
            scores = torch.where(mask[None, None], scores, -1000000000.0)
            p = _softmax(scores, axis=-1)
            o = torch.einsum('bhqk,bhke->bhqe', p, v)
            o = torch.einsum('bhte,hed->btd', o, Wo)
            mem = o[:, :M, :].clone()
            outs.append(o[:, M:, :])
        return torch.concatenate(outs, axis=1)
    x = A_x
    x = x + _A_ref(_layernorm(x, axis=-1), A_w_r, A_w_i)
    x = x + _B_ref(_layernorm(x, axis=-1), B_mem0, B_Wq, B_Wk, B_Wv, B_Wo, B_seg_len)
    x = x + _A_ref(_layernorm(x, axis=-1), A_w_r, A_w_i)
    x = x + _B_ref(_layernorm(x, axis=-1), B_mem0, B_Wq, B_Wk, B_Wv, B_Wo, B_seg_len)
    return x
```

```cpp
#include <hip/hip_runtime.h>
#include <hip/hip_bf16.h>
#include <stdint.h>

typedef unsigned short u16;
typedef __attribute__((ext_vector_type(8))) __bf16 bf16x8;
typedef __attribute__((ext_vector_type(4))) short s16x4;
typedef __attribute__((ext_vector_type(4))) float f32x4;
typedef __attribute__((ext_vector_type(8))) float f32x8;
typedef __attribute__((ext_vector_type(16))) float f32x16;
typedef __attribute__((ext_vector_type(4))) unsigned u32x4;
typedef __attribute__((ext_vector_type(2))) unsigned u32x2;
typedef __attribute__((ext_vector_type(8))) unsigned u32x8;
typedef __attribute__((ext_vector_type(16))) __bf16 bf16x16;
typedef __attribute__((ext_vector_type(2))) unsigned short u16x2;
#define LAS __attribute__((address_space(3)))

constexpr int S_ = 2048, D_ = 640, DF = 321, H_ = 32, DH = 128, HD = 4096, NM = 8, SEG = 32, NSEG = 64;
constexpr int NQKV = 3 * HD;
constexpr int NOWN = 40;
constexpr int ZTR = NSEG;
constexpr size_t ZTSEG = (size_t)NOWN * H_ * 512;
constexpr unsigned PAIR = 16384, HALF = 8192;
constexpr unsigned W_MEM = 0, W_P = W_MEM + 24 * PAIR, W_T = W_P + 40 * PAIR, W_GO = W_T + 8 * PAIR, W_OUT = W_GO + PAIR, W_FZ = W_OUT + PAIR, W_FPT = W_FZ + 4096, W_FZ2 = W_FZ + 8192, W_FQ = W_FZ + 12288, W_H = W_FZ + PAIR, W_PL = W_H + PAIR, W_PROBE = W_PL + 6 * PAIR,
                   W_ZM = 2u * 1024 * 1024, W_END = W_ZM + 120 * PAIR;
static_assert(W_PROBE + 2 * PAIR <= 2u * 1024 * 1024 && W_PROBE % (2 * PAIR) == 0, "window");
constexpr int PTSEG = 32768 + 8192;

#define XA1 0
#define FAUX 0
#define WAUX 16
#define A2AUX 16
#define FPAD 0
constexpr int FS = 1024 + FPAD;
constexpr size_t al(size_t x) { return (x + 255) & ~(size_t)255; }
constexpr size_t OFF_CTL  = 0;
constexpr size_t OFF_WQKVT = 4096 + 4 * 1024 * 1024;
constexpr size_t OFF_WOT  = OFF_WQKVT + al((size_t)NQKV * D_ * 2);
constexpr size_t OFF_WFT  = OFF_WOT + al((size_t)D_ * HD * 2);
constexpr size_t OFF_TW640 = OFF_WFT + al((size_t)DF * S_ * 8);
constexpr size_t OFF_TW2048 = OFF_TW640 + al(640 * 8);
constexpr size_t OFF_F    = OFF_TW2048 + al(20 * 256 * 8);
constexpr size_t OFF_X1   = OFF_F + al((size_t)DF * FS * 16);
constexpr size_t OFF_X2   = OFF_X1 + al((size_t)S_ * D_ * 2);
constexpr size_t OFF_H    = OFF_X2 + al((size_t)S_ * D_ * 2);
constexpr size_t OFF_Z    = OFF_H + al((size_t)S_ * D_ * 2);
constexpr size_t OFF_Z2   = OFF_Z + al(ZTSEG * ZTR * 2);
constexpr size_t OFF_PT   = OFF_Z2 + al((size_t)NSEG * NOWN * 8192);
constexpr size_t OFF_QK   = OFF_PT + al((size_t)NSEG * PTSEG * 2);
constexpr size_t OFF_WIN  = OFF_QK + (size_t)NSEG * H_ * 16384;
constexpr size_t OFF_END  = OFF_WIN + (size_t)6 * 1024 * 1024;
static_assert(OFF_END <= (size_t)256 * 1024 * 1024, "workspace");

#define st128(v, r, vo, so, aux) do { const u32x4 d_ = (v); __builtin_amdgcn_raw_buffer_store_b128(d_, r, vo, so, aux); asm volatile("s_nop 1" :: "v"(d_)); } while (0)
__device__ __forceinline__ float bf2f(u16 v) { return __uint_as_float((unsigned)v << 16); }
__device__ __forceinline__ u16 f2bf(float f) { __bf16 b = (__bf16)f; return __builtin_bit_cast(u16, b); }
__device__ __forceinline__ float bfr(float f) { return (float)(__bf16)f; }
typedef __attribute__((ext_vector_type(2))) float f32x2;
typedef __attribute__((ext_vector_type(2))) __bf16 bf16x2;
__device__ __forceinline__ unsigned pack2(float a, float b) { return __builtin_bit_cast(unsigned, __builtin_convertvector((f32x2){a, b}, bf16x2)); }
__device__ __forceinline__ float lo2f(unsigned v) { return __uint_as_float(v << 16); }
__device__ __forceinline__ float hi2f(unsigned v) { return __uint_as_float(v & 0xFFFF0000u); }
__device__ __forceinline__ float fmx(float a, float b) { return __builtin_elementwise_maximum(a, b); }
__device__ __forceinline__ f32x4 mfma32(u32x4 a, u32x4 b, f32x4 c) {
    return __builtin_amdgcn_mfma_f32_16x16x32_bf16(__builtin_bit_cast(bf16x8, a), __builtin_bit_cast(bf16x8, b), c, 0, 0, 0);
}
__device__ __forceinline__ f32x4 mfma16(u32x2 a, u32x2 b, f32x4 c) {
    return __builtin_amdgcn_mfma_f32_16x16x16bf16_1k(__builtin_bit_cast(s16x4, a), __builtin_bit_cast(s16x4, b), c, 0, 0, 0);
}
__device__ __forceinline__ f32x4 smfmac(u32x4 a, u32x8 b, f32x4 c, int idx) {
    return __builtin_amdgcn_smfmac_f32_16x16x64_bf16(__builtin_bit_cast(bf16x8, a), __builtin_bit_cast(bf16x16, b), c, idx, 0, 0);
}
__device__ __forceinline__ int pi16(int k) { return ((k & 3) << 2) | ((k >> 1) & 2) | (k >> 3); }
__device__ __forceinline__ float addswap32(float a, float b) {
    auto r = __builtin_amdgcn_permlane32_swap(__float_as_uint(a), __float_as_uint(b), false, false);
    return __uint_as_float(r[0]) + __uint_as_float(r[1]);
}
__device__ __forceinline__ float xmax3216(float v) {
    auto r = __builtin_amdgcn_permlane32_swap(__float_as_uint(v), __float_as_uint(v), false, false);
    v = fmx(__uint_as_float(r[0]), __uint_as_float(r[1]));
    auto q = __builtin_amdgcn_permlane16_swap(__float_as_uint(v), __float_as_uint(v), false, false);
    return fmx(__uint_as_float(q[0]), __uint_as_float(q[1]));
}
__device__ __forceinline__ float xsum3216(float v) {
    auto r = __builtin_amdgcn_permlane32_swap(__float_as_uint(v), __float_as_uint(v), false, false);
    v = __uint_as_float(r[0]) + __uint_as_float(r[1]);
    auto q = __builtin_amdgcn_permlane16_swap(__float_as_uint(v), __float_as_uint(v), false, false);
    return __uint_as_float(q[0]) + __uint_as_float(q[1]);
}
template <int CTRL> __device__ __forceinline__ float dppf(float v) { return __uint_as_float((unsigned)__builtin_amdgcn_update_dpp(0, (int)__float_as_uint(v), CTRL, 0xF, 0xF, false)); }
__device__ __forceinline__ float wsum(float v) {
    v += dppf<0xB1>(v); v += dppf<0x4E>(v); v += dppf<0x141>(v); v += dppf<0x140>(v);
    return xsum3216(v);
}
__device__ __forceinline__ unsigned ror8(unsigned v) { return (unsigned)__builtin_amdgcn_update_dpp(0, (int)v, 0x128, 0xF, 0xF, false); }
__device__ __forceinline__ float ror8f(float v) { return __uint_as_float(ror8(__float_as_uint(v))); }
__device__ __forceinline__ unsigned nosent(unsigned v) {
    u16x2 lim = {0xFFFE, 0xFFFE};
    return __builtin_bit_cast(unsigned, __builtin_elementwise_min(__builtin_bit_cast(u16x2, v), lim));
}
__device__ __forceinline__ unsigned pkmax(unsigned a, unsigned b) {
    return __builtin_bit_cast(unsigned, __builtin_elementwise_max(__builtin_bit_cast(u16x2, a), __builtin_bit_cast(u16x2, b)));
}
__device__ __forceinline__ bool has_sent(u32x4 v) {
    unsigned m = pkmax(pkmax(v.x, v.y), pkmax(v.z, v.w));
    return (m & 0xFFFFu) == 0xFFFFu || (m >> 16) == 0xFFFFu;
}

__device__ __forceinline__ float2 cadd(float2 a, float2 b) { return make_float2(a.x + b.x, a.y + b.y); }
__device__ __forceinline__ float2 csub(float2 a, float2 b) { return make_float2(a.x - b.x, a.y - b.y); }
__device__ __forceinline__ float2 cmul(float2 a, float2 b) { return make_float2(a.x * b.x - a.y * b.y, a.x * b.y + a.y * b.x); }
template <int SG> __device__ __forceinline__ float2 muli(float2 a) { return SG < 0 ? make_float2(a.y, -a.x) : make_float2(-a.y, a.x); }
template <int SG> __device__ __forceinline__ float2 ctw(float2 a, float2 w) { if (SG > 0) w.y = -w.y; return cmul(a, w); }

template <int SG> __device__ __forceinline__ void dft4(float2& a, float2& b, float2& c, float2& d) {
    float2 t0 = cadd(a, c), t1 = csub(a, c), t2 = cadd(b, d), t3 = muli<SG>(csub(b, d));
    a = cadd(t0, t2); b = cadd(t1, t3); c = csub(t0, t2); d = csub(t1, t3);
}
template <int SG> __device__ __forceinline__ void dft8(float2 (&u)[8]) {
    float2 e0 = u[0], e1 = u[2], e2 = u[4], e3 = u[6], o0 = u[1], o1 = u[3], o2 = u[5], o3 = u[7];
    dft4<SG>(e0, e1, e2, e3); dft4<SG>(o0, o1, o2, o3);
    const float r = 0.70710678118654752440f;
    float2 t1 = muli<SG>(o1); o1 = make_float2((o1.x + t1.x) * r, (o1.y + t1.y) * r);
    o2 = muli<SG>(o2);
    float2 t3 = muli<SG>(o3); o3 = make_float2((t3.x - o3.x) * r, (t3.y - o3.y) * r);
    u[0] = cadd(e0, o0); u[4] = csub(e0, o0);
    u[1] = cadd(e1, o1); u[5] = csub(e1, o1);
    u[2] = cadd(e2, o2); u[6] = csub(e2, o2);
    u[3] = cadd(e3, o3); u[7] = csub(e3, o3);
}
template <int SG> __device__ __forceinline__ void dft5(float2& x0, float2& x1, float2& x2, float2& x3, float2& x4) {
    const float c1 = 0.30901699437494742410f, c2 = -0.80901699437494742410f, s1 = 0.95105651629515357212f, s2 = 0.58778525229247312917f;
    float2 t1 = cadd(x1, x4), t2 = cadd(x2, x3), t3 = csub(x1, x4), t4 = csub(x2, x3);
    float2 a1 = make_float2(x0.x + c1 * t1.x + c2 * t2.x, x0.y + c1 * t1.y + c2 * t2.y);
    float2 a2 = make_float2(x0.x + c2 * t1.x + c1 * t2.x, x0.y + c2 * t1.y + c1 * t2.y);
    float2 b1 = muli<SG>(make_float2(s1 * t3.x + s2 * t4.x, s1 * t3.y + s2 * t4.y));
    float2 b2 = muli<SG>(make_float2(s2 * t3.x - s1 * t4.x, s2 * t3.y - s1 * t4.y));
    x0 = make_float2(x0.x + t1.x + t2.x, x0.y + t1.y + t2.y);
    x1 = cadd(a1, b1); x4 = csub(a1, b1); x2 = cadd(a2, b2); x3 = csub(a2, b2);
}
template <int SG> __device__ __forceinline__ void dft10(float2 (&u)[10]) {
    float2 e[5] = {u[0], u[2], u[4], u[6], u[8]}, o[5] = {u[1], u[3], u[5], u[7], u[9]};
    dft5<SG>(e[0], e[1], e[2], e[3], e[4]); dft5<SG>(o[0], o[1], o[2], o[3], o[4]);
    const float wc[5] = {1.f, 0.80901699437494742410f, 0.30901699437494742410f, -0.30901699437494742410f, -0.80901699437494742410f};
    const float ws[5] = {0.f, 0.58778525229247312917f, 0.95105651629515357212f, 0.95105651629515357212f, 0.58778525229247312917f};
#pragma unroll
    for (int k = 0; k < 5; ++k) {
        float2 w = make_float2(wc[k], SG < 0 ? -ws[k] : ws[k]);
        float2 t = k == 0 ? o[0] : cmul(o[k], w);
        u[k] = cadd(e[k], t); u[k + 5] = csub(e[k], t);
    }
}
__device__ __forceinline__ void ln_row(float (&v)[10]) {
    float s = 0.f;
#pragma unroll
    for (int r = 0; r < 10; ++r) s += v[r];
    const float mean = wsum(s) * (1.f / 640.f);
    float q = 0.f;
#pragma unroll
    for (int r = 0; r < 10; ++r) { float d = v[r] - mean; q += d * d; }
    const float var = wsum(q) * (1.f / 640.f);
    const float mb = bfr(mean), rs = __builtin_amdgcn_rcpf(bfr(sqrtf(bfr(bfr(var) + 1e-5f))));
#pragma unroll
    for (int r = 0; r < 10; ++r) v[r] = bfr(bfr(v[r] - mb) * rs);
}

__device__ __forceinline__ __amdgpu_buffer_rsrc_t mkrsrc(const void* p, unsigned bytes) {
    return __builtin_amdgcn_make_buffer_rsrc((void*)p, 0, bytes, 0x00020000);
}
template <int NT> __device__ __forceinline__ void frag_tile(const u16* src, int C, int r0, int c0, u16* dst, unsigned gstride, bool perm, u16 (*t)[66]) {
    const int tid = threadIdx.x;
    if (tid < 256) {
        u32x4 v[2 * NT];
#pragma unroll
        for (int i = 0; i < 2 * NT; ++i) {
            const int id = tid + i * 256, r = id / (8 * NT), c8 = (id % (8 * NT)) * 8;
            v[i] = *(const u32x4*)(src + (size_t)(r0 + r) * C + c0 + c8);
        }
#pragma unroll
        for (int i = 0; i < 2 * NT; ++i) {
            const int id = tid + i * 256, r = id / (8 * NT), c8 = (id % (8 * NT)) * 8;
#pragma unroll
            for (int j = 0; j < 4; ++j) { t[c8 + 2 * j][r] = (u16)(v[i][j] & 0xFFFF); t[c8 + 2 * j + 1][r] = (u16)(v[i][j] >> 16); }
        }
    }
    __syncthreads();
    if (tid < 256) {
        const __amdgpu_buffer_rsrc_t rd = mkrsrc(dst, 0x7FFFFFFF);
#pragma unroll
        for (int i = 0; i < 2 * NT; ++i) {
            const int id = tid + i * 256, g = id >> 7, u = (id >> 6) & 1, fq = (id >> 4) & 3, fr = id & 15;
            const int c = 16 * g + (perm ? pi16(fr) : fr), r8 = 32 * u + 8 * fq;
            u32x4 v;
#pragma unroll
            for (int j = 0; j < 4; ++j) v[j] = *(const unsigned*)&t[c][r8 + 2 * j];
            st128(v, rd, (g * gstride + (id & 127) * 8) * 2u, 0, WAUX);
        }
    }
}
template <int NT> __device__ __forceinline__ void qkv_tile(int m, int b, const u16* W, char* ws, u16 (*t)[66]) {
    const int kb = b / (64 / NT), cb = (b % (64 / NT)) * NT;
    frag_tile<NT>(W, HD, kb * 64, cb * 64, (u16*)(ws + OFF_WQKVT) + ((size_t)(m * 256 + cb * 4) * 10 + kb) * 1024, 10240, false, t);
}
template <int NT> __device__ __forceinline__ void wo_tile(int b, const u16* W, char* ws, u16 (*t)[66]) {
    const int eb = b / (10 / NT), cb = (b % (10 / NT)) * NT;
    frag_tile<NT>(W, D_, eb * 64, cb * 64, (u16*)(ws + OFF_WOT) + ((size_t)((eb >> 1) * 40 + cb * 4) * 2 + (eb & 1)) * 1024, 2048, true, t);
}

struct RowLds { float2 bA[4][644]; float2 bB[4][644]; float2 tw[640]; };
__device__ __forceinline__ float2 tw640_at(int i) {
    float sn, cs; sincospif((float)(i > 320 ? 640 - i : -i) * (1.f / 320.f), &sn, &cs);
    return make_float2(cs, sn);
}
__device__ __forceinline__ void a1_unit(__amdgpu_buffer_rsrc_t rx, int row0, float4* F4, RowLds& L, int tid, int xa = 0) {
    const int wave = tid >> 6, lane = tid & 63;
    if (wave < 4) {
        char* st = (char*)L.bB + wave * 2560;
        const unsigned base = (unsigned)(row0 + 2 * wave) * (D_ * 2);
        const u32x4 c0 = __builtin_amdgcn_raw_buffer_load_b128(rx, lane * 16u, base, 16), c1 = __builtin_amdgcn_raw_buffer_load_b128(rx, lane * 16u + 1024u, base, 16);
        if (lane < 32) *(u32x4*)(st + 2048 + lane * 16) = __builtin_amdgcn_raw_buffer_load_b128(rx, lane * 16u + 2048u, base, 16);
        *(u32x4*)(st + lane * 16) = c0; *(u32x4*)(st + 1024 + lane * 16) = c1;
        float a[10], b[10];
#pragma unroll
        for (int r = 0; r < 10; ++r) { a[r] = bf2f(*(const u16*)(st + (lane + 64 * r) * 2)); b[r] = bf2f(*(const u16*)(st + 1280 + (lane + 64 * r) * 2)); }
        ln_row(a); ln_row(b);
        float2 u[10];
#pragma unroll
        for (int r = 0; r < 10; ++r) u[r] = make_float2(a[r], b[r]);
        dft10<-1>(u);
#pragma unroll
        for (int r = 0; r < 10; ++r) L.bA[wave][10 * lane + r] = u[r];
    }
    __syncthreads();
    const int f = tid / 80, i = tid % 80;
    if (tid < 320) {
        const int k = i % 10, j = (i / 10) * 80 + k;
        float2 u[8];
#pragma unroll
        for (int r = 0; r < 8; ++r) { u[r] = L.bA[f][i + 80 * r]; if (r) u[r] = ctw<-1>(u[r], L.tw[8 * k * r]); }
        dft8<-1>(u);
#pragma unroll
        for (int r = 0; r < 8; ++r) L.bB[f][j + 10 * r] = u[r];
    }
    __syncthreads();
    if (tid < 320) {
        float2 u[8];
#pragma unroll
        for (int r = 0; r < 8; ++r) { u[r] = L.bB[f][i + 80 * r]; if (r) u[r] = ctw<-1>(u[r], L.tw[i * r]); }
        dft8<-1>(u);
#pragma unroll
        for (int r = 0; r < 8; ++r) L.bA[f][i + 80 * r] = u[r];
    }
    __syncthreads();
    for (int it = tid; it < (xa == 2 ? 4 : DF * 4); it += blockDim.x) {
        const int k = it >> 2, ff = it & 3;
        if (xa == 3) __builtin_amdgcn_s_sleep(16);
        const float2 zk = L.bA[ff][k], zn = L.bA[ff][k == 0 ? 0 : 640 - k];
        const f32x4 o = {0.5f * (zk.x + zn.x), 0.5f * (zk.y - zn.y), 0.5f * (zk.y + zn.y), 0.5f * (zn.x - zk.x)};
        st128(__builtin_bit_cast(u32x4, o), mkrsrc(F4, DF * FS * 16), (unsigned)(k * FS + (row0 >> 1) + ff) * 16u, 0, FAUX);
    }
}
#define A3S(k) do {} while (0)
__device__ __forceinline__ void a3_unit(const float4* F4, const u16* x, int row0, __amdgpu_buffer_rsrc_t rx1, __amdgpu_buffer_rsrc_t rh, RowLds& L, const float4* tw, unsigned long long* dbg = nullptr) {
    const int tid = threadIdx.x, wave = tid >> 6, lane = tid & 63;
    float4 fv[3];
#pragma unroll
    for (int c = 0; c < 3; ++c) {
        const int it = min(tid + 512 * c, DF * 4 - 1);
        fv[c] = F4[(size_t)(it >> 2) * FS + (row0 >> 1) + (it & 3)];
    }
    const float4 twv = tw[min(tid, 319)];
#pragma unroll
    for (int c = 0; c < 3; ++c) {
        const int it = tid + 512 * c, k = it >> 2, ff = it & 3;
        float4 v = fv[c];
        if (k == 0 || k == 320) { v.y = 0.f; v.w = 0.f; }
        if (it < DF * 4) L.bA[ff][k] = make_float2(v.x - v.w, v.y + v.z);
        if (k > 0 && k < 320) L.bA[ff][640 - k] = make_float2(v.x + v.w, v.z - v.y);
    }
    if (tid < 320) ((float4*)L.tw)[tid] = twv;
    unsigned xa[10], xb[10];
    {
        const u16* xp = x + (size_t)(row0 + 2 * (wave & 3)) * D_ + lane;
#pragma unroll
        for (int r = 0; r < 10; ++r) { xa[r] = xp[64 * r]; xb[r] = xp[D_ + 64 * r]; }
    }
    __syncthreads();
    A3S(0);
    const int f = tid / 80, i = tid % 80;
    if (tid < 320) {
        float2 u[8];
#pragma unroll
        for (int r = 0; r < 8; ++r) u[r] = L.bA[f][i + 80 * r];
        dft8<1>(u);
#pragma unroll
        for (int r = 0; r < 8; ++r) L.bB[f][8 * i + r] = u[r];
    }
    __syncthreads();
    A3S(1);
    if (tid < 320) {
        const int k = i & 7, j = (i >> 3) * 64 + k;
        float2 u[8];
#pragma unroll
        for (int r = 0; r < 8; ++r) { u[r] = L.bB[f][i + 80 * r]; if (r) u[r] = ctw<1>(u[r], L.tw[10 * k * r]); }
        dft8<1>(u);
#pragma unroll
        for (int r = 0; r < 8; ++r) L.bA[f][j + 8 * r] = u[r];
    }
    __syncthreads();
    A3S(2);
    if (wave < 4) {
        float2 u[10];
#pragma unroll
        for (int r = 0; r < 10; ++r) { u[r] = L.bA[wave][lane + 64 * r]; if (r) u[r] = ctw<1>(u[r], L.tw[lane * r]); }
        dft10<1>(u);
        char* st = (char*)L.bB + wave * 5120;
        float a[10], b[10];
#pragma unroll
        for (int r = 0; r < 10; ++r) {
            a[r] = bfr(lo2f(xa[r]) + bfr(u[r].x));
            b[r] = bfr(lo2f(xb[r]) + bfr(u[r].y));
            *(u16*)(st + (lane + 64 * r) * 2) = f2bf(a[r]); *(u16*)(st + 1280 + (lane + 64 * r) * 2) = f2bf(b[r]);
        }
        A3S(3);
        ln_row(a); ln_row(b);
        A3S(4);
#pragma unroll
        for (int r = 0; r < 10; ++r) { *(u16*)(st + 2560 + (lane + 64 * r) * 2) = f2bf(a[r]); *(u16*)(st + 3840 + (lane + 64 * r) * 2) = f2bf(b[r]); }
        const unsigned base = (unsigned)(row0 + 2 * wave) * (D_ * 2);
#pragma unroll
        for (int c = 0; c < 3; ++c) {
            if (c < 2 || lane < 32) {
                const int id = c * 64 + lane, rr = id >= 80, ch = id - 80 * rr, row = (row0 + 2 * wave + rr) & 31;
                st128(*(const u32x4*)(st + 2560 + id * 16), rh, (unsigned)((ch >> 3) * 4096 + row * 128 + (((ch & 7) ^ (row & 7)) << 4)), (unsigned)(row0 >> 5) * 40960u, 16);
                st128(*(const u32x4*)(st + id * 16), rx1, id * 16u, base, 16);
            }
        }
    }
}

constexpr int FL_NEAR = 16;
constexpr int FL_ROT = 40;
__device__ __forceinline__ unsigned xcc_id() { unsigned x; asm volatile("s_getreg_b32 %0, hwreg(HW_REG_XCC_ID)" : "=s"(x)); return x & 15u; }
__global__ __launch_bounds__(320) void k1_kernel(const u16* x, const u16* Wq, const u16* Wk, const u16* wr, const u16* wi, char* ws, char* win) {
    __shared__ __attribute__((aligned(16))) RowLds L;
    const int b = (int)blockIdx.x - 16, tid = threadIdx.x;
    if (b < 0) {
        unsigned* best = (unsigned*)&L;
        const __amdgpu_buffer_rsrc_t rw = mkrsrc(win + (b < -8 ? W_PROBE : W_END), 2 * PAIR);
        const unsigned base = (blockIdx.x & 7u) * 128u + (tid >> 6) * 1664u;
        if (tid < 4) best[tid] = 0u;
        __syncthreads();
        unsigned mn[4] = {~0u, ~0u, ~0u, ~0u};
        for (int r = 0; r < 4; ++r) {
#pragma unroll
            for (int i = 0; i < 4; ++i) __builtin_amdgcn_raw_buffer_store_b32(0u, rw, base + i * HALF, 0, 16);
            asm volatile("s_waitcnt vmcnt(0)" ::: "memory");
#pragma unroll
            for (int i = 0; i < 4; ++i) {
                const unsigned long long t1 = __builtin_amdgcn_s_memtime();
                asm volatile("s_waitcnt lgkmcnt(0)" ::: "memory");
                unsigned v = __builtin_amdgcn_raw_buffer_load_b32(rw, base + i * HALF, 0, 16);
                asm volatile("s_waitcnt vmcnt(0)" : "+v"(v) :: "memory");
                const unsigned long long t2 = __builtin_amdgcn_s_memtime();
                mn[i] = min(mn[i], (unsigned)(t2 - t1) + v);
            }
        }
        if ((tid & 63) == 0) {
#pragma unroll
            for (int i = 0; i < 4; ++i) atomicAdd(&best[i], min(mn[i], 1000u));
        }
        __syncthreads();
        if (tid == 0) ((unsigned*)(ws + OFF_CTL))[FL_NEAR + (xcc_id() & 7u) + (blockIdx.x & 8u)] = min(best[1], best[3]) < min(best[0], best[2]) ? (best[3] < best[1] ? 3u : 1u) : (best[2] < best[0] ? 2u : 0u);
    } else if (b < 256) {
        L.tw[tid] = tw640_at(tid); L.tw[tid + 320] = tw640_at(tid + 320);
        a1_unit(mkrsrc(x, S_ * D_ * 2), b * 8, (float4*)(ws + OFF_F), L, tid);
    } else if (b < 256 + 128) {
        u16* tf = (u16*)&L;
        const int s0 = (b - 256) * 16;
        u32x4 v[5];
#pragma unroll
        for (int i = 0; i < 5; ++i) {
            const int id = min(tid + 320 * i, 1283);
            v[i] = ((const u32x4*)((id < 642 ? wr : wi) + (size_t)s0 * DF))[id < 642 ? id : id - 642];
        }
#pragma unroll
        for (int i = 0; i < 5; ++i) ((u32x4*)tf)[min(tid + 320 * i, 1283)] = v[i];
        __syncthreads();
        const __amdgpu_buffer_rsrc_t rd = mkrsrc(ws + OFF_WFT, DF * S_ * 4);
        for (int i = tid; i < DF * 4; i += 320) {
            const int k = i >> 2, sq = (i & 3) * 4;
            u32x4 o;
#pragma unroll
            for (int j = 0; j < 4; ++j) o[j] = (unsigned)tf[(sq + j) * DF + k] | ((unsigned)tf[16 * DF + (sq + j) * DF + k] << 16);
            st128(o, rd, (unsigned)(k * S_ + s0 + sq) * 4u, 0, WAUX);
        }
    } else if (b < 256 + 128 + 20) {
        const int j = b - 384, i = j * 256 + tid;
        if (tid < 256) {
            const int m = j < 7 ? 32 * (tid & 7) * (j + 1) : j < 14 ? 4 * (tid & 63) * (j - 6) : j < 17 ? tid * (j - 13) : (tid + 256) * (j - 16);
            double sn, cs; sincospi(-2.0 * (double)m / 2048.0, &sn, &cs);
            ((float2*)(ws + OFF_TW2048))[((j >> 1) * 256 + tid) * 2 + (j & 1)] = make_float2((float)cs, (float)sn);
            if (i < 640) ((float2*)(ws + OFF_TW640))[i] = tw640_at(i);
        }
        if (i == 0) *(unsigned*)(ws + OFF_CTL) = 0u;
    } else {
        const int t = b - 404;
        qkv_tile<4>(t / 160, t % 160, t < 160 ? Wq : Wk, ws, (u16 (*)[66])&L);
    }
}

#define PD(i) ((i) + ((i) >> 4))
template <int SG> __device__ __forceinline__ void fft2048(float2 (&u)[8], float2* b0, float2* b1, const float2 (&tw)[20], int tid) {
    dft8<SG>(u);
#pragma unroll
    for (int r = 0; r < 8; ++r) b0[PD(8 * tid + r)] = u[r];
    __syncthreads();
    {
        const int k = tid & 7, j = (tid >> 3) * 64 + k;
#pragma unroll
        for (int r = 0; r < 8; ++r) { u[r] = b0[PD(tid + 256 * r)]; if (r) u[r] = ctw<SG>(u[r], tw[r - 1]); }
        dft8<SG>(u);
#pragma unroll
        for (int r = 0; r < 8; ++r) b1[PD(j + 8 * r)] = u[r];
    }
    __syncthreads();
    {
        const int k = tid & 63, j = (tid >> 6) * 512 + k;
#pragma unroll
        for (int r = 0; r < 8; ++r) { u[r] = b1[PD(tid + 256 * r)]; if (r) u[r] = ctw<SG>(u[r], tw[6 + r]); }
        dft8<SG>(u);
#pragma unroll
        for (int r = 0; r < 8; ++r) b0[PD(j + 64 * r)] = u[r];
    }
    __syncthreads();
#pragma unroll
    for (int hh = 0; hh < 2; ++hh) {
        const int i = tid + 256 * hh;
        float2 v0 = b0[PD(i)], v1 = ctw<SG>(b0[PD(i + 512)], tw[14 + 3 * hh]), v2 = ctw<SG>(b0[PD(i + 1024)], tw[15 + 3 * hh]), v3 = ctw<SG>(b0[PD(i + 1536)], tw[16 + 3 * hh]);
        dft4<SG>(v0, v1, v2, v3);
        u[hh] = v0; u[hh + 2] = v1; u[hh + 4] = v2; u[hh + 6] = v3;
    }
}
#define A2S(k) do {} while (0)
__global__ __launch_bounds__(256) void a2_kernel(const u16* Wv, const u16* Wo, char* ws, char* win) {
    __shared__ float2 bb[2][2048 + 128];
    float2* b0 = bb[0]; float2* b1 = bb[1];
    const int tid = threadIdx.x;
    if (blockIdx.x >= DF) {
        const int t = blockIdx.x - DF;
        A2S(0);
        if (t < 160) qkv_tile<4>(2, t, Wv, ws, (u16 (*)[66])b0);
        else wo_tile<2>(t - 160, Wo, ws, (u16 (*)[66])b0);
        A2S(4);
        return;
    }
    A2S(0);
    const float4* twg = (const float4*)(ws + OFF_TW2048);
    float2* f = (float2*)(ws + OFF_F) + (size_t)blockIdx.x * (2 * FS);
    const unsigned* w = (const unsigned*)(ws + OFF_WFT) + (size_t)blockIdx.x * S_;
    float2 u[8], tw[20];
    unsigned wv[8];
#pragma unroll
    for (int r = 0; r < 8; ++r) u[r] = f[tid + 256 * r];
#pragma unroll
    for (int r = 0; r < 10; ++r) { const float4 t = twg[r * 256 + tid]; tw[2 * r] = make_float2(t.x, t.y); tw[2 * r + 1] = make_float2(t.z, t.w); }
#pragma unroll
    for (int r = 0; r < 8; ++r) wv[r] = w[tid + 256 * r];
    {
        const u32x4 sv = {0xFFFFFFFFu, 0xFFFFFFFFu, 0xFFFFFFFFu, 0xFFFFFFFFu};
        const __amdgpu_buffer_rsrc_t rw = mkrsrc(win, W_END);
        for (int g = blockIdx.x * 256 + tid; g < (int)((W_PROBE + W_END - W_ZM) / 16); g += DF * 256)
            st128(sv, rw, g * 16u + (g < (int)(W_PROBE / 16) ? 0u : W_ZM - W_PROBE), 0, A2AUX);
        if (blockIdx.x == 0 && tid >= 32 && tid < 192) ((u32x4*)(ws + OFF_CTL))[tid] = (u32x4){0u, 0u, 0u, 0u};
        if (blockIdx.x == 0 && tid == 0) ((unsigned*)(ws + OFF_CTL))[FL_ROT] = xcc_id();
    }
    A2S(1);
    fft2048<-1>(u, b0, b1, tw, tid);
    A2S(2);
    const float sc = 1.f / ((float)S_ * (float)D_);
#pragma unroll
    for (int r = 0; r < 8; ++r) u[r] = cmul(u[r], make_float2(lo2f(wv[r]) * sc, hi2f(wv[r]) * sc));
    fft2048<1>(u, b1, b0, tw, tid);
    A2S(3);
    {
        const __amdgpu_buffer_rsrc_t rf = mkrsrc(f, 2048 * 8);
#pragma unroll
        for (int r = 0; r < 8; ++r) __builtin_amdgcn_raw_buffer_store_b64((u32x2){__float_as_uint(u[r].x), __float_as_uint(u[r].y)}, rf, tid * 8u, r * 2048u, A2AUX);
    }
    A2S(4);
}

struct RecArgs {
    unsigned* ctl; char* win; const u16* mem0; const u16* xin; u16* out;
    float4* Fnext;
};
constexpr unsigned O_H = (unsigned)OFF_H, O_X1 = (unsigned)OFF_X1, O_Z = (unsigned)OFF_Z, O_Z2 = (unsigned)OFF_Z2, O_PT = (unsigned)OFF_PT, O_QK = (unsigned)OFF_QK;
#define MXV 0x00808080808080AAull
#define MXQ 0x0008080808080000ull
#define NTAIL 5
static_assert(NTAIL >= 3, "the owners release segments up to NSEG - 4");
constexpr int TAILS = NSEG - NTAIL;
static_assert(!((MXV | MXQ) >> TAILS) && !(MXV & 1) && !(MXQ & 3), "segment sets");
constexpr int NREC = 96 + NOWN, NQT = NREC + NOWN, NXT = NQT + 32, NWRK = NXT + 32;
constexpr unsigned SPIN_CAP = 400000u;
__device__ __forceinline__ bool spin_fail(unsigned& spins, unsigned* tmo) {
    if (++spins > SPIN_CAP) { __hip_atomic_store(tmo, 1u, __ATOMIC_RELAXED, __HIP_MEMORY_SCOPE_AGENT); return true; }
    return (spins & 1023u) == 0u && __builtin_amdgcn_readfirstlane(__hip_atomic_load(tmo, __ATOMIC_RELAXED, __HIP_MEMORY_SCOPE_AGENT));
}
#define SST(k) do {} while (0)
#define LSEL 1
#define RT(k) do {} while (0)
#define RTS() do {} while (0)
#define B3ATTR
#define MTA(k)
#define MTRT() do {} while (0)
#define MT(k) do {} while (0)
#define MTS() do {} while (0)
#define FC(x) do {} while (0)
#define DQC 1200
#define DVC 1200
#define M1 44
#define MZ 40
#define MZP 20
#define M2 234
#define LTP (LT0 + 15)
#define LTZ 340
#define PEN 280
#define KSL 1
#define SVO 64
#define DZP 55
#define LT0 342
#define SAD 1
#define PEN2 80
#define PEN1 50
#define PEN0 150
#define DDM2 0
#define DRA 700
#define TSLP 1
#define PFS 0
#define PFOFF 1
#define OPR 2
#define BSPARSE 0
#define ZLAG 4
#define TKP 0
#define DPS 12
#define RZ 63
#define RQ 63
#define R2 63
#define DXT 3000
#define SSW 99
#define LTM 12
#define XS0 10
#define XSU 8
#define XSM 32
#define LTU 4
#define LTD 1
#define QUIET 0
#define QUAD 1
#define LTS 3
#define XSS 3
#define TKS 3
#define LT1 450
#define FDL 2
#define ZLA 3
#define DPT2 3
#define NORND 0
#define LTX 24
#define TKE 200
#define DVO 6000
#define ZPS 4
#define HLA 8
#define PSLP 16
#define PFD 4
#define NPF 3
#define KSL2 1
#define KBQ 4
#define VB1 6
#define VB2 5
#define KR1 3
#define QB2 2
#define NB2 6
#define DZR 24
#define ML 150
#define MN 12
#define CF 190
#define DB 1500
#define DP 200
#define ZSL 1
#define NB1 3
#define DPT 3
#define DDMA 8
#define DZ2 800
#define QEARLY 2
#define OSLP 32
#define OSLP2 1
#define WSLP 64
#define DO0 12000
#define DW0 1200
#define DQT 9000
#define DQ4 700
#define ZEARLY 3
#define HSLP 4
#define RMAP 0
#define DIE 0
#define VMWAIT() do { asm volatile("" ::: "memory"); __builtin_amdgcn_s_waitcnt(0x0F70); asm volatile("" ::: "memory"); } while (0)
#define SB() __builtin_amdgcn_sched_barrier(0)
#define BAR() asm volatile("s_waitcnt lgkmcnt(0)\n\ts_barrier" ::: "memory")
#define PIN1(a) asm volatile("" : "+v"(a) :: "memory")
#define PIN2(a, b) asm volatile("" : "+v"(a), "+v"(b) :: "memory")
#define BAR0() asm volatile("s_barrier" ::: "memory")
__device__ __forceinline__ unsigned fresh_lane() { unsigned l; asm volatile("v_mbcnt_lo_u32_b32 %0, -1, 0\n\tv_mbcnt_hi_u32_b32 %0, -1, %0" : "=v"(l)); return l; }
__device__ __forceinline__ void wait_until(unsigned long long t) { while ((long long)(__builtin_amdgcn_s_memtime() - t) < 0) __builtin_amdgcn_s_sleep(1); }
struct Clk { unsigned ta, ma, cpq, qpc; };
__device__ __forceinline__ int sx24(unsigned v) { return (int)(v << 8) >> 8; }
#define WAIT_FINE \
        "s_memrealtime vcc\n\t" \
        "s_memtime s[100:101]\n\t" \
        "s_brev_b32 %[fi], -2\n\t" \
        "s_waitcnt lgkmcnt(0)\n\t" \
        "s_mov_b32 %[rp], vcc_lo\n\t" \
        "s_lshl_b32 vcc_hi, vcc_lo, 2\n\t" \
        "s_sub_u32 %[x], %[t], vcc_hi\n\t" \
        "s_bfe_i32 %[x], %[x], 0x180000\n\t" \
        "s_min_i32 %[x], %[x], 4000\n\t" \
        "s_max_i32 %[x], %[x], -4000\n\t" \
        "s_add_u32 %[t], vcc_hi, %[x]\n\t" \
        "s_mul_i32 %[x], %[x], %[cq]\n\t" \
        "s_ashr_i32 %[x], %[x], 8\n\t" \
        "s_sub_u32 %[tm], %[t], %[ta]\n\t" \
        "s_bfe_i32 %[tm], %[tm], 0x180000\n\t" \
        "s_min_i32 %[tm], %[tm], 1000000\n\t" \
        "s_max_i32 %[tm], %[tm], -1000000\n\t" \
        "s_mul_i32 %[tm], %[tm], %[cq]\n\t" \
        "s_ashr_i32 %[tm], %[tm], 8\n\t" \
        "s_add_u32 %[tm], %[tm], %[ma]\n\t" \
        "s_sub_u32 %[tm], %[tm], s100\n\t" \
        "s_lshr_b32 vcc_hi, %[cq], 4\n\t" \
        "s_sub_u32 vcc_hi, %[x], vcc_hi\n\t" \
        "s_max_i32 %[tm], %[tm], vcc_hi\n\t" \
        "s_min_i32 %[tm], %[tm], %[x]\n\t" \
        "s_add_u32 %[tm], %[tm], s100\n" \
        "1:\n\t" \
        "s_memrealtime vcc\n\t" \
        "s_memtime s[100:101]\n\t" \
        "s_waitcnt lgkmcnt(0)\n\t" \
        "s_cmp_eq_u32 vcc_lo, %[rp]\n\t" \
        "s_cbranch_scc1 2f\n\t" \
        "s_mov_b32 %[rp], vcc_lo\n\t" \
        "s_lshl_b32 %[x], vcc_lo, 2\n\t" \
        "s_sub_u32 %[x], %[t], %[x]\n\t" \
        "s_bfe_i32 %[x], %[x], 0x180000\n\t" \
        "s_mul_i32 %[x], %[x], %[cq]\n\t" \
        "s_ashr_i32 %[x], %[x], 8\n\t" \
        "s_add_u32 %[x], %[x], s100\n\t" \
        "s_sub_u32 s101, %[x], %[tm]\n\t" \
        "s_cmp_lt_i32 s101, %[fi]\n\t" \
        "s_cselect_b32 %[tm], %[x], %[tm]\n\t" \
        "s_mov_b32 %[fi], 0\n" \
        "2:\n\t" \
        "s_sub_u32 %[x], %[tm], s100\n\t" \
        "s_cmp_gt_i32 %[x], 260\n\t" \
        "s_cbranch_scc1 1b\n\t" \
        "s_cmp_eq_u32 %[fi], 0\n\t" \
        "s_cselect_b32 %[ta], %[t], %[ta]\n\t" \
        "s_cselect_b32 %[ma], %[tm], %[ma]\n" \
        "3:\n\t" \
        "s_memtime s[100:101]\n\t" \
        "s_waitcnt lgkmcnt(0)\n\t" \
        "s_sub_u32 %[x], %[tm], s100\n\t" \
        "s_cmp_gt_i32 %[x], 92\n\t" \
        "s_cbranch_scc1 3b\n\t" \
        "s_max_i32 %[x], %[x], 0\n\t" \
        "s_andn2_b32 %[x], %[x], 3\n\t" \
        "s_sub_u32 %[x], 104, %[x]\n\t" \
        "s_getpc_b64 vcc\n\t" \
        "s_add_u32 vcc_lo, vcc_lo, %[x]\n\t" \
        "s_addc_u32 vcc_hi, vcc_hi, 0\n\t" \
        "s_setpc_b64 vcc\n\t" \
        ".rept 23\n\t" \
        "s_nop 0\n\t" \
        ".endr\n\t"
#define WAIT_FINE_OUT [tm] "=&s"(tm), [rp] "=&s"(rp), [x] "=&s"(x), [fi] "=&s"(fi), [t] "+s"(t), [ta] "+s"(c.ta), [ma] "+s"(c.ma)
__device__ __forceinline__ void wait_fine(unsigned t, Clk& c) {
    unsigned tm, rp, x, fi;
    asm volatile(WAIT_FINE : WAIT_FINE_OUT : [cq] "s"(c.cpq) : "vcc", "scc", "s100", "s101", "memory");
}
__device__ __forceinline__ void wait_fine_bar(unsigned t, Clk& c) {
    unsigned tm, rp, x, fi;
    asm volatile(WAIT_FINE "s_barrier" : WAIT_FINE_OUT : [cq] "s"(c.cpq) : "vcc", "scc", "s100", "s101", "memory");
}
__device__ __forceinline__ void wait_cyc_bar(unsigned tm) {
    unsigned x;
    asm volatile(
        "3:\n\t"
        "s_memtime s[100:101]\n\t"
        "s_waitcnt lgkmcnt(0)\n\t"
        "s_sub_u32 %0, %1, s100\n\t"
        "s_cmp_gt_i32 %0, 92\n\t"
        "s_cbranch_scc1 3b\n\t"
        "s_max_i32 %0, %0, 0\n\t"
        "s_andn2_b32 %0, %0, 3\n\t"
        "s_sub_u32 %0, 104, %0\n\t"
        "s_getpc_b64 vcc\n\t"
        "s_add_u32 vcc_lo, vcc_lo, %0\n\t"
        "s_addc_u32 vcc_hi, vcc_hi, 0\n\t"
        "s_setpc_b64 vcc\n\t"
        ".rept 23\n\t"
        "s_nop 0\n\t"
        ".endr\n\t"
        "s_barrier"
        : "=&s"(x) : "s"(tm) : "vcc", "scc", "s100", "s101", "memory");
}
__device__ __forceinline__ unsigned cyc_at(const Clk& c, int d) {
    d = min(d, 20000);
    asm volatile("" : "+s"(d));
    return c.ma + (unsigned)((max(d, -4000) * (int)c.cpq) >> 8);
}
__device__ __forceinline__ void clk_rate(Clk& c, unsigned ta0, unsigned ma0) {
    const int dq = sx24(c.ta - ta0);
    if (dq > 200) {
        const float r = fminf(fmaxf((float)(c.ma - ma0) * __builtin_amdgcn_rcpf((float)dq), 1.f), 8.f);
        asm volatile("s_nop 1\n\tv_readfirstlane_b32 %0, %2\n\tv_readfirstlane_b32 %1, %3\n\ts_nop 3" : "=s"(c.cpq), "=s"(c.qpc)
                     : "v"((unsigned)(256.f * r + 0.5f)), "v"((unsigned)(65536.f * __builtin_amdgcn_rcpf(r))));
    }
}
__device__ __forceinline__ void clk_init(Clk& c) {
    c.ta = 4u * (unsigned)__builtin_amdgcn_s_memrealtime(); c.ma = (unsigned)__builtin_amdgcn_s_memtime();
    for (int i = 0; i < 2; ++i) {
        const unsigned ta0 = c.ta, ma0 = c.ma;
        wait_fine(c.ta + (i ? 360u : 60u), c);
        clk_rate(c, ta0, ma0);
    }
}
__device__ __forceinline__ unsigned sched_lane(int s) { const unsigned l = fresh_lane(); return (l & 31u) * 128u + (l >> 5) * 4u; }
__device__ __forceinline__ int read_sched(const RecArgs& a, __amdgpu_buffer_rsrc_t rwin, int s, unsigned vo, unsigned to, unsigned f, unsigned g0, int& lt, int& xs, unsigned& fc) {
    int d = (int)((f - g0) & 0xFFFFFFu);
    if (__builtin_expect(__any((f >> 24) != (unsigned)s) || s == 0, 0)) {
        for (unsigned spins = 0; __any((f >> 24) != (unsigned)s);) {
            FC(fc);
            if (spin_fail(spins, a.ctl)) break;
            __builtin_amdgcn_s_sleep(TSLP);
            asm volatile("" ::: "memory");
            if ((f >> 24) != (unsigned)s) f = __builtin_amdgcn_raw_buffer_load_b32(rwin, vo, to, 16);
        }
        const unsigned f0 = __builtin_amdgcn_readfirstlane(f) - 0x100000u;
        d = (int)((f - f0) & 0xFFFFFFu) + (int)((f0 - g0) & 0xFFFFFFu);
    }
    d = max(d, __builtin_amdgcn_update_dpp(0, d, 0xB1, 0xF, 0xF, true));
    d = max(d, __builtin_amdgcn_update_dpp(0, d, 0x4E, 0xF, 0xF, true));
    d = max(d, __builtin_amdgcn_update_dpp(0, d, 0x141, 0xF, 0xF, true));
    d = max(d, __builtin_amdgcn_update_dpp(0, d, 0x140, 0xF, 0xF, true));
    d = max(d, __builtin_amdgcn_update_dpp(0, d, 0x142, 0xA, 0xF, true));
    d = max(__builtin_amdgcn_readlane(d, 16), __builtin_amdgcn_readlane(d, 48));
    const int l0 = lt, x0 = xs;
    d -= 0x400000;
    const bool miss = d > l0 + min(x0 + LTM, LTX);
    if (s >= SAD) { lt = miss ? l0 : min(max(d, l0 - 1 - ((l0 - d) >> LTS)), l0 + LTU); xs = miss ? min(x0 + XSU, XSM) : max(x0 - 1 - (x0 >> XSS), 0); }
    if (s == 1) lt = LT0;
    return 0x400000 + (s < SAD ? d + PEN0 : miss ? d + PEN : l0 + x0) + M1 + M2 + x0;
}
__device__ __forceinline__ unsigned nosent1(unsigned v) { return min(v, 0xFFFFFFFEu); }
__device__ __forceinline__ unsigned smax(u32x4 v) { return max(v[1], v[3]); }
constexpr int QS = 272;
constexpr int TQS = 1152;
constexpr int L_Q = 10240, L_K = L_Q + 2 * TQS, L_S = L_K + 2 * TQS, L_TOK = L_S + 256, L_H = L_TOK + 49152, L_END = L_H + 2 * 40960;
typedef __attribute__((ext_vector_type(4))) short v4i16_t;
__device__ __forceinline__ u32x2 ldtr(const char* p) { return __builtin_bit_cast(u32x2, __builtin_amdgcn_ds_read_tr16_b64_v4i16((LAS v4i16_t*)p)); }
__device__ __forceinline__ u32x2 ldtr1(const LAS char* p) { return __builtin_bit_cast(u32x2, __builtin_amdgcn_ds_read_tr16_b64_v4i16((LAS v4i16_t*)p)); }
__device__ __forceinline__ u32x4 ldtr2(const LAS char* p0, const LAS char* p1) {
    const u32x2 x = __builtin_bit_cast(u32x2, __builtin_amdgcn_ds_read_tr16_b64_v4i16((LAS v4i16_t*)p0)), y = __builtin_bit_cast(u32x2, __builtin_amdgcn_ds_read_tr16_b64_v4i16((LAS v4i16_t*)p1));
    return (u32x4){x[0], x[1], y[0], y[1]};
}
__device__ __forceinline__ u32x4 ldtr2(const char* p0, const char* p1) { const u32x2 x = ldtr(p0), y = ldtr(p1); return (u32x4){x[0], x[1], y[0], y[1]}; }

__device__ __forceinline__ unsigned poll_lim(int& budget) {
    --budget;
    unsigned lim = budget > 0 ? 0xFFFFFFFEu : 0xFFFFFFFFu;
    asm volatile("" : "+s"(lim));
    return lim;
}
#define GATHER \
        "buffer_load_dwordx4 %[vo], %[rw], %[s0] offen sc1 lds\n\t" \
        "buffer_load_dwordx4 %[vo], %[rw], %[s0] offen offset:1024 sc1 lds\n\t" \
        "buffer_load_dwordx4 %[vo], %[rw], %[s0] offen offset:2048 sc1 lds\n\t" \
        "buffer_load_dwordx4 %[vo], %[rw], %[s0] offen offset:3072 sc1 lds\n\t" \
        "s_movk_i32 m0, 0x1000\n\t" \
        "s_nop 0\n\t" \
        "buffer_load_dwordx4 %[vo], %[rw], %[s4] offen sc1 lds\n\t" \
        "buffer_load_dwordx4 %[vo], %[rw], %[s4] offen offset:1024 sc1 lds\n\t" \
        "buffer_load_dwordx4 %[vo], %[rw], %[s4] offen offset:2048 sc1 lds\n\t" \
        "buffer_load_dwordx4 %[vo], %[rw], %[s4] offen offset:3072 sc1 lds\n\t" \
        "s_movk_i32 m0, 0x2000\n\t" \
        "s_nop 0\n\t" \
        "buffer_load_dwordx4 %[vo], %[rw], %[s1] offen sc1 lds\n\t" \
        "buffer_load_dwordx4 %[vo], %[rw], %[s1] offen offset:1024 sc1 lds"
#define GATHER_IN [vo] "v"(vo), [rw] "s"(rwin), [s0] "s"(so0), [s4] "s"(so0 + 4096u), [s1] "s"(so1)
__device__ __forceinline__ void gather_dma(__amdgpu_buffer_rsrc_t rwin, unsigned vo, unsigned so0, unsigned so1) {
    asm volatile("s_mov_b32 m0, 0\n\ts_nop 0\n\t" GATHER :: GATHER_IN : "m0", "memory");
}
__device__ __forceinline__ void gather_at(unsigned t, Clk& c, __amdgpu_buffer_rsrc_t rwin, unsigned vo, unsigned so0, unsigned so1) {
    unsigned tm, rp, x, fi;
    asm volatile("s_mov_b32 m0, 0\n\t" WAIT_FINE GATHER : WAIT_FINE_OUT : [cq] "s"(c.cpq), GATHER_IN : "vcc", "scc", "s100", "s101", "m0", "memory");
}
__device__ __forceinline__ void dma4k(__amdgpu_buffer_rsrc_t r, unsigned vo, unsigned so, unsigned lds) {
    asm volatile(
        "s_mov_b32 m0, %3\n\t"
        "s_nop 0\n\t"
        "buffer_load_dwordx4 %0, %1, %2 offen sc1 lds\n\t"
        "buffer_load_dwordx4 %0, %1, %2 offen offset:1024 sc1 lds\n\t"
        "buffer_load_dwordx4 %0, %1, %2 offen offset:2048 sc1 lds\n\t"
        "buffer_load_dwordx4 %0, %1, %2 offen offset:3072 sc1 lds"
        :: "v"(vo), "s"(r), "s"(so), "s"(lds) : "m0", "memory");
}
__device__ __forceinline__ int bs2(int c) { return (c & ~3) | ((c & 1) << 1) | ((c >> 1) & 1); }
__device__ __forceinline__ void gather_mem0(const RecArgs& a, char* memL, int t0, int nt) {
    for (int c = t0; c < 640; c += nt) {
        const unsigned* src = (const unsigned*)(a.mem0 + ((c & 6) + ((c >> 3) & 1)) * D_ + (c >> 4) * 16 + (c & 1) * 2);
        *(u32x4*)(memL + c * 16) = (u32x4){src[0], src[2], src[4], src[6]};
    }
}
__device__ __forceinline__ f32x2 hadd(f32x4 c) { return (f32x2){c[0], c[1]} + (f32x2){c[2], c[3]}; }
__device__ __forceinline__ unsigned hpack(f32x4 c) { const f32x2 y = hadd(c); return pack2(y[0], y[1]); }
__device__ __forceinline__ u32x8 cat8(u32x4 lo, u32x4 hi) { return (u32x8){lo[0], lo[1], lo[2], lo[3], hi[0], hi[1], hi[2], hi[3]}; }
__device__ __forceinline__ u32x8 ldw(const u16* p) { return cat8(*(const u32x4*)p, *(const u32x4*)(p + 512)); }
__device__ __forceinline__ u32x4 half8(const u32x8& v, int u) { return u ? (u32x4){v[4], v[5], v[6], v[7]} : (u32x4){v[0], v[1], v[2], v[3]}; }
__device__ __forceinline__ void dma1k(__amdgpu_buffer_rsrc_t r, unsigned vo, unsigned so, unsigned lds) {
    asm volatile("s_mov_b32 m0, %3\n\ts_nop 0\n\tbuffer_load_dwordx4 %0, %1, %2 offen sc1 lds" :: "v"(vo), "s"(r), "s"(so), "s"(lds) : "m0", "memory");
}
__device__ __forceinline__ void stage_h(__amdgpu_buffer_rsrc_t rws, int seg, unsigned lds, int t) {
    const unsigned wo = __builtin_amdgcn_readfirstlane(t >> 6) * 1024;
#pragma unroll
    for (int i = 0; i < 5; ++i) dma1k(rws, (t & 63) * 16u, O_H + (unsigned)seg * 40960u + wo + i * 8192u, lds + wo + i * 8192u);
}
__device__ __forceinline__ void wait_h(const RecArgs& a, __amdgpu_buffer_rsrc_t rfl, int seg, int& hrdy) {
    unsigned lim = 63u;
    if (__builtin_expect(hrdy <= seg, 0)) for (unsigned spins = 0;;) {
        asm volatile("" ::: "memory");
        const u32x4 f = __builtin_amdgcn_raw_buffer_load_b128(rfl, min(fresh_lane(), lim) * 128u, W_H, 16);
        const unsigned long long miss = __ballot((f[0] | f[1] | f[2] | f[3]) != 0u);
        hrdy = miss ? __builtin_ctzll(miss) : (int)lim + 1;
        if (hrdy > seg) break;
        if (spin_fail(spins, a.ctl)) break;
        lim = (unsigned)min(seg + HLA, 63);
        __builtin_amdgcn_s_sleep(HSLP);
    }
}
template <int CTRL> __device__ __forceinline__ unsigned dppu(unsigned v) { return (unsigned)__builtin_amdgcn_mov_dpp((int)v, CTRL, 0xF, 0xF, true); }
template <int CTRL> __device__ __forceinline__ u32x4 ror4(u32x4 v) { return (u32x4){dppu<CTRL>(v[0]), dppu<CTRL>(v[1]), dppu<CTRL>(v[2]), dppu<CTRL>(v[3])}; }
template <int N> struct IC { static constexpr int v = N; };
__device__ __forceinline__ float xmax16(float v) {
    auto q = __builtin_amdgcn_permlane16_swap(__float_as_uint(v), __float_as_uint(v), false, false);
    return fmx(__uint_as_float(q[0]), __uint_as_float(q[1]));
}
__device__ __forceinline__ float xsum16(float v) {
    auto q = __builtin_amdgcn_permlane16_swap(__float_as_uint(v), __float_as_uint(v), false, false);
    return __uint_as_float(q[0]) + __uint_as_float(q[1]);
}
__device__ __forceinline__ float addswap16(float a, float b) {
    auto r = __builtin_amdgcn_permlane16_swap(__float_as_uint(a), __float_as_uint(b), false, false);
    return __uint_as_float(r[0]) + __uint_as_float(r[1]);
}
#define LANES() \
    const int lane = (int)fresh_lane(), tid = w * 64 + lane, fr = lane & 15, fq = lane >> 4; \
    const int sidx = (lane & 2) ? 0xEEEE : 0x4444; \
    const int ma0 = fq * 256 + (fr & 1) * 128 + (fr >> 1) * 16; \
    const int prow = 16 * ((fr >> 2) & 1) + 4 * (fr >> 3) + (fr & 3), hoff = fr * 128; \
    (void)tid; (void)sidx; (void)ma0; (void)prow; (void)hoff

__device__ __forceinline__ int seg_after(unsigned long long m, int j) { m = j >= 63 ? 0ull : m >> (j + 1); return m ? j + 1 + __builtin_ctzll(m) : NSEG; }
__device__ __forceinline__ int seg_done(unsigned long long m, int f0, int f1) { return min(seg_after(~m, f0), seg_after(m, f1)) - 1; }
template <int OFF> __device__ __forceinline__ void ldfrag(u32x4& d, const LAS char* p) { asm volatile("ds_read_b128 %0, %1 offset:%2" : "=v"(d) : "v"(p), "n"(OFF) : "memory"); }
template <int U, class FM, class FV> __device__ __forceinline__ void project(const u32x8 (&wt)[2][10], f32x4 (&tc)[2][2], u32x4 (&bb)[8], const LAS char* c0, const LAS char* c1, const LAS char* n0, const LAS char* n1, FM mid, FV vm) {
    constexpr int t = U >> 2, k = U & 3, v = (U + 8) % 40;
    if (U == 32) mid();
    vm(IC<U>());
    const LAS char* p = U < 32 ? (k >> 1 ? c1 : c0) : (k >> 1 ? n1 : n0);
    if (U < 2) asm volatile("s_nop 0\n\ts_waitcnt lgkmcnt(7)\n\tv_mfma_f32_16x16x32_bf16 %0, %3, %2, 0\n\tv_mfma_f32_16x16x32_bf16 %1, %4, %2, 0\n\tds_read_b128 %2, %5 offset:%6"
                            : "=&v"(tc[0][k & 1]), "=&v"(tc[1][k & 1]), "+v"(bb[U & 7]) : "v"(half8(wt[0][t], k >> 1)), "v"(half8(wt[1][t], k >> 1)), "v"(p), "n"((v >> 2) * 4096 + (k & 1) * 2048) : "memory");
    else asm volatile(".rept %7\n\ts_nop 0\n\t.endr\n\ts_waitcnt lgkmcnt(7)\n\tv_mfma_f32_16x16x32_bf16 %0, %3, %2, %0\n\tv_mfma_f32_16x16x32_bf16 %1, %4, %2, %1\n\tds_read_b128 %2, %5 offset:%6"
                      : "+v"(tc[0][k & 1]), "+v"(tc[1][k & 1]), "+v"(bb[U & 7]) : "v"(half8(wt[0][t], k >> 1)), "v"(half8(wt[1][t], k >> 1)), "v"(p), "n"((v >> 2) * 4096 + (k & 1) * 2048), "n"(U < 8 ? 1 : 0) : "memory");
    if constexpr (U < 39) project<U + 1>(wt, tc, bb, c0, c1, n0, n1, mid, vm);
    else asm volatile("s_nop 7" : "+v"(tc[0][0]), "+v"(tc[0][1]), "+v"(tc[1][0]), "+v"(tc[1][1]));
}
__device__ __forceinline__ void project_end(u32x4 (&bb)[8]) {
    asm volatile("s_waitcnt lgkmcnt(0)" : "+v"(bb[0]), "+v"(bb[1]), "+v"(bb[2]), "+v"(bb[3]), "+v"(bb[4]), "+v"(bb[5]), "+v"(bb[6]), "+v"(bb[7]) :: "memory");
}
__device__ __forceinline__ void project0(u32x4 (&bb)[8], const LAS char* c0, const LAS char* c1) {
    ldfrag<0>(bb[0], c0); ldfrag<2048>(bb[1], c0); ldfrag<0>(bb[2], c1); ldfrag<2048>(bb[3], c1);
    ldfrag<4096>(bb[4], c0); ldfrag<6144>(bb[5], c0); ldfrag<4096>(bb[6], c1); ldfrag<6144>(bb[7], c1);
    project_end(bb);
}

__global__ __launch_bounds__(512, 2) B3ATTR void b3_kernel(RecArgs a) {
    extern __shared__ __attribute__((aligned(16))) char smem[];
    char* memL = smem;
    char* tokL = smem + L_TOK;
    char* hL = smem + L_H;
    const int w = __builtin_amdgcn_readfirstlane(threadIdx.x >> 6);
    const unsigned vx = blockIdx.x + __builtin_amdgcn_readfirstlane(a.ctl[FL_ROT]) + 4u * DIE;
    int bid;
    {
        const int j = blockIdx.x >> 3, x = vx & 3;
        if (!(vx & 4)) bid = j < 8 ? 8 * x + j : j < 16 ? 24 + 8 * x + j : j < 26 ? 80 + 10 * x + j : 255 - 4 * (j - 26) - x;
        else bid = j < 8 ? 64 + 8 * x + j : j < 16 ? NQT - 8 + 8 * x + j : j < 26 ? NREC - 16 + 10 * x + j : NXT - 26 + 6 * x + j;
    }
    const __amdgpu_buffer_rsrc_t rwin = mkrsrc(a.win, W_END);
    char* const ws = (char*)a.ctl;
    const __amdgpu_buffer_rsrc_t rws = mkrsrc(ws, (unsigned)OFF_WIN);
    unsigned long long tref = 0;
    const unsigned long long tstart = __builtin_amdgcn_s_memtime();
    const unsigned nh = __builtin_amdgcn_readfirstlane(a.ctl[FL_NEAR + 4 * DIE] & 1u) * HALF;
    const unsigned nz = __builtin_amdgcn_readfirstlane(a.ctl[FL_NEAR + 8 + 4 * DIE] & 1u) * HALF;
    auto quad = [&](int p) {
        const unsigned* f = a.ctl + FL_NEAR + p + 4 * DIE;
        return (unsigned)__builtin_amdgcn_readfirstlane((3u * (f[0] & 2u) + 2u * ((~f[1] & 2u) + (f[2] & 2u) + (~f[3] & 2u))) > 9u);
    };
    const unsigned mc = (vx & 3u) ^ quad(0);
    const unsigned qz = quad(8);
    auto zpair = [&](unsigned x, unsigned k) { return (((x >> 1) * 10u + k) * 2u + ((x & 1u) ^ qz)) * PAIR + nz; };
    const unsigned mo = W_MEM + mc * PAIR + nh, mo1 = W_MEM + (4u + (mc & 1u)) * PAIR + nh + (mc >> 1) * 2048u;
    const unsigned tro = W_T + (mc & 1u) * PAIR + nh + (mc >> 1) * 4096u;
    auto two = [&]() { const unsigned l = fresh_lane(); return l < 4 ? (l >> 1) * PAIR + (l & 1) * 4096u + nh : 0x40000000u; };
    const u32x4 SENT = {0xFFFFFFFFu, 0xFFFFFFFFu, 0xFFFFFFFFu, 0xFFFFFFFFu};
    const f32x4 z4 = {0.f, 0.f, 0.f, 0.f};
    int hrdy = 0, budget = (int)SPIN_CAP;
    unsigned fc0 = 0, fc1 = 0, fc2 = 0;
    Clk ck = {0u, 0u, 1600u, 10486u};
    SST(0);
    const bool xt = bid >= NXT && bid < NWRK;
    RowLds& L = *(RowLds*)smem;
    unsigned tpp = 0;
    int lt = LT1, xs = XS0;
    struct Tk { unsigned vo, tw, sl; const LAS char* sp; };
    auto tk_init = [&]() {
        const unsigned l = fresh_lane();
        Tk k = {l * 16u, two(), (l & 31u) * 128u + (l >> 5) * 4u, (const LAS char*)(memL + l * 64 + 4 + ((l >> 1) & 7) * 8)};
        PIN2(k.vo, k.tw); PIN2(k.sl, k.sp);
        return k;
    };
    auto sched2 = [&](Tk& k, int s, unsigned f) {
        const unsigned g0 = tpp - 0x400000u;
        tpp = g0 + (unsigned)read_sched(a, rwin, s, k.sl, tro + (unsigned)(s & 3) * 2u * PAIR, f, g0, lt, xs, fc1);
        k.sl &= ~4u;
        RT(6);
    };
    auto sched = [&](Tk& k, int s) { sched2(k, s, __builtin_amdgcn_raw_buffer_load_b32(rwin, k.sl, tro + (unsigned)(s & 3) * 2u * PAIR, 16)); };
    unsigned it = 0;
    auto fetch = [&](const Tk& k, int s, bool first, unsigned lim, unsigned dc, unsigned fo, bool tell) {
        const unsigned sb = (unsigned)(s & 3) * 6u * PAIR, ta0 = ck.ta, mm0 = ck.ma;
        if (__builtin_expect(s > 0, 1)) {
            if (__builtin_expect(first, 1)) { const unsigned te = cyc_at(ck, sx24(tpp - TKE - ck.ta)); while ((int)((unsigned)__builtin_amdgcn_s_memtime() - te) < 0) __builtin_amdgcn_s_sleep(1); }
            if (__builtin_expect(first, 1)) gather_at(tpp, ck, rwin, k.vo, mo + sb, mo1 + sb); else { const unsigned c = (mc + 1u + (it & 1u)) & 3u; gather_dma(rwin, k.vo, W_MEM + c * PAIR + nh + sb, W_MEM + (4u + (c & 1u)) * PAIR + nh + (c >> 1) * 2048u + sb); }
        }
        __builtin_amdgcn_s_setprio(0);
        RT(1);
        clk_rate(ck, ta0, mm0);
        unsigned tb = ck.ma - dc, tg = (unsigned)s << 24, so = W_T + (unsigned)(s & 3) * 2u * PAIR + fo;
        asm volatile("" : "+s"(tb), "+s"(tg), "+s"(so));
        asm volatile("s_waitcnt vmcnt(%1) lgkmcnt(0)\n\ts_barrier\n\ts_waitcnt vmcnt(%2)\n\ts_barrier\n\ts_waitcnt vmcnt(0)\n\ts_memtime %0\n\ts_barrier"
                     : "=s"(tref) : "n"(10 - NB1), "n"(10 - NB2) : "memory");
        MT(1);
        if (!tell) { RT(3); return; }
        unsigned x0 = *(const LAS unsigned*)k.sp, x1 = *(const LAS unsigned*)(k.sp + 4096), x2 = *(const LAS unsigned*)(k.sp + 6144);
        __builtin_amdgcn_s_waitcnt(0xC07F);
        asm volatile("" : "+s"(tref) :: "memory");
        const unsigned fw = ((ck.ta + ((((unsigned)tref - tb) * ck.qpc) >> 16)) & 0xFFFFFFu) | tg;
        __builtin_amdgcn_raw_buffer_store_b32(fw, rwin, k.tw, __any(max(max(x0, x1), x2) > lim) ? 0x40000000u : so, 16);
        RT(3);
    };
    if (bid >= 96 && (bid < NQT || bid >= NWRK)) {
        const int fi = bid >= NWRK ? 255 - bid : NQT + 15 - bid;
        for (int u = fi; u < 256; u += 56) {
            a3_unit((const float4*)(ws + OFF_F), a.xin, u * 8, mkrsrc(ws + OFF_X1, S_ * D_ * 2), mkrsrc(ws + OFF_H, S_ * D_ * 2), L, (const float4*)(ws + OFF_TW640));
            VMWAIT();
            __syncthreads();
            if (threadIdx.x == 0) __builtin_amdgcn_raw_buffer_store_b32(0u, rwin, 0u, W_H + (unsigned)((u >> 2) * 128 + (u & 3) * 4), 16);
            if (u < 96) SST(1);
            if (fi >= 56) break;
            if (u < 96) u += 40;
        }
    }
    if ((bid >= 32 && bid < 96) || xt) {
        const int hh = (xt ? bid - NXT : bid) & 31;
        u32x8 wt[2][10];
        const bool vt = bid >= 64;
        const unsigned long long mv = xt ? MXV : ~MXV;
        auto nextv = [&](int j) { return min(seg_after(mv, j), NSEG - 1); };
        const int jv1 = nextv(-1), jv2 = nextv(jv1);
        {
            LANES();
            if (!vt) gather_mem0(a, memL, tid, 512);
            if (xt || !vt) wait_until(tstart + (xt ? DXT : DVO));
            const u16* pv = w < 4 ? (const u16*)(ws + OFF_WQKVT) + (size_t)2 * HD * D_ + (size_t)(hh * 8 + w) * 10240 + lane * 8 : (const u16*)(ws + OFF_WOT) + (size_t)(hh * 40 + 10 * (w - 4)) * 2048 + lane * 8;
            const int sg = w < 4 ? 40960 : 1024, st = w < 4 ? 1024 : 2048;
#pragma unroll
            for (int t = 0; t < 10; ++t) { wt[0][t] = ldw(pv + t * st); wt[1][t] = ldw(pv + sg + t * st); }
            __builtin_amdgcn_s_waitcnt(0x0F70);
            if (tid < 8) *(unsigned*)(smem + L_S + tid * 4) = 0u;
            if (!vt && w == 7) clk_init(ck);
            SST(1);
            if (vt) {
                if (w == 0) { wait_h(a, rwin, jv2, hrdy); *(volatile LAS int*)(smem + L_S + 32) = hrdy; }
                BAR();
                hrdy = __builtin_amdgcn_readfirstlane(*(volatile LAS int*)(smem + L_S + 32));
                SST(2);
                stage_h(rws, jv1, L_H, tid);
                stage_h(rws, jv2, L_H + 40960, tid);
            }
        }
        unsigned zb = W_ZM;
        auto steps = [&](auto cc) {
            constexpr int C = decltype(cc)::v & 1;
            constexpr bool TK = decltype(cc)::v == 3;
            LANES();
            Tk tk = {0u, 0u, 0x40000000u, nullptr};
            if (TK) { tk = tk_init(); __builtin_amdgcn_s_setprio(TKP); }
            const unsigned zi = (unsigned)(hh * 128 + fr * 8) * 2u;
            unsigned zo0 = zpair(w & 3, fq) + zi, zo1 = zo0 + 8 * PAIR, zo2 = zpair(w & 3, 8 + (fq >> 1)) + zi + (fq & 1) * 8;
            const LAS char* va = (const LAS char*)(smem + L_Q + (fr & 1) * 544 + ((fr >> 1) & 1) * 1152 + (16 * fq + 2 * (fr >> 2)) * 4);
            LAS char* vw = (LAS char*)(smem + L_Q + (fq & 1) * 544 + (fq >> 1) * 1152 + (16 * w + bs2(fr)) * 4);
            const LAS char* mp0 = (const LAS char*)(memL + ma0);
            PIN2(zo0, zo1); PIN1(zo2);
            if (C == 0) { PIN2(mp0, vw); } else PIN1(va);
            u32x4 za = SENT, zc = SENT;
            u32x2 ze = {0u, 0u};
            auto put_z2 = [&](int s) {
                const unsigned z2b = s < NSEG ? O_Z2 + (unsigned)s * (NOWN * 8192u) + (unsigned)(10 * (w & 3) * 8192 + hh * 256) : 0x40000000u, l2 = fresh_lane();
                st128(za, rws, (l2 >> 4) * 8192u + (l2 & 15u) * 16u, z2b, 16);
                st128(zc, rws, (l2 >> 4) * 8192u + (l2 & 15u) * 16u, z2b + 4u * 8192u, 16);
                __builtin_amdgcn_raw_buffer_store_b64(ze, rws, (l2 >> 5) * 8192u + (l2 & 15u) * 16u + ((l2 >> 4) & 1u) * 8u, z2b + 8u * 8192u, 16);
            };
            for (int s = 0; s < NSEG; ++s) {
                RT(0);
                bool redo = false;
                unsigned sf = 0;
                do {
                    ++it;
                    if (C == 0) {
                        const unsigned lim = s > 0 ? poll_lim(budget) : ~0u;
                        asm volatile(
                            "v_mov_b64 v[200:201], 0\n\t"
                            "v_mov_b64 v[202:203], 0\n\t"
                            "v_mov_b64 v[204:205], 0\n\t"
                            "v_mov_b64 v[206:207], 0\n\t"
                            "v_mov_b32 v208, 0\n\t"
                            "s_setprio 2\n\t"
                            "s_waitcnt lgkmcnt(0)\n\t"
                            "s_barrier\n\t"
                            MTA(0)
                            "ds_read_b128 v[212:215], %[mp] offset:0\n\t"
                            "ds_read_b128 v[216:219], %[mp] offset:1024\n\t"
                            "ds_read_b128 v[220:223], %[mp] offset:2048\n\t"
                            "s_barrier\n\t"
                            "ds_read_b128 v[224:227], %[mp] offset:3072\n\t"
                            "ds_read_b128 v[228:231], %[mp] offset:4096\n\t"
                            "ds_read_b128 v[232:235], %[mp] offset:5120\n\t"
                            "s_waitcnt lgkmcnt(5)\n\t"
                            "v_smfmac_f32_16x16x64_bf16 v[200:203], v[212:215], %[w00], %[ix]\n\t"
                            "s_waitcnt lgkmcnt(4)\n\t"
                            "v_smfmac_f32_16x16x64_bf16 v[200:203], v[216:219], %[w01], %[ix]\n\t"
                            "s_barrier\n\t"
                            MTA(1)
                            "ds_read_b128 v[236:239], %[mp] offset:6144\n\t"
                            "s_waitcnt lgkmcnt(4)\n\t"
                            "v_smfmac_f32_16x16x64_bf16 v[200:203], v[220:223], %[w02], %[ix]\n\t"
                            "ds_read_b128 v[240:243], %[mp] offset:7168\n\t"
                            "s_waitcnt lgkmcnt(4)\n\t"
                            "v_smfmac_f32_16x16x64_bf16 v[200:203], v[224:227], %[w03], %[ix]\n\t"
                            "ds_read_b128 v[244:247], %[mp] offset:8192\n\t"
                            "s_waitcnt lgkmcnt(4)\n\t"
                            "v_smfmac_f32_16x16x64_bf16 v[200:203], v[228:231], %[w04], %[ix]\n\t"
                            "ds_read_b128 v[248:251], %[mp] offset:9216\n\t"
                            "s_waitcnt lgkmcnt(4)\n\t"
                            "v_smfmac_f32_16x16x64_bf16 v[200:203], v[232:235], %[w05], %[ix]\n\t"
                            "s_waitcnt lgkmcnt(3)\n\t"
                            "v_smfmac_f32_16x16x64_bf16 v[200:203], v[236:239], %[w06], %[ix]\n\t"
                            "s_waitcnt lgkmcnt(2)\n\t"
                            "v_smfmac_f32_16x16x64_bf16 v[200:203], v[240:243], %[w07], %[ix]\n\t"
                            "s_waitcnt lgkmcnt(1)\n\t"
                            "v_smfmac_f32_16x16x64_bf16 v[200:203], v[244:247], %[w08], %[ix]\n\t"
                            "s_waitcnt lgkmcnt(0)\n\t"
                            "v_smfmac_f32_16x16x64_bf16 v[200:203], v[248:251], %[w09], %[ix]\n\t"
                            "s_nop 2\n\t"
                            "v_smfmac_f32_16x16x64_bf16 v[204:207], v[212:215], %[w10], %[ix]\n\t"
                            "s_nop 2\n\t"
                            "v_smfmac_f32_16x16x64_bf16 v[204:207], v[216:219], %[w11], %[ix]\n\t"
                            "v_add_f32 v200, v200, v202\n\t"
                            "v_add_f32 v201, v201, v203\n\t"
                            "v_cvt_pk_bf16_f32 v200, v200, v201\n\t"
                            "ds_write_b32 %[vw], v200\n\t"
                            MTA(2)
                            "v_smfmac_f32_16x16x64_bf16 v[204:207], v[220:223], %[w12], %[ix]\n\t"
                            "v_max3_u32 v208, v208, v212, v213\n\t"
                            "v_max3_u32 v208, v208, v214, v215\n\t"
                            "v_smfmac_f32_16x16x64_bf16 v[204:207], v[224:227], %[w13], %[ix]\n\t"
                            "v_max3_u32 v208, v208, v216, v217\n\t"
                            "v_max3_u32 v208, v208, v218, v219\n\t"
                            "v_smfmac_f32_16x16x64_bf16 v[204:207], v[228:231], %[w14], %[ix]\n\t"
                            "v_max3_u32 v208, v208, v220, v221\n\t"
                            "v_max3_u32 v208, v208, v222, v223\n\t"
                            "v_smfmac_f32_16x16x64_bf16 v[204:207], v[232:235], %[w15], %[ix]\n\t"
                            "v_max3_u32 v208, v208, v224, v225\n\t"
                            "v_max3_u32 v208, v208, v226, v227\n\t"
                            "v_smfmac_f32_16x16x64_bf16 v[204:207], v[236:239], %[w16], %[ix]\n\t"
                            "v_max3_u32 v208, v208, v228, v229\n\t"
                            "v_max3_u32 v208, v208, v230, v231\n\t"
                            "v_smfmac_f32_16x16x64_bf16 v[204:207], v[240:243], %[w17], %[ix]\n\t"
                            "s_waitcnt lgkmcnt(0)\n\t"
                            "s_barrier\n\t"
                            MTA(5)
                            "v_smfmac_f32_16x16x64_bf16 v[204:207], v[244:247], %[w18], %[ix]\n\t"
                            "v_max3_u32 v208, v208, v232, v233\n\t"
                            "v_max3_u32 v208, v208, v234, v235\n\t"
                            "v_smfmac_f32_16x16x64_bf16 v[204:207], v[248:251], %[w19], %[ix]\n\t"
                            "v_max3_u32 v208, v208, v236, v237\n\t"
                            "v_max3_u32 v208, v208, v238, v239\n\t"
                            "v_max3_u32 v208, v208, v240, v241\n\t"
                            "v_max3_u32 v208, v208, v242, v243\n\t"
                            "v_max3_u32 v208, v208, v244, v245\n\t"
                            "v_max3_u32 v208, v208, v246, v247\n\t"
                            "v_max3_u32 v208, v208, v248, v249\n\t"
                            "v_max3_u32 v208, v208, v250, v251\n\t"
                            "v_cmp_lt_u32 vcc, %[lim], v208\n\t"
                            "v_add_f32 v204, v204, v206\n\t"
                            "v_add_f32 v205, v205, v207\n\t"
                            "v_cvt_pk_bf16_f32 v204, v204, v205\n\t"
                            "ds_write_b32 %[vw], v204 offset:256\n\t"
                            MTA(3)
                            "s_cbranch_vccz 1f\n\t"
                            "v_mov_b32 v208, %[it]\n\t"
                            "v_mov_b32 v209, 0\n\t"
                            "ds_write_b32 v209, v208 offset:%[ls]\n\t"
                            "1:\n\t"
                            "s_waitcnt vmcnt(0) lgkmcnt(0)\n\t"
                            "s_barrier\n\t"
                            "s_setprio 0\n\t"
                            :: [mp] "v"(mp0), [vw] "v"(vw), [ix] "v"(sidx), [lim] "s"(lim), [it] "s"(it), [ls] "n"(L_S),
                              [w00] "v"(wt[0][0]), [w01] "v"(wt[0][1]), [w02] "v"(wt[0][2]), [w03] "v"(wt[0][3]), [w04] "v"(wt[0][4]), [w05] "v"(wt[0][5]), [w06] "v"(wt[0][6]), [w07] "v"(wt[0][7]), [w08] "v"(wt[0][8]), [w09] "v"(wt[0][9]), [w10] "v"(wt[1][0]), [w11] "v"(wt[1][1]), [w12] "v"(wt[1][2]), [w13] "v"(wt[1][3]), [w14] "v"(wt[1][4]), [w15] "v"(wt[1][5]), [w16] "v"(wt[1][6]), [w17] "v"(wt[1][7]), [w18] "v"(wt[1][8]), [w19] "v"(wt[1][9])
                            : "v200", "v201", "v202", "v203", "v204", "v205", "v206", "v207", "v208", "v209", "v212", "v213", "v214", "v215", "v216", "v217", "v218", "v219", "v220", "v221", "v222", "v223", "v224", "v225", "v226", "v227", "v228", "v229", "v230", "v231", "v232", "v233", "v234", "v235", "v236", "v237", "v238", "v239", "v240", "v241", "v242", "v243", "v244", "v245", "v246", "v247", "v248", "v249", "v250", "v251", "vcc", "memory");
                        MT(4);
                        redo = __builtin_amdgcn_readfirstlane(*(volatile LAS unsigned*)(smem + L_S)) == it;
                        if (redo) FC(fc0);
                    } else {
                        f32x16 ca = {}, cb = {};
                        f32x8 cc = {};
                        asm volatile("" : "+{v[212:227]}"(ca), "+{v[228:243]}"(cb), "+{v[244:251]}"(cc));
                        unsigned sr = tro + (unsigned)(s & 3) * 2u * PAIR;
                        asm volatile("" : "+s"(sr));
                        if (TK) fetch(tk, s, !redo, s > 0 ? poll_lim(budget) : ~0u, DVC, (unsigned)hh * 128u + 4u, __builtin_expect(s == 0, 0));
                        else asm volatile("s_waitcnt lgkmcnt(0)\n\ts_barrier\n\ts_barrier\n\ts_barrier" ::: "memory");
                        unsigned vd, vv, zs = zb;
                        u32x4 af1;
                        asm volatile(
                            "s_setprio 1\n\t"
                            "s_waitcnt lgkmcnt(0)\n\t"
                            "s_barrier\n\t"
                            "ds_read_b64_tr_b16 v[192:193], %[va]\n\t"
                            "ds_read_b64_tr_b16 v[194:195], %[va] offset:32\n\t"
                            "s_waitcnt lgkmcnt(0)\n\t"
                            "v_smfmac_f32_16x16x64_bf16 v[212:215], v[192:195], %[w0], %[ix]\n\t"
                            "v_smfmac_f32_16x16x64_bf16 v[220:223], v[192:195], %[w2], %[ix]\n\t"
                            "v_smfmac_f32_16x16x64_bf16 v[216:219], v[192:195], %[w1], %[ix]\n\t"
                            "v_smfmac_f32_16x16x64_bf16 v[224:227], v[192:195], %[w3], %[ix]\n\t"
                            "s_barrier\n\t"
                            MTA(4)
                            "ds_read_b64_tr_b16 v[196:197], %[va] offset:256\n\t"
                            "v_smfmac_f32_16x16x64_bf16 v[228:231], v[192:195], %[w4], %[ix]\n\t"
                            "ds_read_b64_tr_b16 v[198:199], %[va] offset:288\n\t"
                            "v_smfmac_f32_16x16x64_bf16 v[236:239], v[192:195], %[w6], %[ix]\n\t"
                            "ds_read_b32 v210, %[vz] offset:%[ls]\n\t"
                            "v_smfmac_f32_16x16x64_bf16 v[232:235], v[192:195], %[w5], %[ix]\n\t"
                            "v_smfmac_f32_16x16x64_bf16 v[240:243], v[192:195], %[w7], %[ix]\n\t"
                            "v_smfmac_f32_16x16x64_bf16 v[244:247], v[192:195], %[w8], %[ix]\n\t"
                            "v_smfmac_f32_16x16x64_bf16 v[248:251], v[192:195], %[w9], %[ix]\n\t"
                            "buffer_load_dword %[sf], %[sl], %[rw], %[sr] offen sc1\n\t"
                            : [sf] "=&v"(sf), "+{v[212:227]}"(ca), "+{v[228:243]}"(cb), "+{v[244:251]}"(cc), "=&{v[196:199]}"(af1), "=&{v210}"(vv)
                            : [va] "v"(va), [vz] "v"(0u), [ls] "n"(L_S), [ix] "v"(sidx), [sl] "v"(tk.sl), [sr] "s"(sr), [rw] "s"(rwin),
                              [w0] "v"(wt[0][0]), [w1] "v"(wt[0][1]), [w2] "v"(wt[0][2]), [w3] "v"(wt[0][3]), [w4] "v"(wt[0][4]), [w5] "v"(wt[0][5]), [w6] "v"(wt[0][6]), [w7] "v"(wt[0][7]), [w8] "v"(wt[0][8]), [w9] "v"(wt[0][9])
                            : "v192", "v193", "v194", "v195", "memory");
                        asm volatile(
                            "s_waitcnt lgkmcnt(0)\n\t"
                            "v_smfmac_f32_16x16x64_bf16 v[212:215], v[196:199], %[w0], %[ix]\n\t"
                            "v_readfirstlane_b32 %[vd], v210\n\t"
                            "v_smfmac_f32_16x16x64_bf16 v[220:223], v[196:199], %[w2], %[ix]\n\t"
                            "s_cmp_eq_u32 %[vd], %[it]\n\t"
                            "s_cselect_b32 %[zs], 2.0, %[zs]\n\t"
                            "v_smfmac_f32_16x16x64_bf16 v[216:219], v[196:199], %[w1], %[ix]\n\t"
                            "s_nop 1\n\t"
                            "v_smfmac_f32_16x16x64_bf16 v[224:227], v[196:199], %[w3], %[ix]\n\t"
                            "v_add_f32 v212, v212, v214\n\t"
                            "v_smfmac_f32_16x16x64_bf16 v[228:231], v[196:199], %[w4], %[ix]\n\t"
                            "v_add_f32 v213, v213, v215\n\t"
                            "v_smfmac_f32_16x16x64_bf16 v[236:239], v[196:199], %[w6], %[ix]\n\t"
                            "v_add_f32 v220, v220, v222\n\t"
                            "v_smfmac_f32_16x16x64_bf16 v[232:235], v[196:199], %[w5], %[ix]\n\t"
                            "v_add_f32 v221, v221, v223\n\t"
                            "v_smfmac_f32_16x16x64_bf16 v[240:243], v[196:199], %[w7], %[ix]\n\t"
                            "v_cvt_pk_bf16_f32 v200, v212, v213\n\t"
                            "v_smfmac_f32_16x16x64_bf16 v[244:247], v[196:199], %[w8], %[ix]\n\t"
                            "v_cvt_pk_bf16_f32 v201, v220, v221\n\t"
                            "v_smfmac_f32_16x16x64_bf16 v[248:251], v[196:199], %[w9], %[ix]\n\t"
                            "v_add_f32 v216, v216, v218\n\t"
                            "v_add_f32 v217, v217, v219\n\t"
                            "v_add_f32 v224, v224, v226\n\t"
                            "v_add_f32 v225, v225, v227\n\t"
                            "v_cvt_pk_bf16_f32 v202, v216, v217\n\t"
                            "v_cvt_pk_bf16_f32 v203, v224, v225\n\t"
                            "v_permlane32_swap_b32 v200, v201\n\t"
                            "v_pk_add_f32 v[228:229], v[228:229], v[230:231]\n\t"
                            "v_permlane32_swap_b32 v202, v203\n\t"
                            "v_min_u32 v201, -2, v201\n\t"
                            "v_pk_add_f32 v[236:237], v[236:237], v[238:239]\n\t"
                            "v_min_u32 v203, -2, v203\n\t"
                            "v_permlane16_swap_b32 v200, v202\n\t"
                            "v_pk_add_f32 v[232:233], v[232:233], v[234:235]\n\t"
                            "v_permlane16_swap_b32 v201, v203\n\t"
                            "v_pk_add_f32 v[240:241], v[240:241], v[242:243]\n\t"
                            MTA(5)
                            "buffer_store_dwordx4 v[200:203], %[z0], %[rw], %[zs] offen sc1\n\t"
                            "v_cvt_pk_bf16_f32 v204, v228, v229\n\t"
                            "v_cvt_pk_bf16_f32 v205, v236, v237\n\t"
                            "v_cvt_pk_bf16_f32 v206, v232, v233\n\t"
                            "v_cvt_pk_bf16_f32 v207, v240, v241\n\t"
                            "v_permlane32_swap_b32 v204, v205\n\t"
                            "v_pk_add_f32 v[244:245], v[244:245], v[246:247]\n\t"
                            "v_permlane32_swap_b32 v206, v207\n\t"
                            "v_min_u32 v205, -2, v205\n\t"
                            "v_pk_add_f32 v[248:249], v[248:249], v[250:251]\n\t"
                            "v_min_u32 v207, -2, v207\n\t"
                            "v_permlane16_swap_b32 v204, v206\n\t"
                            "v_cvt_pk_bf16_f32 v208, v244, v245\n\t"
                            "v_permlane16_swap_b32 v205, v207\n\t"
                            "v_cvt_pk_bf16_f32 v209, v248, v249\n\t"
                            "buffer_store_dwordx4 v[204:207], %[z1], %[rw], %[zs] offen sc1\n\t"
                            "s_nop 0\n\t"
                            "v_permlane32_swap_b32 v208, v209\n\t"
                            "v_min_u32 v209, -2, v209\n\t"
                            "buffer_store_dwordx2 v[208:209], %[z2], %[rw], %[zs] offen sc1\n\t"
                            : [vd] "=&s"(vd), [zs] "+s"(zs), "=&{v[200:203]}"(za), "=&{v[204:207]}"(zc), "=&{v[208:209]}"(ze), "+{v[212:227]}"(ca), "+{v[228:243]}"(cb), "+{v[244:251]}"(cc), "+v"(sf)
                            : [ix] "v"(sidx), [it] "s"(it), [rw] "s"(rwin), [z0] "v"(zo0), [z1] "v"(zo1), [z2] "v"(zo2), "{v[196:199]}"(af1), "{v210}"(vv),
                              [w0] "v"(wt[1][0]), [w1] "v"(wt[1][1]), [w2] "v"(wt[1][2]), [w3] "v"(wt[1][3]), [w4] "v"(wt[1][4]), [w5] "v"(wt[1][5]), [w6] "v"(wt[1][6]), [w7] "v"(wt[1][7]), [w8] "v"(wt[1][8]), [w9] "v"(wt[1][9])
                            : "scc", "memory");
                        redo = vd == it;
                        RT(4);
                        MT(6);
                        tref = __builtin_amdgcn_s_memtime();
                        __builtin_amdgcn_s_setprio(TKP);
                        asm volatile("s_waitcnt vmcnt(3)" : "+v"(sf) :: "memory");
                    }
                } while (__builtin_expect(redo, 0));
                if (s < 8) SST(8 + s);
                if (C == 0) {
                    if (s > 0) {
                        __builtin_amdgcn_s_sleep(DZR);
                        const unsigned zq = zb == W_ZM ? W_ZM + 2u * NOWN * PAIR : zb - NOWN * PAIR;
                        unsigned s1;
                        asm volatile("v_mov_b32 %0, -1" : "=v"(s1));
                        st128(((u32x4){s1, s1, s1, s1}), rwin, zo0, zq, 16);
                        st128(((u32x4){s1, s1, s1, s1}), rwin, zo1, zq, 16);
                        __builtin_amdgcn_raw_buffer_store_b64((u32x2){s1, s1}, rwin, zo2, zq, 16);
                    }
                } else {
                    if (s > TAILS) __builtin_amdgcn_raw_buffer_store_b32((unsigned)s - 1u, rwin, 0u, W_FZ2 + (unsigned)(hh * 128 + (w & 3) * 4), 16);
                    if (TK && s + 1 < NSEG) sched2(tk, s, sf);
                    if (!TK) wait_until(tref + DZ2);
                    if (s + 1 < NSEG) put_z2(s);
                }
                if (budget <= 0) __hip_atomic_store(a.ctl, 1u, __ATOMIC_RELAXED, __HIP_MEMORY_SCOPE_AGENT);
                zb = zb == W_ZM + 2u * NOWN * PAIR ? W_ZM : zb + NOWN * PAIR;
                RT(5);
                RTS();
                MT(7);
                MTS();
            }
        };
        if (!vt) {
            if (w < 4) steps(IC<0>()); else if (w < 7) steps(IC<1>()); else steps(IC<3>());
        } else {
            asm volatile("s_waitcnt vmcnt(5) lgkmcnt(0)\n\ts_barrier" ::: "memory");
            SST(3);
            LANES();
            const int ni = __builtin_popcountll(mv) + 1;
            if (w < 4) {
                const LAS char* c0 = (const LAS char*)(hL + hoff + ((fq ^ (fr & 7)) << 4));
                const LAS char* c1 = (const LAS char*)(hL + hoff + (((4 + fq) ^ (fr & 7)) << 4));
                const LAS char* n0 = c0 + 40960; const LAS char* n1 = c1 + 40960;
                u32x4 bb[8];
                project0(bb, c0, c1);
                for (int i = 1; i < ni; ++i) {
                    if (vt) MT(0);
                    f32x4 tc[2][2];
                    project<0>(wt, tc, bb, c0, c1, n0, n1,
                        [&]() {
                            MT(1);
                            asm volatile("s_waitcnt vmcnt(0) lgkmcnt(0)\n\ts_barrier" ::: "memory");
                            MT(2);
                        }, [](auto) {});
                    { const LAS char* x = c0; c0 = n0; n0 = x; x = c1; c1 = n1; n1 = x; }
                    char* tb = tokL + (i & 1) * 32 * QS + (16 * w + 4 * fq) * 2;
#pragma unroll
                    for (int g = 0; g < 2; ++g)
#pragma unroll
                        for (int tt = 0; tt < 2; ++tt) *(u32x2*)(tb + (prow + 8 * tt) * QS + g * 128) = (u32x2){pack2(tc[g][tt][0], tc[g][tt][1]), pack2(tc[g][tt][2], tc[g][tt][3])};
                    asm volatile("" ::: "memory");
                    *(volatile LAS int*)(smem + L_S + 16 + w * 4) = i;
                    if (vt) { MT(3); const int s = i & 63; MTRT(); MTS(); }
                }
                project_end(bb);
            } else {
                __builtin_amdgcn_s_setprio(OPR);
                const unsigned hv = lane * 16u, fz_ = W_FZ + (unsigned)(hh * 128 + xt * 64 + (w - 4) * 4);
                unsigned fp = ~0u;
                int jo = jv1, jh = jv2;
                for (int i = 1; i < ni; ++i, jo = nextv(jo)) {
                    const int b = i + (i >= ZLAG);
                    if (__builtin_expect(i == 1 || i == ZLAG, 0)) {
                        VMWAIT();
                        if (i == ZLAG) {
                            jh = nextv(jh);
                            wait_h(a, rwin, jh, hrdy);
                            BAR0();
                            for (int t = 0; t < 10; ++t) dma1k(rws, hv, O_H + (unsigned)(jh * 40960 + (w - 4) * 1024 + t * 4096), (unsigned)(L_H + ((i + 1) & 1) * 40960 + (w - 4) * 1024 + t * 4096));
                            VMWAIT();
                        }
                    }
                    jh = nextv(jh);
                    wait_h(a, rwin, jh, hrdy);
                    if (b < ni) BAR0();
                    if (vt) MT(1);
                    for (;;) {
                        const u32x4 f = *(volatile LAS u32x4*)(smem + L_S + 16);
                        if (__builtin_amdgcn_readfirstlane(min(min(f[0], f[1]), min(f[2], f[3]))) >= (unsigned)i) break;
                        __builtin_amdgcn_s_sleep(1);
                    }
                    asm volatile("" ::: "memory");
                    if (vt) MT(0);
                    u32x4 vf[2][4];
#pragma unroll
                    for (int t = 0; t < 2; ++t)
#pragma unroll
                        for (int ks = 0; ks < 4; ++ks) vf[t][ks] = *(const u32x4*)(tokL + (i & 1) * 32 * QS + (16 * t + fr) * QS + ks * 64 + fq * 16);
                    const unsigned ztb = O_Z + (unsigned)(jo * ZTSEG) * 2u + (unsigned)(10 * (w - 4) * H_ + hh) * 1024u;
                    f32x4 c[2][2];
                    u32x4 r;
                    unsigned hs = O_H + (unsigned)(jh * 40960 + (w - 4) * 1024), hd = (unsigned)(L_H + ((b + 1) & 1) * 40960 + (w - 4) * 1024);
                    asm volatile("" : "+s"(hs), "+s"(hd));
#pragma unroll
                    for (int nt = 0; nt < 11; ++nt) {
                        auto& cc = c[nt & 1]; auto& cp = c[(nt + 1) & 1];
#pragma unroll
                        for (int ks = 0; ks < 4; ++ks) {
                            if (nt < 10) { cc[0] = mfma32(vf[0][ks], half8(wt[ks >> 1][nt], ks & 1), ks ? cc[0] : z4); PIN1(cc[0]); }
                            if (nt > 0 && ks >= 1 && ks < 3) { r[2 * ks - 2] = pack2(cp[ks - 1][0], cp[ks - 1][1]); PIN1(r); }
                            if (nt < 10) { cc[1] = mfma32(vf[1][ks], half8(wt[ks >> 1][nt], ks & 1), ks ? cc[1] : z4); PIN1(cc[1]); }
                            if (nt > 0 && ks >= 1 && ks < 3) { r[2 * ks - 1] = pack2(cp[ks - 1][2], cp[ks - 1][3]); PIN1(r); }
                            if (nt > 0 && ks == 2) st128(r, rws, lane * 16u, ztb + (unsigned)((nt - 1) * H_) * 1024u, 16);
                            if (nt < 5 && !(ks & 1)) dma1k(rws, hv, hs + (2 * nt + (ks >> 1)) * 4096u, hd + (2 * nt + (ks >> 1)) * 4096u);
                            if (nt == 5 && ks == 0) {
                                asm volatile("s_waitcnt vmcnt(14)" ::: "memory");
                                __builtin_amdgcn_raw_buffer_store_b32(fp, rwin, 0u, fz_, 16);
                            }
                            SB();
                        }
                    }
                    if (vt) MT(2);
                    fp = (unsigned)jo;
                    if (__builtin_expect(jo <= ZEARLY || i + 1 == ni, 0)) {
                        VMWAIT();
                        __builtin_amdgcn_raw_buffer_store_b32(fp, rwin, 0u, fz_, 16);
                    } else asm volatile("s_waitcnt vmcnt(7)" ::: "memory");
                    if (jo < 8) SST(8 + jo);
                    if (vt && (!BSPARSE || jo % 6 == 2)) { const int s = jo; RT(0); RTS(); }
                    if (vt) { MT(3); const int s = i & 63; MTRT(); MTS(); }
                }
            }
            __builtin_amdgcn_s_setprio(0);
            VMWAIT();
            BAR();
        }
    }
    const bool qt = bid >= NQT && bid < NWRK;
    if (bid < 32 || qt) {
        LANES();
        const int hh = qt ? (bid - NQT) & 31 : bid;
        const unsigned long long mq = xt ? MXQ : ~MXQ;
        auto nextq = [&](int j) { return min(seg_after(mq, j), NSEG - 1); };
        const int jq0 = qt ? nextq(1) : 0, jq1 = nextq(jq0);
        u32x8 wt[2][10];
        if (w > 0) {
            const unsigned long long t0 = __builtin_amdgcn_s_memtime();
            if (!qt) gather_mem0(a, memL, tid - 64, 448);
            if (w == 7) clk_init(ck);
            wait_until(t0 + DW0);
        }
        if (DQT && qt) wait_until(tstart + DQT);
        {
            const u16* pq = (const u16*)(ws + OFF_WQKVT) + (size_t)((w >> 2) * 256 + hh * 8 + 2 * (w & 3)) * 10240 + lane * 8;
#pragma unroll
            for (int t = 0; t < 10; ++t) { wt[0][t] = ldw(pq + t * 1024); wt[1][t] = ldw(pq + 10240 + t * 1024); }
        }
        if (tid < 4) *(unsigned*)(smem + L_S + tid * 4) = 0u;
        SST(1);
        if (w == 0) { wait_h(a, rwin, jq1, hrdy); *(volatile LAS int*)(smem + L_S + 32) = hrdy; }
        BAR();
        hrdy = __builtin_amdgcn_readfirstlane(*(volatile LAS int*)(smem + L_S + 32));
        SST(2);
        stage_h(rws, jq0, L_H, tid);
        stage_h(rws, jq1, L_H + 40960, tid);
        VMWAIT();
        BAR();
        SST(3);
        int qrdy = 0;
        {
            LANES();
            const unsigned to0 = (unsigned)((w ^ 4) >> 2) * 8192u + (unsigned)((w & 3) * 64 + (lane ^ ((fq & 1) * (w < 4 ? 8 : 5)))) * 16u, to1 = to0 + 4096u, hv = lane * 16u;
            const LAS char* c0 = (const LAS char*)(hL + hoff + ((fq ^ (fr & 7)) << 4));
            const LAS char* c1 = (const LAS char*)(hL + hoff + (((4 + fq) ^ (fr & 7)) << 4));
            u32x4 bb[8], g0 = SENT, g1 = SENT;
            f32x4 tc[2][2];
            auto pack = [&]() {
                g0 = (u32x4){pack2(tc[0][0][0], tc[0][0][1]), pack2(tc[0][0][2], tc[0][0][3]), pack2(tc[1][0][0], tc[1][0][1]), pack2(tc[1][0][2], tc[1][0][3])};
                g1 = (u32x4){pack2(tc[0][1][0], tc[0][1][1]), pack2(tc[0][1][2], tc[0][1][3]), pack2(tc[1][1][0], tc[1][1][1]), pack2(tc[1][1][2], tc[1][1][3])};
            };
            const LAS char* n0 = c0 + 40960; const LAS char* n1 = c1 + 40960;
            if (!qt) {
                project0(bb, c0, c1);
                for (int j = 0; j < 2; ++j) {
                    project<0>(wt, tc, bb, c0, c1, n0, n1, []() {}, [](auto) {});
                    pack();
                    *(u32x4*)(tokL + j * 16384 + to0) = g0; *(u32x4*)(tokL + j * 16384 + to1) = g1;
                    c0 = n0; c1 = n1;
                }
                project_end(bb);
            } else {
                project0(bb, c0, c1);
                unsigned hs = O_H + (unsigned)(jq1 * 40960 + w * 1024), hd = (unsigned)(L_H + 40960 + w * 1024), fv = tid == 0 ? 0u : 0x40000000u;
                const unsigned fq_ = W_FQ + (unsigned)(hh * 128 + xt * 64);
                const int jl = 63 - __builtin_clzll(mq);
                for (int jp = 1, j = jq0, jn = jq1;; jp = j, j = jn, jn = nextq(jn)) {
                    MT(0);
                    project<0>(wt, tc, bb, c0, c1, n0, n1,
                        [&]() {
                            wait_h(a, rwin, nextq(jn), hrdy);
                            hs = O_H + (unsigned)(nextq(jn) * 40960 + w * 1024); hd = (unsigned)(2 * L_H + 40960 + 2 * w * 1024) - hd;
                            unsigned fj = (unsigned)jp;
                            asm volatile("" : "+s"(hs), "+s"(hd));
                            PIN1(fj);
                            MT(1);
                            VMWAIT();
                            BAR();
                            MT(2);
                            if (w == 0) __builtin_amdgcn_raw_buffer_store_b32(fj, rwin, fv, fq_, 16);
                        },
                        [&](auto uu) {
                            constexpr int u = decltype(uu)::v;
                            if (u == 34 || u == 38 || u == 2 || u == 6 || u == 10) { constexpr int t = ((u + 6) % 40) / 4; dma1k(rws, hv, hs + t * 8192u, hd + t * 8192u); }
                        });
                    pack();
                    const unsigned qs = O_QK + (unsigned)(j * H_ + hh) * 16384u;
                    st128(g0, rws, to0, qs, 16);
                    st128(g1, rws, to1, qs, 16);
                    { const LAS char* x = c0; c0 = n0; n0 = x; x = c1; c1 = n1; n1 = x; }
                    if (__builtin_expect(j <= QEARLY || j == jl, 0)) {
                        VMWAIT();
                        BAR0();
                        if (tid == 0) __builtin_amdgcn_raw_buffer_store_b32((unsigned)j, rwin, 0u, fq_, 16);
                        if (j == jl) break;
                    }
                    MT(3);
                    if (j < 8) SST(8 + j);
                    if (!BSPARSE || j % 6 == 2) { const int s = j; RT(0); RTS(); MTRT(); MTS(); }
                }
                project_end(bb);
            }
        }
        SST(4);
        auto steps = [&](auto cc) {
        constexpr int C = decltype(cc)::v >> 1, R = decltype(cc)::v & 1;
        LANES();
        const LAS char* ka = (const LAS char*)(smem + L_K + (fr & 1) * TQS + (4 * fq + (fr >> 2)) * 8);
        const LAS char* qa = (const LAS char*)(smem + L_Q + (fr & 1) * 544 + ((fr >> 1) & 1) * 1152 + (16 * fq + 2 * (fr >> 2)) * 4);
        LAS char* qw = (LAS char*)(C < 2 ? smem + L_Q + (fq & 1) * 544 + (fq >> 1) * 1152 + (32 * w + 8 * (fr >> 2) + bs2(fr & 3)) * 4
                                         : smem + L_K + (fq >> 1) * TQS + (32 * (w & 3) + fr) * 8 + (fq & 1) * 4);
        const LAS char* mp0 = (const LAS char*)(memL + ma0);
        const int z = 16 * (fr >> 3) + 4 * (fr & 3) + 2 * ((fr >> 2) & 1);
        const int ko = ((z >> 4) * 256 + fq * 16 + ((z & 15) ^ ((fq & 1) * 5))) * 16;
        unsigned pa0 = (4u * C + 2u * (hh >> 4)) * PAIR + (unsigned)((hh & 15) * 512 + R * 256 + lane * 4);
        unsigned pa2 = ((8u + (fr >> 3)) * PAIR + C * 4096u + (unsigned)((hh >> 1) * 256 + R * 128 + (hh & 1) * 64 + fq * 16 + (fr & 6) * 2)) | (fr & 1) << 30;
        PIN2(ka, qw); PIN1(mp0);
        constexpr bool TK = C == 2 && R == 1;
        Tk tk = {0u, 0u, 0x40000000u, nullptr};
        if (TK) { tk = tk_init(); __builtin_amdgcn_s_setprio(TKP); }
        if (C < 2) { PIN2(qa, pa0); PIN1(pa2); }
        int qf_ = 0;
        int tbo = 0;
        for (int s = 0; s < NSEG; ++s) {
            RT(0);
            const char* tb = tokL + tbo;
            unsigned pb = __builtin_amdgcn_readfirstlane(s + 1 < NSEG ? W_P + (unsigned)(s & 3) * 10u * PAIR + nh : 0x40000000u);
            asm volatile("" : "+s"(pb));
            if (s == 0) BAR();
            u32x8 kx[2][2];
            if (C < 2) {
#pragma unroll
                for (int u = 0; u < 4; ++u) kx[u >> 1][u & 1] = cat8(*(const u32x4*)(tb + (ko ^ ((u >> 1) * 16)) + (u & 1) * 2048), *(const u32x4*)(tb + (ko ^ ((u >> 1) * 16)) + (u & 1) * 2048 + 1024));
                PIN2(kx[0][0], kx[0][1]); PIN2(kx[1][0], kx[1][1]);
            }
            bool redo = false;
            unsigned sf = 0;
            do {
                ++it;
                f32x4 a0 = z4, a1 = z4;
                PIN2(a0, a1);
                u32x4 m[10];
                if (C < 2) {
                    __builtin_amdgcn_s_setprio(2);
                    BAR();
                    MT(0);
#pragma unroll
                    for (int t = 0; t < NB1; ++t) { m[t] = *(const LAS u32x4*)(mp0 + t * 1024); SB(); }
                    BAR0();
#pragma unroll
                    for (int t = 0; t < 10; ++t) {
                        if (t == QB2) { BAR0(); MT(1); }
                        if (t + NB1 < 10) m[t + NB1] = *(const LAS u32x4*)(mp0 + (t + NB1) * 1024);
                        SB();
                        a0 = smfmac(m[t], wt[0][t], a0, sidx); a1 = smfmac(m[t], wt[1][t], a1, sidx);
                        PIN2(a0, a1);
                    }
                } else {
                    unsigned mx = 0;
                    asm volatile("" : "+{v[200:203]}"(a0), "+{v[204:207]}"(a1), "+{v208}"(mx));
                    unsigned lim = s > 0 ? poll_lim(budget) : ~0u, sr = tro + (unsigned)(s & 3) * 2u * PAIR;
                    asm volatile("" : "+s"(lim), "+s"(sr));
                    if (TK) fetch(tk, s, !redo, lim, DQC, (unsigned)hh * 128u, true);
                    else asm volatile("s_waitcnt lgkmcnt(0)\n\ts_barrier\n\ts_barrier\n\ts_barrier" ::: "memory");
                    asm volatile(
                        "ds_read_b128 v[212:215], %[mp] offset:0\n\t"
                        "ds_read_b128 v[216:219], %[mp] offset:1024\n\t"
                        "ds_read_b128 v[220:223], %[mp] offset:2048\n\t"
                        "ds_read_b128 v[224:227], %[mp] offset:3072\n\t"
                        "ds_read_b128 v[228:231], %[mp] offset:4096\n\t"
                        "ds_read_b128 v[232:235], %[mp] offset:5120\n\t"
                        "ds_read_b128 v[236:239], %[mp] offset:6144\n\t"
                        "ds_read_b128 v[240:243], %[mp] offset:7168\n\t"
                        "ds_read_b128 v[244:247], %[mp] offset:8192\n\t"
                        "ds_read_b128 v[248:251], %[mp] offset:9216\n\t"
                        MTA(4)
                        "s_waitcnt lgkmcnt(9)\n\t"
                        "v_smfmac_f32_16x16x64_bf16 v[200:203], v[212:215], %[w00], %[ix]\n\t"
                        "v_max3_u32 v208, v208, v212, v213\n\t"
                        "v_smfmac_f32_16x16x64_bf16 v[204:207], v[212:215], %[w10], %[ix]\n\t"
                        "s_waitcnt lgkmcnt(8)\n\t"
                        "v_max3_u32 v208, v208, v214, v215\n\t"
                        "v_smfmac_f32_16x16x64_bf16 v[200:203], v[216:219], %[w01], %[ix]\n\t"
                        "v_max3_u32 v208, v208, v216, v217\n\t"
                        "v_smfmac_f32_16x16x64_bf16 v[204:207], v[216:219], %[w11], %[ix]\n\t"
                        "s_waitcnt lgkmcnt(7)\n\t"
                        "v_max3_u32 v208, v208, v218, v219\n\t"
                        "v_smfmac_f32_16x16x64_bf16 v[200:203], v[220:223], %[w02], %[ix]\n\t"
                        "v_max3_u32 v208, v208, v220, v221\n\t"
                        "v_smfmac_f32_16x16x64_bf16 v[204:207], v[220:223], %[w12], %[ix]\n\t"
                        "s_waitcnt lgkmcnt(6)\n\t"
                        "v_max3_u32 v208, v208, v222, v223\n\t"
                        "v_smfmac_f32_16x16x64_bf16 v[200:203], v[224:227], %[w03], %[ix]\n\t"
                        "v_max3_u32 v208, v208, v224, v225\n\t"
                        "v_smfmac_f32_16x16x64_bf16 v[204:207], v[224:227], %[w13], %[ix]\n\t"
                        "s_waitcnt lgkmcnt(0)\n\t"
                        "v_max3_u32 v208, v208, v226, v227\n\t"
                        "v_smfmac_f32_16x16x64_bf16 v[200:203], v[228:231], %[w04], %[ix]\n\t"
                        "v_max3_u32 v208, v208, v228, v229\n\t"
                        "v_smfmac_f32_16x16x64_bf16 v[204:207], v[228:231], %[w14], %[ix]\n\t"
                        "v_max3_u32 v208, v208, v230, v231\n\t"
                        "v_smfmac_f32_16x16x64_bf16 v[200:203], v[232:235], %[w05], %[ix]\n\t"
                        "v_max3_u32 v208, v208, v232, v233\n\t"
                        "v_smfmac_f32_16x16x64_bf16 v[204:207], v[232:235], %[w15], %[ix]\n\t"
                        "v_max3_u32 v208, v208, v234, v235\n\t"
                        "v_smfmac_f32_16x16x64_bf16 v[200:203], v[236:239], %[w06], %[ix]\n\t"
                        "v_max3_u32 v208, v208, v236, v237\n\t"
                        "v_smfmac_f32_16x16x64_bf16 v[204:207], v[236:239], %[w16], %[ix]\n\t"
                        "v_max3_u32 v208, v208, v238, v239\n\t"
                        "v_smfmac_f32_16x16x64_bf16 v[200:203], v[240:243], %[w07], %[ix]\n\t"
                        "v_max3_u32 v208, v208, v240, v241\n\t"
                        "v_smfmac_f32_16x16x64_bf16 v[204:207], v[240:243], %[w17], %[ix]\n\t"
                        "v_max3_u32 v208, v208, v242, v243\n\t"
                        "v_smfmac_f32_16x16x64_bf16 v[200:203], v[244:247], %[w08], %[ix]\n\t"
                        "v_max3_u32 v208, v208, v244, v245\n\t"
                        "v_smfmac_f32_16x16x64_bf16 v[204:207], v[244:247], %[w18], %[ix]\n\t"
                        "v_max3_u32 v208, v208, v246, v247\n\t"
                        "v_smfmac_f32_16x16x64_bf16 v[200:203], v[248:251], %[w09], %[ix]\n\t"
                        "v_max3_u32 v208, v208, v248, v249\n\t"
                        "v_smfmac_f32_16x16x64_bf16 v[204:207], v[248:251], %[w19], %[ix]\n\t"
                        "v_max3_u32 v208, v208, v250, v251\n\t"
                        "buffer_load_dword %[sf], %[sl], %[rw], %[sr] offen sc1\n\t"
                        "v_cmp_lt_u32 vcc, %[lim], v208\n\t"
                        "s_nop 3\n\t"
                        "v_pk_add_f32 v[200:201], v[200:201], v[202:203]\n\t"
                        "v_pk_add_f32 v[204:205], v[204:205], v[206:207]\n\t"
                        "v_cvt_pk_bf16_f32 v200, v200, v201\n\t"
                        "v_cvt_pk_bf16_f32 v204, v204, v205\n\t"
                        "ds_write2_b32 %[qw], v200, v204 offset1:32\n\t"
                        MTA(2)
                        "s_cbranch_vccz 1f\n\t"
                        "v_mov_b32 v208, %[it]\n\t"
                        "v_mov_b32 v209, 0\n\t"
                        "ds_write_b32 v209, v208 offset:%[ls]\n\t"
                        "1:\n\t"
                        "s_waitcnt vmcnt(%[nv]) lgkmcnt(0)\n\t"
                        "s_barrier\n\t"
                        "s_waitcnt vmcnt(0)\n\t"
                        : [sf] "=&v"(sf), "+v"(qf_), "+{v[200:203]}"(a0), "+{v[204:207]}"(a1), "+{v208}"(mx)
                        : [mp] "v"(mp0), [qw] "v"(qw), [ix] "v"(sidx), [lim] "s"(lim), [it] "s"(it), [ls] "n"(L_S), [sl] "v"(tk.sl), [sr] "s"(sr), [rw] "s"(rwin), [nv] "n"(TK ? 2 : 1),
                          [w00] "v"(wt[0][0]), [w01] "v"(wt[0][1]), [w02] "v"(wt[0][2]), [w03] "v"(wt[0][3]), [w04] "v"(wt[0][4]), [w05] "v"(wt[0][5]), [w06] "v"(wt[0][6]), [w07] "v"(wt[0][7]), [w08] "v"(wt[0][8]), [w09] "v"(wt[0][9]), [w10] "v"(wt[1][0]), [w11] "v"(wt[1][1]), [w12] "v"(wt[1][2]), [w13] "v"(wt[1][3]), [w14] "v"(wt[1][4]), [w15] "v"(wt[1][5]), [w16] "v"(wt[1][6]), [w17] "v"(wt[1][7]), [w18] "v"(wt[1][8]), [w19] "v"(wt[1][9])
                        : "v209", "v212", "v213", "v214", "v215", "v216", "v217", "v218", "v219", "v220", "v221", "v222", "v223", "v224", "v225", "v226", "v227", "v228", "v229", "v230", "v231", "v232", "v233", "v234", "v235", "v236", "v237", "v238", "v239", "v240", "v241", "v242", "v243", "v244", "v245", "v246", "v247", "v248", "v249", "v250", "v251", "vcc", "memory");
                }
                if (C < 2) {
                    *(LAS unsigned*)qw = hpack(a0); *(LAS unsigned*)(qw + 16) = hpack(a1);
                    SB();
                    MT(2);
                    unsigned vd;
#define SC0 "v[212:215]"
#define SC1 "v[216:219]"
#define SC2 "v[220:223]"
#define DPP(op, d, ctl) op " " d ", " d ", " d " " ctl " row_mask:0xf bank_mask:0xf\n\t"
#define TAIL(A0, A2, B0, B2, C0, C2) \
                        "v_mov_b64 v[212:213], 0\n\tv_mov_b64 v[214:215], 0\n\tv_mov_b64 v[216:217], 0\n\tv_mov_b64 v[218:219], 0\n\tv_mov_b64 v[220:221], 0\n\tv_mov_b64 v[222:223], 0\n\t" \
                        "s_setprio 3\n\t" \
                        "s_waitcnt lgkmcnt(0)\n\t" \
                        "s_barrier\n\t" MTA(3) \
                        "ds_read_b64_tr_b16 v[224:225], %[qa]\n\t" \
                        "ds_read_b64_tr_b16 v[226:227], %[qa] offset:32\n\t" \
                        "ds_read_b64_tr_b16 v[228:229], %[qa] offset:256\n\t" \
                        "ds_read_b64_tr_b16 v[230:231], %[qa] offset:288\n\t" \
                        "ds_read_b64_tr_b16 v[232:233], %[ka]\n\t" \
                        "ds_read_b64_tr_b16 v[234:235], %[ka] offset:128\n\t" \
                        "ds_read_b64_tr_b16 v[236:237], %[ka] offset:256\n\t" \
                        "ds_read_b64_tr_b16 v[238:239], %[ka] offset:384\n\t" \
                        "ds_read_b64_tr_b16 v[240:241], %[ka] offset:512\n\t" \
                        "ds_read_b64_tr_b16 v[242:243], %[ka] offset:640\n\t" \
                        "s_waitcnt lgkmcnt(8)\n\t" \
                        "v_smfmac_f32_16x16x64_bf16 " SC0 ", v[224:227], %[k00], %[ix]\n\t" \
                        "ds_read_b64_tr_b16 v[244:245], %[ka] offset:768\n\t" \
                        "v_smfmac_f32_16x16x64_bf16 " SC1 ", v[224:227], %[k10], %[ix]\n\t" \
                        "ds_read_b64_tr_b16 v[246:247], %[ka] offset:896\n\t" \
                        "s_waitcnt lgkmcnt(8)\n\t" \
                        "v_smfmac_f32_16x16x64_bf16 " SC0 ", v[228:231], %[k01], %[ix]\n\t" \
                        "ds_read_b32 v248, %[vz] offset:%[ls]\n\t" \
                        "v_smfmac_f32_16x16x64_bf16 " SC1 ", v[228:231], %[k11], %[ix]\n\t" \
                        "s_waitcnt lgkmcnt(5)\n\t" \
                        "v_smfmac_f32_16x16x64_bf16 " SC2 ", v[224:227], v[232:239], %[ix]\n\t" \
                        "s_waitcnt lgkmcnt(1)\n\t" \
                        "v_smfmac_f32_16x16x64_bf16 " SC2 ", v[228:231], v[240:247], %[ix]\n\t" \
                        "s_waitcnt lgkmcnt(0)\n\t" \
                        MTA(4) "v_readfirstlane_b32 %[vd], v248\n\t" \
                        "v_add_f32 v232, " A0 ", " A2 "\n\t" \
                        "s_nop 0\n\t" \
                        "v_add_f32 v233, " B0 ", " B2 "\n\t" \
                        "v_max_f32 v235, v232, v233\n\t" \
                        "s_cmp_eq_u32 %[vd], %[it]\n\t" \
                        "s_cselect_b32 %[p0], 2.0, %[p0]\n\t" \
                        DPP("v_max_f32_dpp", "v235", "row_ror:8") \
                        "v_add_f32 v234, " C0 ", " C2 "\n\t" \
                        "v_max_f32 v235, v235, v234\n\t" \
                        "s_add_u32 %[p1], %[p0], 0x4000\n\t" \
                        "s_nop 0\n\t" DPP("v_max_f32_dpp", "v235", "row_ror:4") \
                        "s_nop 1\n\t" DPP("v_max_f32_dpp", "v235", "row_ror:2") \
                        "s_nop 1\n\t" DPP("v_max_f32_dpp", "v235", "row_ror:1") \
                        "v_mul_f32 v235, 0xbe0293ee, v235\n\t" \
                        "v_fmamk_f32 v232, v232, 0x3e0293ee, v235\n\t" \
                        "v_fmamk_f32 v233, v233, 0x3e0293ee, v235\n\t" \
                        "v_exp_f32 v232, v232\n\t" \
                        "v_exp_f32 v233, v233\n\t" \
                        "v_fmamk_f32 v234, v234, 0x3e0293ee, v235\n\t" \
                        "v_add_f32 v235, v232, v233\n\t" \
                        "v_exp_f32 v234, v234\n\t" \
                        "s_nop 0\n\t" DPP("v_add_f32_dpp", "v235", "row_ror:8") \
                        "v_add_f32 v235, v235, v234\n\t" \
                        "v_mov_b32_dpp v236, v234 quad_perm:[1,1,3,3] row_mask:0xf bank_mask:0xf\n\t" \
                        "s_nop 0\n\t" DPP("v_add_f32_dpp", "v235", "row_ror:4") \
                        "s_nop 1\n\t" DPP("v_add_f32_dpp", "v235", "row_ror:2") \
                        "s_nop 1\n\t" DPP("v_add_f32_dpp", "v235", "row_ror:1") \
                        "v_rcp_f32 v235, v235\n\t" \
                        "s_nop 0\n\t" \
                        "v_mul_f32_e64 v232, v232, v235 clamp\n\t" \
                        "v_mul_f32_e64 v233, v233, v235 clamp\n\t" \
                        "v_cvt_pk_bf16_f32 v232, v232, v233\n\t" \
                        MTA(5) "buffer_store_dword v232, %[pa], %[rw], %[p0] offen sc1\n\t" \
                        "v_mul_f32_e64 v234, v234, v235 clamp\n\t" \
                        "v_mul_f32_e64 v236, v236, v235 clamp\n\t" \
                        "buffer_store_dword v232, %[pa], %[rw], %[p1] offen sc1\n\t" \
                        "v_cvt_pk_bf16_f32 v234, v234, v236\n\t" \
                        "buffer_store_dword v234, %[pm], %[rw], %[p0] offen sc1\n\t" \
                        "s_setprio 0"
#define TAILIO : [vd] "=&s"(vd), [p0] "+s"(ps), [p1] "=&s"(ps1) \
                        : [qa] "v"(qa), [ka] "v"(ka), [vz] "v"(0u), [ls] "n"(L_S), [ix] "v"(sidx), [it] "s"(it), [pa] "v"(pa0), [pm] "v"(pa2), [rw] "s"(rwin), \
                          [k00] "v"(kx[0][0]), [k01] "v"(kx[0][1]), [k10] "v"(kx[1][0]), [k11] "v"(kx[1][1]) \
                        : "v212", "v213", "v214", "v215", "v216", "v217", "v218", "v219", "v220", "v221", "v222", "v223", "v224", "v225", "v226", "v227", "v228", "v229", "v230", "v231", "v232", \
                          "v233", "v234", "v235", "v236", "v237", "v238", "v239", "v240", "v241", "v242", "v243", "v244", "v245", "v246", "v247", "v248", "scc", "memory"
                    unsigned ps = pb, ps1;
                    if (R == 0) asm volatile(TAIL("v212", "v214", "v216", "v218", "v220", "v222") TAILIO);
                    else asm volatile(TAIL("v213", "v215", "v217", "v219", "v221", "v223") TAILIO);
                    redo = vd == it;
                    RT(4);
                    MT(6);
                } else {
                    MT(3);
                    if (TK) { __builtin_amdgcn_s_setprio(TKP); if (TKS) __builtin_amdgcn_s_sleep(TKS); }
                    redo = __builtin_amdgcn_readfirstlane(*(volatile LAS unsigned*)(smem + L_S)) == it;
                    if (redo) FC(fc0);
                }
            } while (__builtin_expect(redo, 0));
            if (s < 8) SST(8 + s);
            if (s >= 56) SST(s - 40);
            if (C + R == 1) {
                if (s > 0 && s + 1 < NSEG) {
                    __builtin_amdgcn_s_sleep(DDMA);
                    unsigned l2 = fresh_lane(), s1;
                    asm volatile("v_mov_b32 %0, -1" : "=v"(s1));
                    const unsigned base = W_P + (unsigned)((s - 1) & 3) * 10u * PAIR + nh;
                    asm volatile("s_waitcnt vmcnt(3)" ::: "memory");
                    st128(((u32x4){s1, s1, s1, s1}), rwin, (l2 >> 5) * PAIR + (l2 & 31u) * 16u, base + (unsigned)(R * 4 + 2 * (hh >> 4)) * PAIR + (unsigned)(hh & 15) * 512u, 16);
                    if (R) __builtin_amdgcn_raw_buffer_store_b64((u32x2){s1, s1}, rwin, (l2 >> 5) * PAIR + (l2 & 16u) * 256u + (l2 & 8u) * 16u + (l2 & 7u) * 8u, base + 8u * PAIR + (unsigned)((hh >> 1) * 256 + (hh & 1) * 64), 16);
                    else __builtin_amdgcn_raw_buffer_store_b32(s1, rwin, two(), W_T + (unsigned)((s + 2) & 3) * 2u * PAIR + (unsigned)hh * 128u, 16);
                }
            } else if (TK) {
                if (s + 1 < NSEG) sched2(tk, s, sf);
            } else if (C == 2 && w == 4) {
                if (s + 2 < NSEG) {
                    qrdy = max(qrdy, seg_done(MXQ, __builtin_amdgcn_readlane(qf_, 0), __builtin_amdgcn_readlane(qf_, 1)));
                    if (__builtin_expect(qrdy < s + 2, 0)) for (unsigned spins = 0;;) {
                        asm volatile("" ::: "memory");
                        const int f = (int)__builtin_amdgcn_raw_buffer_load_b32(rwin, (unsigned)(lane & 1) * 64u, W_FQ + (unsigned)hh * 128u, 16);
                        qrdy = seg_done(MXQ, __builtin_amdgcn_readlane(f, 0), __builtin_amdgcn_readlane(f, 1));
                        if (qrdy >= s + 2) break;
                        FC(fc2);
                        if (spin_fail(spins, a.ctl)) break;
                        __builtin_amdgcn_s_sleep(2);
                    }
                    const unsigned so = O_QK + (unsigned)(((s + 2) * H_ + hh) * 16384), lo = (unsigned)(L_TOK + (tbo ? tbo - 16384 : 32768)), l2 = fresh_lane();
#pragma unroll
                    for (int i = 0; i < 4; ++i) dma4k(rws, l2 * 16u, so + i * 4096u, lo + i * 4096u);
                    if (qrdy < min(s + 5, NSEG - 1))
                        asm volatile("buffer_load_dword %0, %1, %2, %3 offen sc1" : "+v"(qf_) : "v"((l2 & 1u) * 64u), "s"(rwin), "s"(W_FQ + (unsigned)hh * 128u) : "memory");
                }
            } else {
                asm volatile("s_waitcnt vmcnt(3)" ::: "memory");
                if (s > TAILS) __builtin_amdgcn_raw_buffer_store_b32((unsigned)s - 1u, rwin, 0u, W_FPT + (unsigned)(hh * 128 + (w & 3) * 4), 16);
                if (C == 2 && s + 1 < NSEG) __builtin_amdgcn_s_sleep(DPT);
                const float scale = 0.08838834764831845f, l2e = 1.44269504088896340736f * (NORND ? scale : 1.f);
                const int tq = (w >> 1) & 1, qh = w & 1;
                f32x4 sc[3] = {z4, z4, z4};
                u32x4 kt[2][4], qf[4], kf[4];
                const unsigned l2 = fresh_lane(), kto = ((l2 & 8u) * 512u + (l2 & 51u) * 16u + (l2 & 4u) * 32u) ^ ((l2 & 16u) * 5u), qto = ((l2 & 55u) * 16u + qh * 128u) ^ ((l2 & 16u) * 8u);
#pragma unroll
                for (int ks = 0; ks < 4; ++ks) {
                    qf[ks] = *(const u32x4*)(tb + 8192 + (tq * 4 + ks) * 1024 + qto);
                    kt[0][ks] = *(const u32x4*)(tb + ks * 1024 + kto);
                    kt[1][ks] = *(const u32x4*)(tb + ks * 1024 + (kto ^ 64u));
                    SB();
                }
#pragma unroll
                for (int ks = 0; ks < 4; ++ks) {
                    sc[0] = mfma32(kt[0][ks], qf[ks], sc[0]); PIN1(sc[0]);
                    kf[ks] = ldtr2(ka + ks * 256, ka + ks * 256 + 128);
                    SB();
                    sc[1] = mfma32(kt[1][ks], qf[ks], sc[1]); PIN1(sc[1]);
                }
#pragma unroll
                for (int ks = 0; ks < 4; ++ks) sc[2] = mfma32(kf[ks], qf[ks], sc[2]);
                const bool hi = (l2 & 8u) != 0u;
                float v[6];
#pragma unroll
                for (int r = 0; r < 4; ++r) v[r] = hi ? sc[1][r] : sc[0][r];
                v[4] = hi ? sc[2][2] : sc[2][0]; v[5] = hi ? sc[2][3] : sc[2][1];
#pragma unroll
                for (int r = 0; r < 6; r += 2) {
                    if (NORND) break;
                    const unsigned b0 = pack2(v[r], v[r + 1]);
                    const unsigned b1 = pack2(lo2f(b0) * scale, hi2f(b0) * scale);
                    v[r] = lo2f(b1); v[r + 1] = hi2f(b1);
                }
                const int lim = 8 * (w & 3) + (int)(l2 & 7u) - 8 * (int)(l2 >> 4) - (int)((l2 & 8u) >> 1);
#pragma unroll
                for (int r = 0; r < 4; ++r) if (r > lim) v[r] = NORND ? -1000000000.0f / scale : bfr(-1000000000.0f);
                float mx = fmx(fmx(fmx(fmx(fmx(v[0], v[1]), v[2]), v[3]), v[4]), v[5]);
                mx = xmax3216(fmx(mx, ror8f(mx)));
                const float mb = -mx * l2e;
#pragma unroll
                for (int r = 0; r < 6; ++r) v[r] = __builtin_amdgcn_exp2f(fmaf(v[r], l2e, mb));
                float sum = fmaf(v[4] + v[5], 0.5f, (v[0] + v[1]) + (v[2] + v[3]));
                sum = xsum3216(sum + ror8f(sum));
                const float inv = __builtin_amdgcn_rcpf(sum);
                const unsigned p0 = pack2(v[0] * inv, v[1] * inv), p1 = nosent1(pack2(v[2] * inv, v[3] * inv)), p2 = nosent1(pack2(v[4] * inv, v[5] * inv));
                const unsigned base = s + 1 < NSEG ? O_PT + (unsigned)(s * PTSEG) * 2u : W_PL;
                const __amdgpu_buffer_rsrc_t rp = s + 1 < NSEG ? rws : rwin;
                const unsigned oob = (l2 & 8u) << 27;
                st128(((u32x4){p0, p1, dppu<0x128>(p0), dppu<0x128>(p1)}), rp, (l2 * 16u) | oob, base + (unsigned)((hh * 2 + tq) * 1024 + qh * 128), 16);
                __builtin_amdgcn_raw_buffer_store_b64((u32x2){p2, dppu<0x128>(p2)}, rp, ((l2 & 7u) * 16u + (l2 >> 4) * 8u) | oob | (l2 & 32u) << 25, base + 65536u + (unsigned)((tq * 32 + hh) * 256 + qh * 128), 16);
            }
            tbo = tbo == 32768 ? 0 : tbo + 16384;
            if (C == 2 && __builtin_expect(budget <= 0, 0)) __hip_atomic_store(a.ctl, 1u, __ATOMIC_RELAXED, __HIP_MEMORY_SCOPE_AGENT);
            RT(5);
            RTS();
            MT(7);
            MTS();
        }
        };
        if (qt) {} else if (w == 0) steps(IC<0>()); else if (w == 1) steps(IC<1>()); else if (w == 2) steps(IC<2>()); else if (w == 3) steps(IC<3>()); else if (w < 7) steps(IC<4>()); else steps(IC<5>());
    } else if (bid >= 96 && bid < NREC) {
        LANES();
        const int o = bid - 96, hg = w & 3;
        const unsigned ztoff = (unsigned)((o * H_ + 8 * hg) * 64 + lane) * 16u;
        const unsigned zoff = zpair(o / 10, o % 10) + (unsigned)(lane * 16);
        const unsigned poff = (4u * (mc >> 1) + 2u * (hg >> 1) + (mc & 1u)) * PAIR + nh + (unsigned)((hg & 1) * 4096 + (fq >> 1) * 512 + (fr >> 2) * 128 + (fr & 1) * 64 + (fq & 1) * 32 + (fr & 2) * 8);
        const unsigned pmoff = (8u + (mc & 1u)) * PAIR + nh + (mc >> 1) * 4096u + (unsigned)(fq * 256 + (fr >> 3) * 128 + (fr & 2) * 32 + (fr & 4) * 8 + (fr & 1) * 16);
        const unsigned fzo = (unsigned)(8 * hg + (lane & 7)) * 128u + (unsigned)(o / 10) * 4u + (lane & 8) * 8u;
        const unsigned moff = (o < 32 ? (unsigned)o * 256u : 4u * PAIR + (unsigned)(o - 32) * 256u) + nh + (unsigned)(lane * 4);
        const unsigned mc2 = o < 32 ? 2u * PAIR : 2048u;
        char* op = smem + 8192;
        SST(2);
        if (w == 7) clk_init(ck);
        wait_until(tstart + DO0);
        if (w < 4) {
            u32x8 zf[4];
            int zr = -1;
            unsigned zb = W_ZM, zso = O_Z;
            const unsigned ra = (o < 32 ? (unsigned)fq * PAIR + (unsigned)o * 256u : (4u + (fq & 1)) * PAIR + (unsigned)(fq >> 1) * 2048u + (unsigned)(o - 32) * 256u) + nh + (unsigned)(fr * 16);
            auto zt_miss = [&](int f, int s) { return __any(f < s && ((lane >> 3) & 1) == (int)((MXV >> s) & 1)); };
            for (int s = 0;; ++s) {

                const unsigned pb = W_P + (unsigned)(s & 3) * 10u * PAIR;
                if (__builtin_expect(zr < s, 0)) for (unsigned spins = 0;;) {
                    asm volatile("" ::: "memory");
                    if (!zt_miss((int)__builtin_amdgcn_raw_buffer_load_b32(rwin, fzo, W_FZ, 16), s)) break;
                    FC(fc2);
                    if (spin_fail(spins, a.ctl)) break;
                    __builtin_amdgcn_s_sleep(ZPS);
                }
                if (s == 0) SST(1);
                asm volatile(
                    "buffer_load_dwordx4 v[160:163], %[zt], %[rs], %[so] offen sc1\n\t"
                    "buffer_load_dwordx4 v[164:167], %[zt], %[rs], %[so] offen offset:1024 sc1\n\t"
                    "buffer_load_dwordx4 v[168:171], %[zt], %[rs], %[so] offen offset:2048 sc1\n\t"
                    "buffer_load_dwordx4 v[172:175], %[zt], %[rs], %[so] offen offset:3072 sc1\n\t"
                    "buffer_load_dwordx4 v[176:179], %[zt], %[rs], %[s4] offen sc1\n\t"
                    "buffer_load_dwordx4 v[180:183], %[zt], %[rs], %[s4] offen offset:1024 sc1\n\t"
                    "buffer_load_dwordx4 v[184:187], %[zt], %[rs], %[s4] offen offset:2048 sc1\n\t"
                    "buffer_load_dwordx4 v[188:191], %[zt], %[rs], %[s4] offen offset:3072 sc1"
                    : "=&{v[160:167]}"(zf[0]), "=&{v[168:175]}"(zf[1]), "=&{v[176:183]}"(zf[2]), "=&{v[184:191]}"(zf[3])
                    : [zt] "v"(ztoff), [rs] "s"(rws), [so] "s"(zso), [s4] "s"(zso + 4096u) : "memory");
                zso = O_Z + (unsigned)((s + 1) * ZTSEG) * 2u;
                if (__builtin_expect(budget <= 0, 0)) __hip_atomic_store(a.ctl, 1u, __ATOMIC_RELAXED, __HIP_MEMORY_SCOPE_AGENT);
                RT(6);
                if (s + 1 == NSEG) break;
                const unsigned spa = (unsigned)((s & 1) * 2048 + (w < 3 ? w * 512 : 0) + lane * 8);
#define OST(r)
                unsigned long long t0_, t1_, t2_, t3_, t4_ = 0, bad;
                int tries = budget;
                unsigned po = poff, pm = pmoff, x0 = 4u * PAIR, x1 = 4096u;
                const unsigned mb = W_MEM + (unsigned)((s + 1) & 3) * 6u * PAIR;
                if (w < 3) asm volatile(
                    "s_barrier\n\t"
                    OST("%[t0]")
                    "v_mov_b64 v[96:97], 0\n\t"
                    "v_mov_b64 v[98:99], 0\n\t"
                    "v_mov_b32 v100, 0\n\t"
                    "s_barrier\n\t"
                    "1:\n\t"
                    "buffer_load_dwordx4 v[208:211], %[po], %[rw], %[pb] offen sc1\n\t"
                    "buffer_load_dwordx4 v[212:215], %[po], %[rw], %[pb] offen offset:1024 sc1\n\t"
                    "buffer_load_dwordx4 v[216:219], %[po], %[rw], %[pb] offen offset:2048 sc1\n\t"
                    "buffer_load_dwordx4 v[220:223], %[po], %[rw], %[pb] offen offset:3072 sc1\n\t"
                    OST("%[t1]")
                    "s_waitcnt vmcnt(3)\n\t"
                    OST("%[t3]")
                    "v_smfmac_f32_16x16x64_bf16 v[96:99], v[208:211], v[160:167], %[ix]\n\t"
                    "v_max3_u32 v100, v100, v208, v209\n\t"
                    "v_max3_u32 v100, v100, v210, v211\n\t"
                    "s_waitcnt vmcnt(2)\n\t"
                    "v_smfmac_f32_16x16x64_bf16 v[96:99], v[212:215], v[168:175], %[ix]\n\t"
                    "v_max3_u32 v100, v100, v212, v213\n\t"
                    "v_max3_u32 v100, v100, v214, v215\n\t"
                    "s_waitcnt vmcnt(1)\n\t"
                    "v_smfmac_f32_16x16x64_bf16 v[96:99], v[216:219], v[176:183], %[ix]\n\t"
                    "v_max3_u32 v100, v100, v216, v217\n\t"
                    "v_max3_u32 v100, v100, v218, v219\n\t"
                    "s_waitcnt vmcnt(0)\n\t"
                    "v_smfmac_f32_16x16x64_bf16 v[96:99], v[220:223], v[184:191], %[ix]\n\t"
                    "v_max3_u32 v100, v100, v220, v221\n\t"
                    "v_max3_u32 v100, v100, v222, v223\n\t"
                    "v_cmp_eq_u32 vcc, -1, v100\n\t"
                    OST("%[t2]")
                    "s_nop 4\n\t"
                    "v_pk_add_f32 v[96:97], v[96:97], v[98:99]\n\t"
                    "ds_write_b64 %[sp], v[96:97]\n\t"
                    "s_cbranch_vccz 2f\n\t"
                    "s_sub_i32 %[tr], %[tr], 1\n\t"
                    "s_cmp_lt_i32 %[tr], 1\n\t"
                    "s_cbranch_scc1 2f\n\t"
                    "v_xor_b32 %[po], %[x0], %[po]\n\t"
                    "s_xor_b32 %[x0], %[x0], 0x14000\n\t"
                    "s_waitcnt lgkmcnt(0)\n\t"
                    "v_mov_b64 v[96:97], 0\n\t"
                    "v_mov_b64 v[98:99], 0\n\t"
                    "v_mov_b32 v100, 0\n\t"
                    "s_branch 1b\n\t"
                    "2:\n\t"
                    "s_waitcnt lgkmcnt(0)\n\t"
                    "s_barrier\n\t"
                    "s_sleep 3\n\t"
                    : [tr] "+s"(tries), [po] "+v"(po), [x0] "+s"(x0), [t0] "=&s"(t0_), [t1] "=&s"(t1_), [t2] "=&s"(t2_), [t3] "=&s"(t3_)
                    : [sp] "v"(spa), [ix] "v"(sidx), [rw] "s"(rwin), [pb] "s"(pb), "{v[160:167]}"(zf[0]), "{v[168:175]}"(zf[1]), "{v[176:183]}"(zf[2]), "{v[184:191]}"(zf[3])
                    : "v96", "v97", "v98", "v99", "v100", "v208", "v209", "v210", "v211", "v212", "v213", "v214", "v215", "v216", "v217", "v218", "v219", "v220", "v221", "v222", "v223", "vcc", "scc", "memory");
                else asm volatile(
                    "s_barrier\n\t"
                    "buffer_load_dwordx4 v[128:131], %[zo], %[rw], %[zb] offen sc1\n\t"
                    "buffer_load_dwordx4 v[132:135], %[zo], %[rw], %[zb] offen offset:1024 sc1\n\t"
                    "buffer_load_dwordx4 v[136:139], %[zo], %[rw], %[zb] offen offset:2048 sc1\n\t"
                    "buffer_load_dwordx4 v[140:143], %[zo], %[rw], %[zb] offen offset:3072 sc1\n\t"
                    "buffer_load_dwordx4 v[144:147], %[zo], %[rw], %[z4] offen sc1\n\t"
                    "buffer_load_dwordx4 v[148:151], %[zo], %[rw], %[z4] offen offset:1024 sc1\n\t"
                    "buffer_load_dwordx4 v[152:155], %[zo], %[rw], %[z4] offen offset:2048 sc1\n\t"
                    "buffer_load_dwordx4 v[156:159], %[zo], %[rw], %[z4] offen offset:3072 sc1\n\t"
                    OST("%[t0]")
                    "v_mov_b64 v[96:97], 0\n\t"
                    "v_mov_b64 v[98:99], 0\n\t"
                    "v_mov_b32 v100, 0\n\t"
                    "s_barrier\n\t"
                    "s_sleep %[fd]\n\t"
                    "buffer_load_dwordx4 v[208:211], %[po], %[rw], %[pb] offen sc1\n\t"
                    "buffer_load_dwordx4 v[212:215], %[po], %[rw], %[pb] offen offset:1024 sc1\n\t"
                    "buffer_load_dwordx4 v[216:219], %[po], %[rw], %[pb] offen offset:2048 sc1\n\t"
                    "buffer_load_dwordx4 v[220:223], %[po], %[rw], %[pb] offen offset:3072 sc1\n\t"
                    "buffer_load_dwordx4 v[224:227], %[pm], %[rw], %[pb] offen sc1\n\t"
                    "buffer_load_dwordx4 v[228:231], %[pm], %[rw], %[pb] offen offset:1024 sc1\n\t"
                    "buffer_load_dwordx4 v[232:235], %[pm], %[rw], %[pb] offen offset:2048 sc1\n\t"
                    "buffer_load_dwordx4 v[236:239], %[pm], %[rw], %[pb] offen offset:3072 sc1\n\t"
                    OST("%[t1]")
                    "s_waitcnt vmcnt(8)\n\t"
                    "v_max3_u32 v101, v129, v131, v133\n\t"
                    "v_max3_u32 v101, v101, v135, v137\n\t"
                    "v_max3_u32 v101, v101, v139, v141\n\t"
                    "v_max3_u32 v101, v101, v143, v145\n\t"
                    "v_max3_u32 v101, v101, v147, v149\n\t"
                    "v_max3_u32 v101, v101, v151, v153\n\t"
                    "v_max3_u32 v101, v101, v155, v157\n\t"
                    "v_max_u32 v101, v101, v159\n\t"
                    "v_cmp_eq_u32 vcc, -1, v101\n\t"
                    "s_cbranch_vccz 3f\n\t"
                    "buffer_load_dwordx4 v[128:131], %[zo], %[rw], %[zb] offen sc1\n\t"
                    "buffer_load_dwordx4 v[132:135], %[zo], %[rw], %[zb] offen offset:1024 sc1\n\t"
                    "buffer_load_dwordx4 v[136:139], %[zo], %[rw], %[zb] offen offset:2048 sc1\n\t"
                    "buffer_load_dwordx4 v[140:143], %[zo], %[rw], %[zb] offen offset:3072 sc1\n\t"
                    "buffer_load_dwordx4 v[144:147], %[zo], %[rw], %[z4] offen sc1\n\t"
                    "buffer_load_dwordx4 v[148:151], %[zo], %[rw], %[z4] offen offset:1024 sc1\n\t"
                    "buffer_load_dwordx4 v[152:155], %[zo], %[rw], %[z4] offen offset:2048 sc1\n\t"
                    "buffer_load_dwordx4 v[156:159], %[zo], %[rw], %[z4] offen offset:3072 sc1\n\t"
                    "s_waitcnt vmcnt(0)\n\t"
                    "v_max3_u32 v101, v129, v131, v133\n\t"
                    "v_max3_u32 v101, v101, v135, v137\n\t"
                    "v_max3_u32 v101, v101, v139, v141\n\t"
                    "v_max3_u32 v101, v101, v143, v145\n\t"
                    "v_max3_u32 v101, v101, v147, v149\n\t"
                    "v_max3_u32 v101, v101, v151, v153\n\t"
                    "v_max3_u32 v101, v101, v155, v157\n\t"
                    "v_max_u32 v101, v101, v159\n\t"
                    "v_mov_b32 v100, v101\n\t"
                    "3:\n\t"
                    "v_permlane16_swap_b32 v128, v129\n\t"
                    "v_permlane16_swap_b32 v130, v131\n\t"
                    "v_permlane16_swap_b32 v132, v133\n\t"
                    "v_permlane16_swap_b32 v134, v135\n\t"
                    "v_permlane16_swap_b32 v136, v137\n\t"
                    "v_permlane16_swap_b32 v138, v139\n\t"
                    "v_permlane16_swap_b32 v140, v141\n\t"
                    "v_permlane16_swap_b32 v142, v143\n\t"
                    "v_permlane16_swap_b32 v144, v145\n\t"
                    "v_permlane16_swap_b32 v146, v147\n\t"
                    "v_permlane16_swap_b32 v148, v149\n\t"
                    "v_permlane16_swap_b32 v150, v151\n\t"
                    "v_permlane16_swap_b32 v152, v153\n\t"
                    "v_permlane16_swap_b32 v154, v155\n\t"
                    "v_permlane16_swap_b32 v156, v157\n\t"
                    "v_permlane16_swap_b32 v158, v159\n\t"
                    "s_mov_b32 vcc_lo, 0x00ff00ff\n\t"
                    "s_mov_b32 vcc_hi, 0x00ff00ff\n\t"
                    "s_waitcnt vmcnt(7)\n\t"
                    OST("%[t3]")
                    "v_smfmac_f32_16x16x64_bf16 v[96:99], v[208:211], v[160:167], %[ix]\n\t"
                    "v_max3_u32 v100, v100, v208, v209\n\t"
                    "v_max3_u32 v100, v100, v210, v211\n\t"
                    "s_waitcnt vmcnt(6)\n\t"
                    "v_smfmac_f32_16x16x64_bf16 v[96:99], v[212:215], v[168:175], %[ix]\n\t"
                    "v_max3_u32 v100, v100, v212, v213\n\t"
                    "v_max3_u32 v100, v100, v214, v215\n\t"
                    "s_waitcnt vmcnt(5)\n\t"
                    "v_smfmac_f32_16x16x64_bf16 v[96:99], v[216:219], v[176:183], %[ix]\n\t"
                    "v_max3_u32 v100, v100, v216, v217\n\t"
                    "v_max3_u32 v100, v100, v218, v219\n\t"
                    "s_waitcnt vmcnt(4)\n\t"
                    "v_smfmac_f32_16x16x64_bf16 v[96:99], v[220:223], v[184:191], %[ix]\n\t"
                    "v_max3_u32 v100, v100, v220, v221\n\t"
                    "v_max3_u32 v100, v100, v222, v223\n\t"
                    "s_waitcnt vmcnt(3)\n\t"
                    "v_smfmac_f32_16x16x64_bf16 v[96:99], v[224:227], v[128:135], %[ix]\n\t"
                    "v_max3_u32 v100, v100, v224, v225\n\t"
                    "v_max3_u32 v100, v100, v226, v227\n\t"
                    "s_waitcnt vmcnt(2)\n\t"
                    "v_smfmac_f32_16x16x64_bf16 v[96:99], v[228:231], v[136:143], %[ix]\n\t"
                    "s_barrier\n\t"
                    "ds_read_b64 v[102:103], %[sp]\n\t"
                    "ds_read_b64 v[104:105], %[sp] offset:512\n\t"
                    "ds_read_b64 v[106:107], %[sp] offset:1024\n\t"
                    "v_max3_u32 v100, v100, v228, v229\n\t"
                    "v_max3_u32 v100, v100, v230, v231\n\t"
                    "s_waitcnt vmcnt(1)\n\t"
                    "v_smfmac_f32_16x16x64_bf16 v[96:99], v[232:235], v[144:151], %[ix]\n\t"
                    "v_max3_u32 v100, v100, v232, v233\n\t"
                    "v_max3_u32 v100, v100, v234, v235\n\t"
                    "s_waitcnt vmcnt(0)\n\t"
                    "v_smfmac_f32_16x16x64_bf16 v[96:99], v[236:239], v[152:159], %[ix]\n\t"
                    "v_max3_u32 v100, v100, v236, v237\n\t"
                    "v_max3_u32 v100, v100, v238, v239\n\t"
                    OST("%[t2]")
                    "v_cmp_eq_u32 %[bad], -1, v100\n\t"
                    "s_waitcnt lgkmcnt(0)\n\t"
                    "v_add_f32 v102, v102, v104\n\t"
                    "v_add_f32 v103, v103, v105\n\t"
                    "v_add_f32 v102, v102, v106\n\t"
                    "v_add_f32 v103, v103, v107\n\t"
                    "v_add_f32 v96, v96, v102\n\t"
                    "v_add_f32 v97, v97, v103\n\t"
                    "v_add_f32 v96, v96, v98\n\t"
                    "v_add_f32 v97, v97, v99\n\t"
                    "s_cmp_lg_u64 %[bad], 0\n\t"
                    "s_cbranch_scc1 4f\n\t"
                    "2:\n\t"
                    "v_cndmask_b32_dpp v98, v97, v96, vcc row_ror:8 row_mask:0xf bank_mask:0xf\n\t"
                    "s_not_b64 vcc, vcc\n\t"
                    "v_cndmask_b32_dpp v99, v96, v97, vcc row_ror:8 row_mask:0xf bank_mask:0xf\n\t"
                    "v_cvt_pk_bf16_f32 v98, v98, v99\n\t"
                    "v_min_u32 v98, -2, v98\n\t"
                    "buffer_store_dword v98, %[mo], %[rw], %[m0] offen sc1\n\t"
                    "buffer_store_dword v98, %[mo], %[rw], %[m1] offen sc1\n\t"
                    "buffer_store_dword v98, %[mo], %[rw], %[m2] offen sc1\n\t"
                    "buffer_store_dword v98, %[mo], %[rw], %[m3] offen sc1\n\t"
                    OST("%[t4]")
                    "s_branch 5f\n\t"
                    "4:\n\t"
                    "s_sub_i32 %[tr], %[tr], 1\n\t"
                    "s_cmp_lt_i32 %[tr], 1\n\t"
                    "s_cbranch_scc1 2b\n\t"
                    "v_xor_b32 %[po], %[x0], %[po]\n\t"
                    "s_xor_b32 %[x0], %[x0], 0x14000\n\t"
                    "v_xor_b32 %[pm], %[x1], %[pm]\n\t"
                    "s_xor_b32 %[x1], %[x1], 0x5000\n\t"
                    "buffer_load_dwordx4 v[128:131], %[zo], %[rw], %[zb] offen sc1\n\t"
                    "buffer_load_dwordx4 v[132:135], %[zo], %[rw], %[zb] offen offset:1024 sc1\n\t"
                    "buffer_load_dwordx4 v[136:139], %[zo], %[rw], %[zb] offen offset:2048 sc1\n\t"
                    "buffer_load_dwordx4 v[140:143], %[zo], %[rw], %[zb] offen offset:3072 sc1\n\t"
                    "buffer_load_dwordx4 v[144:147], %[zo], %[rw], %[z4] offen sc1\n\t"
                    "buffer_load_dwordx4 v[148:151], %[zo], %[rw], %[z4] offen offset:1024 sc1\n\t"
                    "buffer_load_dwordx4 v[152:155], %[zo], %[rw], %[z4] offen offset:2048 sc1\n\t"
                    "buffer_load_dwordx4 v[156:159], %[zo], %[rw], %[z4] offen offset:3072 sc1\n\t"
                    "buffer_load_dwordx4 v[208:211], %[po], %[rw], %[pb] offen sc1\n\t"
                    "buffer_load_dwordx4 v[212:215], %[po], %[rw], %[pb] offen offset:1024 sc1\n\t"
                    "buffer_load_dwordx4 v[216:219], %[po], %[rw], %[pb] offen offset:2048 sc1\n\t"
                    "buffer_load_dwordx4 v[220:223], %[po], %[rw], %[pb] offen offset:3072 sc1\n\t"
                    "buffer_load_dwordx4 v[224:227], %[pm], %[rw], %[pb] offen sc1\n\t"
                    "buffer_load_dwordx4 v[228:231], %[pm], %[rw], %[pb] offen offset:1024 sc1\n\t"
                    "buffer_load_dwordx4 v[232:235], %[pm], %[rw], %[pb] offen offset:2048 sc1\n\t"
                    "buffer_load_dwordx4 v[236:239], %[pm], %[rw], %[pb] offen offset:3072 sc1\n\t"
                    "v_mov_b64 v[96:97], 0\n\t"
                    "v_mov_b64 v[98:99], 0\n\t"
                    "v_mov_b32 v100, 0\n\t"
                    "s_waitcnt vmcnt(0)\n\t"
                    "v_max3_u32 v101, v129, v131, v133\n\t"
                    "v_max3_u32 v101, v101, v135, v137\n\t"
                    "v_max3_u32 v101, v101, v139, v141\n\t"
                    "v_max3_u32 v101, v101, v143, v145\n\t"
                    "v_max3_u32 v101, v101, v147, v149\n\t"
                    "v_max3_u32 v101, v101, v151, v153\n\t"
                    "v_max3_u32 v101, v101, v155, v157\n\t"
                    "v_max_u32 v101, v101, v159\n\t"
                    "v_mov_b32 v100, v101\n\t"
                    "v_permlane16_swap_b32 v128, v129\n\t"
                    "v_permlane16_swap_b32 v130, v131\n\t"
                    "v_permlane16_swap_b32 v132, v133\n\t"
                    "v_permlane16_swap_b32 v134, v135\n\t"
                    "v_permlane16_swap_b32 v136, v137\n\t"
                    "v_permlane16_swap_b32 v138, v139\n\t"
                    "v_permlane16_swap_b32 v140, v141\n\t"
                    "v_permlane16_swap_b32 v142, v143\n\t"
                    "v_permlane16_swap_b32 v144, v145\n\t"
                    "v_permlane16_swap_b32 v146, v147\n\t"
                    "v_permlane16_swap_b32 v148, v149\n\t"
                    "v_permlane16_swap_b32 v150, v151\n\t"
                    "v_permlane16_swap_b32 v152, v153\n\t"
                    "v_permlane16_swap_b32 v154, v155\n\t"
                    "v_permlane16_swap_b32 v156, v157\n\t"
                    "v_permlane16_swap_b32 v158, v159\n\t"
                    "s_nop 1\n\t"
                    "v_smfmac_f32_16x16x64_bf16 v[96:99], v[208:211], v[160:167], %[ix]\n\t"
                    "v_max3_u32 v100, v100, v208, v209\n\t"
                    "v_max3_u32 v100, v100, v210, v211\n\t"
                    "v_smfmac_f32_16x16x64_bf16 v[96:99], v[212:215], v[168:175], %[ix]\n\t"
                    "v_max3_u32 v100, v100, v212, v213\n\t"
                    "v_max3_u32 v100, v100, v214, v215\n\t"
                    "v_smfmac_f32_16x16x64_bf16 v[96:99], v[216:219], v[176:183], %[ix]\n\t"
                    "v_max3_u32 v100, v100, v216, v217\n\t"
                    "v_max3_u32 v100, v100, v218, v219\n\t"
                    "v_smfmac_f32_16x16x64_bf16 v[96:99], v[220:223], v[184:191], %[ix]\n\t"
                    "v_max3_u32 v100, v100, v220, v221\n\t"
                    "v_max3_u32 v100, v100, v222, v223\n\t"
                    "v_smfmac_f32_16x16x64_bf16 v[96:99], v[224:227], v[128:135], %[ix]\n\t"
                    "v_max3_u32 v100, v100, v224, v225\n\t"
                    "v_max3_u32 v100, v100, v226, v227\n\t"
                    "v_smfmac_f32_16x16x64_bf16 v[96:99], v[228:231], v[136:143], %[ix]\n\t"
                    "v_max3_u32 v100, v100, v228, v229\n\t"
                    "v_max3_u32 v100, v100, v230, v231\n\t"
                    "v_smfmac_f32_16x16x64_bf16 v[96:99], v[232:235], v[144:151], %[ix]\n\t"
                    "v_max3_u32 v100, v100, v232, v233\n\t"
                    "v_max3_u32 v100, v100, v234, v235\n\t"
                    "v_smfmac_f32_16x16x64_bf16 v[96:99], v[236:239], v[152:159], %[ix]\n\t"
                    "v_max3_u32 v100, v100, v236, v237\n\t"
                    "v_max3_u32 v100, v100, v238, v239\n\t"
                    "v_cmp_eq_u32 %[bad], -1, v100\n\t"
                    "s_nop 5\n\t"
                    "v_add_f32 v96, v96, v102\n\t"
                    "v_add_f32 v97, v97, v103\n\t"
                    "v_add_f32 v96, v96, v98\n\t"
                    "v_add_f32 v97, v97, v99\n\t"
                    "s_cmp_lg_u64 %[bad], 0\n\t"
                    "s_cbranch_scc1 4b\n\t"
                    "s_branch 2b\n\t"
                    "5:\n\t"
                    : [tr] "+s"(tries), [po] "+v"(po), [pm] "+v"(pm), [x0] "+s"(x0), [x1] "+s"(x1), [bad] "=&s"(bad), [t0] "=&s"(t0_), [t1] "=&s"(t1_), [t2] "=&s"(t2_), [t3] "=&s"(t3_), [t4] "=&s"(t4_)
                    : [zo] "v"(zoff), [sp] "v"(spa), [ix] "v"(sidx), [rw] "s"(rwin), [zb] "s"(zb), [z4] "s"(zb + 4096u), [pb] "s"(pb), [mo] "v"(moff), [fd] "n"(FDL),
                      [m0] "s"(mb), [m1] "s"(mb + PAIR), [m2] "s"(mb + mc2), [m3] "s"(mb + mc2 + PAIR), "{v[160:167]}"(zf[0]), "{v[168:175]}"(zf[1]), "{v[176:183]}"(zf[2]), "{v[184:191]}"(zf[3])
                    : "v96", "v97", "v98", "v99", "v100", "v101", "v102", "v103", "v104", "v105", "v106", "v107", "v128", "v129", "v130", "v131", "v132", "v133", "v134", "v135", "v136", "v137", "v138", "v139", "v140", "v141", "v142", "v143", "v144", "v145", "v146", "v147", "v148", "v149", "v150", "v151", "v152", "v153", "v154", "v155", "v156", "v157", "v158", "v159", "v208", "v209", "v210", "v211", "v212", "v213", "v214", "v215", "v216", "v217", "v218", "v219", "v220", "v221", "v222", "v223", "v224", "v225", "v226", "v227", "v228", "v229", "v230", "v231", "v232", "v233", "v234", "v235", "v236", "v237", "v238", "v239", "vcc", "scc", "memory");
                budget = tries;
                if (w == 3) *(volatile LAS int*)(smem + 4100) = s;
                __builtin_amdgcn_s_sleep(DPS - 4);
                if (w == 3) {
                    st128(SENT, rwin, ra, W_MEM + (unsigned)(s & 3) * 6u * PAIR, 16);
                    if (lane == 0 && s > 1) __builtin_amdgcn_raw_buffer_store_b32((unsigned)s - 2u, rwin, 0u, W_GO + (unsigned)o * 128u, 16);
                }
                for (;;) {
                    const u32x2 x = *(volatile LAS u32x2*)(smem + 4096);
                    zr = __builtin_amdgcn_readfirstlane(x[0]);
                    if (__builtin_amdgcn_readfirstlane(x[1]) == s) break;
                    __builtin_amdgcn_s_sleep(2);
                }
                zb = zb == W_ZM + 2u * NOWN * PAIR ? W_ZM : zb + NOWN * PAIR;
            }
            asm volatile("s_waitcnt vmcnt(0)" : "+{v[160:167]}"(zf[0]), "+{v[168:175]}"(zf[1]), "+{v[176:183]}"(zf[2]), "+{v[184:191]}"(zf[3]) :: "memory");
            BAR0();
            BAR0();
            f32x4 c[2];
            for (unsigned spins = 0;;) {
                asm volatile("" ::: "memory");
                u32x4 pt[16], ptm[2][2], zm[2];
#pragma unroll
                for (int q = 0; q < 2; ++q) zm[q] = __builtin_amdgcn_raw_buffer_load_b128(rwin, zoff + hg * 2048u + q * 1024u, zb, 16);
#pragma unroll
                for (int q = 0; q < 16; ++q) pt[q] = __builtin_amdgcn_raw_buffer_load_b128(rwin, (unsigned)(16 * hg) * 1024u + lane * 16u, W_PL + q * 1024u, 16);
#pragma unroll
                for (int q = 0; q < 4; ++q) ptm[q >> 1][q & 1] = __builtin_amdgcn_raw_buffer_load_b128(rwin, (unsigned)(hg * 2048 + lane * 16), W_PL + 65536u + (q >> 1) * 8192u + (q & 1) * 1024u, 16);
                unsigned m = max(smax(zm[0]), smax(zm[1]));
#pragma unroll
                for (int t = 0; t < 2; ++t) {
                    c[t] = z4;
#pragma unroll
                    for (int q = 0; q < 8; ++q) { c[t] = mfma32(half8(zf[q >> 1], q & 1), pt[2 * q + t], c[t]); m = max(m, smax(pt[2 * q + t])); }
#pragma unroll
                    for (int q = 0; q < 2; ++q) { c[t] = mfma32((u32x4){zm[q][0], zm[q][2], zm[q][1], zm[q][3]}, ptm[t][q], c[t]); m = max(m, smax(ptm[t][q])); }
                }
                if (!__any(m == 0xFFFFFFFFu)) break;
                if (spin_fail(spins, a.ctl)) break;
            }
#pragma unroll
            for (int t = 0; t < 2; ++t)
#pragma unroll
                for (int r = 0; r < 4; ++r) *(float*)(op + hg * 2048 + (16 * t + fr) * 64 + pi16(4 * fq + r) * 4) = c[t][r];
            BAR();
        } else if (w == 4) {
            const unsigned fo = (unsigned)(lane & 31) * 128u + (unsigned)(o / 10) * 4u + (lane & 32) * 2u;
            int zk = -1;
            if (lane < 2) *(volatile LAS int*)(smem + 4096 + lane * 4) = -1;
            for (int s = 0; s < NSEG; ++s) {
                asm volatile("s_waitcnt lgkmcnt(0)\n\ts_barrier\n\ts_barrier" ::: "memory");
                int f = 0;
                const bool look = zk < min(s + ZLA, NSEG - 1);
                if (look) { __builtin_amdgcn_s_sleep(6); f = (int)__builtin_amdgcn_raw_buffer_load_b32(rwin, fo, W_FZ, 16); }
                SB();
                BAR0();
                if (look) {
                    f = min(f, __builtin_amdgcn_update_dpp(f, f, 0xB1, 0xF, 0xF, false)); f = min(f, __builtin_amdgcn_update_dpp(f, f, 0x4E, 0xF, 0xF, false));
                    f = min(f, __builtin_amdgcn_update_dpp(f, f, 0x141, 0xF, 0xF, false)); f = min(f, __builtin_amdgcn_update_dpp(f, f, 0x140, 0xF, 0xF, false));
                    zk = seg_done(MXV, min(__builtin_amdgcn_readlane(f, 0), __builtin_amdgcn_readlane(f, 16)), min(__builtin_amdgcn_readlane(f, 32), __builtin_amdgcn_readlane(f, 48)));
                    *(volatile LAS int*)(smem + 4096) = zk;
                }
            }
        } else if (w == 5) {
            for (int s = 0; s < 3 * NSEG; ++s) BAR0();
        } else if (w == 6) {
            const unsigned ooff = (unsigned)((S_ - SEG + (lane >> 1)) * D_ + o * 16 + (lane & 1) * 8) * 2u;
            for (int s = 0; s < 3 * NSEG - 2; ++s) BAR0();
            const u32x4 xr = __builtin_amdgcn_raw_buffer_load_b128(rws, ooff, O_X1, 16);
            asm volatile("s_barrier\n\ts_barrier ; 6" ::: "memory");
            const char* src = op + lane * 32;
            f32x4 x = *(const f32x4*)src, y = *(const f32x4*)(src + 16);
#pragma unroll
            for (int ww = 1; ww < 4; ++ww) { x += *(const f32x4*)(src + ww * 2048); y += *(const f32x4*)(src + ww * 2048 + 16); }
            const u32x4 r = {pack2(lo2f(xr[0]) + bfr(x[0]), hi2f(xr[0]) + bfr(x[1])), pack2(lo2f(xr[1]) + bfr(x[2]), hi2f(xr[1]) + bfr(x[3])),
                             pack2(lo2f(xr[2]) + bfr(y[0]), hi2f(xr[2]) + bfr(y[1])), pack2(lo2f(xr[3]) + bfr(y[2]), hi2f(xr[3]) + bfr(y[3]))};
            st128(r, mkrsrc(a.out, S_ * D_ * 2), ooff, 0u, 16);
            if (a.Fnext) {
                VMWAIT();
                if (lane == 0) __builtin_amdgcn_raw_buffer_store_b32(0u, rwin, 0u, W_OUT + (unsigned)(NSEG - 1) * 256u + (unsigned)o * 4u, 16);
            }
        } else {
            Tk tk = tk_init();
            __builtin_amdgcn_s_setprio(3);
            for (int s = 0; s < NSEG; ++s) {
                const unsigned ta0 = ck.ta, mm0 = ck.ma;
                unsigned tp = tpp + lt + xs + M1;
                if (s < SAD) { sched(tk, s); tp = tpp - M2 - XS0 - PEN1; }
                wait_fine_bar(tp - DZP, ck);
                wait_cyc_bar(cyc_at(ck, sx24(tp + (s + 1 < NSEG ? 0 : ML) - ck.ta)));
                clk_rate(ck, ta0, mm0);
                asm volatile("s_barrier ; 7" ::: "memory");
                if (s >= SAD && s + 1 < NSEG) sched(tk, s);
                RTS();
            }
        }
        SST(23);
    }
    LANES();
    for (int ph = 0; ph < 2; ++ph) {
        if (ph ? bid >= NQT && bid < NQT + NOWN : bid >= NREC && bid < NQT) {
            const int o = bid - (ph ? NQT : NREC), cg = o >> 1, sbeg = (ph ? TAILS : 0) + (o & 1), send = ph ? NSEG - 1 : TAILS;
            const unsigned ztoff = (unsigned)((2 * cg * H_ + 4 * w) * 64 + lane) * 16u;
            const unsigned ptoff = (unsigned)(8 * w * 64 + lane) * 16u, ptmoff = 65536u + (unsigned)(4 * w * 16 + lane) * 16u;
            const unsigned ooff = (unsigned)(((w & 1) * 16 + (lane >> 2)) * D_ + cg * 32 + (lane & 3) * 8) * 2u;
            const unsigned tfoff = lane < 16 ? W_FPT + (unsigned)((4 * w + (lane & 3)) * 128 + (lane >> 2) * 4) : W_FZ2 + (unsigned)((4 * w + (lane & 3)) * 128 + (cg / 5) * 4);
            int rdy = -1;
            if (ph) {
                if (w == 0) for (unsigned spins = 0;;) {
                    asm volatile("" ::: "memory");
                    if ((int)__builtin_amdgcn_readfirstlane(__builtin_amdgcn_raw_buffer_load_b32(rwin, 0u, W_GO + (unsigned)o * 128u, 16)) >= TAILS - 3) break;
                    if (spin_fail(spins, a.ctl)) break;
                    __builtin_amdgcn_s_sleep(OSLP);
                }
                __syncthreads();
                for (unsigned spins = 0;;) {
                    asm volatile("" ::: "memory");
                    if (!__any((int)__builtin_amdgcn_raw_buffer_load_b32(rwin, (unsigned)(4 * w + (lane & 3)) * 128u + (unsigned)(cg / 5) * 4u, W_FZ, 16) < NSEG - 2)) break;
                    if (spin_fail(spins, a.ctl)) break;
                    __builtin_amdgcn_s_sleep(8);
                }
            }
            auto wait_rdy = [&](int s) {
                for (unsigned spins = 0; rdy < s;) {
                    asm volatile("" ::: "memory");
                    if (!ph) {
                        rdy = (int)__builtin_amdgcn_readfirstlane(__builtin_amdgcn_raw_buffer_load_b32(rwin, 0u, W_GO + (unsigned)o * 128u, 16));
                    } else {
                        const int f = (int)__builtin_amdgcn_raw_buffer_load_b32(rwin, tfoff, 0u, 16);
                        if (!__any(f < s)) rdy = s;
                    }
                    if (rdy >= s) break;
                    if (spin_fail(spins, a.ctl)) break;
                    if (ph) __builtin_amdgcn_s_sleep(OSLP2); else __builtin_amdgcn_s_sleep(OSLP);
                }
            };
            for (int s = sbeg; s < send; s += 2) {
                RT(0);
                u32x4 xr, zf[2][4], zx[2], pt[8], ptm[2];
                if (!ph) wait_rdy(max(s - 2, 0));
                if (w >= 6) xr = __builtin_amdgcn_raw_buffer_load_b128(rws, ooff, O_X1 + (unsigned)(s * SEG * D_ * 2), 16);
#pragma unroll
                for (int q = 0; q < 8; ++q) zf[q >> 2][q & 3] = __builtin_amdgcn_raw_buffer_load_b128(rws, ztoff + (q & 3) * 1024u, O_Z + (unsigned)(s * ZTSEG) * 2u + (q >> 2) * (H_ * 1024u), 16);
                wait_rdy(s);
                RT(1);
                const unsigned ptb = O_PT + (unsigned)(s * PTSEG) * 2u;
#pragma unroll
                for (int q = 0; q < 8; ++q) pt[q] = __builtin_amdgcn_raw_buffer_load_b128(rws, ptoff + q * 1024u, ptb, 16);
#pragma unroll
                for (int q = 0; q < 2; ++q) {
                    zx[q] = __builtin_amdgcn_raw_buffer_load_b128(rws, tid * 16u, O_Z2 + (unsigned)(s * NOWN + 2 * cg + q) * 8192u, 16);
                    ptm[q] = __builtin_amdgcn_raw_buffer_load_b128(rws, ptmoff + q * 8192u, ptb, 16);
                }
                char* op = smem + ((s >> 1) & 1) * 32768;
#pragma unroll
                for (int u = 0; u < 4; ++u) {
                    const int tq = u >> 1, t = u & 1;
                    f32x4 c = z4;
#pragma unroll
                    for (int q = 0; q < 4; ++q) c = mfma32(zf[t][q], pt[2 * q + tq], c);
                    c = mfma32((u32x4){zx[t][0], zx[t][2], zx[t][1], zx[t][3]}, ptm[tq], c);
                    *(f32x4*)(op + w * 4096 + (16 * tq + fr) * 128 + (((t * 4 + fq) ^ (fr & 7)) << 4)) = c;
                }
                __syncthreads();
                if (w >= 6) {
                    const int tk = (w & 1) * 16 + (lane >> 2);
                    f32x2 x[4];
#pragma unroll
                    for (int q = 0; q < 4; ++q) {
                        const char* sp = op + tk * 128 + ((((lane & 2) * 2 + q) ^ (tk & 7)) << 4) + (lane & 1) * 8;
                        x[q] = *(const f32x2*)sp;
#pragma unroll
                        for (int ww = 1; ww < 8; ++ww) x[q] += *(const f32x2*)(sp + ww * 4096);
                    }
                    const u32x4 r = {pack2(lo2f(xr[0]) + bfr(x[0][0]), hi2f(xr[0]) + bfr(x[2][0])), pack2(lo2f(xr[1]) + bfr(x[1][0]), hi2f(xr[1]) + bfr(x[3][0])),
                                     pack2(lo2f(xr[2]) + bfr(x[0][1]), hi2f(xr[2]) + bfr(x[2][1])), pack2(lo2f(xr[3]) + bfr(x[1][1]), hi2f(xr[3]) + bfr(x[3][1]))};
                    if (!a.Fnext) st128(r, mkrsrc(a.out, S_ * D_ * 2), ooff, (unsigned)(s * SEG * D_ * 2), 0);
                    else {
                        st128(r, mkrsrc(a.out, S_ * D_ * 2), ooff, (unsigned)(s * SEG * D_ * 2), 16);
                        VMWAIT();
                        if (lane == 0) __builtin_amdgcn_raw_buffer_store_b32(0u, rwin, 0u, W_OUT + (unsigned)(s * 64 + 2 * cg + (w & 1)) * 4u, 16);
                    }
                }
                RT(2);
                RTS();
                if (s < 8) SST(8 + s);
                if (s >= 56) SST(s - 40);
            }
        }
        if (a.Fnext && ph == 0 && bid >= NQT + NOWN) {
            if (tid < 320) ((float4*)L.tw)[tid] = ((const float4*)(ws + OFF_TW640))[tid];
            for (int u = bid - (NQT + NOWN); u < 256; u += 40) {
                if (w == 0) {
                    for (unsigned spins = 0;;) {
                        asm volatile("" ::: "memory");
                        const unsigned f = __builtin_amdgcn_raw_buffer_load_b32(rwin, (unsigned)min(lane, NOWN - 1) * 4u, W_OUT + (unsigned)(u >> 2) * 256u, 16);
                        if (!__any(f != 0u)) break;
                        if (spin_fail(spins, a.ctl)) break;
                        if (u < 216) __builtin_amdgcn_s_sleep(WSLP); else __builtin_amdgcn_s_sleep(4);
                    }
                }
                __syncthreads();
                SST(24);
                a1_unit(mkrsrc(a.out, S_ * D_ * 2), u * 8, a.Fnext, L, tid, XA1);
                SST(25);
            }
        }
    }
    SST(31);
}

constexpr int L_DBG = 0;
extern "C" size_t dbg_offset(int i) {
    const size_t o[] = {OFF_CTL, OFF_WQKVT, OFF_WOT, OFF_WFT, OFF_TW640, OFF_TW2048, OFF_F, OFF_X1, OFF_X2, OFF_H, OFF_Z, OFF_WIN, OFF_WIN, OFF_PT, OFF_WIN, OFF_END};
    return o[i];
}

extern "C" void kernel_launch(void* const* d_in, const int* in_sizes, int n_in, void* d_out, int out_size, void* d_ws, size_t ws_size,
                              hipStream_t stream) {
    const u16* A_x = (const u16*)d_in[0];
    const u16* w_r = (const u16*)d_in[1];
    const u16* w_i = (const u16*)d_in[2];
    const u16* mem0 = (const u16*)d_in[4];
    const u16* Wq = (const u16*)d_in[5];
    const u16* Wk = (const u16*)d_in[6];
    const u16* Wv = (const u16*)d_in[7];
    const u16* Wo = (const u16*)d_in[8];
    char* ws = (char*)d_ws;
    u16* X2 = (u16*)(ws + OFF_X2);

    hipFuncSetAttribute((const void*)b3_kernel, hipFuncAttributeMaxDynamicSharedMemorySize, L_END + L_DBG);
    char* win = (char*)(((uintptr_t)(ws + OFF_WIN) + 0x1FFFFFu) & ~(uintptr_t)0x1FFFFFu);
    k1_kernel<<<16 + 404 + 320, 320, 0, stream>>>(A_x, Wq, Wk, w_r, w_i, ws, win);
    for (int layer = 0; layer < 2; ++layer) {
        const u16* xin = layer == 0 ? A_x : X2;
        u16* xout = layer == 0 ? X2 : (u16*)d_out;
        a2_kernel<<<layer == 0 ? DF + 160 + 320 : DF, 256, 0, stream>>>(Wv, Wo, ws, win);
        RecArgs ra{(unsigned*)ws, win, mem0, xin, xout, layer == 0 ? (float4*)(ws + OFF_F) : nullptr};
        b3_kernel<<<256, 512, L_END + L_DBG, stream>>>(ra);
    }
}
```

```cpp
#include <hip/hip_runtime.h>
#include <hip/hip_bf16.h>
#include <stdint.h>

typedef unsigned short u16;
typedef unsigned long long u64;
typedef __attribute__((ext_vector_type(8))) __bf16 bf16x8;
typedef __attribute__((ext_vector_type(4))) short s16x4;
typedef __attribute__((ext_vector_type(4))) float f32x4;
typedef __attribute__((ext_vector_type(8))) float f32x8;
typedef __attribute__((ext_vector_type(16))) float f32x16;
typedef __attribute__((ext_vector_type(4))) unsigned u32x4;
typedef __attribute__((ext_vector_type(2))) unsigned u32x2;
typedef __attribute__((ext_vector_type(8))) unsigned u32x8;
typedef __attribute__((ext_vector_type(16))) __bf16 bf16x16;
typedef __attribute__((ext_vector_type(2))) unsigned short u16x2;
#define LAS __attribute__((address_space(3)))
#define INL __attribute__((always_inline))

constexpr int S_ = 2048, D_ = 640, DF = 321, H_ = 32, DH = 128, HD = 4096, NM = 8, SEG = 32, NSEG = 64;
constexpr int NOWN = 40;
constexpr int NCOL = 160;
constexpr size_t ZTSEG = (size_t)NOWN * H_ * 512;
constexpr unsigned PAIR = 16384, HALF = 8192;
constexpr unsigned W_MEM = 0, W_P = W_MEM + 24 * PAIR, W_T = W_P + 40 * PAIR, W_GO = W_T + 8 * PAIR, W_FZ = W_GO + HALF, W_FPT = W_FZ + 4096, W_FZ2 = W_FZ + 8192, W_FQ = W_FZ + 12288,
                   W_H = W_FZ + PAIR, W_COL = W_H + HALF, W_PL = W_COL + 24576, W_LAYER = W_PL + 81920 - W_GO, W_OUT = W_GO + 2 * W_LAYER, W_MISC = W_OUT + PAIR, W_ARM = W_MISC + 4096,
                   W_A1 = 92 * PAIR, W_PROBE = W_A1 + 2 * PAIR, W_ZM = 2u * 1024 * 1024, W_END = W_ZM + 120 * PAIR;
static_assert(W_ARM <= W_A1 && W_PROBE + 2 * PAIR <= 2u * 1024 * 1024 && W_PROBE % (2 * PAIR) == 0 && W_ARM % 1024 == 0, "window");
constexpr unsigned W_TMO = W_MISC, W_NEAR = W_MISC + 128;
constexpr int PTSEG = 32768 + 8192;

constexpr size_t al(size_t x) { return (x + 255) & ~(size_t)255; }
constexpr size_t OFF_CTL  = 0;
constexpr size_t OFF_WFT  = 4096 + 4 * 1024 * 1024;
constexpr size_t OFF_F    = OFF_WFT + al((size_t)(NCOL + 1) * S_ * 8);
constexpr size_t OFF_X1   = OFF_F + al((size_t)NCOL * S_ * 16);
constexpr size_t OFF_X2   = OFF_X1 + al((size_t)S_ * D_ * 2);
constexpr size_t OFF_H    = OFF_X2 + al((size_t)S_ * D_ * 2);
constexpr size_t OFF_Z    = OFF_H + al((size_t)S_ * D_ * 2);
constexpr size_t OFF_Z2   = OFF_Z + al(ZTSEG * NSEG * 2);
constexpr size_t OFF_PT   = OFF_Z2 + al((size_t)NSEG * NOWN * 8192);
constexpr size_t OFF_QK   = OFF_PT + al((size_t)NSEG * PTSEG * 2);
constexpr size_t OFF_WIN  = OFF_QK + (size_t)NSEG * H_ * 16384;
constexpr size_t OFF_END  = OFF_WIN + (size_t)6 * 1024 * 1024;
static_assert(OFF_END <= (size_t)256 * 1024 * 1024, "workspace");
constexpr unsigned O_WFT = (unsigned)OFF_WFT, O_F = (unsigned)OFF_F, O_H = (unsigned)OFF_H, O_X1 = (unsigned)OFF_X1, O_Z = (unsigned)OFF_Z, O_Z2 = (unsigned)OFF_Z2, O_PT = (unsigned)OFF_PT, O_QK = (unsigned)OFF_QK;

constexpr double PI_ = 3.14159265358979323846;
constexpr double tay(double x, int s) {
    double t = s ? x : 1.0, r = t;
    for (int i = s + 1; i < 18; i += 2) { t *= -x * x / (double)(i * (i + 1)); r += t; }
    return r;
}
struct Cs { float c, s; };
constexpr Cs cis(int m, int n) {
    m %= n;
    const int q = (m * 8 / n + 1) / 2;
    const double x = 2.0 * PI_ * (double)(4 * m - q * n) / (double)(4 * n), c = tay(x, 0), s = tay(x, 1);
    return (q & 3) == 0 ? Cs{(float)c, (float)-s} : (q & 3) == 1 ? Cs{(float)-s, (float)-c} : (q & 3) == 2 ? Cs{(float)-c, (float)s} : Cs{(float)s, (float)c};
}
template <int NI> struct alignas(16) Tw7 { Cs v[4][NI][2]; };
template <int NI, int N, int KIND> constexpr Tw7<NI> mk_tw() {
    Tw7<NI> r{};
    for (int i = 0; i < NI; ++i)
        for (int q = 1; q <= 8; ++q) r.v[(q - 1) >> 1][i][(q - 1) & 1] = cis((KIND == 0 ? i : KIND == 1 ? 8 * (i >> 3) : (i >> 3) + 10 * (i & 7)) * q, N);
    return r;
}
static __device__ const Tw7<256> TWC1 = mk_tw<256, 2048, 0>();
static __device__ const Tw7<32> TWC2 = mk_tw<32, 256, 0>();
static __device__ const Tw7<4> TWC3 = mk_tw<4, 32, 0>();
static __device__ const Tw7<80> TWR2 = mk_tw<80, 640, 1>();
static __device__ const Tw7<80> TWR3 = mk_tw<80, 640, 2>();

#define st128(v, r, vo, so, aux) do { const u32x4 d_ = (v); __builtin_amdgcn_raw_buffer_store_b128(d_, r, vo, so, aux); asm volatile("s_nop 1" :: "v"(d_)); } while (0)
__device__ __forceinline__ float bf2f(u16 v) { return __uint_as_float((unsigned)v << 16); }
__device__ __forceinline__ u16 f2bf(float f) { __bf16 b = (__bf16)f; return __builtin_bit_cast(u16, b); }
__device__ __forceinline__ float bfr(float f) { return (float)(__bf16)f; }
typedef __attribute__((ext_vector_type(2))) float f32x2;
typedef __attribute__((ext_vector_type(2))) __bf16 bf16x2;
__device__ __forceinline__ unsigned pack2(float a, float b) { return __builtin_bit_cast(unsigned, __builtin_convertvector((f32x2){a, b}, bf16x2)); }
__device__ __forceinline__ float lo2f(unsigned v) { return __uint_as_float(v << 16); }
__device__ __forceinline__ float hi2f(unsigned v) { return __uint_as_float(v & 0xFFFF0000u); }
__device__ __forceinline__ float fmx(float a, float b) { return __builtin_elementwise_maximum(a, b); }
__device__ __forceinline__ f32x4 mfma32(u32x4 a, u32x4 b, f32x4 c) {
    return __builtin_amdgcn_mfma_f32_16x16x32_bf16(__builtin_bit_cast(bf16x8, a), __builtin_bit_cast(bf16x8, b), c, 0, 0, 0);
}
__device__ __forceinline__ f32x4 mfma16(u32x2 a, u32x2 b, f32x4 c) {
    return __builtin_amdgcn_mfma_f32_16x16x16bf16_1k(__builtin_bit_cast(s16x4, a), __builtin_bit_cast(s16x4, b), c, 0, 0, 0);
}
__device__ __forceinline__ f32x4 smfmac(u32x4 a, u32x8 b, f32x4 c, int idx) {
    return __builtin_amdgcn_smfmac_f32_16x16x64_bf16(__builtin_bit_cast(bf16x8, a), __builtin_bit_cast(bf16x16, b), c, idx, 0, 0);
}
__device__ __forceinline__ int pi16(int k) { return ((k & 3) << 2) | ((k >> 1) & 2) | (k >> 3); }
__device__ __forceinline__ float addswap32(float a, float b) {
    auto r = __builtin_amdgcn_permlane32_swap(__float_as_uint(a), __float_as_uint(b), false, false);
    return __uint_as_float(r[0]) + __uint_as_float(r[1]);
}
__device__ __forceinline__ float xmax3216(float v) {
    auto r = __builtin_amdgcn_permlane32_swap(__float_as_uint(v), __float_as_uint(v), false, false);
    v = fmx(__uint_as_float(r[0]), __uint_as_float(r[1]));
    auto q = __builtin_amdgcn_permlane16_swap(__float_as_uint(v), __float_as_uint(v), false, false);
    return fmx(__uint_as_float(q[0]), __uint_as_float(q[1]));
}
__device__ __forceinline__ float xsum3216(float v) {
    auto r = __builtin_amdgcn_permlane32_swap(__float_as_uint(v), __float_as_uint(v), false, false);
    v = __uint_as_float(r[0]) + __uint_as_float(r[1]);
    auto q = __builtin_amdgcn_permlane16_swap(__float_as_uint(v), __float_as_uint(v), false, false);
    return __uint_as_float(q[0]) + __uint_as_float(q[1]);
}
template <int CTRL> __device__ __forceinline__ float dppf(float v) { return __uint_as_float((unsigned)__builtin_amdgcn_update_dpp(0, (int)__float_as_uint(v), CTRL, 0xF, 0xF, false)); }
__device__ __forceinline__ float wsum(float v) {
    v += dppf<0xB1>(v); v += dppf<0x4E>(v); v += dppf<0x141>(v); v += dppf<0x140>(v);
    return xsum3216(v);
}
__device__ __forceinline__ unsigned ror8(unsigned v) { return (unsigned)__builtin_amdgcn_update_dpp(0, (int)v, 0x128, 0xF, 0xF, false); }
__device__ __forceinline__ float ror8f(float v) { return __uint_as_float(ror8(__float_as_uint(v))); }
__device__ __forceinline__ unsigned nosent(unsigned v) {
    u16x2 lim = {0xFFFE, 0xFFFE};
    return __builtin_bit_cast(unsigned, __builtin_elementwise_min(__builtin_bit_cast(u16x2, v), lim));
}
__device__ __forceinline__ unsigned pkmax(unsigned a, unsigned b) {
    return __builtin_bit_cast(unsigned, __builtin_elementwise_max(__builtin_bit_cast(u16x2, a), __builtin_bit_cast(u16x2, b)));
}
__device__ __forceinline__ bool has_sent(u32x4 v) {
    unsigned m = pkmax(pkmax(v.x, v.y), pkmax(v.z, v.w));
    return (m & 0xFFFFu) == 0xFFFFu || (m >> 16) == 0xFFFFu;
}

typedef f32x2 c2;
__device__ __forceinline__ c2 swp(c2 a) { return __builtin_shufflevector(a, a, 1, 0); }
__device__ __forceinline__ c2 fm(c2 a, c2 b, c2 c) { return __builtin_elementwise_fma(a, b, c); }
__device__ __forceinline__ c2 bc(float x) { return (c2){x, x}; }
__device__ __forceinline__ c2 sgn() { c2 s = {-1.f, 1.f}; asm("" : "+s"(s)); return s; }
template <int SG> __device__ __forceinline__ c2 addi(c2 a, c2 b) { return fm(swp(b), SG > 0 ? sgn() : swp(sgn()), a); }
__device__ __forceinline__ c2 cmul(c2 a, c2 b) { return fm(swp(a), bc(b.y) * sgn(), a * bc(b.x)); }
struct Tw { c2 c[7], s[7]; };
template <int NI> __device__ __forceinline__ void ldtw(Tw& t, const Tw7<NI>& tab, int i) {
#pragma unroll
    for (int j = 0; j < 4; ++j) {
        const f32x4 x = *(const f32x4*)tab.v[j][i];
        t.c[2 * j] = (c2){x[0], x[1]}; t.s[2 * j] = bc(x[1]) * sgn();
        if (j < 3) { t.c[2 * j + 1] = (c2){x[2], x[3]}; t.s[2 * j + 1] = bc(x[3]) * sgn(); }
    }
}
template <int SG> __device__ __forceinline__ void twid(c2 (&u)[8], const Tw& t) {
#pragma unroll
    for (int r = 1; r < 8; ++r) u[r] = fm(swp(u[r]), SG < 0 ? t.s[r - 1] : swp(t.s[r - 1]), u[r] * bc(t.c[r - 1].x));
}
template <int SG> __device__ __forceinline__ void dft4(c2& a, c2& b, c2& c, c2& d) {
    const c2 t0 = a + c, t1 = a - c, t2 = b + d, t3 = b - d;
    a = t0 + t2; c = t0 - t2; b = addi<SG>(t1, t3); d = addi<-SG>(t1, t3);
}
template <int SG> __device__ __forceinline__ void dft8(c2 (&u)[8]) {
    c2 e0 = u[0], e1 = u[2], e2 = u[4], e3 = u[6], o0 = u[1], o1 = u[3], o2 = u[5], o3 = u[7];
    dft4<SG>(e0, e1, e2, e3); dft4<SG>(o0, o1, o2, o3);
    const c2 r = bc(0.70710678118654752440f);
    o1 = addi<SG>(o1, o1); o3 = addi<-SG>(o3, o3);
    u[0] = e0 + o0; u[4] = e0 - o0;
    u[1] = fm(o1, r, e1); u[5] = fm(o1, -r, e1);
    u[2] = addi<SG>(e2, o2); u[6] = addi<-SG>(e2, o2);
    u[3] = fm(o3, -r, e3); u[7] = fm(o3, r, e3);
}
template <int SG> __device__ __forceinline__ void dft5(c2& x0, c2& x1, c2& x2, c2& x3, c2& x4) {
    const c2 c1 = bc(0.30901699437494742410f), c2_ = bc(-0.80901699437494742410f), s1 = bc(0.95105651629515357212f), s2 = bc(0.58778525229247312917f);
    const c2 t1 = x1 + x4, t2 = x2 + x3, t3 = x1 - x4, t4 = x2 - x3;
    const c2 a1 = fm(c2_, t2, fm(c1, t1, x0)), a2 = fm(c1, t2, fm(c2_, t1, x0));
    const c2 b1 = fm(s2, t4, s1 * t3), b2 = fm(-s1, t4, s2 * t3);
    x0 = x0 + t1 + t2;
    x1 = addi<SG>(a1, b1); x4 = addi<-SG>(a1, b1); x2 = addi<SG>(a2, b2); x3 = addi<-SG>(a2, b2);
}
template <int SG> __device__ __forceinline__ void dft10(c2 (&u)[10]) {
    c2 e0 = u[0], e1 = u[2], e2 = u[4], e3 = u[6], e4 = u[8], o0 = u[5], o1 = u[7], o2 = u[9], o3 = u[1], o4 = u[3];
    dft5<SG>(e0, e1, e2, e3, e4); dft5<SG>(o0, o1, o2, o3, o4);
    u[0] = e0 + o0; u[5] = e0 - o0; u[6] = e1 + o1; u[1] = e1 - o1; u[2] = e2 + o2; u[7] = e2 - o2; u[8] = e3 + o3; u[3] = e3 - o3; u[4] = e4 + o4; u[9] = e4 - o4;
}
__device__ __forceinline__ void swap32(c2& v) { auto r = __builtin_amdgcn_permlane32_swap(__float_as_uint(v.x), __float_as_uint(v.y), false, false); v = (c2){__uint_as_float(r[0]), __uint_as_float(r[1])}; }
template <int B> __device__ __forceinline__ void swapl(c2& a, c2& b) {
#pragma unroll
    for (int i = 0; i < 2; ++i) {
        auto r = B == 5 ? __builtin_amdgcn_permlane32_swap(__float_as_uint(a[i]), __float_as_uint(b[i]), false, false) : __builtin_amdgcn_permlane16_swap(__float_as_uint(a[i]), __float_as_uint(b[i]), false, false);
        a[i] = __uint_as_float(r[0]); b[i] = __uint_as_float(r[1]);
    }
}
__device__ __forceinline__ c2 unpk(unsigned v) { return (c2){lo2f(v), hi2f(v)}; }
__device__ __forceinline__ float hsum32(c2 s) {
    float v = s.x + s.y;
    v += dppf<0xB1>(v); v += dppf<0x4E>(v); v += dppf<0x141>(v); v += dppf<0x140>(v);
    auto q = __builtin_amdgcn_permlane16_swap(__float_as_uint(v), __float_as_uint(v), false, false);
    return __uint_as_float(q[0]) + __uint_as_float(q[1]);
}
__device__ __forceinline__ void ln_row(const c2 (&v)[10], unsigned (&o)[10]) {
    const c2 s = ((v[0] + v[1]) + (v[2] + v[3])) + ((v[4] + v[5]) + (v[6] + v[7])) + (v[8] + v[9]);
    const float mean = hsum32(s) * (1.f / 640.f);
    c2 q0 = bc(0.f), q1 = bc(0.f);
#pragma unroll
    for (int r = 0; r < 10; r += 2) { const c2 d = v[r] - bc(mean), e = v[r + 1] - bc(mean); q0 = fm(d, d, q0); q1 = fm(e, e, q1); }
    const float mb = bfr(mean);
    c2 d[10];
#pragma unroll
    for (int r = 0; r < 10; ++r) { const c2 x = v[r] - bc(mb); d[r] = unpk(pack2(x.x, x.y)); }
    const float var = hsum32(q0 + q1) * (1.f / 640.f);
    const float rs = __builtin_amdgcn_rcpf(bfr(sqrtf(bfr(bfr(var) + 1e-5f))));
#pragma unroll
    for (int r = 0; r < 10; ++r) { const c2 x = d[r] * bc(rs); o[r] = pack2(x.x, x.y); }
}

__device__ __forceinline__ __amdgpu_buffer_rsrc_t mkrsrc(const void* p, unsigned bytes) {
    return __builtin_amdgcn_make_buffer_rsrc((void*)p, 0, bytes, 0x00020000);
}
__device__ __forceinline__ unsigned xcc_id() { unsigned x; asm volatile("s_getreg_b32 %0, hwreg(HW_REG_XCC_ID)" : "=s"(x)); return x & 15u; }
#define VMWAIT() do { asm volatile("" ::: "memory"); __builtin_amdgcn_s_waitcnt(0x0F70); asm volatile("" ::: "memory"); } while (0)
#define BAR() asm volatile("s_waitcnt lgkmcnt(0)\n\ts_barrier" ::: "memory")
#define BAR0() asm volatile("s_barrier" ::: "memory")

#define FST(k) do {} while (0)
#define FSW(k) do {} while (0)
extern __shared__ __attribute__((aligned(16))) char smem[];
constexpr int R_C = 8 * 10 * 72 * 8;
__device__ __forceinline__ c2 ldv(const LAS void* p) { return *(const volatile LAS c2*)p; }
struct RowTw { Tw a, b; };
__device__ __forceinline__ void row_tw(RowTw& t, int tid) { const int i = tid % 80; ldtw(t.a, TWR2, i); ldtw(t.b, TWR3, i); }
template <int NF> __device__ __forceinline__ void a1_unit(__amdgpu_buffer_rsrc_t rx, int row0, __amdgpu_buffer_rsrc_t rws, const RowTw& tw, int tid, unsigned (&fs)[12]) {
    const int wave = __builtin_amdgcn_readfirstlane(tid >> 6), lane = tid & 63;
    LAS c2* A = (LAS c2*)smem; LAS c2* C = (LAS c2*)(smem + R_C);
    FST(0);
    if (wave < NF) {
        unsigned d[10];
#pragma unroll
        for (int r = 0; r < 10; ++r) d[r] = __builtin_amdgcn_raw_buffer_load_b32(rx, (lane & 31) * 4u + (lane >> 5) * (D_ * 2u) + r * 128u, (unsigned)(row0 + 2 * wave) * (D_ * 2), 16);
        c2 u[10];
#pragma unroll
        for (int r = 0; r < 10; ++r) u[r] = unpk(d[r]);
        FST(1);
        ln_row(u, d);
        FST(2);
#pragma unroll
        for (int r = 0; r < 10; ++r) { u[r] = unpk(d[r]); swap32(u[r]); }
        dft10<-1>(u);
#pragma unroll
        for (int r = 0; r < 10; ++r) A[(wave * 10 + r) * 72 + (lane & 31) + 36 * (lane >> 5)] = u[r];
        FST(3);
    }
    BAR();
    FST(4);
#pragma unroll
    for (int c = 0; c < (NF > 4 ? 2 : 1); ++c) {
        if (tid < (NF > 4 ? (c ? 160 : 480) : 80 * NF)) {
            const int K = (tid >> 3) + 60 * c, m = tid & 7;
            LAS c2* a = A + K * 72;
            c2 u[8];
#pragma unroll
            for (int q = 0; q < 8; ++q) u[q] = ldv(a + (m >> 1) + 36 * (m & 1) + 4 * q);
            twid<-1>(u, tw.a);
            dft8<-1>(u);
#pragma unroll
            for (int p = 0; p < 8; ++p) a[9 * p + m] = u[p];
            asm volatile("" ::: "memory");
#pragma unroll
            for (int q = 0; q < 8; ++q) u[q] = ldv(a + 9 * m + q);
            twid<-1>(u, tw.b);
            dft8<-1>(u);
            LAS c2* o = C + (K / 10) * 638 + K + 10 * m;
#pragma unroll
            for (int p = 0; p < 8; ++p) o[80 * p] = u[p];
        }
    }
    FST(5);
    BAR();
    FST(6);
#pragma unroll
    for (int c = 0; c < (NF > 4 ? 5 : 3); ++c) {
        const int it = tid + 512 * c, kp = it / (2 * NF), row = it % (2 * NF);
        if (NF > 4 || c < 2 || tid < 256) {
            const LAS c2* z = C + (row >> 1) * 648;
            const f32x4 zk = *(const LAS f32x4*)(z + 2 * kp);
            const c2 n0 = ldv(z + (kp ? 640 - 2 * kp : 320)), n1 = ldv(z + 639 - 2 * kp);
            const bool odd = it & 1;
            f32x4 o = odd ? (f32x4){zk[1] + n0.y, n0.x - zk[0], zk[3] + n1.y, n1.x - zk[2]} : (f32x4){zk[0] + n0.x, zk[1] - n0.y, zk[2] + n1.x, zk[3] - n1.y};
            if (c == 0 && kp == 0) { o[0] = 2.f * (odd ? zk[1] : zk[0]); o[1] = 2.f * (odd ? n0.y : n0.x); }
            st128(__builtin_bit_cast(u32x4, o), rws, (unsigned)(kp * S_ + row) * 16u, O_F + (unsigned)row0 * 16u, 16);
        }
    }
    FST(7);
}
template <class FG> __device__ __forceinline__ void a3_unit(__amdgpu_buffer_rsrc_t rx, int row0, __amdgpu_buffer_rsrc_t rws, const RowTw& tw, int tid, FG go, unsigned (&fs)[12]) {
    const int wave = __builtin_amdgcn_readfirstlane(tid >> 6), lane = tid & 63;
    LAS c2* A = (LAS c2*)smem; LAS c2* C = (LAS c2*)(smem + R_C);
    const unsigned n0 = (lane & 7) | ((lane & 24) << 1);
    c2 x[10];
    if (wave < 4) {
        const unsigned base = (unsigned)(row0 + 2 * wave) * (D_ * 2), vo = n0 * 2u + (lane >> 5) * (D_ * 2u);
#pragma unroll
        for (int r = 0; r < 10; ++r) x[r] = (c2){bf2f(__builtin_amdgcn_raw_buffer_load_b16(rx, vo + 128u * r, base, 16)), bf2f(__builtin_amdgcn_raw_buffer_load_b16(rx, vo + 16u + 128u * r, base, 16))};
    }
    go();
    FST(0);
    u32x4 fv[3];
#pragma unroll
    for (int c = 0; c < 3; ++c) {
        const int it = tid + 512 * c;
        fv[c] = __builtin_amdgcn_raw_buffer_load_b128(rws, c < 2 || tid < 256 ? (unsigned)((it >> 3) * S_ + (it & 7)) * 16u : 0x40000000u, O_F + (unsigned)row0 * 16u, 16);
    }
#pragma unroll
    for (int c = 0; c < 3; ++c) {
        const int it = tid + 512 * c, kp = it >> 3;
        if (c < 2 || tid < 256) {
            const bool odd = it & 1;
            const f32x4 v = __builtin_bit_cast(f32x4, fv[c]), p = {dppf<0xB1>(v[0]), dppf<0xB1>(v[1]), dppf<0xB1>(v[2]), dppf<0xB1>(v[3])};
            c2 z0 = odd ? (c2){p[0] + v[1], v[0] - p[1]} : (c2){v[0] - p[1], p[0] + v[1]};
            const c2 z1 = odd ? (c2){p[2] + v[3], v[2] - p[3]} : (c2){v[2] - p[3], p[2] + v[3]};
            int i0 = odd ? 640 - 2 * kp : 2 * kp;
            if (c == 0 && kp == 0) { z0 = odd ? (c2){p[1], v[1]} : (c2){v[0], p[0]}; i0 >>= 1; }
            LAS c2* z = C + ((it >> 1) & 3) * 648;
            z[i0] = z0; z[odd ? 639 - 2 * kp : 2 * kp + 1] = z1;
        }
    }
    FST(1);
    BAR();
    FST(2);
    if (tid < 320) {
        const int K = tid >> 3, m = tid & 7;
        LAS c2* a = A + K * 72;
        const LAS c2* o = C + (K / 10) * 638 + K + 10 * m;
        c2 u[8];
#pragma unroll
        for (int p = 0; p < 8; ++p) u[p] = ldv(o + 80 * p);
        dft8<1>(u);
        twid<1>(u, tw.b);
#pragma unroll
        for (int q = 0; q < 8; ++q) a[9 * q + m] = u[q];
        asm volatile("" ::: "memory");
#pragma unroll
        for (int p = 0; p < 8; ++p) u[p] = ldv(a + 9 * m + p);
        dft8<1>(u);
        twid<1>(u, tw.a);
#pragma unroll
        for (int q = 0; q < 8; ++q) a[m + 8 * (q >> 1) + 32 * (q & 1)] = u[q];
    }
    FST(3);
    BAR();
    FST(4);
    if (wave < 4) {
        c2 u[10];
#pragma unroll
        for (int r = 0; r < 10; ++r) u[r] = ldv(A + (wave * 10 + r) * 72 + lane);
        dft10<1>(u);
        unsigned x1[10], h[10];
#pragma unroll
        for (int r = 0; r < 10; ++r) {
            swap32(u[r]);
            const c2 y = x[r] + unpk(pack2(u[r].x, u[r].y));
            x1[r] = pack2(y.x, y.y);
            u[r] = unpk(x1[r]);
        }
        FST(5);
        ln_row(u, h);
        FST(6);
        LAS char* st = (LAS char*)C + wave * 5120;
        LAS char* hp = st + (lane >> 5) * 1280 + (n0 & 48) * 2 + pi16(n0 & 15) * 2;
#pragma unroll
        for (int r = 0; r < 10; ++r) *(LAS unsigned*)(hp + 128 * r) = h[r];
#pragma unroll
        for (int c = 0; c < 3; ++c) {
            if (c < 2 || lane < 32) {
                const int id = c * 64 + lane, rr = id >= 80, ch = id - 80 * rr, row = (row0 + 2 * wave + rr) & 31;
                st128(*(const LAS u32x4*)(st + id * 16), rws, (unsigned)((ch >> 3) * 4096 + row * 128 + (((ch & 7) ^ (row & 7)) << 4)), O_H + (unsigned)(row0 >> 5) * 40960u, 16);
            }
        }
        LAS char* xp = st + 2560 + (lane >> 5) * 1280 + n0 * 2;
#pragma unroll
        for (int r = 0; r < 10; ++r) { *(LAS u16*)(xp + 128 * r) = (u16)x1[r]; *(LAS u16*)(xp + 128 * r + 16) = (u16)(x1[r] >> 16); }
#pragma unroll
        for (int c = 0; c < 3; ++c)
            if (c < 2 || lane < 32) st128(*(const LAS u32x4*)(st + 2560 + (c * 64 + lane) * 16), rws, (c * 64 + lane) * 16u, O_X1 + (unsigned)(row0 + 2 * wave) * (D_ * 2), 16);
        FST(7);
    }
}

constexpr int C_W = 90112, C_W2 = C_W + 8 * 516 * 4, C_Z = C_W2 + 8 * 516 * 4;
template <class FG> __device__ __forceinline__ void a2_unit(int kp, __amdgpu_buffer_rsrc_t rws, int tid, FG go, unsigned (&fs)[12]) {
    const int w = __builtin_amdgcn_readfirstlane(tid >> 6), l = tid & 63;
    Tw t1, t2, t3;
    ldtw(t1, TWC1, tid >> 1); ldtw(t2, TWC2, l >> 1); ldtw(t3, TWC3, l >> 4);
    const unsigned fb = O_F + (unsigned)kp * (S_ * 16u), wb = O_WFT + (unsigned)kp * (S_ * 8u);
    LAS c2* e0 = (LAS c2*)smem + tid;
    LAS c2* e1 = (LAS c2*)smem + w * 544 + l;
    LAS c2* e2 = (LAS c2*)smem + w * 544 + 2 * (l >> 4) + (l & 1);
    const int b = (l >> 1) & 7;
    LAS unsigned* wl = (LAS unsigned*)(smem + C_W) + ((tid >> 1) & 7) * 516 + (tid >> 4) * 2 + (tid & 1);
    c2 u[8];
    unsigned wv[8];
    go(0);
#pragma unroll
    for (int r = 0; r < 8; ++r) wv[r] = __builtin_amdgcn_raw_buffer_load_b32(rws, tid * 4u, wb + r * 2048u, 16);
    if (kp == 0) {
#pragma unroll
        for (int r = 0; r < 8; ++r) wl[8 * 516 + 64 * r] = __builtin_amdgcn_raw_buffer_load_b32(rws, tid * 4u, O_WFT + NCOL * S_ * 8u + r * 2048u, 16);
    }
    go(1);
    FST(0);
#pragma unroll
    for (int r = 0; r < 7; ++r) u[r] = __builtin_bit_cast(c2, __builtin_amdgcn_raw_buffer_load_b64(rws, tid * 8u, fb + r * 4096u, 16));
    go(2);
    FST(1);
    u[7] = __builtin_bit_cast(c2, __builtin_amdgcn_raw_buffer_load_b64(rws, tid * 8u, fb + 7 * 4096u, 16));
#pragma unroll
    for (int r = 0; r < 8; ++r) wl[64 * r] = wv[r];
    FST(2);
    dft8<-1>(u);
    twid<-1>(u, t1);
#pragma unroll
    for (int r = 0; r < 8; ++r) e0[544 * r] = u[r];
    BAR();
#pragma unroll
    for (int r = 0; r < 8; ++r) u[r] = ldv(e1 + 64 * r);
    dft8<-1>(u);
    twid<-1>(u, t2);
#pragma unroll
    for (int r = 0; r < 8; ++r) e1[68 * r] = u[r];
    asm volatile("" ::: "memory");
#pragma unroll
    for (int r = 0; r < 8; ++r) u[r] = ldv(e2 + 68 * b + 8 * r);
    const LAS unsigned* wr = (const LAS unsigned*)(smem + C_W) + w * 516 + (l & 15) + 32 * (l >> 4);
#pragma unroll
    for (int r = 0; r < 8; ++r) wv[r] = wr[16 * (r & 1) + 128 * (r >> 1)];
    dft8<-1>(u);
    twid<-1>(u, t3);
#pragma unroll
    for (int r = 0; r < 4; ++r) swapl<5>(u[r], u[r + 4]);
#pragma unroll
    for (int r = 0; r < 4; ++r) swapl<4>(u[(r & 1) + 2 * (r & 2)], u[(r & 1) + 2 * (r & 2) + 2]);
    dft4<-1>(u[0], u[2], u[4], u[6]); dft4<-1>(u[1], u[3], u[5], u[7]);
    FST(3);
    const float sc = 0.5f / ((float)S_ * (float)D_);
    if (kp == 0) {
        LAS c2* z = (LAS c2*)(smem + C_Z);
        const int k0 = w + 8 * b + 128 * (l >> 4);
#pragma unroll
        for (int r = 0; r < 8; ++r) if (!(l & 1)) z[w * 258 + b + 16 * (l >> 4) + 8 * (r & 1) + 64 * (r >> 1)] = u[r];
        BAR();
#pragma unroll
        for (int r = 0; r < 8; ++r) {
            const int kn = (S_ - k0 - 64 * (r & 1) - 512 * (r >> 1)) & (S_ - 1);
            const LAS unsigned* wn = (const LAS unsigned*)(smem + C_W) + (kn & 7) * 516 + (kn >> 3) * 2;
            const c2 h0 = unpk(wv[r]) - unpk(wn[0]) * sgn(), h1 = unpk(wr[8 * 516 + 16 * (r & 1) + 128 * (r >> 1)]) - unpk(wn[8 * 516]) * sgn();
            const c2 y = cmul(u[r], (h0 + h1) * bc(0.25f * sc)) + cmul(z[(kn & 7) * 258 + (kn >> 3)] * swp(sgn()), (h0 - h1) * bc(0.25f * sc));
            u[r] = l & 1 ? cmul(u[r], unpk(wv[r]) * bc(sc)) : y;
        }
    } else {
#pragma unroll
        for (int r = 0; r < 8; ++r) u[r] = cmul(u[r], unpk(wv[r]) * bc(sc));
    }
    FST(4);
    dft4<1>(u[0], u[2], u[4], u[6]); dft4<1>(u[1], u[3], u[5], u[7]);
#pragma unroll
    for (int r = 0; r < 4; ++r) swapl<4>(u[(r & 1) + 2 * (r & 2)], u[(r & 1) + 2 * (r & 2) + 2]);
#pragma unroll
    for (int r = 0; r < 4; ++r) swapl<5>(u[r], u[r + 4]);
    twid<1>(u, t3);
    dft8<1>(u);
#pragma unroll
    for (int r = 0; r < 8; ++r) e2[66 * b + 8 * r] = u[r];
    asm volatile("" ::: "memory");
#pragma unroll
    for (int r = 0; r < 8; ++r) u[r] = ldv(e1 + 66 * r);
    twid<1>(u, t2);
    dft8<1>(u);
#pragma unroll
    for (int r = 0; r < 8; ++r) e1[64 * r] = u[r];
    BAR();
#pragma unroll
    for (int r = 0; r < 8; ++r) u[r] = ldv(e0 + 544 * r);
    twid<1>(u, t1);
    dft8<1>(u);
    FST(5);
#pragma unroll
    for (int r = 0; r < 8; ++r) __builtin_amdgcn_raw_buffer_store_b64(__builtin_bit_cast(u32x2, u[r]), rws, tid * 8u, fb + r * 4096u, 16);
    FST(6);
}

struct Args { char* ws; char* win; const u16 *x, *wr, *wi, *mem0, *wq, *wk, *wv, *wo; u16* out; };
__device__ __forceinline__ const u16* arg(int i) {
    const __attribute__((address_space(4))) u64* p = (const __attribute__((address_space(4))) u64*)__builtin_amdgcn_kernarg_segment_ptr();
    asm volatile("" : "+s"(p));
    return (const u16*)p[i];
}
enum { A_X = 2, A_WR, A_WI, A_MEM0, A_WQ, A_WK, A_WV, A_WO, A_OUT };
#define MXV 0x00808080808080AAull
#define MXQ 0x0008080808080000ull
#define NTAIL 5
static_assert(NTAIL >= 3, "the owners release segments up to NSEG - 4");
constexpr int TAILS = NSEG - NTAIL;
static_assert(!((MXV | MXQ) >> TAILS) && !(MXV & 1) && !(MXQ & 3), "segment sets");
constexpr int NREC = 96 + NOWN, NQT = NREC + NOWN, NXT = NQT + 32, NWRK = NXT + 32;
#define SPIN_CAP 400000u
__device__ __forceinline__ void set_tmo(__amdgpu_buffer_rsrc_t rwin) { __builtin_amdgcn_raw_buffer_store_b32(0u, rwin, 0u, W_TMO, 16); }
__device__ __forceinline__ bool spin_fail(unsigned& spins, __amdgpu_buffer_rsrc_t rwin) {
    if (++spins > SPIN_CAP) { set_tmo(rwin); return true; }
    return (spins & 1023u) == 0u && __builtin_amdgcn_readfirstlane(__builtin_amdgcn_raw_buffer_load_b32(rwin, 0u, W_TMO, 16)) == 0u;
}
#define SST(k) do {} while (0)
#define LSEL 0
#define RT(k) do {} while (0)
#define RTS() do {} while (0)
#define B3ATTR
#define MTA(k)
#define MTRT() do {} while (0)
#define MT(k) do {} while (0)
#define MTS() do {} while (0)
#define FC(x) do {} while (0)
#define DQC 1200
#define DVC 1200
#define M1 44
#define M2 234
#define PEN 280
#define DZP 55
#define LT0 342
#define SAD 1
#define PEN1 50
#define PEN0 150
#define TSLP 1
#define OPR 2
#define BSPARSE 0
#define ZLAG 4
#define TKP 0
#define DPS 12
#define DXT 3000
#define LTM 12
#define XS0 10
#define XSU 8
#define XSM 32
#define LTU 4
#define LTS 3
#define XSS 3
#define TKS 3
#define LT1 450
#define FDL 2
#define ZLA 3
#define NORND 0
#define LTX 24
#define TKE 200
#define DVO 6000
#define ZPS 4
#define HLA 8
#define QB2 2
#define NB2 6
#define DZR 24
#define ML 150
#define NB1 3
#define DPT 3
#define DDMA 8
#define DZ2 800
#define QEARLY 2
#define OSLP 32
#define OSLP2 1
#define WSLP 64
#define DO0 3000
#define DW0 1200
#define DQT 2000
#define ZEARLY 3
#define HSLP 4
#define ASLP 4
#define CSLP 4
#define NPR 3
#define DIE 0
#define SB() __builtin_amdgcn_sched_barrier(0)
#define PIN1(a) asm volatile("" : "+v"(a) :: "memory")
#define PIN2(a, b) asm volatile("" : "+v"(a), "+v"(b) :: "memory")
__device__ __forceinline__ unsigned fresh_lane() { unsigned l; asm volatile("v_mbcnt_lo_u32_b32 %0, -1, 0\n\tv_mbcnt_hi_u32_b32 %0, -1, %0" : "=v"(l)); return l; }
#define LANE0 (min(fresh_lane(), 1u) << 30)
__device__ __forceinline__ void wait_until(unsigned long long t) { while ((long long)(__builtin_amdgcn_s_memtime() - t) < 0) __builtin_amdgcn_s_sleep(1); }
struct Clk { unsigned ta, ma, cpq, qpc; };
__device__ __forceinline__ int sx24(unsigned v) { return (int)(v << 8) >> 8; }
#define WAIT_FINE \
        "s_memrealtime vcc\n\t" \
        "s_memtime s[100:101]\n\t" \
        "s_brev_b32 %[fi], -2\n\t" \
        "s_waitcnt lgkmcnt(0)\n\t" \
        "s_mov_b32 %[rp], vcc_lo\n\t" \
        "s_lshl_b32 vcc_hi, vcc_lo, 2\n\t" \
        "s_sub_u32 %[x], %[t], vcc_hi\n\t" \
        "s_bfe_i32 %[x], %[x], 0x180000\n\t" \
        "s_min_i32 %[x], %[x], 4000\n\t" \
        "s_max_i32 %[x], %[x], -4000\n\t" \
        "s_add_u32 %[t], vcc_hi, %[x]\n\t" \
        "s_mul_i32 %[x], %[x], %[cq]\n\t" \
        "s_ashr_i32 %[x], %[x], 8\n\t" \
        "s_sub_u32 %[tm], %[t], %[ta]\n\t" \
        "s_bfe_i32 %[tm], %[tm], 0x180000\n\t" \
        "s_min_i32 %[tm], %[tm], 1000000\n\t" \
        "s_max_i32 %[tm], %[tm], -1000000\n\t" \
        "s_mul_i32 %[tm], %[tm], %[cq]\n\t" \
        "s_ashr_i32 %[tm], %[tm], 8\n\t" \
        "s_add_u32 %[tm], %[tm], %[ma]\n\t" \
        "s_sub_u32 %[tm], %[tm], s100\n\t" \
        "s_lshr_b32 vcc_hi, %[cq], 4\n\t" \
        "s_sub_u32 vcc_hi, %[x], vcc_hi\n\t" \
        "s_max_i32 %[tm], %[tm], vcc_hi\n\t" \
        "s_min_i32 %[tm], %[tm], %[x]\n\t" \
        "s_add_u32 %[tm], %[tm], s100\n" \
        "1:\n\t" \
        "s_memrealtime vcc\n\t" \
        "s_memtime s[100:101]\n\t" \
        "s_waitcnt lgkmcnt(0)\n\t" \
        "s_cmp_eq_u32 vcc_lo, %[rp]\n\t" \
        "s_cbranch_scc1 2f\n\t" \
        "s_mov_b32 %[rp], vcc_lo\n\t" \
        "s_lshl_b32 %[x], vcc_lo, 2\n\t" \
        "s_sub_u32 %[x], %[t], %[x]\n\t" \
        "s_bfe_i32 %[x], %[x], 0x180000\n\t" \
        "s_mul_i32 %[x], %[x], %[cq]\n\t" \
        "s_ashr_i32 %[x], %[x], 8\n\t" \
        "s_add_u32 %[x], %[x], s100\n\t" \
        "s_sub_u32 s101, %[x], %[tm]\n\t" \
        "s_cmp_lt_i32 s101, %[fi]\n\t" \
        "s_cselect_b32 %[tm], %[x], %[tm]\n\t" \
        "s_mov_b32 %[fi], 0\n" \
        "2:\n\t" \
        "s_sub_u32 %[x], %[tm], s100\n\t" \
        "s_cmp_gt_i32 %[x], 260\n\t" \
        "s_cbranch_scc1 1b\n\t" \
        "s_cmp_eq_u32 %[fi], 0\n\t" \
        "s_cselect_b32 %[ta], %[t], %[ta]\n\t" \
        "s_cselect_b32 %[ma], %[tm], %[ma]\n" \
        "3:\n\t" \
        "s_memtime s[100:101]\n\t" \
        "s_waitcnt lgkmcnt(0)\n\t" \
        "s_sub_u32 %[x], %[tm], s100\n\t" \
        "s_cmp_gt_i32 %[x], 92\n\t" \
        "s_cbranch_scc1 3b\n\t" \
        "s_max_i32 %[x], %[x], 0\n\t" \
        "s_andn2_b32 %[x], %[x], 3\n\t" \
        "s_sub_u32 %[x], 104, %[x]\n\t" \
        "s_getpc_b64 vcc\n\t" \
        "s_add_u32 vcc_lo, vcc_lo, %[x]\n\t" \
        "s_addc_u32 vcc_hi, vcc_hi, 0\n\t" \
        "s_setpc_b64 vcc\n\t" \
        ".rept 23\n\t" \
        "s_nop 0\n\t" \
        ".endr\n\t"
#define WAIT_FINE_OUT [tm] "=&s"(tm), [rp] "=&s"(rp), [x] "=&s"(x), [fi] "=&s"(fi), [t] "+s"(t), [ta] "+s"(c.ta), [ma] "+s"(c.ma)
__device__ __forceinline__ void wait_fine(unsigned t, Clk& c) {
    unsigned tm, rp, x, fi;
    asm volatile(WAIT_FINE : WAIT_FINE_OUT : [cq] "s"(c.cpq) : "vcc", "scc", "s100", "s101", "memory");
}
__device__ __forceinline__ void wait_fine_bar(unsigned t, Clk& c) {
    unsigned tm, rp, x, fi;
    asm volatile(WAIT_FINE "s_barrier" : WAIT_FINE_OUT : [cq] "s"(c.cpq) : "vcc", "scc", "s100", "s101", "memory");
}
__device__ __forceinline__ void wait_cyc_bar(unsigned tm) {
    unsigned x;
    asm volatile(
        "3:\n\t"
        "s_memtime s[100:101]\n\t"
        "s_waitcnt lgkmcnt(0)\n\t"
        "s_sub_u32 %0, %1, s100\n\t"
        "s_cmp_gt_i32 %0, 92\n\t"
        "s_cbranch_scc1 3b\n\t"
        "s_max_i32 %0, %0, 0\n\t"
        "s_andn2_b32 %0, %0, 3\n\t"
        "s_sub_u32 %0, 104, %0\n\t"
        "s_getpc_b64 vcc\n\t"
        "s_add_u32 vcc_lo, vcc_lo, %0\n\t"
        "s_addc_u32 vcc_hi, vcc_hi, 0\n\t"
        "s_setpc_b64 vcc\n\t"
        ".rept 23\n\t"
        "s_nop 0\n\t"
        ".endr\n\t"
        "s_barrier"
        : "=&s"(x) : "s"(tm) : "vcc", "scc", "s100", "s101", "memory");
}
__device__ __forceinline__ unsigned cyc_at(const Clk& c, int d) {
    d = min(d, 20000);
    asm volatile("" : "+s"(d));
    return c.ma + (unsigned)((max(d, -4000) * (int)c.cpq) >> 8);
}
__device__ __forceinline__ void clk_rate(Clk& c, unsigned ta0, unsigned ma0) {
    const int dq = sx24(c.ta - ta0);
    if (dq > 200) {
        const float r = fminf(fmaxf((float)(c.ma - ma0) * __builtin_amdgcn_rcpf((float)dq), 1.f), 8.f);
        asm volatile("s_nop 1\n\tv_readfirstlane_b32 %0, %2\n\tv_readfirstlane_b32 %1, %3\n\ts_nop 3" : "=s"(c.cpq), "=s"(c.qpc)
                     : "v"((unsigned)(256.f * r + 0.5f)), "v"((unsigned)(65536.f * __builtin_amdgcn_rcpf(r))));
    }
}
__device__ __forceinline__ void clk_init(Clk& c) {
    c.ta = 4u * (unsigned)__builtin_amdgcn_s_memrealtime(); c.ma = (unsigned)__builtin_amdgcn_s_memtime();
    for (int i = 0; i < 2; ++i) {
        const unsigned ta0 = c.ta, ma0 = c.ma;
        wait_fine(c.ta + (i ? 360u : 60u), c);
        clk_rate(c, ta0, ma0);
    }
}
__device__ __forceinline__ unsigned sched_lane(int s) { const unsigned l = fresh_lane(); return (l & 31u) * 128u + (l >> 5) * 4u; }
__device__ __forceinline__ int read_sched(__amdgpu_buffer_rsrc_t rwin, int s, unsigned vo, unsigned to, unsigned f, unsigned g0, int& lt, int& xs, unsigned& fc) {
    int d = (int)((f - g0) & 0xFFFFFFu);
    if (__builtin_expect(__any((f >> 24) != (unsigned)s) || s == 0, 0)) {
        for (unsigned spins = 0; __any((f >> 24) != (unsigned)s);) {
            FC(fc);
            if (spin_fail(spins, rwin)) break;
            __builtin_amdgcn_s_sleep(TSLP);
            asm volatile("" ::: "memory");
            if ((f >> 24) != (unsigned)s) f = __builtin_amdgcn_raw_buffer_load_b32(rwin, vo, to, 16);
        }
        const unsigned f0 = __builtin_amdgcn_readfirstlane(f) - 0x100000u;
        d = (int)((f - f0) & 0xFFFFFFu) + (int)((f0 - g0) & 0xFFFFFFu);
    }
    d = max(d, __builtin_amdgcn_update_dpp(0, d, 0xB1, 0xF, 0xF, true));
    d = max(d, __builtin_amdgcn_update_dpp(0, d, 0x4E, 0xF, 0xF, true));
    d = max(d, __builtin_amdgcn_update_dpp(0, d, 0x141, 0xF, 0xF, true));
    d = max(d, __builtin_amdgcn_update_dpp(0, d, 0x140, 0xF, 0xF, true));
    d = max(d, __builtin_amdgcn_update_dpp(0, d, 0x142, 0xA, 0xF, true));
    d = max(__builtin_amdgcn_readlane(d, 16), __builtin_amdgcn_readlane(d, 48));
    const int l0 = lt, x0 = xs;
    d -= 0x400000;
    const bool miss = d > l0 + min(x0 + LTM, LTX);
    if (s >= SAD) { lt = miss ? l0 : min(max(d, l0 - 1 - ((l0 - d) >> LTS)), l0 + LTU); xs = miss ? min(x0 + XSU, XSM) : max(x0 - 1 - (x0 >> XSS), 0); }
    if (s == 1) lt = LT0;
    return 0x400000 + (s < SAD ? d + PEN0 : miss ? d + PEN : l0 + x0) + M1 + M2 + x0;
}
__device__ __forceinline__ unsigned nosent1(unsigned v) { return min(v, 0xFFFFFFFEu); }
__device__ __forceinline__ unsigned smax(u32x4 v) { return max(v[1], v[3]); }
constexpr int QS = 272;
constexpr int TQS = 1152;
constexpr int L_Q = 10240, L_K = L_Q + 2 * TQS, L_S = L_K + 2 * TQS, L_TOK = L_S + 256, L_H = L_TOK + 49152, L_END = L_H + 2 * 40960;
typedef __attribute__((ext_vector_type(4))) short v4i16_t;
__device__ __forceinline__ u32x2 ldtr(const char* p) { return __builtin_bit_cast(u32x2, __builtin_amdgcn_ds_read_tr16_b64_v4i16((LAS v4i16_t*)p)); }
__device__ __forceinline__ u32x2 ldtr1(const LAS char* p) { return __builtin_bit_cast(u32x2, __builtin_amdgcn_ds_read_tr16_b64_v4i16((LAS v4i16_t*)p)); }
__device__ __forceinline__ u32x4 ldtr2(const LAS char* p0, const LAS char* p1) {
    const u32x2 x = __builtin_bit_cast(u32x2, __builtin_amdgcn_ds_read_tr16_b64_v4i16((LAS v4i16_t*)p0)), y = __builtin_bit_cast(u32x2, __builtin_amdgcn_ds_read_tr16_b64_v4i16((LAS v4i16_t*)p1));
    return (u32x4){x[0], x[1], y[0], y[1]};
}
__device__ __forceinline__ u32x4 ldtr2(const char* p0, const char* p1) { const u32x2 x = ldtr(p0), y = ldtr(p1); return (u32x4){x[0], x[1], y[0], y[1]}; }

__device__ __forceinline__ unsigned poll_lim(int& budget) {
    --budget;
    unsigned lim = budget > 0 ? 0xFFFFFFFEu : 0xFFFFFFFFu;
    asm volatile("" : "+s"(lim));
    return lim;
}
#define GATHER \
        "buffer_load_dwordx4 %[vo], %[rw], %[s0] offen sc1 lds\n\t" \
        "buffer_load_dwordx4 %[vo], %[rw], %[s0] offen offset:1024 sc1 lds\n\t" \
        "buffer_load_dwordx4 %[vo], %[rw], %[s0] offen offset:2048 sc1 lds\n\t" \
        "buffer_load_dwordx4 %[vo], %[rw], %[s0] offen offset:3072 sc1 lds\n\t" \
        "s_movk_i32 m0, 0x1000\n\t" \
        "s_nop 0\n\t" \
        "buffer_load_dwordx4 %[vo], %[rw], %[s4] offen sc1 lds\n\t" \
        "buffer_load_dwordx4 %[vo], %[rw], %[s4] offen offset:1024 sc1 lds\n\t" \
        "buffer_load_dwordx4 %[vo], %[rw], %[s4] offen offset:2048 sc1 lds\n\t" \
        "buffer_load_dwordx4 %[vo], %[rw], %[s4] offen offset:3072 sc1 lds\n\t" \
        "s_movk_i32 m0, 0x2000\n\t" \
        "s_nop 0\n\t" \
        "buffer_load_dwordx4 %[vo], %[rw], %[s1] offen sc1 lds\n\t" \
        "buffer_load_dwordx4 %[vo], %[rw], %[s1] offen offset:1024 sc1 lds"
#define GATHER_IN [vo] "v"(vo), [rw] "s"(rwin), [s0] "s"(so0), [s4] "s"(so0 + 4096u), [s1] "s"(so1)
__device__ __forceinline__ void gather_dma(__amdgpu_buffer_rsrc_t rwin, unsigned vo, unsigned so0, unsigned so1) {
    asm volatile("s_mov_b32 m0, 0\n\ts_nop 0\n\t" GATHER :: GATHER_IN : "m0", "memory");
}
__device__ __forceinline__ void gather_at(unsigned t, Clk& c, __amdgpu_buffer_rsrc_t rwin, unsigned vo, unsigned so0, unsigned so1) {
    unsigned tm, rp, x, fi;
    asm volatile("s_mov_b32 m0, 0\n\t" WAIT_FINE GATHER : WAIT_FINE_OUT : [cq] "s"(c.cpq), GATHER_IN : "vcc", "scc", "s100", "s101", "m0", "memory");
}
__device__ __forceinline__ void dma4k(__amdgpu_buffer_rsrc_t r, unsigned vo, unsigned so, unsigned lds) {
    asm volatile(
        "s_mov_b32 m0, %3\n\t"
        "s_nop 0\n\t"
        "buffer_load_dwordx4 %0, %1, %2 offen sc1 lds\n\t"
        "buffer_load_dwordx4 %0, %1, %2 offen offset:1024 sc1 lds\n\t"
        "buffer_load_dwordx4 %0, %1, %2 offen offset:2048 sc1 lds\n\t"
        "buffer_load_dwordx4 %0, %1, %2 offen offset:3072 sc1 lds"
        :: "v"(vo), "s"(r), "s"(so), "s"(lds) : "m0", "memory");
}
__device__ __forceinline__ int bs2(int c) { return (c & ~3) | ((c & 1) << 1) | ((c >> 1) & 1); }
__device__ __forceinline__ void gather_mem0(char* memL, int tid) {
    const u16* mem0 = arg(A_MEM0);
    for (int c = tid; c < 640; c += 512) {
        const u32x2* src = (const u32x2*)(mem0 + ((c & 6) + ((c >> 3) & 1)) * D_ + (c >> 4) * 16 + (c & 1) * 4);
        const u32x2 x = src[0], y = src[2];
        *(u32x4*)(memL + c * 16) = (u32x4){(x[0] & 0xFFFFu) | (y[0] << 16), (x[0] >> 16) | (y[0] & 0xFFFF0000u), (x[1] & 0xFFFFu) | (y[1] << 16), (x[1] >> 16) | (y[1] & 0xFFFF0000u)};
    }
}
__device__ __forceinline__ f32x2 hadd(f32x4 c) { return (f32x2){c[0], c[1]} + (f32x2){c[2], c[3]}; }
__device__ __forceinline__ unsigned hpack(f32x4 c) { const f32x2 y = hadd(c); return pack2(y[0], y[1]); }
__device__ __forceinline__ u32x8 cat8(u32x4 lo, u32x4 hi) { return (u32x8){lo[0], lo[1], lo[2], lo[3], hi[0], hi[1], hi[2], hi[3]}; }
__device__ __forceinline__ u32x4 half8(const u32x8& v, int u) { return u ? (u32x4){v[4], v[5], v[6], v[7]} : (u32x4){v[0], v[1], v[2], v[3]}; }
__device__ __forceinline__ void dma1k(__amdgpu_buffer_rsrc_t r, unsigned vo, unsigned so, unsigned lds) {
    asm volatile("s_mov_b32 m0, %3\n\ts_nop 0\n\tbuffer_load_dwordx4 %0, %1, %2 offen sc1 lds" :: "v"(vo), "s"(r), "s"(so), "s"(lds) : "m0", "memory");
}
__device__ __forceinline__ void load_w(u32x8 (&wt)[2][10], const u16* src, unsigned rs, bool perm, unsigned b, unsigned c, int w) {
    const __amdgpu_buffer_rsrc_t r = mkrsrc(src, 0x7FFFFFFFu);
    const unsigned l = fresh_lane(), q = l >> 4, lds = (unsigned)(w >> 2) * 65536u, v = w & 3;
    const unsigned vo0 = (perm ? ((q & 1u) << 3) | ((q & 2u) << 1) : q) * rs + ((l & 15u) ^ (q << 1)) * 16u, vo1 = vo0 ^ 128u, a = perm ? rs : 4u * rs;
    const LAS char* p0 = (const LAS char*)(size_t)(lds + q * 2048u + ((l >> 2) & 3u) * 256u + ((2u * v) ^ ((l >> 2) & 7u)) * 32u + (l & 3u) * 8u);
    const LAS char* p1 = (const LAS char*)((size_t)p0 ^ 32u);
    auto issue = [&](int g) {
#pragma unroll
        for (int i = 0; i < 4; ++i)
            asm volatile("s_mov_b32 m0, %3\n\ts_nop 0\n\tbuffer_load_dwordx4 %0, %1, %2 offen lds" :: "v"(i < 2 ? vo0 : vo1), "s"(r), "s"(i * a + (g & 1) * b + (g >> 1) * c), "s"(lds + (g & 3) * 16384u + v * 4096u + i * 1024u) : "m0", "memory");
    };
#pragma unroll
    for (int g = 0; g < 3; ++g) issue(g);
#pragma unroll
    for (int g = 0; g < 10; ++g) {
        asm volatile("s_waitcnt vmcnt(%0)\n\ts_barrier" :: "n"(g < 8 ? 8 : 4 * (9 - g)) : "memory");
        if (g + 3 < 10) issue(g + 3);
        const int o = (g & 3) * 16384;
        wt[0][g] = cat8(ldtr2(p0 + o, p0 + o + 1024), ldtr2(p0 + o + 8192, p0 + o + 9216));
        wt[1][g] = cat8(ldtr2(p1 + o, p1 + o + 1024), ldtr2(p1 + o + 8192, p1 + o + 9216));
        asm volatile("s_waitcnt lgkmcnt(0)" : "+v"(wt[0][g]), "+v"(wt[1][g]) :: "memory");
    }
    BAR0();
}
__device__ __forceinline__ int tile_of(int o) { return 8 * ((o % 10) >> 1) + 2 * (o / 10) + (o & 1); }
__device__ __forceinline__ void wait_tags(__amdgpu_buffer_rsrc_t rwin, int g0, int g1, unsigned n1, u64 val, bool boot) {
    const unsigned l = fresh_lane();
    unsigned o[4];
#pragma unroll
    for (int g = 0; g < 4; ++g) o[g] = g >= g0 && g < g1 && (g + 1 < g1 || l < n1) ? l * 128u + g * 8192u : 0x40000000u;
    for (unsigned spins = 0;;) {
        asm volatile("" ::: "memory");
        u32x2 v[4];
#pragma unroll
        for (int g = 0; g < 4; ++g) v[g] = __builtin_amdgcn_raw_buffer_load_b64(rwin, o[g], W_A1, 16);
#pragma unroll
        for (int g = 0; g < 4; ++g) if (__builtin_bit_cast(u64, v[g]) == val) o[g] = 0x40000000u;
        if (!__any((o[0] & o[1] & o[2] & o[3]) != 0x40000000u)) break;
        if (boot ? ++spins > SPIN_CAP : spin_fail(spins, rwin)) break;
        __builtin_amdgcn_s_sleep(ASLP);
    }
}
__device__ __forceinline__ void stage_h(__amdgpu_buffer_rsrc_t rws, int seg, unsigned lds, int t) {
    const unsigned wo = __builtin_amdgcn_readfirstlane(t >> 6) * 1024;
#pragma unroll
    for (int i = 0; i < 5; ++i) dma1k(rws, (t & 63) * 16u, O_H + (unsigned)seg * 40960u + wo + i * 8192u, lds + wo + i * 8192u);
}
__device__ __forceinline__ void wait_h(__amdgpu_buffer_rsrc_t rwin, unsigned fo, int seg, int& hrdy) {
    unsigned lim = 63u;
    if (__builtin_expect(hrdy <= seg, 0)) for (unsigned spins = 0;;) {
        asm volatile("" ::: "memory");
        const u32x4 f = __builtin_amdgcn_raw_buffer_load_b128(rwin, min(fresh_lane(), lim) * 128u, W_H + fo, 16);
        const unsigned long long miss = __ballot((f[0] | f[1] | f[2] | f[3]) != 0u);
        hrdy = miss ? __builtin_ctzll(miss) : (int)lim + 1;
        if (hrdy > seg) break;
        if (spin_fail(spins, rwin)) break;
        lim = (unsigned)min(seg + HLA, 63);
        __builtin_amdgcn_s_sleep(HSLP);
    }
}
template <int CTRL> __device__ __forceinline__ unsigned dppu(unsigned v) { return (unsigned)__builtin_amdgcn_mov_dpp((int)v, CTRL, 0xF, 0xF, true); }
template <int CTRL> __device__ __forceinline__ u32x4 ror4(u32x4 v) { return (u32x4){dppu<CTRL>(v[0]), dppu<CTRL>(v[1]), dppu<CTRL>(v[2]), dppu<CTRL>(v[3])}; }
template <int N> struct IC { static constexpr int v = N; };
__device__ __forceinline__ float xmax16(float v) {
    auto q = __builtin_amdgcn_permlane16_swap(__float_as_uint(v), __float_as_uint(v), false, false);
    return fmx(__uint_as_float(q[0]), __uint_as_float(q[1]));
}
__device__ __forceinline__ float xsum16(float v) {
    auto q = __builtin_amdgcn_permlane16_swap(__float_as_uint(v), __float_as_uint(v), false, false);
    return __uint_as_float(q[0]) + __uint_as_float(q[1]);
}
__device__ __forceinline__ float addswap16(float a, float b) {
    auto r = __builtin_amdgcn_permlane16_swap(__float_as_uint(a), __float_as_uint(b), false, false);
    return __uint_as_float(r[0]) + __uint_as_float(r[1]);
}
#define LANES() \
    const int lane = (int)fresh_lane(), tid = w * 64 + lane, fr = lane & 15, fq = lane >> 4; \
    const int sidx = (lane & 2) ? 0xEEEE : 0x4444; \
    const int ma0 = fq * 256 + (fr & 1) * 128 + (fr >> 1) * 16; \
    const int prow = 16 * ((fr >> 2) & 1) + 4 * (fr >> 3) + (fr & 3), hoff = fr * 128; \
    (void)tid; (void)sidx; (void)ma0; (void)prow; (void)hoff

__device__ __forceinline__ int seg_after(unsigned long long m, int j) { m = j >= 63 ? 0ull : m >> (j + 1); return m ? j + 1 + __builtin_ctzll(m) : NSEG; }
__device__ __forceinline__ int seg_done(unsigned long long m, int f0, int f1) { return min(seg_after(~m, f0), seg_after(m, f1)) - 1; }
template <int OFF> __device__ __forceinline__ void ldfrag(u32x4& d, const LAS char* p) { asm volatile("ds_read_b128 %0, %1 offset:%2" : "=v"(d) : "v"(p), "n"(OFF) : "memory"); }
template <int U, class FM, class FV> __device__ __forceinline__ void project(const u32x8 (&wt)[2][10], f32x4 (&tc)[2][2], u32x4 (&bb)[8], const LAS char* c0, const LAS char* c1, const LAS char* n0, const LAS char* n1, FM mid, FV vm) {
    constexpr int t = U >> 2, k = U & 3, v = (U + 8) % 40;
    if (U == 32) mid();
    vm(IC<U>());
    const LAS char* p = U < 32 ? (k >> 1 ? c1 : c0) : (k >> 1 ? n1 : n0);
    if (U < 2) asm volatile("s_nop 0\n\ts_waitcnt lgkmcnt(7)\n\tv_mfma_f32_16x16x32_bf16 %0, %3, %2, 0\n\tv_mfma_f32_16x16x32_bf16 %1, %4, %2, 0\n\tds_read_b128 %2, %5 offset:%6"
                            : "=&v"(tc[0][k & 1]), "=&v"(tc[1][k & 1]), "+v"(bb[U & 7]) : "v"(half8(wt[0][t], k >> 1)), "v"(half8(wt[1][t], k >> 1)), "v"(p), "n"((v >> 2) * 4096 + (k & 1) * 2048) : "memory");
    else asm volatile(".rept %7\n\ts_nop 0\n\t.endr\n\ts_waitcnt lgkmcnt(7)\n\tv_mfma_f32_16x16x32_bf16 %0, %3, %2, %0\n\tv_mfma_f32_16x16x32_bf16 %1, %4, %2, %1\n\tds_read_b128 %2, %5 offset:%6"
                      : "+v"(tc[0][k & 1]), "+v"(tc[1][k & 1]), "+v"(bb[U & 7]) : "v"(half8(wt[0][t], k >> 1)), "v"(half8(wt[1][t], k >> 1)), "v"(p), "n"((v >> 2) * 4096 + (k & 1) * 2048), "n"(U < 8 ? 1 : 0) : "memory");
    if constexpr (U < 39) project<U + 1>(wt, tc, bb, c0, c1, n0, n1, mid, vm);
    else asm volatile("s_nop 7" : "+v"(tc[0][0]), "+v"(tc[0][1]), "+v"(tc[1][0]), "+v"(tc[1][1]));
}
__device__ __forceinline__ void project_end(u32x4 (&bb)[8]) {
    asm volatile("s_waitcnt lgkmcnt(0)" : "+v"(bb[0]), "+v"(bb[1]), "+v"(bb[2]), "+v"(bb[3]), "+v"(bb[4]), "+v"(bb[5]), "+v"(bb[6]), "+v"(bb[7]) :: "memory");
}
__device__ __forceinline__ void project0(u32x4 (&bb)[8], const LAS char* c0, const LAS char* c1) {
    ldfrag<0>(bb[0], c0); ldfrag<2048>(bb[1], c0); ldfrag<0>(bb[2], c1); ldfrag<2048>(bb[3], c1);
    ldfrag<4096>(bb[4], c0); ldfrag<6144>(bb[5], c0); ldfrag<4096>(bb[6], c1); ldfrag<6144>(bb[7], c1);
    project_end(bb);
}

__global__ __launch_bounds__(512, 2) B3ATTR void mega_kernel(Args a) {
    char* memL = smem;
    char* tokL = smem + L_TOK;
    char* hL = smem + L_H;
    int w = __builtin_amdgcn_readfirstlane(threadIdx.x >> 6);
    const __amdgpu_buffer_rsrc_t rwin = mkrsrc(a.win, W_END);
    char* const ws = a.ws;
    const __amdgpu_buffer_rsrc_t rws = mkrsrc(ws, (unsigned)OFF_WIN);
    const u32x4 SENT = {0xFFFFFFFFu, 0xFFFFFFFFu, 0xFFFFFFFFu, 0xFFFFFFFFu};
    unsigned fs[12] = {};
    const u32x2 tg0 = __builtin_amdgcn_raw_buffer_load_b64(rws, 0u, 0u, 16);
    u64 tag;
    if (blockIdx.x >= 64) {
        const int tid = threadIdx.x;
        if (blockIdx.x >= 128) {
            RowTw tw;
            row_tw(tw, tid);
            a1_unit<8>(mkrsrc(a.x, S_ * D_ * 2), (blockIdx.x - 128) * 16, rws, tw, tid, fs);
        } else {
            const unsigned j = blockIdx.x - 64;
            u32x4 v[6];
#pragma unroll
            for (int i = 0; i < 6; ++i) {
                const int id = min(tid + 512 * i, 8 * DF - 1);
                v[i] = ((const u32x4*)((id < 4 * DF ? a.wr : a.wi) + (size_t)j * 32 * DF))[id < 4 * DF ? id : id - 4 * DF];
            }
#pragma unroll
            for (int i = 0; i < 6; ++i) ((u32x4*)smem)[min(tid + 512 * i, 8 * DF - 1)] = v[i];
            BAR();
#pragma unroll
            for (int i = 0; i < 6; ++i) {
                const int id = tid + 512 * i, kp = id >> 4;
                if (i < 5 || tid < 16) {
                    const u16* q = (const u16*)smem + (id & 15) * 2 * DF + 2 * kp;
                    u32x4 o;
#pragma unroll
                    for (int e = 0; e < 4; ++e) o[e] = (unsigned)q[(e >> 1) * DF + (e & 1)] | ((unsigned)q[(32 + (e >> 1)) * DF + (e & 1)] << 16);
                    st128(o, rws, (unsigned)(kp * S_ + (id & 15) * 2) * 8u, O_WFT + j * 256u, 16);
                }
            }
#pragma unroll
            for (int i = 0; i < 7; ++i) {
                const unsigned g = (j * 512u + tid + i * 32768u) * 16u;
                st128(SENT, rwin, g + (g < W_ARM ? 0u : W_ZM - W_ARM), 0, 16);
            }
            static_assert(W_ARM + W_END - W_ZM <= 7u * 32768u * 16u, "arming");
        }
        VMWAIT();
        FST(8);
        BAR0();
        FST(9);
    }
    tag = (((u64)__builtin_amdgcn_readfirstlane(tg0[1]) << 32) | (__builtin_amdgcn_readfirstlane(tg0[0]) & ~15u)) + 16ull;
    if (w == 0) {
        if (blockIdx.x >= 64) __builtin_amdgcn_raw_buffer_store_b64(__builtin_bit_cast(u32x2, tag + 8ull), rwin, LANE0, W_A1 + blockIdx.x * 128u, 16);
        else if (blockIdx.x == 0) __builtin_amdgcn_raw_buffer_store_b64(__builtin_bit_cast(u32x2, tag + (xcc_id() & 7u)), rws, LANE0, 128u, 16);
    }
    if (blockIdx.x >= 128) FSW(0);
    unsigned vx;
    int bid;
    auto set_role = [&](unsigned x0) INL {
        vx = x0;
        const int j = blockIdx.x >> 3, x = vx & 3;
        if (!(vx & 4)) bid = j < 8 ? 8 * x + j : j < 16 ? 24 + 8 * x + j : j < 26 ? 80 + 10 * x + j : j < 30 ? NWRK + 4 * (j - 26) + x : NXT - 24 + 8 * x + j;
        else bid = j < 8 ? 64 + 8 * x + j : j < 16 ? NQT - 8 + 8 * x + j : j < 26 ? NREC - 16 + 10 * x + j : NXT - 26 + 8 * x + j;
    };
    auto get_rot = [&]() INL {
        unsigned r = 0;
        for (unsigned spins = 0; spins < SPIN_CAP; ++spins) {
            asm volatile("" ::: "memory");
            const u64 x = __builtin_bit_cast(u64, __builtin_amdgcn_raw_buffer_load_b64(rws, 0u, 128u, 16)) - tag;
            r = __builtin_amdgcn_readfirstlane((unsigned)x);
            if (__all(x < 8ull)) break;
            __builtin_amdgcn_s_sleep(2);
        }
        return (r + blockIdx.x + 4u * DIE) & 7u;
    };
    set_role(blockIdx.x < 64 ? (xcc_id() ^ (4u * DIE)) & 7u : get_rot());
    unsigned long long tref = 0;
    int layer = 0;
    unsigned fo = 0;
    unsigned nh = 0, nz = 0, mc = 0, qz = 0, mo = 0, mo1 = 0, tro = 0;
    auto near_init = [&]() INL {
        unsigned f;
        for (unsigned spins = 0;;) {
            asm volatile("" ::: "memory");
            f = __builtin_amdgcn_raw_buffer_load_b32(rwin, (fresh_lane() & 7u) * 4u, W_NEAR, 16);
            if (!__any(f == ~0u) || spin_fail(spins, rwin)) break;
            __builtin_amdgcn_s_sleep(8);
        }
        const unsigned h = (unsigned)__ballot(f & 1u), q = (unsigned)__ballot(f & 2u) ^ 0xAAu;
        auto quad = [&](unsigned m) { return (3u * (m & 1u) + 2u * __builtin_popcount(m & 14u) + 3u) >> 3; };
        nh = (h & 1u) * HALF; nz = ((h >> 4) & 1u) * HALF;
        mc = (vx & 3u) ^ quad(q);
        qz = quad(q >> 4);
        mo = W_MEM + mc * PAIR + nh; mo1 = W_MEM + (4u + (mc & 1u)) * PAIR + nh + (mc >> 1) * 2048u;
        tro = W_T + (mc & 1u) * PAIR + nh + (mc >> 1) * 4096u;
    };
    auto zpair = [&](unsigned x, unsigned k) { return (((x >> 1) * 10u + k) * 2u + ((x & 1u) ^ qz)) * PAIR + nz; };
    auto two = [&]() { const unsigned l = fresh_lane(); return l < 4 ? (l >> 1) * PAIR + (l & 1) * 4096u + nh : 0x40000000u; };
    const f32x4 z4 = {0.f, 0.f, 0.f, 0.f};
    int hrdy = 0, budget = (int)SPIN_CAP;
    unsigned fc0 = 0, fc1 = 0, fc2 = 0;
    Clk ck = {0u, 0u, 1600u, 10486u};
    SST(5);
#define xt (bid >= NXT && bid < NWRK)
    unsigned tpp = 0;
    int lt = LT1, xs = XS0;
    struct Tk { unsigned vo, tw, sl; const LAS char* sp; };
    auto tk_init = [&]() {
        const unsigned l = fresh_lane();
        Tk k = {l * 16u, two(), (l & 31u) * 128u + (l >> 5) * 4u, (const LAS char*)(memL + l * 64 + 4 + ((l >> 1) & 7) * 8)};
        PIN2(k.vo, k.tw); PIN2(k.sl, k.sp);
        return k;
    };
    auto sched2 = [&](Tk& k, int s, unsigned f) INL {
        const unsigned g0 = tpp - 0x400000u;
        tpp = g0 + (unsigned)read_sched(rwin, s, k.sl, tro + (unsigned)(s & 3) * 2u * PAIR, f, g0, lt, xs, fc1);
        k.sl &= ~4u;
        RT(6);
    };
    auto sched = [&](Tk& k, int s) INL { sched2(k, s, __builtin_amdgcn_raw_buffer_load_b32(rwin, k.sl, tro + (unsigned)(s & 3) * 2u * PAIR, 16)); };
    unsigned it = 0;
    auto fetch = [&](const Tk& k, int s, bool first, unsigned lim, unsigned dc, unsigned fo, bool tell) INL {
        const unsigned sb = (unsigned)(s & 3) * 6u * PAIR, ta0 = ck.ta, mm0 = ck.ma;
        if (__builtin_expect(s > 0, 1)) {
            if (__builtin_expect(first, 1)) { const unsigned te = cyc_at(ck, sx24(tpp - TKE - ck.ta)); while ((int)((unsigned)__builtin_amdgcn_s_memtime() - te) < 0) __builtin_amdgcn_s_sleep(1); }
            if (__builtin_expect(first, 1)) gather_at(tpp, ck, rwin, k.vo, mo + sb, mo1 + sb); else { const unsigned c = (mc + 1u + (it & 1u)) & 3u; gather_dma(rwin, k.vo, W_MEM + c * PAIR + nh + sb, W_MEM + (4u + (c & 1u)) * PAIR + nh + (c >> 1) * 2048u + sb); }
        }
        __builtin_amdgcn_s_setprio(0);
        RT(1);
        clk_rate(ck, ta0, mm0);
        unsigned tb = ck.ma - dc, tg = (unsigned)s << 24, so = W_T + (unsigned)(s & 3) * 2u * PAIR + fo;
        asm volatile("" : "+s"(tb), "+s"(tg), "+s"(so));
        asm volatile("s_waitcnt vmcnt(%1) lgkmcnt(0)\n\ts_barrier\n\ts_waitcnt vmcnt(%2)\n\ts_barrier\n\ts_waitcnt vmcnt(0)\n\ts_memtime %0\n\ts_barrier"
                     : "=s"(tref) : "n"(10 - NB1), "n"(10 - NB2) : "memory");
        MT(1);
        if (!tell) { RT(3); return; }
        unsigned x0 = *(const LAS unsigned*)k.sp, x1 = *(const LAS unsigned*)(k.sp + 4096), x2 = *(const LAS unsigned*)(k.sp + 6144);
        __builtin_amdgcn_s_waitcnt(0xC07F);
        asm volatile("" : "+s"(tref) :: "memory");
        const unsigned fw = ((ck.ta + ((((unsigned)tref - tb) * ck.qpc) >> 16)) & 0xFFFFFFu) | tg;
        __builtin_amdgcn_raw_buffer_store_b32(fw, rwin, k.tw, __any(max(max(x0, x1), x2) > lim) ? 0x40000000u : so, 16);
        RT(3);
    };
    auto rout = [&]() { return mkrsrc(layer ? arg(A_OUT) : (const u16*)(ws + OFF_X2), S_ * D_ * 2); };
    auto mytid = [&]() { return w * 64 + (int)fresh_lane(); };
    auto new_layer = [&]() { fo = (unsigned)layer * W_LAYER; asm volatile("" : "+s"(fo), "+s"(w), "+s"(bid), "+s"(nh), "+s"(nz), "+s"(mc), "+s"(qz)); hrdy = 0; tpp = 0; lt = LT1; xs = XS0; };
    auto load_qv = [&](u32x8 (&wt)[2][10], int hh, bool v) INL {
        const bool o = v && w >= 4;
        load_w(wt, arg(A_WQ + 2 * v + (w >> 2)) + (o ? (hh * 128 + (w & 3) * 32) * D_ : hh * 128 + (w & 3) * 16 * HD), o ? D_ * 2 : HD * 2, !o, o ? 16 * D_ * 2 : 64 * HD * 2, o ? 256 : 128 * HD * 2, w);
    };
    auto stage2 = [&](int j0, int j1) INL {
        if (w == 0) { wait_h(rwin, fo, j1, hrdy); *(volatile LAS int*)(smem + L_S + 32) = hrdy; }
        BAR();
        hrdy = __builtin_amdgcn_readfirstlane(*(volatile LAS int*)(smem + L_S + 32));
        SST(2);
        const int tid = w * 64 + (int)fresh_lane();
        stage_h(rws, j0, L_H, tid);
        stage_h(rws, j1, L_H + 40960, tid);
    };
#define WO(eb, nt) wt[(nt) & 1][((nt) & ~1) + (eb)]
    auto vo_role = [&]() INL {
        int hh = bid & 31;
        u32x8 wt[2][10];
        if ((bid & 7) < 2) {
            unsigned* best = (unsigned*)smem;
            const int tid = mytid();
            const __amdgpu_buffer_rsrc_t rw = mkrsrc(a.win + (bid & 1 ? W_END : W_PROBE), 2 * PAIR);
            const unsigned base = (unsigned)(hh >> 3) * 128u + w * 896u;
            if (tid < 4) best[tid] = 0u;
            BAR();
            unsigned mn[4] = {~0u, ~0u, ~0u, ~0u};
            for (int r = 0; r < NPR; ++r) {
#pragma unroll
                for (int i = 0; i < 4; ++i) __builtin_amdgcn_raw_buffer_store_b32(0u, rw, base + i * HALF, 0, 16);
                asm volatile("s_waitcnt vmcnt(0)" ::: "memory");
#pragma unroll
                for (int i = 0; i < 4; ++i) {
                    const unsigned long long t1 = __builtin_amdgcn_s_memtime();
                    asm volatile("s_waitcnt lgkmcnt(0)" ::: "memory");
                    unsigned v = __builtin_amdgcn_raw_buffer_load_b32(rw, base + i * HALF, 0, 16);
                    asm volatile("s_waitcnt vmcnt(0)" : "+v"(v) :: "memory");
                    const unsigned long long t2 = __builtin_amdgcn_s_memtime();
                    mn[i] = min(mn[i], (unsigned)(t2 - t1) + v);
                }
            }
            if ((tid & 63) == 0) {
#pragma unroll
                for (int i = 0; i < 4; ++i) atomicAdd(&best[i], min(mn[i], 1000u));
            }
            if (w == 0) wait_tags(rwin, 1, 2, 64u, tag + 8ull, true);
            BAR();
            if (w == 0) __builtin_amdgcn_raw_buffer_store_b32(min(best[1], best[3]) < min(best[0], best[2]) ? (best[3] < best[1] ? 3u : 1u) : (best[2] < best[0] ? 2u : 0u), rwin, LANE0, W_NEAR + (unsigned)((hh >> 3) + (bid & 1) * 4) * 4u, 16);
            BAR();
        } else wait_until(__builtin_amdgcn_s_memtime() + DVO);
        load_qv(wt, hh, true);
        SST(1);
        if (w == 0) wait_tags(rwin, 1, 2, 64u, tag + 8ull, true);
        BAR();
        near_init();
        unsigned zb = W_ZM;
        auto steps = [&](auto cc) INL {
            constexpr int C = decltype(cc)::v & 1;
            constexpr bool TK = decltype(cc)::v == 3;
            LANES();
            Tk tk = {0u, 0u, 0x40000000u, nullptr};
            if (TK) { tk = tk_init(); __builtin_amdgcn_s_setprio(TKP); }
            const unsigned zi = (unsigned)(hh * 128 + fr * 8) * 2u;
            unsigned zo0 = zpair(w & 3, fq) + zi, zo1 = zo0 + 8 * PAIR, zo2 = zpair(w & 3, 8 + (fq >> 1)) + zi + (fq & 1) * 8;
            const LAS char* va = (const LAS char*)(smem + L_Q + (fr & 1) * 544 + ((fr >> 1) & 1) * 1152 + (16 * fq + 2 * (fr >> 2)) * 4);
            LAS char* vw = (LAS char*)(smem + L_Q + (fq & 1) * 544 + (fq >> 1) * 1152 + (16 * w + bs2(fr)) * 4);
            const LAS char* mp0 = (const LAS char*)(memL + ma0);
            PIN2(zo0, zo1); PIN1(zo2);
            if (C == 0) { PIN2(mp0, vw); } else PIN1(va);
            u32x4 za = SENT, zc = SENT;
            u32x2 ze = {0u, 0u};
            auto put_z2 = [&](int s) {
                const unsigned z2b = s < NSEG ? O_Z2 + (unsigned)s * (NOWN * 8192u) + (unsigned)(10 * (w & 3) * 8192 + hh * 256) : 0x40000000u, l2 = fresh_lane();
                st128(za, rws, (l2 >> 4) * 8192u + (l2 & 15u) * 16u, z2b, 16);
                st128(zc, rws, (l2 >> 4) * 8192u + (l2 & 15u) * 16u, z2b + 4u * 8192u, 16);
                __builtin_amdgcn_raw_buffer_store_b64(ze, rws, (l2 >> 5) * 8192u + (l2 & 15u) * 16u + ((l2 >> 4) & 1u) * 8u, z2b + 8u * 8192u, 16);
            };
            for (int s = 0; s < NSEG; ++s) {
                RT(0);
                bool redo = false;
                unsigned sf = 0;
                do {
                    ++it;
                    if (C == 0) {
                        const unsigned lim = s > 0 ? poll_lim(budget) : ~0u;
                        asm volatile(
                            "v_mov_b64 v[200:201], 0\n\t"
                            "v_mov_b64 v[202:203], 0\n\t"
                            "v_mov_b64 v[204:205], 0\n\t"
                            "v_mov_b64 v[206:207], 0\n\t"
                            "v_mov_b32 v208, 0\n\t"
                            "s_setprio 2\n\t"
                            "s_waitcnt lgkmcnt(0)\n\t"
                            "s_barrier\n\t"
                            MTA(0)
                            "ds_read_b128 v[212:215], %[mp] offset:0\n\t"
                            "ds_read_b128 v[216:219], %[mp] offset:1024\n\t"
                            "ds_read_b128 v[220:223], %[mp] offset:2048\n\t"
                            "s_barrier\n\t"
                            "ds_read_b128 v[224:227], %[mp] offset:3072\n\t"
                            "ds_read_b128 v[228:231], %[mp] offset:4096\n\t"
                            "ds_read_b128 v[232:235], %[mp] offset:5120\n\t"
                            "s_waitcnt lgkmcnt(5)\n\t"
                            "v_smfmac_f32_16x16x64_bf16 v[200:203], v[212:215], %[w00], %[ix]\n\t"
                            "s_waitcnt lgkmcnt(4)\n\t"
                            "v_smfmac_f32_16x16x64_bf16 v[200:203], v[216:219], %[w01], %[ix]\n\t"
                            "s_barrier\n\t"
                            MTA(1)
                            "ds_read_b128 v[236:239], %[mp] offset:6144\n\t"
                            "s_waitcnt lgkmcnt(4)\n\t"
                            "v_smfmac_f32_16x16x64_bf16 v[200:203], v[220:223], %[w02], %[ix]\n\t"
                            "ds_read_b128 v[240:243], %[mp] offset:7168\n\t"
                            "s_waitcnt lgkmcnt(4)\n\t"
                            "v_smfmac_f32_16x16x64_bf16 v[200:203], v[224:227], %[w03], %[ix]\n\t"
                            "ds_read_b128 v[244:247], %[mp] offset:8192\n\t"
                            "s_waitcnt lgkmcnt(4)\n\t"
                            "v_smfmac_f32_16x16x64_bf16 v[200:203], v[228:231], %[w04], %[ix]\n\t"
                            "ds_read_b128 v[248:251], %[mp] offset:9216\n\t"
                            "s_waitcnt lgkmcnt(4)\n\t"
                            "v_smfmac_f32_16x16x64_bf16 v[200:203], v[232:235], %[w05], %[ix]\n\t"
                            "s_waitcnt lgkmcnt(3)\n\t"
                            "v_smfmac_f32_16x16x64_bf16 v[200:203], v[236:239], %[w06], %[ix]\n\t"
                            "s_waitcnt lgkmcnt(2)\n\t"
                            "v_smfmac_f32_16x16x64_bf16 v[200:203], v[240:243], %[w07], %[ix]\n\t"
                            "s_waitcnt lgkmcnt(1)\n\t"
                            "v_smfmac_f32_16x16x64_bf16 v[200:203], v[244:247], %[w08], %[ix]\n\t"
                            "s_waitcnt lgkmcnt(0)\n\t"
                            "v_smfmac_f32_16x16x64_bf16 v[200:203], v[248:251], %[w09], %[ix]\n\t"
                            "s_nop 2\n\t"
                            "v_smfmac_f32_16x16x64_bf16 v[204:207], v[212:215], %[w10], %[ix]\n\t"
                            "s_nop 2\n\t"
                            "v_smfmac_f32_16x16x64_bf16 v[204:207], v[216:219], %[w11], %[ix]\n\t"
                            "v_add_f32 v200, v200, v202\n\t"
                            "v_add_f32 v201, v201, v203\n\t"
                            "v_cvt_pk_bf16_f32 v200, v200, v201\n\t"
                            "ds_write_b32 %[vw], v200\n\t"
                            MTA(2)
                            "v_smfmac_f32_16x16x64_bf16 v[204:207], v[220:223], %[w12], %[ix]\n\t"
                            "v_max3_u32 v208, v208, v212, v213\n\t"
                            "v_max3_u32 v208, v208, v214, v215\n\t"
                            "v_smfmac_f32_16x16x64_bf16 v[204:207], v[224:227], %[w13], %[ix]\n\t"
                            "v_max3_u32 v208, v208, v216, v217\n\t"
                            "v_max3_u32 v208, v208, v218, v219\n\t"
                            "v_smfmac_f32_16x16x64_bf16 v[204:207], v[228:231], %[w14], %[ix]\n\t"
                            "v_max3_u32 v208, v208, v220, v221\n\t"
                            "v_max3_u32 v208, v208, v222, v223\n\t"
                            "v_smfmac_f32_16x16x64_bf16 v[204:207], v[232:235], %[w15], %[ix]\n\t"
                            "v_max3_u32 v208, v208, v224, v225\n\t"
                            "v_max3_u32 v208, v208, v226, v227\n\t"
                            "v_smfmac_f32_16x16x64_bf16 v[204:207], v[236:239], %[w16], %[ix]\n\t"
                            "v_max3_u32 v208, v208, v228, v229\n\t"
                            "v_max3_u32 v208, v208, v230, v231\n\t"
                            "v_smfmac_f32_16x16x64_bf16 v[204:207], v[240:243], %[w17], %[ix]\n\t"
                            "s_waitcnt lgkmcnt(0)\n\t"
                            "s_barrier\n\t"
                            MTA(5)
                            "v_smfmac_f32_16x16x64_bf16 v[204:207], v[244:247], %[w18], %[ix]\n\t"
                            "v_max3_u32 v208, v208, v232, v233\n\t"
                            "v_max3_u32 v208, v208, v234, v235\n\t"
                            "v_smfmac_f32_16x16x64_bf16 v[204:207], v[248:251], %[w19], %[ix]\n\t"
                            "v_max3_u32 v208, v208, v236, v237\n\t"
                            "v_max3_u32 v208, v208, v238, v239\n\t"
                            "v_max3_u32 v208, v208, v240, v241\n\t"
                            "v_max3_u32 v208, v208, v242, v243\n\t"
                            "v_max3_u32 v208, v208, v244, v245\n\t"
                            "v_max3_u32 v208, v208, v246, v247\n\t"
                            "v_max3_u32 v208, v208, v248, v249\n\t"
                            "v_max3_u32 v208, v208, v250, v251\n\t"
                            "v_cmp_lt_u32 vcc, %[lim], v208\n\t"
                            "v_add_f32 v204, v204, v206\n\t"
                            "v_add_f32 v205, v205, v207\n\t"
                            "v_cvt_pk_bf16_f32 v204, v204, v205\n\t"
                            "ds_write_b32 %[vw], v204 offset:256\n\t"
                            MTA(3)
                            "s_cbranch_vccz 1f\n\t"
                            "v_mov_b32 v208, %[it]\n\t"
                            "v_mov_b32 v209, 0\n\t"
                            "ds_write_b32 v209, v208 offset:%[ls]\n\t"
                            "1:\n\t"
                            "s_waitcnt vmcnt(0) lgkmcnt(0)\n\t"
                            "s_barrier\n\t"
                            "s_setprio 0\n\t"
                            :: [mp] "v"(mp0), [vw] "v"(vw), [ix] "v"(sidx), [lim] "s"(lim), [it] "s"(it), [ls] "n"(L_S),
                              [w00] "v"(wt[0][0]), [w01] "v"(wt[0][1]), [w02] "v"(wt[0][2]), [w03] "v"(wt[0][3]), [w04] "v"(wt[0][4]), [w05] "v"(wt[0][5]), [w06] "v"(wt[0][6]), [w07] "v"(wt[0][7]), [w08] "v"(wt[0][8]), [w09] "v"(wt[0][9]), [w10] "v"(wt[1][0]), [w11] "v"(wt[1][1]), [w12] "v"(wt[1][2]), [w13] "v"(wt[1][3]), [w14] "v"(wt[1][4]), [w15] "v"(wt[1][5]), [w16] "v"(wt[1][6]), [w17] "v"(wt[1][7]), [w18] "v"(wt[1][8]), [w19] "v"(wt[1][9])
                            : "v200", "v201", "v202", "v203", "v204", "v205", "v206", "v207", "v208", "v209", "v212", "v213", "v214", "v215", "v216", "v217", "v218", "v219", "v220", "v221", "v222", "v223", "v224", "v225", "v226", "v227", "v228", "v229", "v230", "v231", "v232", "v233", "v234", "v235", "v236", "v237", "v238", "v239", "v240", "v241", "v242", "v243", "v244", "v245", "v246", "v247", "v248", "v249", "v250", "v251", "vcc", "memory");
                        MT(4);
                        redo = __builtin_amdgcn_readfirstlane(*(volatile LAS unsigned*)(smem + L_S)) == it;
                        if (redo) FC(fc0);
                    } else {
                        f32x16 ca = {}, cb = {};
                        f32x8 cc = {};
                        asm volatile("" : "+{v[212:227]}"(ca), "+{v[228:243]}"(cb), "+{v[244:251]}"(cc));
                        unsigned sr = tro + (unsigned)(s & 3) * 2u * PAIR;
                        asm volatile("" : "+s"(sr));
                        if (TK) fetch(tk, s, !redo, s > 0 ? poll_lim(budget) : ~0u, DVC, (unsigned)hh * 128u + 4u, __builtin_expect(s == 0, 0));
                        else asm volatile("s_waitcnt lgkmcnt(0)\n\ts_barrier\n\ts_barrier\n\ts_barrier" ::: "memory");
                        unsigned vd, vv, zs = zb;
                        u32x4 af1;
                        asm volatile(
                            "s_setprio 1\n\t"
                            "s_waitcnt lgkmcnt(0)\n\t"
                            "s_barrier\n\t"
                            "ds_read_b64_tr_b16 v[192:193], %[va]\n\t"
                            "ds_read_b64_tr_b16 v[194:195], %[va] offset:32\n\t"
                            "s_waitcnt lgkmcnt(0)\n\t"
                            "v_smfmac_f32_16x16x64_bf16 v[212:215], v[192:195], %[w0], %[ix]\n\t"
                            "v_smfmac_f32_16x16x64_bf16 v[220:223], v[192:195], %[w2], %[ix]\n\t"
                            "v_smfmac_f32_16x16x64_bf16 v[216:219], v[192:195], %[w1], %[ix]\n\t"
                            "v_smfmac_f32_16x16x64_bf16 v[224:227], v[192:195], %[w3], %[ix]\n\t"
                            "s_barrier\n\t"
                            MTA(4)
                            "ds_read_b64_tr_b16 v[196:197], %[va] offset:256\n\t"
                            "v_smfmac_f32_16x16x64_bf16 v[228:231], v[192:195], %[w4], %[ix]\n\t"
                            "ds_read_b64_tr_b16 v[198:199], %[va] offset:288\n\t"
                            "v_smfmac_f32_16x16x64_bf16 v[236:239], v[192:195], %[w6], %[ix]\n\t"
                            "ds_read_b32 v210, %[vz] offset:%[ls]\n\t"
                            "v_smfmac_f32_16x16x64_bf16 v[232:235], v[192:195], %[w5], %[ix]\n\t"
                            "v_smfmac_f32_16x16x64_bf16 v[240:243], v[192:195], %[w7], %[ix]\n\t"
                            "v_smfmac_f32_16x16x64_bf16 v[244:247], v[192:195], %[w8], %[ix]\n\t"
                            "v_smfmac_f32_16x16x64_bf16 v[248:251], v[192:195], %[w9], %[ix]\n\t"
                            "buffer_load_dword %[sf], %[sl], %[rw], %[sr] offen sc1\n\t"
                            : [sf] "=&v"(sf), "+{v[212:227]}"(ca), "+{v[228:243]}"(cb), "+{v[244:251]}"(cc), "=&{v[196:199]}"(af1), "=&{v210}"(vv)
                            : [va] "v"(va), [vz] "v"(0u), [ls] "n"(L_S), [ix] "v"(sidx), [sl] "v"(tk.sl), [sr] "s"(sr), [rw] "s"(rwin),
                              [w0] "v"(WO(0, 0)), [w1] "v"(WO(0, 1)), [w2] "v"(WO(0, 2)), [w3] "v"(WO(0, 3)), [w4] "v"(WO(0, 4)), [w5] "v"(WO(0, 5)), [w6] "v"(WO(0, 6)), [w7] "v"(WO(0, 7)), [w8] "v"(WO(0, 8)), [w9] "v"(WO(0, 9))
                            : "v192", "v193", "v194", "v195", "memory");
                        asm volatile(
                            "s_waitcnt lgkmcnt(0)\n\t"
                            "v_smfmac_f32_16x16x64_bf16 v[212:215], v[196:199], %[w0], %[ix]\n\t"
                            "v_readfirstlane_b32 %[vd], v210\n\t"
                            "v_smfmac_f32_16x16x64_bf16 v[220:223], v[196:199], %[w2], %[ix]\n\t"
                            "s_cmp_eq_u32 %[vd], %[it]\n\t"
                            "s_cselect_b32 %[zs], 2.0, %[zs]\n\t"
                            "v_smfmac_f32_16x16x64_bf16 v[216:219], v[196:199], %[w1], %[ix]\n\t"
                            "s_nop 1\n\t"
                            "v_smfmac_f32_16x16x64_bf16 v[224:227], v[196:199], %[w3], %[ix]\n\t"
                            "v_add_f32 v212, v212, v214\n\t"
                            "v_smfmac_f32_16x16x64_bf16 v[228:231], v[196:199], %[w4], %[ix]\n\t"
                            "v_add_f32 v213, v213, v215\n\t"
                            "v_smfmac_f32_16x16x64_bf16 v[236:239], v[196:199], %[w6], %[ix]\n\t"
                            "v_add_f32 v220, v220, v222\n\t"
                            "v_smfmac_f32_16x16x64_bf16 v[232:235], v[196:199], %[w5], %[ix]\n\t"
                            "v_add_f32 v221, v221, v223\n\t"
                            "v_smfmac_f32_16x16x64_bf16 v[240:243], v[196:199], %[w7], %[ix]\n\t"
                            "v_cvt_pk_bf16_f32 v200, v212, v213\n\t"
                            "v_smfmac_f32_16x16x64_bf16 v[244:247], v[196:199], %[w8], %[ix]\n\t"
                            "v_cvt_pk_bf16_f32 v201, v220, v221\n\t"
                            "v_smfmac_f32_16x16x64_bf16 v[248:251], v[196:199], %[w9], %[ix]\n\t"
                            "v_add_f32 v216, v216, v218\n\t"
                            "v_add_f32 v217, v217, v219\n\t"
                            "v_add_f32 v224, v224, v226\n\t"
                            "v_add_f32 v225, v225, v227\n\t"
                            "v_cvt_pk_bf16_f32 v202, v216, v217\n\t"
                            "v_cvt_pk_bf16_f32 v203, v224, v225\n\t"
                            "v_permlane32_swap_b32 v200, v201\n\t"
                            "v_pk_add_f32 v[228:229], v[228:229], v[230:231]\n\t"
                            "v_permlane32_swap_b32 v202, v203\n\t"
                            "v_min_u32 v201, -2, v201\n\t"
                            "v_pk_add_f32 v[236:237], v[236:237], v[238:239]\n\t"
                            "v_min_u32 v203, -2, v203\n\t"
                            "v_permlane16_swap_b32 v200, v202\n\t"
                            "v_pk_add_f32 v[232:233], v[232:233], v[234:235]\n\t"
                            "v_permlane16_swap_b32 v201, v203\n\t"
                            "v_pk_add_f32 v[240:241], v[240:241], v[242:243]\n\t"
                            MTA(5)
                            "buffer_store_dwordx4 v[200:203], %[z0], %[rw], %[zs] offen sc1\n\t"
                            "v_cvt_pk_bf16_f32 v204, v228, v229\n\t"
                            "v_cvt_pk_bf16_f32 v205, v236, v237\n\t"
                            "v_cvt_pk_bf16_f32 v206, v232, v233\n\t"
                            "v_cvt_pk_bf16_f32 v207, v240, v241\n\t"
                            "v_permlane32_swap_b32 v204, v205\n\t"
                            "v_pk_add_f32 v[244:245], v[244:245], v[246:247]\n\t"
                            "v_permlane32_swap_b32 v206, v207\n\t"
                            "v_min_u32 v205, -2, v205\n\t"
                            "v_pk_add_f32 v[248:249], v[248:249], v[250:251]\n\t"
                            "v_min_u32 v207, -2, v207\n\t"
                            "v_permlane16_swap_b32 v204, v206\n\t"
                            "v_cvt_pk_bf16_f32 v208, v244, v245\n\t"
                            "v_permlane16_swap_b32 v205, v207\n\t"
                            "v_cvt_pk_bf16_f32 v209, v248, v249\n\t"
                            "buffer_store_dwordx4 v[204:207], %[z1], %[rw], %[zs] offen sc1\n\t"
                            "s_nop 0\n\t"
                            "v_permlane32_swap_b32 v208, v209\n\t"
                            "v_min_u32 v209, -2, v209\n\t"
                            "buffer_store_dwordx2 v[208:209], %[z2], %[rw], %[zs] offen sc1\n\t"
                            : [vd] "=&s"(vd), [zs] "+s"(zs), "=&{v[200:203]}"(za), "=&{v[204:207]}"(zc), "=&{v[208:209]}"(ze), "+{v[212:227]}"(ca), "+{v[228:243]}"(cb), "+{v[244:251]}"(cc), "+v"(sf)
                            : [ix] "v"(sidx), [it] "s"(it), [rw] "s"(rwin), [z0] "v"(zo0), [z1] "v"(zo1), [z2] "v"(zo2), "{v[196:199]}"(af1), "{v210}"(vv),
                              [w0] "v"(WO(1, 0)), [w1] "v"(WO(1, 1)), [w2] "v"(WO(1, 2)), [w3] "v"(WO(1, 3)), [w4] "v"(WO(1, 4)), [w5] "v"(WO(1, 5)), [w6] "v"(WO(1, 6)), [w7] "v"(WO(1, 7)), [w8] "v"(WO(1, 8)), [w9] "v"(WO(1, 9))
                            : "scc", "memory");
                        redo = vd == it;
                        RT(4);
                        MT(6);
                        tref = __builtin_amdgcn_s_memtime();
                        __builtin_amdgcn_s_setprio(TKP);
                        asm volatile("s_waitcnt vmcnt(3)" : "+v"(sf) :: "memory");
                    }
                } while (__builtin_expect(redo, 0));
                if (s < 8) SST(8 + s);
                if (C == 0) {
                    if (s > 0) {
                        __builtin_amdgcn_s_sleep(DZR);
                        const unsigned zq = zb == W_ZM ? W_ZM + 2u * NOWN * PAIR : zb - NOWN * PAIR;
                        unsigned s1;
                        asm volatile("v_mov_b32 %0, -1" : "=v"(s1));
                        st128(((u32x4){s1, s1, s1, s1}), rwin, zo0, zq, 16);
                        st128(((u32x4){s1, s1, s1, s1}), rwin, zo1, zq, 16);
                        __builtin_amdgcn_raw_buffer_store_b64((u32x2){s1, s1}, rwin, zo2, zq, 16);
                    }
                } else {
                    if (s > TAILS) __builtin_amdgcn_raw_buffer_store_b32((unsigned)s - 1u, rwin, 0u, W_FZ2 + fo + (unsigned)(hh * 128 + (w & 3) * 4), 16);
                    if (TK && s + 1 < NSEG) sched2(tk, s, sf);
                    if (!TK) wait_until(tref + DZ2);
                    if (s + 1 < NSEG) put_z2(s);
                }
                if (budget <= 0) set_tmo(rwin);
                zb = zb == W_ZM + 2u * NOWN * PAIR ? W_ZM : zb + NOWN * PAIR;
                RT(5);
                RTS();
                MT(7);
                MTS();
            }
        };
        for (; layer < 2; ++layer) {
            new_layer();
            hh = bid & 31;
            const int tid = mytid();
            gather_mem0(memL, tid);
            if (tid < 8) *(unsigned*)(smem + L_S + tid * 4) = 0u;
            if (w == 7) { wait_h(rwin, fo, 0, hrdy); clk_init(ck); }
            if (w < 4) steps(IC<0>()); else if (w < 7) steps(IC<1>()); else steps(IC<3>());
            SST(31);
        }
    };
    auto vt_role = [&](const u32x8 (&wt)[2][10], int hh, unsigned long long mv) INL {
        constexpr bool vt = true;
        auto nextv = [&](int j) { return min(seg_after(mv, j), NSEG - 1); };
        const int jv1 = nextv(-1), jv2 = nextv(jv1);
        if (w == 0) *(unsigned*)(smem + L_S + (fresh_lane() & 7u) * 4) = 0u;
        stage2(jv1, jv2);
        {
            asm volatile("s_waitcnt vmcnt(5) lgkmcnt(0)\n\ts_barrier" ::: "memory");
            SST(3);
            LANES();
            const int ni = __builtin_popcountll(mv) + 1;
            if (w < 4) {
                const LAS char* c0 = (const LAS char*)(hL + hoff + ((fq ^ (fr & 7)) << 4));
                const LAS char* c1 = (const LAS char*)(hL + hoff + (((4 + fq) ^ (fr & 7)) << 4));
                const LAS char* n0 = c0 + 40960; const LAS char* n1 = c1 + 40960;
                u32x4 bb[8];
                project0(bb, c0, c1);
                for (int i = 1; i < ni; ++i) {
                    if (vt) MT(0);
                    f32x4 tc[2][2];
                    project<0>(wt, tc, bb, c0, c1, n0, n1,
                        [&]() {
                            MT(1);
                            asm volatile("s_waitcnt vmcnt(0) lgkmcnt(0)\n\ts_barrier" ::: "memory");
                            MT(2);
                        }, [](auto) {});
                    { const LAS char* x = c0; c0 = n0; n0 = x; x = c1; c1 = n1; n1 = x; }
                    char* tb = tokL + (i & 1) * 32 * QS + (16 * w + 4 * fq) * 2;
#pragma unroll
                    for (int g = 0; g < 2; ++g)
#pragma unroll
                        for (int tt = 0; tt < 2; ++tt) *(u32x2*)(tb + (prow + 8 * tt) * QS + g * 128) = (u32x2){pack2(tc[g][tt][0], tc[g][tt][1]), pack2(tc[g][tt][2], tc[g][tt][3])};
                    asm volatile("" ::: "memory");
                    *(volatile LAS int*)(smem + L_S + 16 + w * 4) = i;
                    if (vt) { MT(3); const int s = i & 63; MTRT(); MTS(); }
                }
                project_end(bb);
            } else {
                __builtin_amdgcn_s_setprio(OPR);
                const unsigned hv = lane * 16u, fz_ = W_FZ + fo + (unsigned)(hh * 128 + xt * 64 + (w - 4) * 4);
                unsigned fp = ~0u;
                int jo = jv1, jh = jv2;
                for (int i = 1; i < ni; ++i, jo = nextv(jo)) {
                    const int b = i + (i >= ZLAG);
                    if (__builtin_expect(i == 1 || i == ZLAG, 0)) {
                        VMWAIT();
                        if (i == ZLAG) {
                            jh = nextv(jh);
                            wait_h(rwin, fo, jh, hrdy);
                            BAR0();
                            for (int t = 0; t < 10; ++t) dma1k(rws, hv, O_H + (unsigned)(jh * 40960 + (w - 4) * 1024 + t * 4096), (unsigned)(L_H + ((i + 1) & 1) * 40960 + (w - 4) * 1024 + t * 4096));
                            VMWAIT();
                        }
                    }
                    jh = nextv(jh);
                    wait_h(rwin, fo, jh, hrdy);
                    if (b < ni) BAR0();
                    if (vt) MT(1);
                    for (;;) {
                        const u32x4 f = *(volatile LAS u32x4*)(smem + L_S + 16);
                        if (__builtin_amdgcn_readfirstlane(min(min(f[0], f[1]), min(f[2], f[3]))) >= (unsigned)i) break;
                        __builtin_amdgcn_s_sleep(1);
                    }
                    asm volatile("" ::: "memory");
                    if (vt) MT(0);
                    u32x4 vf[2][4];
#pragma unroll
                    for (int t = 0; t < 2; ++t)
#pragma unroll
                        for (int ks = 0; ks < 4; ++ks) vf[t][ks] = *(const u32x4*)(tokL + (i & 1) * 32 * QS + (16 * t + fr) * QS + ks * 64 + fq * 16);
                    const unsigned ztb = O_Z + (unsigned)(jo * ZTSEG) * 2u + (unsigned)(10 * (w - 4) * H_ + hh) * 1024u;
                    f32x4 c[2][2];
                    u32x4 r;
                    unsigned hs = O_H + (unsigned)(jh * 40960 + (w - 4) * 1024), hd = (unsigned)(L_H + ((b + 1) & 1) * 40960 + (w - 4) * 1024);
                    asm volatile("" : "+s"(hs), "+s"(hd));
#pragma unroll
                    for (int nt = 0; nt < 11; ++nt) {
                        auto& cc = c[nt & 1]; auto& cp = c[(nt + 1) & 1];
#pragma unroll
                        for (int ks = 0; ks < 4; ++ks) {
                            if (nt < 10) { cc[0] = mfma32(vf[0][ks], half8(WO(ks >> 1, nt < 10 ? nt : 0), ks & 1), ks ? cc[0] : z4); PIN1(cc[0]); }
                            if (nt > 0 && ks >= 1 && ks < 3) { r[2 * ks - 2] = pack2(cp[ks - 1][0], cp[ks - 1][1]); PIN1(r); }
                            if (nt < 10) { cc[1] = mfma32(vf[1][ks], half8(WO(ks >> 1, nt < 10 ? nt : 0), ks & 1), ks ? cc[1] : z4); PIN1(cc[1]); }
                            if (nt > 0 && ks >= 1 && ks < 3) { r[2 * ks - 1] = pack2(cp[ks - 1][2], cp[ks - 1][3]); PIN1(r); }
                            if (nt > 0 && ks == 2) st128(r, rws, lane * 16u, ztb + (unsigned)((nt - 1) * H_) * 1024u, 16);
                            if (nt < 5 && !(ks & 1)) dma1k(rws, hv, hs + (2 * nt + (ks >> 1)) * 4096u, hd + (2 * nt + (ks >> 1)) * 4096u);
                            if (nt == 5 && ks == 0) {
                                asm volatile("s_waitcnt vmcnt(14)" ::: "memory");
                                __builtin_amdgcn_raw_buffer_store_b32(fp, rwin, 0u, fz_, 16);
                            }
                            SB();
                        }
                    }
                    if (vt) MT(2);
                    fp = (unsigned)jo;
                    if (__builtin_expect(jo <= ZEARLY || i + 1 == ni, 0)) {
                        VMWAIT();
                        __builtin_amdgcn_raw_buffer_store_b32(fp, rwin, 0u, fz_, 16);
                    } else asm volatile("s_waitcnt vmcnt(7)" ::: "memory");
                    if (jo < 8) SST(8 + jo);
                    if (vt && (!BSPARSE || jo % 6 == 2)) { const int s = jo; RT(0); RTS(); }
                    if (vt) { MT(3); const int s = i & 63; MTRT(); MTS(); }
                }
            }
            __builtin_amdgcn_s_setprio(0);
            VMWAIT();
            BAR();
        }
    };
    auto qt_role = [&](const u32x8 (&wt)[2][10], int hh, auto qq) INL {
        constexpr bool qt = decltype(qq)::v;
        const unsigned long long mq = xt ? MXQ : ~MXQ;
        auto nextq = [&](int j) { return min(seg_after(mq, j), NSEG - 1); };
        const int jq0 = qt ? nextq(1) : 0, jq1 = qt ? nextq(jq0) : 1;
        stage2(jq0, jq1);
        VMWAIT();
        BAR();
        SST(3);
        {
            LANES();
            const unsigned to0 = (unsigned)((w ^ 4) >> 2) * 8192u + (unsigned)((w & 3) * 64 + (lane ^ ((fq & 1) * (w < 4 ? 8 : 5)))) * 16u, to1 = to0 + 4096u, hv = lane * 16u;
            const LAS char* c0 = (const LAS char*)(hL + hoff + ((fq ^ (fr & 7)) << 4));
            const LAS char* c1 = (const LAS char*)(hL + hoff + (((4 + fq) ^ (fr & 7)) << 4));
            u32x4 bb[8], g0 = SENT, g1 = SENT;
            f32x4 tc[2][2];
            auto pack = [&]() {
                g0 = (u32x4){pack2(tc[0][0][0], tc[0][0][1]), pack2(tc[0][0][2], tc[0][0][3]), pack2(tc[1][0][0], tc[1][0][1]), pack2(tc[1][0][2], tc[1][0][3])};
                g1 = (u32x4){pack2(tc[0][1][0], tc[0][1][1]), pack2(tc[0][1][2], tc[0][1][3]), pack2(tc[1][1][0], tc[1][1][1]), pack2(tc[1][1][2], tc[1][1][3])};
            };
            const LAS char* n0 = c0 + 40960; const LAS char* n1 = c1 + 40960;
            if (!qt) {
                project0(bb, c0, c1);
                for (int j = 0; j < 2; ++j) {
                    project<0>(wt, tc, bb, c0, c1, n0, n1, []() {}, [](auto) {});
                    pack();
                    *(u32x4*)(tokL + j * 16384 + to0) = g0; *(u32x4*)(tokL + j * 16384 + to1) = g1;
                    c0 = n0; c1 = n1;
                }
                project_end(bb);
            } else {
                project0(bb, c0, c1);
                unsigned hs = O_H + (unsigned)(jq1 * 40960 + w * 1024), hd = (unsigned)(L_H + 40960 + w * 1024), fv = tid == 0 ? 0u : 0x40000000u;
                const unsigned fq_ = W_FQ + fo + (unsigned)(hh * 128 + xt * 64);
                const int jl = 63 - __builtin_clzll(mq);
                for (int jp = 1, j = jq0, jn = jq1;; jp = j, j = jn, jn = nextq(jn)) {
                    MT(0);
                    project<0>(wt, tc, bb, c0, c1, n0, n1,
                        [&]() {
                            wait_h(rwin, fo, nextq(jn), hrdy);
                            hs = O_H + (unsigned)(nextq(jn) * 40960 + w * 1024); hd = (unsigned)(2 * L_H + 40960 + 2 * w * 1024) - hd;
                            unsigned fj = (unsigned)jp;
                            asm volatile("" : "+s"(hs), "+s"(hd));
                            PIN1(fj);
                            MT(1);
                            VMWAIT();
                            BAR();
                            MT(2);
                            if (w == 0) __builtin_amdgcn_raw_buffer_store_b32(fj, rwin, fv, fq_, 16);
                        },
                        [&](auto uu) {
                            constexpr int u = decltype(uu)::v;
                            if (u == 34 || u == 38 || u == 2 || u == 6 || u == 10) { constexpr int t = ((u + 6) % 40) / 4; dma1k(rws, hv, hs + t * 8192u, hd + t * 8192u); }
                        });
                    pack();
                    const unsigned qs = O_QK + (unsigned)(j * H_ + hh) * 16384u;
                    st128(g0, rws, to0, qs, 16);
                    st128(g1, rws, to1, qs, 16);
                    { const LAS char* x = c0; c0 = n0; n0 = x; x = c1; c1 = n1; n1 = x; }
                    if (__builtin_expect(j <= QEARLY || j == jl, 0)) {
                        VMWAIT();
                        BAR0();
                        if (w == 0) __builtin_amdgcn_raw_buffer_store_b32((unsigned)j, rwin, fv, fq_, 16);
                        if (j == jl) break;
                    }
                    MT(3);
                    if (j < 8) SST(8 + j);
                    if (!BSPARSE || j % 6 == 2) { const int s = j; RT(0); RTS(); MTRT(); MTS(); }
                }
                project_end(bb);
                VMWAIT();
                BAR0();
            }
        }
        SST(4);
    };
    auto qk_role = [&](const u32x8 (&wt)[2][10]) INL {
        int hh = bid;
        int qrdy = 0;
        auto steps = [&](auto cc) INL {
        constexpr int C = decltype(cc)::v >> 1, R = decltype(cc)::v & 1;
        LANES();
        const LAS char* ka = (const LAS char*)(smem + L_K + (fr & 1) * TQS + (4 * fq + (fr >> 2)) * 8);
        const LAS char* qa = (const LAS char*)(smem + L_Q + (fr & 1) * 544 + ((fr >> 1) & 1) * 1152 + (16 * fq + 2 * (fr >> 2)) * 4);
        LAS char* qw = (LAS char*)(C < 2 ? smem + L_Q + (fq & 1) * 544 + (fq >> 1) * 1152 + (32 * w + 8 * (fr >> 2) + bs2(fr & 3)) * 4
                                         : smem + L_K + (fq >> 1) * TQS + (32 * (w & 3) + fr) * 8 + (fq & 1) * 4);
        const LAS char* mp0 = (const LAS char*)(memL + ma0);
        const int z = 16 * (fr >> 3) + 4 * (fr & 3) + 2 * ((fr >> 2) & 1);
        const int ko = ((z >> 4) * 256 + fq * 16 + ((z & 15) ^ ((fq & 1) * 5))) * 16;
        unsigned pa0 = (4u * C + 2u * (hh >> 4)) * PAIR + (unsigned)((hh & 15) * 512 + R * 256 + lane * 4);
        unsigned pa2 = ((8u + (fr >> 3)) * PAIR + C * 4096u + (unsigned)((hh >> 1) * 256 + R * 128 + (hh & 1) * 64 + fq * 16 + (fr & 6) * 2)) | (fr & 1) << 30;
        PIN2(ka, qw); PIN1(mp0);
        constexpr bool TK = C == 2 && R == 1;
        Tk tk = {0u, 0u, 0x40000000u, nullptr};
        if (TK) { tk = tk_init(); __builtin_amdgcn_s_setprio(TKP); }
        if (C < 2) { PIN2(qa, pa0); PIN1(pa2); }
        int qf_ = 0;
        int tbo = 0;
        for (int s = 0; s < NSEG; ++s) {
            RT(0);
            const char* tb = tokL + tbo;
            unsigned pb = __builtin_amdgcn_readfirstlane(s + 1 < NSEG ? W_P + (unsigned)(s & 3) * 10u * PAIR + nh : 0x40000000u);
            asm volatile("" : "+s"(pb));
            if (s == 0) BAR();
            u32x8 kx[2][2];
            if (C < 2) {
#pragma unroll
                for (int u = 0; u < 4; ++u) kx[u >> 1][u & 1] = cat8(*(const u32x4*)(tb + (ko ^ ((u >> 1) * 16)) + (u & 1) * 2048), *(const u32x4*)(tb + (ko ^ ((u >> 1) * 16)) + (u & 1) * 2048 + 1024));
                PIN2(kx[0][0], kx[0][1]); PIN2(kx[1][0], kx[1][1]);
            }
            bool redo = false;
            unsigned sf = 0;
            do {
                ++it;
                f32x4 a0 = z4, a1 = z4;
                PIN2(a0, a1);
                u32x4 m[10];
                if (C < 2) {
                    __builtin_amdgcn_s_setprio(2);
                    BAR();
                    MT(0);
#pragma unroll
                    for (int t = 0; t < NB1; ++t) { m[t] = *(const LAS u32x4*)(mp0 + t * 1024); SB(); }
                    BAR0();
#pragma unroll
                    for (int t = 0; t < 10; ++t) {
                        if (t == QB2) { BAR0(); MT(1); }
                        if (t + NB1 < 10) m[t + NB1] = *(const LAS u32x4*)(mp0 + (t + NB1) * 1024);
                        SB();
                        a0 = smfmac(m[t], wt[0][t], a0, sidx); a1 = smfmac(m[t], wt[1][t], a1, sidx);
                        PIN2(a0, a1);
                    }
                } else {
                    unsigned mx = 0;
                    asm volatile("" : "+{v[200:203]}"(a0), "+{v[204:207]}"(a1), "+{v208}"(mx));
                    unsigned lim = s > 0 ? poll_lim(budget) : ~0u, sr = tro + (unsigned)(s & 3) * 2u * PAIR;
                    asm volatile("" : "+s"(lim), "+s"(sr));
                    if (TK) fetch(tk, s, !redo, lim, DQC, (unsigned)hh * 128u, true);
                    else asm volatile("s_waitcnt lgkmcnt(0)\n\ts_barrier\n\ts_barrier\n\ts_barrier" ::: "memory");
                    asm volatile(
                        "ds_read_b128 v[212:215], %[mp] offset:0\n\t"
                        "ds_read_b128 v[216:219], %[mp] offset:1024\n\t"
                        "ds_read_b128 v[220:223], %[mp] offset:2048\n\t"
                        "ds_read_b128 v[224:227], %[mp] offset:3072\n\t"
                        "ds_read_b128 v[228:231], %[mp] offset:4096\n\t"
                        "ds_read_b128 v[232:235], %[mp] offset:5120\n\t"
                        "ds_read_b128 v[236:239], %[mp] offset:6144\n\t"
                        "ds_read_b128 v[240:243], %[mp] offset:7168\n\t"
                        "ds_read_b128 v[244:247], %[mp] offset:8192\n\t"
                        "ds_read_b128 v[248:251], %[mp] offset:9216\n\t"
                        MTA(4)
                        "s_waitcnt lgkmcnt(9)\n\t"
                        "v_smfmac_f32_16x16x64_bf16 v[200:203], v[212:215], %[w00], %[ix]\n\t"
                        "v_max3_u32 v208, v208, v212, v213\n\t"
                        "v_smfmac_f32_16x16x64_bf16 v[204:207], v[212:215], %[w10], %[ix]\n\t"
                        "s_waitcnt lgkmcnt(8)\n\t"
                        "v_max3_u32 v208, v208, v214, v215\n\t"
                        "v_smfmac_f32_16x16x64_bf16 v[200:203], v[216:219], %[w01], %[ix]\n\t"
                        "v_max3_u32 v208, v208, v216, v217\n\t"
                        "v_smfmac_f32_16x16x64_bf16 v[204:207], v[216:219], %[w11], %[ix]\n\t"
                        "s_waitcnt lgkmcnt(7)\n\t"
                        "v_max3_u32 v208, v208, v218, v219\n\t"
                        "v_smfmac_f32_16x16x64_bf16 v[200:203], v[220:223], %[w02], %[ix]\n\t"
                        "v_max3_u32 v208, v208, v220, v221\n\t"
                        "v_smfmac_f32_16x16x64_bf16 v[204:207], v[220:223], %[w12], %[ix]\n\t"
                        "s_waitcnt lgkmcnt(6)\n\t"
                        "v_max3_u32 v208, v208, v222, v223\n\t"
                        "v_smfmac_f32_16x16x64_bf16 v[200:203], v[224:227], %[w03], %[ix]\n\t"
                        "v_max3_u32 v208, v208, v224, v225\n\t"
                        "v_smfmac_f32_16x16x64_bf16 v[204:207], v[224:227], %[w13], %[ix]\n\t"
                        "s_waitcnt lgkmcnt(0)\n\t"
                        "v_max3_u32 v208, v208, v226, v227\n\t"
                        "v_smfmac_f32_16x16x64_bf16 v[200:203], v[228:231], %[w04], %[ix]\n\t"
                        "v_max3_u32 v208, v208, v228, v229\n\t"
                        "v_smfmac_f32_16x16x64_bf16 v[204:207], v[228:231], %[w14], %[ix]\n\t"
                        "v_max3_u32 v208, v208, v230, v231\n\t"
                        "v_smfmac_f32_16x16x64_bf16 v[200:203], v[232:235], %[w05], %[ix]\n\t"
                        "v_max3_u32 v208, v208, v232, v233\n\t"
                        "v_smfmac_f32_16x16x64_bf16 v[204:207], v[232:235], %[w15], %[ix]\n\t"
                        "v_max3_u32 v208, v208, v234, v235\n\t"
                        "v_smfmac_f32_16x16x64_bf16 v[200:203], v[236:239], %[w06], %[ix]\n\t"
                        "v_max3_u32 v208, v208, v236, v237\n\t"
                        "v_smfmac_f32_16x16x64_bf16 v[204:207], v[236:239], %[w16], %[ix]\n\t"
                        "v_max3_u32 v208, v208, v238, v239\n\t"
                        "v_smfmac_f32_16x16x64_bf16 v[200:203], v[240:243], %[w07], %[ix]\n\t"
                        "v_max3_u32 v208, v208, v240, v241\n\t"
                        "v_smfmac_f32_16x16x64_bf16 v[204:207], v[240:243], %[w17], %[ix]\n\t"
                        "v_max3_u32 v208, v208, v242, v243\n\t"
                        "v_smfmac_f32_16x16x64_bf16 v[200:203], v[244:247], %[w08], %[ix]\n\t"
                        "v_max3_u32 v208, v208, v244, v245\n\t"
                        "v_smfmac_f32_16x16x64_bf16 v[204:207], v[244:247], %[w18], %[ix]\n\t"
                        "v_max3_u32 v208, v208, v246, v247\n\t"
                        "v_smfmac_f32_16x16x64_bf16 v[200:203], v[248:251], %[w09], %[ix]\n\t"
                        "v_max3_u32 v208, v208, v248, v249\n\t"
                        "v_smfmac_f32_16x16x64_bf16 v[204:207], v[248:251], %[w19], %[ix]\n\t"
                        "v_max3_u32 v208, v208, v250, v251\n\t"
                        "buffer_load_dword %[sf], %[sl], %[rw], %[sr] offen sc1\n\t"
                        "v_cmp_lt_u32 vcc, %[lim], v208\n\t"
                        "s_nop 3\n\t"
                        "v_pk_add_f32 v[200:201], v[200:201], v[202:203]\n\t"
                        "v_pk_add_f32 v[204:205], v[204:205], v[206:207]\n\t"
                        "v_cvt_pk_bf16_f32 v200, v200, v201\n\t"
                        "v_cvt_pk_bf16_f32 v204, v204, v205\n\t"
                        "ds_write2_b32 %[qw], v200, v204 offset1:32\n\t"
                        MTA(2)
                        "s_cbranch_vccz 1f\n\t"
                        "v_mov_b32 v208, %[it]\n\t"
                        "v_mov_b32 v209, 0\n\t"
                        "ds_write_b32 v209, v208 offset:%[ls]\n\t"
                        "1:\n\t"
                        "s_waitcnt vmcnt(%[nv]) lgkmcnt(0)\n\t"
                        "s_barrier\n\t"
                        "s_waitcnt vmcnt(0)\n\t"
                        : [sf] "=&v"(sf), "+v"(qf_), "+{v[200:203]}"(a0), "+{v[204:207]}"(a1), "+{v208}"(mx)
                        : [mp] "v"(mp0), [qw] "v"(qw), [ix] "v"(sidx), [lim] "s"(lim), [it] "s"(it), [ls] "n"(L_S), [sl] "v"(tk.sl), [sr] "s"(sr), [rw] "s"(rwin), [nv] "n"(TK ? 2 : 1),
                          [w00] "v"(wt[0][0]), [w01] "v"(wt[0][1]), [w02] "v"(wt[0][2]), [w03] "v"(wt[0][3]), [w04] "v"(wt[0][4]), [w05] "v"(wt[0][5]), [w06] "v"(wt[0][6]), [w07] "v"(wt[0][7]), [w08] "v"(wt[0][8]), [w09] "v"(wt[0][9]), [w10] "v"(wt[1][0]), [w11] "v"(wt[1][1]), [w12] "v"(wt[1][2]), [w13] "v"(wt[1][3]), [w14] "v"(wt[1][4]), [w15] "v"(wt[1][5]), [w16] "v"(wt[1][6]), [w17] "v"(wt[1][7]), [w18] "v"(wt[1][8]), [w19] "v"(wt[1][9])
                        : "v209", "v212", "v213", "v214", "v215", "v216", "v217", "v218", "v219", "v220", "v221", "v222", "v223", "v224", "v225", "v226", "v227", "v228", "v229", "v230", "v231", "v232", "v233", "v234", "v235", "v236", "v237", "v238", "v239", "v240", "v241", "v242", "v243", "v244", "v245", "v246", "v247", "v248", "v249", "v250", "v251", "vcc", "memory");
                }
                if (C < 2) {
                    *(LAS unsigned*)qw = hpack(a0); *(LAS unsigned*)(qw + 16) = hpack(a1);
                    SB();
                    MT(2);
                    unsigned vd;
#define SC0 "v[212:215]"
#define SC1 "v[216:219]"
#define SC2 "v[220:223]"
#define DPP(op, d, ctl) op " " d ", " d ", " d " " ctl " row_mask:0xf bank_mask:0xf\n\t"
#define TAIL(A0, A2, B0, B2, C0, C2) \
                        "v_mov_b64 v[212:213], 0\n\tv_mov_b64 v[214:215], 0\n\tv_mov_b64 v[216:217], 0\n\tv_mov_b64 v[218:219], 0\n\tv_mov_b64 v[220:221], 0\n\tv_mov_b64 v[222:223], 0\n\t" \
                        "s_setprio 3\n\t" \
                        "s_waitcnt lgkmcnt(0)\n\t" \
                        "s_barrier\n\t" MTA(3) \
                        "ds_read_b64_tr_b16 v[224:225], %[qa]\n\t" \
                        "ds_read_b64_tr_b16 v[226:227], %[qa] offset:32\n\t" \
                        "ds_read_b64_tr_b16 v[228:229], %[qa] offset:256\n\t" \
                        "ds_read_b64_tr_b16 v[230:231], %[qa] offset:288\n\t" \
                        "ds_read_b64_tr_b16 v[232:233], %[ka]\n\t" \
                        "ds_read_b64_tr_b16 v[234:235], %[ka] offset:128\n\t" \
                        "ds_read_b64_tr_b16 v[236:237], %[ka] offset:256\n\t" \
                        "ds_read_b64_tr_b16 v[238:239], %[ka] offset:384\n\t" \
                        "ds_read_b64_tr_b16 v[240:241], %[ka] offset:512\n\t" \
                        "ds_read_b64_tr_b16 v[242:243], %[ka] offset:640\n\t" \
                        "s_waitcnt lgkmcnt(8)\n\t" \
                        "v_smfmac_f32_16x16x64_bf16 " SC0 ", v[224:227], %[k00], %[ix]\n\t" \
                        "ds_read_b64_tr_b16 v[244:245], %[ka] offset:768\n\t" \
                        "v_smfmac_f32_16x16x64_bf16 " SC1 ", v[224:227], %[k10], %[ix]\n\t" \
                        "ds_read_b64_tr_b16 v[246:247], %[ka] offset:896\n\t" \
                        "s_waitcnt lgkmcnt(8)\n\t" \
                        "v_smfmac_f32_16x16x64_bf16 " SC0 ", v[228:231], %[k01], %[ix]\n\t" \
                        "ds_read_b32 v248, %[vz] offset:%[ls]\n\t" \
                        "v_smfmac_f32_16x16x64_bf16 " SC1 ", v[228:231], %[k11], %[ix]\n\t" \
                        "s_waitcnt lgkmcnt(5)\n\t" \
                        "v_smfmac_f32_16x16x64_bf16 " SC2 ", v[224:227], v[232:239], %[ix]\n\t" \
                        "s_waitcnt lgkmcnt(1)\n\t" \
                        "v_smfmac_f32_16x16x64_bf16 " SC2 ", v[228:231], v[240:247], %[ix]\n\t" \
                        "s_waitcnt lgkmcnt(0)\n\t" \
                        MTA(4) "v_readfirstlane_b32 %[vd], v248\n\t" \
                        "v_add_f32 v232, " A0 ", " A2 "\n\t" \
                        "s_nop 0\n\t" \
                        "v_add_f32 v233, " B0 ", " B2 "\n\t" \
                        "v_max_f32 v235, v232, v233\n\t" \
                        "s_cmp_eq_u32 %[vd], %[it]\n\t" \
                        "s_cselect_b32 %[p0], 2.0, %[p0]\n\t" \
                        DPP("v_max_f32_dpp", "v235", "row_ror:8") \
                        "v_add_f32 v234, " C0 ", " C2 "\n\t" \
                        "v_max_f32 v235, v235, v234\n\t" \
                        "s_add_u32 %[p1], %[p0], 0x4000\n\t" \
                        "s_nop 0\n\t" DPP("v_max_f32_dpp", "v235", "row_ror:4") \
                        "s_nop 1\n\t" DPP("v_max_f32_dpp", "v235", "row_ror:2") \
                        "s_nop 1\n\t" DPP("v_max_f32_dpp", "v235", "row_ror:1") \
                        "v_mul_f32 v235, 0xbe0293ee, v235\n\t" \
                        "v_fmamk_f32 v232, v232, 0x3e0293ee, v235\n\t" \
                        "v_fmamk_f32 v233, v233, 0x3e0293ee, v235\n\t" \
                        "v_exp_f32 v232, v232\n\t" \
                        "v_exp_f32 v233, v233\n\t" \
                        "v_fmamk_f32 v234, v234, 0x3e0293ee, v235\n\t" \
                        "v_add_f32 v235, v232, v233\n\t" \
                        "v_exp_f32 v234, v234\n\t" \
                        "s_nop 0\n\t" DPP("v_add_f32_dpp", "v235", "row_ror:8") \
                        "v_add_f32 v235, v235, v234\n\t" \
                        "v_mov_b32_dpp v236, v234 quad_perm:[1,1,3,3] row_mask:0xf bank_mask:0xf\n\t" \
                        "s_nop 0\n\t" DPP("v_add_f32_dpp", "v235", "row_ror:4") \
                        "s_nop 1\n\t" DPP("v_add_f32_dpp", "v235", "row_ror:2") \
                        "s_nop 1\n\t" DPP("v_add_f32_dpp", "v235", "row_ror:1") \
                        "v_rcp_f32 v235, v235\n\t" \
                        "s_nop 0\n\t" \
                        "v_mul_f32_e64 v232, v232, v235 clamp\n\t" \
                        "v_mul_f32_e64 v233, v233, v235 clamp\n\t" \
                        "v_cvt_pk_bf16_f32 v232, v232, v233\n\t" \
                        MTA(5) "buffer_store_dword v232, %[pa], %[rw], %[p0] offen sc1\n\t" \
                        "v_mul_f32_e64 v234, v234, v235 clamp\n\t" \
                        "v_mul_f32_e64 v236, v236, v235 clamp\n\t" \
                        "buffer_store_dword v232, %[pa], %[rw], %[p1] offen sc1\n\t" \
                        "v_cvt_pk_bf16_f32 v234, v234, v236\n\t" \
                        "buffer_store_dword v234, %[pm], %[rw], %[p0] offen sc1\n\t" \
                        "s_setprio 0"
#define TAILIO : [vd] "=&s"(vd), [p0] "+s"(ps), [p1] "=&s"(ps1) \
                        : [qa] "v"(qa), [ka] "v"(ka), [vz] "v"(0u), [ls] "n"(L_S), [ix] "v"(sidx), [it] "s"(it), [pa] "v"(pa0), [pm] "v"(pa2), [rw] "s"(rwin), \
                          [k00] "v"(kx[0][0]), [k01] "v"(kx[0][1]), [k10] "v"(kx[1][0]), [k11] "v"(kx[1][1]) \
                        : "v212", "v213", "v214", "v215", "v216", "v217", "v218", "v219", "v220", "v221", "v222", "v223", "v224", "v225", "v226", "v227", "v228", "v229", "v230", "v231", "v232", \
                          "v233", "v234", "v235", "v236", "v237", "v238", "v239", "v240", "v241", "v242", "v243", "v244", "v245", "v246", "v247", "v248", "scc", "memory"
                    unsigned ps = pb, ps1;
                    if (R == 0) asm volatile(TAIL("v212", "v214", "v216", "v218", "v220", "v222") TAILIO);
                    else asm volatile(TAIL("v213", "v215", "v217", "v219", "v221", "v223") TAILIO);
                    redo = vd == it;
                    RT(4);
                    MT(6);
                } else {
                    MT(3);
                    if (TK) { __builtin_amdgcn_s_setprio(TKP); if (TKS) __builtin_amdgcn_s_sleep(TKS); }
                    redo = __builtin_amdgcn_readfirstlane(*(volatile LAS unsigned*)(smem + L_S)) == it;
                    if (redo) FC(fc0);
                }
            } while (__builtin_expect(redo, 0));
            if (s < 8) SST(8 + s);
            if (s >= 56) SST(s - 40);
            if (C + R == 1) {
                if (s + layer > 0) {
                    __builtin_amdgcn_s_sleep(DDMA);
                    unsigned l2 = fresh_lane(), s1;
                    asm volatile("v_mov_b32 %0, -1" : "=v"(s1));
                    const unsigned base = W_P + (unsigned)((s - 1) & 3) * 10u * PAIR + nh;
                    asm volatile("s_waitcnt vmcnt(3)" ::: "memory");
                    st128(((u32x4){s1, s1, s1, s1}), rwin, (l2 >> 5) * PAIR + (l2 & 31u) * 16u, base + (unsigned)(R * 4 + 2 * (hh >> 4)) * PAIR + (unsigned)(hh & 15) * 512u, 16);
                    if (R) __builtin_amdgcn_raw_buffer_store_b64((u32x2){s1, s1}, rwin, (l2 >> 5) * PAIR + (l2 & 16u) * 256u + (l2 & 8u) * 16u + (l2 & 7u) * 8u, base + 8u * PAIR + (unsigned)((hh >> 1) * 256 + (hh & 1) * 64), 16);
                    else __builtin_amdgcn_raw_buffer_store_b32(s1, rwin, two(), W_T + (unsigned)((s + 2) & 3) * 2u * PAIR + (unsigned)hh * 128u, 16);
                }
            } else if (TK) {
                if (s + 1 < NSEG) sched2(tk, s, sf);
            } else if (C == 2 && w == 4) {
                if (s + 2 < NSEG) {
                    qrdy = max(qrdy, seg_done(MXQ, __builtin_amdgcn_readlane(qf_, 0), __builtin_amdgcn_readlane(qf_, 1)));
                    if (__builtin_expect(qrdy < s + 2, 0)) for (unsigned spins = 0;;) {
                        asm volatile("" ::: "memory");
                        const int f = (int)__builtin_amdgcn_raw_buffer_load_b32(rwin, (unsigned)(lane & 1) * 64u, W_FQ + fo + (unsigned)hh * 128u, 16);
                        qrdy = seg_done(MXQ, __builtin_amdgcn_readlane(f, 0), __builtin_amdgcn_readlane(f, 1));
                        if (qrdy >= s + 2) break;
                        FC(fc2);
                        if (spin_fail(spins, rwin)) break;
                        __builtin_amdgcn_s_sleep(2);
                    }
                    const unsigned so = O_QK + (unsigned)(((s + 2) * H_ + hh) * 16384), lo = (unsigned)(L_TOK + (tbo ? tbo - 16384 : 32768)), l2 = fresh_lane();
#pragma unroll
                    for (int i = 0; i < 4; ++i) dma4k(rws, l2 * 16u, so + i * 4096u, lo + i * 4096u);
                    if (qrdy < min(s + 5, NSEG - 1))
                        asm volatile("buffer_load_dword %0, %1, %2, %3 offen sc1" : "+v"(qf_) : "v"((l2 & 1u) * 64u), "s"(rwin), "s"(W_FQ + fo + (unsigned)hh * 128u) : "memory");
                }
            } else {
                asm volatile("s_waitcnt vmcnt(3)" ::: "memory");
                if (s > TAILS) __builtin_amdgcn_raw_buffer_store_b32((unsigned)s - 1u, rwin, 0u, W_FPT + fo + (unsigned)(hh * 128 + (w & 3) * 4), 16);
                if (C == 2 && s + 1 < NSEG) __builtin_amdgcn_s_sleep(DPT);
                const float scale = 0.08838834764831845f, l2e = 1.44269504088896340736f * (NORND ? scale : 1.f);
                const int tq = (w >> 1) & 1, qh = w & 1;
                f32x4 sc[3] = {z4, z4, z4};
                u32x4 kt[2][4], qf[4], kf[4];
                const unsigned l2 = fresh_lane(), kto = ((l2 & 8u) * 512u + (l2 & 51u) * 16u + (l2 & 4u) * 32u) ^ ((l2 & 16u) * 5u), qto = ((l2 & 55u) * 16u + qh * 128u) ^ ((l2 & 16u) * 8u);
#pragma unroll
                for (int ks = 0; ks < 4; ++ks) {
                    qf[ks] = *(const u32x4*)(tb + 8192 + (tq * 4 + ks) * 1024 + qto);
                    kt[0][ks] = *(const u32x4*)(tb + ks * 1024 + kto);
                    kt[1][ks] = *(const u32x4*)(tb + ks * 1024 + (kto ^ 64u));
                    SB();
                }
#pragma unroll
                for (int ks = 0; ks < 4; ++ks) {
                    sc[0] = mfma32(kt[0][ks], qf[ks], sc[0]); PIN1(sc[0]);
                    kf[ks] = ldtr2(ka + ks * 256, ka + ks * 256 + 128);
                    SB();
                    sc[1] = mfma32(kt[1][ks], qf[ks], sc[1]); PIN1(sc[1]);
                }
#pragma unroll
                for (int ks = 0; ks < 4; ++ks) sc[2] = mfma32(kf[ks], qf[ks], sc[2]);
                const bool hi = (l2 & 8u) != 0u;
                float v[6];
#pragma unroll
                for (int r = 0; r < 4; ++r) v[r] = hi ? sc[1][r] : sc[0][r];
                v[4] = hi ? sc[2][2] : sc[2][0]; v[5] = hi ? sc[2][3] : sc[2][1];
#pragma unroll
                for (int r = 0; r < 6; r += 2) {
                    if (NORND) break;
                    const unsigned b0 = pack2(v[r], v[r + 1]);
                    const unsigned b1 = pack2(lo2f(b0) * scale, hi2f(b0) * scale);
                    v[r] = lo2f(b1); v[r + 1] = hi2f(b1);
                }
                const int lim = 8 * (w & 3) + (int)(l2 & 7u) - 8 * (int)(l2 >> 4) - (int)((l2 & 8u) >> 1);
#pragma unroll
                for (int r = 0; r < 4; ++r) if (r > lim) v[r] = NORND ? -1000000000.0f / scale : bfr(-1000000000.0f);
                float mx = fmx(fmx(fmx(fmx(fmx(v[0], v[1]), v[2]), v[3]), v[4]), v[5]);
                mx = xmax3216(fmx(mx, ror8f(mx)));
                const float mb = -mx * l2e;
#pragma unroll
                for (int r = 0; r < 6; ++r) v[r] = __builtin_amdgcn_exp2f(fmaf(v[r], l2e, mb));
                float sum = fmaf(v[4] + v[5], 0.5f, (v[0] + v[1]) + (v[2] + v[3]));
                sum = xsum3216(sum + ror8f(sum));
                const float inv = __builtin_amdgcn_rcpf(sum);
                const unsigned p0 = pack2(v[0] * inv, v[1] * inv), p1 = nosent1(pack2(v[2] * inv, v[3] * inv)), p2 = nosent1(pack2(v[4] * inv, v[5] * inv));
                const unsigned base = s + 1 < NSEG ? O_PT + (unsigned)(s * PTSEG) * 2u : W_PL + fo;
                const __amdgpu_buffer_rsrc_t rp = s + 1 < NSEG ? rws : rwin;
                const unsigned oob = (l2 & 8u) << 27;
                st128(((u32x4){p0, p1, dppu<0x128>(p0), dppu<0x128>(p1)}), rp, (l2 * 16u) | oob, base + (unsigned)((hh * 2 + tq) * 1024 + qh * 128), 16);
                __builtin_amdgcn_raw_buffer_store_b64((u32x2){p2, dppu<0x128>(p2)}, rp, ((l2 & 7u) * 16u + (l2 >> 4) * 8u) | oob | (l2 & 32u) << 25, base + 65536u + (unsigned)((tq * 32 + hh) * 256 + qh * 128), 16);
            }
            tbo = tbo == 32768 ? 0 : tbo + 16384;
            if (C == 2 && __builtin_expect(budget <= 0, 0)) set_tmo(rwin);
            RT(5);
            RTS();
            MT(7);
            MTS();
        }
        };
        for (; layer < 2; ++layer) {
            new_layer();
            qrdy = 0;
            BAR();
            hh = bid;
            const int tid = mytid();
            gather_mem0(memL, tid);
            if (tid < 4) *(unsigned*)(smem + L_S + tid * 4) = 0u;
            if (layer == 0) near_init();
            if (w == 7) clk_init(ck);
            qt_role(wt, hh, IC<0>());
            if (w == 0) steps(IC<0>()); else if (w == 1) steps(IC<1>()); else if (w == 2) steps(IC<2>()); else if (w == 3) steps(IC<3>()); else if (w < 7) steps(IC<4>()); else steps(IC<5>());
            SST(31);
        }
    };
    unsigned zbo = W_ZM;
    auto own_role = [&]() INL {
        const unsigned long long tstart = __builtin_amdgcn_s_memtime();
        if (layer == 0) near_init();
        LANES();
        const int o = bid - 96, hg = w & 3, ot = tile_of(o);
        const unsigned ztoff = (unsigned)((o * H_ + 8 * hg) * 64 + lane) * 16u;
        const unsigned zoff = zpair(o / 10, o % 10) + (unsigned)(lane * 16);
        const unsigned poff = (4u * (mc >> 1) + 2u * (hg >> 1) + (mc & 1u)) * PAIR + nh + (unsigned)((hg & 1) * 4096 + (fq >> 1) * 512 + (fr >> 2) * 128 + (fr & 1) * 64 + (fq & 1) * 32 + (fr & 2) * 8);
        const unsigned pmoff = (8u + (mc & 1u)) * PAIR + nh + (mc >> 1) * 4096u + (unsigned)(fq * 256 + (fr >> 3) * 128 + (fr & 2) * 32 + (fr & 4) * 8 + (fr & 1) * 16);
        const unsigned fzo = (unsigned)(8 * hg + (lane & 7)) * 128u + (unsigned)(o / 10) * 4u + (lane & 8) * 8u;
        const unsigned moff = (ot < 32 ? (unsigned)ot * 256u : 4u * PAIR + (unsigned)(ot - 32) * 256u) + nh + (unsigned)(lane * 4);
        const unsigned mc2 = ot < 32 ? 2u * PAIR : 2048u;
        char* op = smem + 8192;
        SST(2);
        if (w == 7) clk_init(ck);
        wait_until(tstart + DO0);
        if (w < 4) {
            u32x8 zf[4];
            int zr = -1;
            unsigned zb = zbo, zso = O_Z;
            const unsigned ra = (ot < 32 ? (unsigned)fq * PAIR + (unsigned)ot * 256u : (4u + (fq & 1)) * PAIR + (unsigned)(fq >> 1) * 2048u + (unsigned)(ot - 32) * 256u) + nh + (unsigned)(fr * 16);
            auto zt_miss = [&](int f, int s) { return __any(f < s && ((lane >> 3) & 1) == (int)((MXV >> s) & 1)); };
            for (int s = 0;; ++s) {

                const unsigned pb = W_P + (unsigned)(s & 3) * 10u * PAIR;
                if (__builtin_expect(zr < s, 0)) for (unsigned spins = 0;;) {
                    asm volatile("" ::: "memory");
                    if (!zt_miss((int)__builtin_amdgcn_raw_buffer_load_b32(rwin, fzo, W_FZ + fo, 16), s)) break;
                    FC(fc2);
                    if (spin_fail(spins, rwin)) break;
                    __builtin_amdgcn_s_sleep(ZPS);
                }
                if (s == 0) SST(1);
                asm volatile(
                    "buffer_load_dwordx4 v[160:163], %[zt], %[rs], %[so] offen sc1\n\t"
                    "buffer_load_dwordx4 v[164:167], %[zt], %[rs], %[so] offen offset:1024 sc1\n\t"
                    "buffer_load_dwordx4 v[168:171], %[zt], %[rs], %[so] offen offset:2048 sc1\n\t"
                    "buffer_load_dwordx4 v[172:175], %[zt], %[rs], %[so] offen offset:3072 sc1\n\t"
                    "buffer_load_dwordx4 v[176:179], %[zt], %[rs], %[s4] offen sc1\n\t"
                    "buffer_load_dwordx4 v[180:183], %[zt], %[rs], %[s4] offen offset:1024 sc1\n\t"
                    "buffer_load_dwordx4 v[184:187], %[zt], %[rs], %[s4] offen offset:2048 sc1\n\t"
                    "buffer_load_dwordx4 v[188:191], %[zt], %[rs], %[s4] offen offset:3072 sc1"
                    : "=&{v[160:167]}"(zf[0]), "=&{v[168:175]}"(zf[1]), "=&{v[176:183]}"(zf[2]), "=&{v[184:191]}"(zf[3])
                    : [zt] "v"(ztoff), [rs] "s"(rws), [so] "s"(zso), [s4] "s"(zso + 4096u) : "memory");
                zso = O_Z + (unsigned)((s + 1) * ZTSEG) * 2u;
                if (__builtin_expect(budget <= 0, 0)) set_tmo(rwin);
                RT(6);
                if (s + 1 == NSEG) break;
                const unsigned spa = (unsigned)((s & 1) * 2048 + (w < 3 ? w * 512 : 0) + lane * 8);
#define OST(r)
                unsigned long long t0_, t1_, t2_, t3_, t4_ = 0, bad;
                int tries = budget;
                unsigned po = poff, pm = pmoff, x0 = 4u * PAIR, x1 = 4096u;
                const unsigned mb = W_MEM + (unsigned)((s + 1) & 3) * 6u * PAIR;
                if (w < 3) asm volatile(
                    "s_barrier\n\t"
                    OST("%[t0]")
                    "v_mov_b64 v[96:97], 0\n\t"
                    "v_mov_b64 v[98:99], 0\n\t"
                    "v_mov_b32 v100, 0\n\t"
                    "s_barrier\n\t"
                    "1:\n\t"
                    "buffer_load_dwordx4 v[208:211], %[po], %[rw], %[pb] offen sc1\n\t"
                    "buffer_load_dwordx4 v[212:215], %[po], %[rw], %[pb] offen offset:1024 sc1\n\t"
                    "buffer_load_dwordx4 v[216:219], %[po], %[rw], %[pb] offen offset:2048 sc1\n\t"
                    "buffer_load_dwordx4 v[220:223], %[po], %[rw], %[pb] offen offset:3072 sc1\n\t"
                    OST("%[t1]")
                    "s_waitcnt vmcnt(3)\n\t"
                    OST("%[t3]")
                    "v_smfmac_f32_16x16x64_bf16 v[96:99], v[208:211], v[160:167], %[ix]\n\t"
                    "v_max3_u32 v100, v100, v208, v209\n\t"
                    "v_max3_u32 v100, v100, v210, v211\n\t"
                    "s_waitcnt vmcnt(2)\n\t"
                    "v_smfmac_f32_16x16x64_bf16 v[96:99], v[212:215], v[168:175], %[ix]\n\t"
                    "v_max3_u32 v100, v100, v212, v213\n\t"
                    "v_max3_u32 v100, v100, v214, v215\n\t"
                    "s_waitcnt vmcnt(1)\n\t"
                    "v_smfmac_f32_16x16x64_bf16 v[96:99], v[216:219], v[176:183], %[ix]\n\t"
                    "v_max3_u32 v100, v100, v216, v217\n\t"
                    "v_max3_u32 v100, v100, v218, v219\n\t"
                    "s_waitcnt vmcnt(0)\n\t"
                    "v_smfmac_f32_16x16x64_bf16 v[96:99], v[220:223], v[184:191], %[ix]\n\t"
                    "v_max3_u32 v100, v100, v220, v221\n\t"
                    "v_max3_u32 v100, v100, v222, v223\n\t"
                    "v_cmp_eq_u32 vcc, -1, v100\n\t"
                    OST("%[t2]")
                    "s_nop 4\n\t"
                    "v_pk_add_f32 v[96:97], v[96:97], v[98:99]\n\t"
                    "ds_write_b64 %[sp], v[96:97]\n\t"
                    "s_cbranch_vccz 2f\n\t"
                    "s_sub_i32 %[tr], %[tr], 1\n\t"
                    "s_cmp_lt_i32 %[tr], 1\n\t"
                    "s_cbranch_scc1 2f\n\t"
                    "v_xor_b32 %[po], %[x0], %[po]\n\t"
                    "s_xor_b32 %[x0], %[x0], 0x14000\n\t"
                    "s_waitcnt lgkmcnt(0)\n\t"
                    "v_mov_b64 v[96:97], 0\n\t"
                    "v_mov_b64 v[98:99], 0\n\t"
                    "v_mov_b32 v100, 0\n\t"
                    "s_branch 1b\n\t"
                    "2:\n\t"
                    "s_waitcnt lgkmcnt(0)\n\t"
                    "s_barrier\n\t"
                    "s_sleep 3\n\t"
                    : [tr] "+s"(tries), [po] "+v"(po), [x0] "+s"(x0), [t0] "=&s"(t0_), [t1] "=&s"(t1_), [t2] "=&s"(t2_), [t3] "=&s"(t3_)
                    : [sp] "v"(spa), [ix] "v"(sidx), [rw] "s"(rwin), [pb] "s"(pb), "{v[160:167]}"(zf[0]), "{v[168:175]}"(zf[1]), "{v[176:183]}"(zf[2]), "{v[184:191]}"(zf[3])
                    : "v96", "v97", "v98", "v99", "v100", "v208", "v209", "v210", "v211", "v212", "v213", "v214", "v215", "v216", "v217", "v218", "v219", "v220", "v221", "v222", "v223", "vcc", "scc", "memory");
                else asm volatile(
                    "s_barrier\n\t"
                    "buffer_load_dwordx4 v[128:131], %[zo], %[rw], %[zb] offen sc1\n\t"
                    "buffer_load_dwordx4 v[132:135], %[zo], %[rw], %[zb] offen offset:1024 sc1\n\t"
                    "buffer_load_dwordx4 v[136:139], %[zo], %[rw], %[zb] offen offset:2048 sc1\n\t"
                    "buffer_load_dwordx4 v[140:143], %[zo], %[rw], %[zb] offen offset:3072 sc1\n\t"
                    "buffer_load_dwordx4 v[144:147], %[zo], %[rw], %[z4] offen sc1\n\t"
                    "buffer_load_dwordx4 v[148:151], %[zo], %[rw], %[z4] offen offset:1024 sc1\n\t"
                    "buffer_load_dwordx4 v[152:155], %[zo], %[rw], %[z4] offen offset:2048 sc1\n\t"
                    "buffer_load_dwordx4 v[156:159], %[zo], %[rw], %[z4] offen offset:3072 sc1\n\t"
                    OST("%[t0]")
                    "v_mov_b64 v[96:97], 0\n\t"
                    "v_mov_b64 v[98:99], 0\n\t"
                    "v_mov_b32 v100, 0\n\t"
                    "s_barrier\n\t"
                    "s_sleep %[fd]\n\t"
                    "buffer_load_dwordx4 v[208:211], %[po], %[rw], %[pb] offen sc1\n\t"
                    "buffer_load_dwordx4 v[212:215], %[po], %[rw], %[pb] offen offset:1024 sc1\n\t"
                    "buffer_load_dwordx4 v[216:219], %[po], %[rw], %[pb] offen offset:2048 sc1\n\t"
                    "buffer_load_dwordx4 v[220:223], %[po], %[rw], %[pb] offen offset:3072 sc1\n\t"
                    "buffer_load_dwordx4 v[224:227], %[pm], %[rw], %[pb] offen sc1\n\t"
                    "buffer_load_dwordx4 v[228:231], %[pm], %[rw], %[pb] offen offset:1024 sc1\n\t"
                    "buffer_load_dwordx4 v[232:235], %[pm], %[rw], %[pb] offen offset:2048 sc1\n\t"
                    "buffer_load_dwordx4 v[236:239], %[pm], %[rw], %[pb] offen offset:3072 sc1\n\t"
                    OST("%[t1]")
                    "s_waitcnt vmcnt(8)\n\t"
                    "v_max3_u32 v101, v129, v131, v133\n\t"
                    "v_max3_u32 v101, v101, v135, v137\n\t"
                    "v_max3_u32 v101, v101, v139, v141\n\t"
                    "v_max3_u32 v101, v101, v143, v145\n\t"
                    "v_max3_u32 v101, v101, v147, v149\n\t"
                    "v_max3_u32 v101, v101, v151, v153\n\t"
                    "v_max3_u32 v101, v101, v155, v157\n\t"
                    "v_max_u32 v101, v101, v159\n\t"
                    "v_cmp_eq_u32 vcc, -1, v101\n\t"
                    "s_cbranch_vccz 3f\n\t"
                    "buffer_load_dwordx4 v[128:131], %[zo], %[rw], %[zb] offen sc1\n\t"
                    "buffer_load_dwordx4 v[132:135], %[zo], %[rw], %[zb] offen offset:1024 sc1\n\t"
                    "buffer_load_dwordx4 v[136:139], %[zo], %[rw], %[zb] offen offset:2048 sc1\n\t"
                    "buffer_load_dwordx4 v[140:143], %[zo], %[rw], %[zb] offen offset:3072 sc1\n\t"
                    "buffer_load_dwordx4 v[144:147], %[zo], %[rw], %[z4] offen sc1\n\t"
                    "buffer_load_dwordx4 v[148:151], %[zo], %[rw], %[z4] offen offset:1024 sc1\n\t"
                    "buffer_load_dwordx4 v[152:155], %[zo], %[rw], %[z4] offen offset:2048 sc1\n\t"
                    "buffer_load_dwordx4 v[156:159], %[zo], %[rw], %[z4] offen offset:3072 sc1\n\t"
                    "s_waitcnt vmcnt(0)\n\t"
                    "v_max3_u32 v101, v129, v131, v133\n\t"
                    "v_max3_u32 v101, v101, v135, v137\n\t"
                    "v_max3_u32 v101, v101, v139, v141\n\t"
                    "v_max3_u32 v101, v101, v143, v145\n\t"
                    "v_max3_u32 v101, v101, v147, v149\n\t"
                    "v_max3_u32 v101, v101, v151, v153\n\t"
                    "v_max3_u32 v101, v101, v155, v157\n\t"
                    "v_max_u32 v101, v101, v159\n\t"
                    "v_mov_b32 v100, v101\n\t"
                    "3:\n\t"
                    "v_permlane16_swap_b32 v128, v129\n\t"
                    "v_permlane16_swap_b32 v130, v131\n\t"
                    "v_permlane16_swap_b32 v132, v133\n\t"
                    "v_permlane16_swap_b32 v134, v135\n\t"
                    "v_permlane16_swap_b32 v136, v137\n\t"
                    "v_permlane16_swap_b32 v138, v139\n\t"
                    "v_permlane16_swap_b32 v140, v141\n\t"
                    "v_permlane16_swap_b32 v142, v143\n\t"
                    "v_permlane16_swap_b32 v144, v145\n\t"
                    "v_permlane16_swap_b32 v146, v147\n\t"
                    "v_permlane16_swap_b32 v148, v149\n\t"
                    "v_permlane16_swap_b32 v150, v151\n\t"
                    "v_permlane16_swap_b32 v152, v153\n\t"
                    "v_permlane16_swap_b32 v154, v155\n\t"
                    "v_permlane16_swap_b32 v156, v157\n\t"
                    "v_permlane16_swap_b32 v158, v159\n\t"
                    "s_mov_b32 vcc_lo, 0x00ff00ff\n\t"
                    "s_mov_b32 vcc_hi, 0x00ff00ff\n\t"
                    "s_waitcnt vmcnt(7)\n\t"
                    OST("%[t3]")
                    "v_smfmac_f32_16x16x64_bf16 v[96:99], v[208:211], v[160:167], %[ix]\n\t"
                    "v_max3_u32 v100, v100, v208, v209\n\t"
                    "v_max3_u32 v100, v100, v210, v211\n\t"
                    "s_waitcnt vmcnt(6)\n\t"
                    "v_smfmac_f32_16x16x64_bf16 v[96:99], v[212:215], v[168:175], %[ix]\n\t"
                    "v_max3_u32 v100, v100, v212, v213\n\t"
                    "v_max3_u32 v100, v100, v214, v215\n\t"
                    "s_waitcnt vmcnt(5)\n\t"
                    "v_smfmac_f32_16x16x64_bf16 v[96:99], v[216:219], v[176:183], %[ix]\n\t"
                    "v_max3_u32 v100, v100, v216, v217\n\t"
                    "v_max3_u32 v100, v100, v218, v219\n\t"
                    "s_waitcnt vmcnt(4)\n\t"
                    "v_smfmac_f32_16x16x64_bf16 v[96:99], v[220:223], v[184:191], %[ix]\n\t"
                    "v_max3_u32 v100, v100, v220, v221\n\t"
                    "v_max3_u32 v100, v100, v222, v223\n\t"
                    "s_waitcnt vmcnt(3)\n\t"
                    "v_smfmac_f32_16x16x64_bf16 v[96:99], v[224:227], v[128:135], %[ix]\n\t"
                    "v_max3_u32 v100, v100, v224, v225\n\t"
                    "v_max3_u32 v100, v100, v226, v227\n\t"
                    "s_waitcnt vmcnt(2)\n\t"
                    "v_smfmac_f32_16x16x64_bf16 v[96:99], v[228:231], v[136:143], %[ix]\n\t"
                    "s_barrier\n\t"
                    "ds_read_b64 v[102:103], %[sp]\n\t"
                    "ds_read_b64 v[104:105], %[sp] offset:512\n\t"
                    "ds_read_b64 v[106:107], %[sp] offset:1024\n\t"
                    "v_max3_u32 v100, v100, v228, v229\n\t"
                    "v_max3_u32 v100, v100, v230, v231\n\t"
                    "s_waitcnt vmcnt(1)\n\t"
                    "v_smfmac_f32_16x16x64_bf16 v[96:99], v[232:235], v[144:151], %[ix]\n\t"
                    "v_max3_u32 v100, v100, v232, v233\n\t"
                    "v_max3_u32 v100, v100, v234, v235\n\t"
                    "s_waitcnt vmcnt(0)\n\t"
                    "v_smfmac_f32_16x16x64_bf16 v[96:99], v[236:239], v[152:159], %[ix]\n\t"
                    "v_max3_u32 v100, v100, v236, v237\n\t"
                    "v_max3_u32 v100, v100, v238, v239\n\t"
                    OST("%[t2]")
                    "v_cmp_eq_u32 %[bad], -1, v100\n\t"
                    "s_waitcnt lgkmcnt(0)\n\t"
                    "v_add_f32 v102, v102, v104\n\t"
                    "v_add_f32 v103, v103, v105\n\t"
                    "v_add_f32 v102, v102, v106\n\t"
                    "v_add_f32 v103, v103, v107\n\t"
                    "v_add_f32 v96, v96, v102\n\t"
                    "v_add_f32 v97, v97, v103\n\t"
                    "v_add_f32 v96, v96, v98\n\t"
                    "v_add_f32 v97, v97, v99\n\t"
                    "s_cmp_lg_u64 %[bad], 0\n\t"
                    "s_cbranch_scc1 4f\n\t"
                    "2:\n\t"
                    "v_cndmask_b32_dpp v98, v97, v96, vcc row_ror:8 row_mask:0xf bank_mask:0xf\n\t"
                    "s_not_b64 vcc, vcc\n\t"
                    "v_cndmask_b32_dpp v99, v96, v97, vcc row_ror:8 row_mask:0xf bank_mask:0xf\n\t"
                    "v_cvt_pk_bf16_f32 v98, v98, v99\n\t"
                    "v_min_u32 v98, -2, v98\n\t"
                    "buffer_store_dword v98, %[mo], %[rw], %[m0] offen sc1\n\t"
                    "buffer_store_dword v98, %[mo], %[rw], %[m1] offen sc1\n\t"
                    "buffer_store_dword v98, %[mo], %[rw], %[m2] offen sc1\n\t"
                    "buffer_store_dword v98, %[mo], %[rw], %[m3] offen sc1\n\t"
                    OST("%[t4]")
                    "s_branch 5f\n\t"
                    "4:\n\t"
                    "s_sub_i32 %[tr], %[tr], 1\n\t"
                    "s_cmp_lt_i32 %[tr], 1\n\t"
                    "s_cbranch_scc1 2b\n\t"
                    "v_xor_b32 %[po], %[x0], %[po]\n\t"
                    "s_xor_b32 %[x0], %[x0], 0x14000\n\t"
                    "v_xor_b32 %[pm], %[x1], %[pm]\n\t"
                    "s_xor_b32 %[x1], %[x1], 0x5000\n\t"
                    "buffer_load_dwordx4 v[128:131], %[zo], %[rw], %[zb] offen sc1\n\t"
                    "buffer_load_dwordx4 v[132:135], %[zo], %[rw], %[zb] offen offset:1024 sc1\n\t"
                    "buffer_load_dwordx4 v[136:139], %[zo], %[rw], %[zb] offen offset:2048 sc1\n\t"
                    "buffer_load_dwordx4 v[140:143], %[zo], %[rw], %[zb] offen offset:3072 sc1\n\t"
                    "buffer_load_dwordx4 v[144:147], %[zo], %[rw], %[z4] offen sc1\n\t"
                    "buffer_load_dwordx4 v[148:151], %[zo], %[rw], %[z4] offen offset:1024 sc1\n\t"
                    "buffer_load_dwordx4 v[152:155], %[zo], %[rw], %[z4] offen offset:2048 sc1\n\t"
                    "buffer_load_dwordx4 v[156:159], %[zo], %[rw], %[z4] offen offset:3072 sc1\n\t"
                    "buffer_load_dwordx4 v[208:211], %[po], %[rw], %[pb] offen sc1\n\t"
                    "buffer_load_dwordx4 v[212:215], %[po], %[rw], %[pb] offen offset:1024 sc1\n\t"
                    "buffer_load_dwordx4 v[216:219], %[po], %[rw], %[pb] offen offset:2048 sc1\n\t"
                    "buffer_load_dwordx4 v[220:223], %[po], %[rw], %[pb] offen offset:3072 sc1\n\t"
                    "buffer_load_dwordx4 v[224:227], %[pm], %[rw], %[pb] offen sc1\n\t"
                    "buffer_load_dwordx4 v[228:231], %[pm], %[rw], %[pb] offen offset:1024 sc1\n\t"
                    "buffer_load_dwordx4 v[232:235], %[pm], %[rw], %[pb] offen offset:2048 sc1\n\t"
                    "buffer_load_dwordx4 v[236:239], %[pm], %[rw], %[pb] offen offset:3072 sc1\n\t"
                    "v_mov_b64 v[96:97], 0\n\t"
                    "v_mov_b64 v[98:99], 0\n\t"
                    "v_mov_b32 v100, 0\n\t"
                    "s_waitcnt vmcnt(0)\n\t"
                    "v_max3_u32 v101, v129, v131, v133\n\t"
                    "v_max3_u32 v101, v101, v135, v137\n\t"
                    "v_max3_u32 v101, v101, v139, v141\n\t"
                    "v_max3_u32 v101, v101, v143, v145\n\t"
                    "v_max3_u32 v101, v101, v147, v149\n\t"
                    "v_max3_u32 v101, v101, v151, v153\n\t"
                    "v_max3_u32 v101, v101, v155, v157\n\t"
                    "v_max_u32 v101, v101, v159\n\t"
                    "v_mov_b32 v100, v101\n\t"
                    "v_permlane16_swap_b32 v128, v129\n\t"
                    "v_permlane16_swap_b32 v130, v131\n\t"
                    "v_permlane16_swap_b32 v132, v133\n\t"
                    "v_permlane16_swap_b32 v134, v135\n\t"
                    "v_permlane16_swap_b32 v136, v137\n\t"
                    "v_permlane16_swap_b32 v138, v139\n\t"
                    "v_permlane16_swap_b32 v140, v141\n\t"
                    "v_permlane16_swap_b32 v142, v143\n\t"
                    "v_permlane16_swap_b32 v144, v145\n\t"
                    "v_permlane16_swap_b32 v146, v147\n\t"
                    "v_permlane16_swap_b32 v148, v149\n\t"
                    "v_permlane16_swap_b32 v150, v151\n\t"
                    "v_permlane16_swap_b32 v152, v153\n\t"
                    "v_permlane16_swap_b32 v154, v155\n\t"
                    "v_permlane16_swap_b32 v156, v157\n\t"
                    "v_permlane16_swap_b32 v158, v159\n\t"
                    "s_nop 1\n\t"
                    "v_smfmac_f32_16x16x64_bf16 v[96:99], v[208:211], v[160:167], %[ix]\n\t"
                    "v_max3_u32 v100, v100, v208, v209\n\t"
                    "v_max3_u32 v100, v100, v210, v211\n\t"
                    "v_smfmac_f32_16x16x64_bf16 v[96:99], v[212:215], v[168:175], %[ix]\n\t"
                    "v_max3_u32 v100, v100, v212, v213\n\t"
                    "v_max3_u32 v100, v100, v214, v215\n\t"
                    "v_smfmac_f32_16x16x64_bf16 v[96:99], v[216:219], v[176:183], %[ix]\n\t"
                    "v_max3_u32 v100, v100, v216, v217\n\t"
                    "v_max3_u32 v100, v100, v218, v219\n\t"
                    "v_smfmac_f32_16x16x64_bf16 v[96:99], v[220:223], v[184:191], %[ix]\n\t"
                    "v_max3_u32 v100, v100, v220, v221\n\t"
                    "v_max3_u32 v100, v100, v222, v223\n\t"
                    "v_smfmac_f32_16x16x64_bf16 v[96:99], v[224:227], v[128:135], %[ix]\n\t"
                    "v_max3_u32 v100, v100, v224, v225\n\t"
                    "v_max3_u32 v100, v100, v226, v227\n\t"
                    "v_smfmac_f32_16x16x64_bf16 v[96:99], v[228:231], v[136:143], %[ix]\n\t"
                    "v_max3_u32 v100, v100, v228, v229\n\t"
                    "v_max3_u32 v100, v100, v230, v231\n\t"
                    "v_smfmac_f32_16x16x64_bf16 v[96:99], v[232:235], v[144:151], %[ix]\n\t"
                    "v_max3_u32 v100, v100, v232, v233\n\t"
                    "v_max3_u32 v100, v100, v234, v235\n\t"
                    "v_smfmac_f32_16x16x64_bf16 v[96:99], v[236:239], v[152:159], %[ix]\n\t"
                    "v_max3_u32 v100, v100, v236, v237\n\t"
                    "v_max3_u32 v100, v100, v238, v239\n\t"
                    "v_cmp_eq_u32 %[bad], -1, v100\n\t"
                    "s_nop 5\n\t"
                    "v_add_f32 v96, v96, v102\n\t"
                    "v_add_f32 v97, v97, v103\n\t"
                    "v_add_f32 v96, v96, v98\n\t"
                    "v_add_f32 v97, v97, v99\n\t"
                    "s_cmp_lg_u64 %[bad], 0\n\t"
                    "s_cbranch_scc1 4b\n\t"
                    "s_branch 2b\n\t"
                    "5:\n\t"
                    : [tr] "+s"(tries), [po] "+v"(po), [pm] "+v"(pm), [x0] "+s"(x0), [x1] "+s"(x1), [bad] "=&s"(bad), [t0] "=&s"(t0_), [t1] "=&s"(t1_), [t2] "=&s"(t2_), [t3] "=&s"(t3_), [t4] "=&s"(t4_)
                    : [zo] "v"(zoff), [sp] "v"(spa), [ix] "v"(sidx), [rw] "s"(rwin), [zb] "s"(zb), [z4] "s"(zb + 4096u), [pb] "s"(pb), [mo] "v"(moff), [fd] "n"(FDL),
                      [m0] "s"(mb), [m1] "s"(mb + PAIR), [m2] "s"(mb + mc2), [m3] "s"(mb + mc2 + PAIR), "{v[160:167]}"(zf[0]), "{v[168:175]}"(zf[1]), "{v[176:183]}"(zf[2]), "{v[184:191]}"(zf[3])
                    : "v96", "v97", "v98", "v99", "v100", "v101", "v102", "v103", "v104", "v105", "v106", "v107", "v128", "v129", "v130", "v131", "v132", "v133", "v134", "v135", "v136", "v137", "v138", "v139", "v140", "v141", "v142", "v143", "v144", "v145", "v146", "v147", "v148", "v149", "v150", "v151", "v152", "v153", "v154", "v155", "v156", "v157", "v158", "v159", "v208", "v209", "v210", "v211", "v212", "v213", "v214", "v215", "v216", "v217", "v218", "v219", "v220", "v221", "v222", "v223", "v224", "v225", "v226", "v227", "v228", "v229", "v230", "v231", "v232", "v233", "v234", "v235", "v236", "v237", "v238", "v239", "vcc", "scc", "memory");
                budget = tries;
                if (w == 3) *(volatile LAS int*)(smem + 4100) = s;
                __builtin_amdgcn_s_sleep(DPS - 4);
                if (w == 3) {
                    st128(SENT, rwin, ra, W_MEM + (unsigned)(s & 3) * 6u * PAIR, 16);
                    if (lane == 0 && s > 1) __builtin_amdgcn_raw_buffer_store_b32((unsigned)s - 2u, rwin, 0u, W_GO + fo + (unsigned)o * 128u, 16);
                }
                for (;;) {
                    const u32x2 x = *(volatile LAS u32x2*)(smem + 4096);
                    zr = __builtin_amdgcn_readfirstlane(x[0]);
                    if (__builtin_amdgcn_readfirstlane(x[1]) == s) break;
                    __builtin_amdgcn_s_sleep(2);
                }
                zb = zb == W_ZM + 2u * NOWN * PAIR ? W_ZM : zb + NOWN * PAIR;
            }
            asm volatile("s_waitcnt vmcnt(0)" : "+{v[160:167]}"(zf[0]), "+{v[168:175]}"(zf[1]), "+{v[176:183]}"(zf[2]), "+{v[184:191]}"(zf[3]) :: "memory");
            BAR0();
            BAR0();
            f32x4 c[2];
            for (unsigned spins = 0;;) {
                asm volatile("" ::: "memory");
                u32x4 pt[16], ptm[2][2], zm[2];
#pragma unroll
                for (int q = 0; q < 2; ++q) zm[q] = __builtin_amdgcn_raw_buffer_load_b128(rwin, zoff + hg * 2048u + q * 1024u, zb, 16);
#pragma unroll
                for (int q = 0; q < 16; ++q) pt[q] = __builtin_amdgcn_raw_buffer_load_b128(rwin, (unsigned)(16 * hg) * 1024u + lane * 16u, W_PL + fo + q * 1024u, 16);
#pragma unroll
                for (int q = 0; q < 4; ++q) ptm[q >> 1][q & 1] = __builtin_amdgcn_raw_buffer_load_b128(rwin, (unsigned)(hg * 2048 + lane * 16), W_PL + fo + 65536u + (q >> 1) * 8192u + (q & 1) * 1024u, 16);
                unsigned m = max(smax(zm[0]), smax(zm[1]));
#pragma unroll
                for (int t = 0; t < 2; ++t) {
                    c[t] = z4;
#pragma unroll
                    for (int q = 0; q < 8; ++q) { c[t] = mfma32(half8(zf[q >> 1], q & 1), pt[2 * q + t], c[t]); m = max(m, smax(pt[2 * q + t])); }
#pragma unroll
                    for (int q = 0; q < 2; ++q) { c[t] = mfma32((u32x4){zm[q][0], zm[q][2], zm[q][1], zm[q][3]}, ptm[t][q], c[t]); m = max(m, smax(ptm[t][q])); }
                }
                if (!__any(m == 0xFFFFFFFFu)) break;
                if (spin_fail(spins, rwin)) break;
            }
#pragma unroll
            for (int t = 0; t < 2; ++t) *(f32x4*)(op + hg * 2048 + (16 * t + fr) * 64 + fq * 16) = c[t];
            BAR();
#pragma unroll
            for (int q = 0; q < 2; ++q) st128(SENT, rwin, zoff + hg * 2048u + q * 1024u, zb, 16);
            if (w == 3) st128(SENT, rwin, ra, W_MEM + 3u * 6u * PAIR, 16);
        } else if (w == 4) {
            const unsigned fl = (unsigned)(lane & 31) * 128u + (unsigned)(o / 10) * 4u + (lane & 32) * 2u;
            int zk = -1;
            if (lane < 2) *(volatile LAS int*)(smem + 4096 + lane * 4) = -1;
            for (int s = 0; s < NSEG; ++s) {
                asm volatile("s_waitcnt lgkmcnt(0)\n\ts_barrier\n\ts_barrier" ::: "memory");
                int f = 0;
                const bool look = zk < min(s + ZLA, NSEG - 1);
                if (look) { __builtin_amdgcn_s_sleep(6); f = (int)__builtin_amdgcn_raw_buffer_load_b32(rwin, fl, W_FZ + fo, 16); }
                SB();
                BAR0();
                if (look) {
                    f = min(f, __builtin_amdgcn_update_dpp(f, f, 0xB1, 0xF, 0xF, false)); f = min(f, __builtin_amdgcn_update_dpp(f, f, 0x4E, 0xF, 0xF, false));
                    f = min(f, __builtin_amdgcn_update_dpp(f, f, 0x141, 0xF, 0xF, false)); f = min(f, __builtin_amdgcn_update_dpp(f, f, 0x140, 0xF, 0xF, false));
                    zk = seg_done(MXV, min(__builtin_amdgcn_readlane(f, 0), __builtin_amdgcn_readlane(f, 16)), min(__builtin_amdgcn_readlane(f, 32), __builtin_amdgcn_readlane(f, 48)));
                    *(volatile LAS int*)(smem + 4096) = zk;
                }
            }
        } else if (w == 5) {
            for (int s = 0; s < 3 * NSEG; ++s) BAR0();
        } else if (w == 6) {
            const unsigned ooff = (unsigned)((S_ - SEG + (lane >> 1)) * D_ + ot * 16 + (lane & 1) * 8) * 2u;
            for (int s = 0; s < 3 * NSEG - 2; ++s) BAR0();
            const u32x4 xr = __builtin_amdgcn_raw_buffer_load_b128(rws, ooff, O_X1, 16);
            asm volatile("s_barrier\n\ts_barrier ; 6" ::: "memory");
            const char* src = op + lane * 32;
            f32x4 x = *(const f32x4*)src, y = *(const f32x4*)(src + 16);
#pragma unroll
            for (int ww = 1; ww < 4; ++ww) { x += *(const f32x4*)(src + ww * 2048); y += *(const f32x4*)(src + ww * 2048 + 16); }
            const u32x4 r = {pack2(lo2f(xr[0]) + bfr(x[0]), hi2f(xr[0]) + bfr(x[1])), pack2(lo2f(xr[1]) + bfr(x[2]), hi2f(xr[1]) + bfr(x[3])),
                             pack2(lo2f(xr[2]) + bfr(y[0]), hi2f(xr[2]) + bfr(y[1])), pack2(lo2f(xr[3]) + bfr(y[2]), hi2f(xr[3]) + bfr(y[3]))};
            st128(r, rout(), ooff, 0u, 16);
            if (layer == 0) {
                VMWAIT();
                __builtin_amdgcn_raw_buffer_store_b32(0u, rwin, LANE0, W_OUT + (unsigned)(NSEG - 1) * 256u + (unsigned)o * 4u, 16);
            }
        } else {
            Tk tk = tk_init();
            __builtin_amdgcn_s_setprio(3);
            for (int s = 0; s < NSEG; ++s) {
                const unsigned ta0 = ck.ta, mm0 = ck.ma;
                unsigned tp = tpp + lt + xs + M1;
                if (s < SAD) { sched(tk, s); tp = tpp - M2 - XS0 - PEN1; }
                wait_fine_bar(tp - DZP, ck);
                wait_cyc_bar(cyc_at(ck, sx24(tp + (s + 1 < NSEG ? 0 : ML) - ck.ta)));
                clk_rate(ck, ta0, mm0);
                asm volatile("s_barrier ; 7" ::: "memory");
                if (s >= SAD && s + 1 < NSEG) sched(tk, s);
                RTS();
            }
        }
        __builtin_amdgcn_s_setprio(0);
        zbo = W_ZM + NOWN * PAIR;
        SST(23);
    };
    static_assert(NSEG % 3 == 1, "ring");
    auto out_role = [&](int ph) INL {
        LANES();
        {
            const int o = bid - (ph ? NQT : NREC), cg = o >> 1, sbeg = (ph ? TAILS : 0) + (o & 1), send = ph ? NSEG - 1 : TAILS;
            const unsigned ztoff = (unsigned)((2 * cg * H_ + 4 * w) * 64 + lane) * 16u;
            const unsigned ptoff = (unsigned)(8 * w * 64 + lane) * 16u, ptmoff = 65536u + (unsigned)(4 * w * 16 + lane) * 16u;
            const unsigned ooff = (unsigned)(((w & 1) * 16 + (lane >> 2)) * D_ + tile_of(2 * cg) * 16 + (lane & 3) * 8) * 2u;
            const unsigned tfoff = fo + (lane < 16 ? W_FPT + (unsigned)((4 * w + (lane & 3)) * 128 + (lane >> 2) * 4) : W_FZ2 + (unsigned)((4 * w + (lane & 3)) * 128 + (cg / 5) * 4));
            int rdy = -1;
            if (ph) {
                if (w == 0) for (unsigned spins = 0;;) {
                    asm volatile("" ::: "memory");
                    if ((int)__builtin_amdgcn_readfirstlane(__builtin_amdgcn_raw_buffer_load_b32(rwin, 0u, W_GO + fo + (unsigned)o * 128u, 16)) >= TAILS - 3) break;
                    if (spin_fail(spins, rwin)) break;
                    __builtin_amdgcn_s_sleep(OSLP);
                }
                __syncthreads();
                for (unsigned spins = 0;;) {
                    asm volatile("" ::: "memory");
                    if (!__any((int)__builtin_amdgcn_raw_buffer_load_b32(rwin, (unsigned)(4 * w + (lane & 3)) * 128u + (unsigned)(cg / 5) * 4u, W_FZ + fo, 16) < NSEG - 2)) break;
                    if (spin_fail(spins, rwin)) break;
                    __builtin_amdgcn_s_sleep(8);
                }
            }
            auto wait_rdy = [&](int s) INL {
                for (unsigned spins = 0; rdy < s;) {
                    asm volatile("" ::: "memory");
                    if (!ph) {
                        rdy = (int)__builtin_amdgcn_readfirstlane(__builtin_amdgcn_raw_buffer_load_b32(rwin, 0u, W_GO + fo + (unsigned)o * 128u, 16));
                    } else {
                        const int f = (int)__builtin_amdgcn_raw_buffer_load_b32(rwin, tfoff, 0u, 16);
                        if (!__any(f < s)) rdy = s;
                    }
                    if (rdy >= s) break;
                    if (spin_fail(spins, rwin)) break;
                    if (ph) __builtin_amdgcn_s_sleep(OSLP2); else __builtin_amdgcn_s_sleep(OSLP);
                }
            };
            u32x4 xr, xn, zf[2][4], zx[2], pt[8], ptm[2];
            auto fetch1 = [&](int s) INL {
                if (w >= 6) xn = __builtin_amdgcn_raw_buffer_load_b128(rws, ooff, O_X1 + (unsigned)(s * SEG * D_ * 2), 16);
#pragma unroll
                for (int q = 0; q < 8; ++q) zf[q >> 2][q & 3] = __builtin_amdgcn_raw_buffer_load_b128(rws, ztoff + (q & 3) * 1024u, O_Z + (unsigned)(s * ZTSEG) * 2u + (q >> 2) * (H_ * 1024u), 16);
            };
            auto fetch2 = [&](int s) INL {
                const unsigned ptb = O_PT + (unsigned)(s * PTSEG) * 2u;
#pragma unroll
                for (int q = 0; q < 8; ++q) pt[q] = __builtin_amdgcn_raw_buffer_load_b128(rws, ptoff + q * 1024u, ptb, 16);
#pragma unroll
                for (int q = 0; q < 2; ++q) {
                    zx[q] = __builtin_amdgcn_raw_buffer_load_b128(rws, tid * 16u, O_Z2 + (unsigned)(s * NOWN + 2 * cg + q) * 8192u, 16);
                    ptm[q] = __builtin_amdgcn_raw_buffer_load_b128(rws, ptmoff + q * 8192u, ptb, 16);
                }
            };
            if (!ph) wait_rdy(0);
            fetch1(sbeg);
            wait_rdy(sbeg);
            fetch2(sbeg);
            unsigned pend = 0x40000000u;
            for (int s = sbeg; s < send; s += 2) {
                RT(0);
                char* op = smem + ((s >> 1) & 1) * 32768;
#pragma unroll
                for (int u = 0; u < 4; ++u) {
                    const int tq = u >> 1, t = u & 1;
                    f32x4 c = z4;
#pragma unroll
                    for (int q = 0; q < 4; ++q) c = mfma32(zf[t][q], pt[2 * q + tq], c);
                    c = mfma32((u32x4){zx[t][0], zx[t][2], zx[t][1], zx[t][3]}, ptm[tq], c);
                    *(f32x4*)(op + w * 4096 + (16 * tq + fr) * 128 + (((t * 4 + fq) ^ (fr & 7)) << 4)) = c;
                }
                if (w >= 6) {
                    VMWAIT();
                    __builtin_amdgcn_raw_buffer_store_b32(0u, rwin, LANE0, pend, 16);
                    xr = xn;
                }
                const bool more = s + 2 < send, early = rdy >= s + 2;
                if (more) { fetch1(s + 2); if (early) fetch2(s + 2); }
                RT(1);
                BAR();
                if (w >= 6) {
                    const int tk = (w & 1) * 16 + (lane >> 2);
                    const char* s0 = op + tk * 128 + (((2 * (lane & 3)) ^ (tk & 7)) << 4);
                    const char* s1 = op + tk * 128 + (((2 * (lane & 3) + 1) ^ (tk & 7)) << 4);
                    f32x4 x = *(const f32x4*)s0, y = *(const f32x4*)s1;
#pragma unroll
                    for (int ww = 1; ww < 8; ++ww) { x += *(const f32x4*)(s0 + ww * 4096); y += *(const f32x4*)(s1 + ww * 4096); }
                    const u32x4 r = {pack2(lo2f(xr[0]) + bfr(x[0]), hi2f(xr[0]) + bfr(x[1])), pack2(lo2f(xr[1]) + bfr(x[2]), hi2f(xr[1]) + bfr(x[3])),
                                     pack2(lo2f(xr[2]) + bfr(y[0]), hi2f(xr[2]) + bfr(y[1])), pack2(lo2f(xr[3]) + bfr(y[2]), hi2f(xr[3]) + bfr(y[3]))};
                    if (layer) st128(r, rout(), ooff, (unsigned)(s * SEG * D_ * 2), 0);
                    else {
                        st128(r, rout(), ooff, (unsigned)(s * SEG * D_ * 2), 16);
                        pend = W_OUT + (unsigned)(s * 64 + 2 * cg + (w & 1)) * 4u;
                        if (!(more && early)) {
                            VMWAIT();
                            __builtin_amdgcn_raw_buffer_store_b32(0u, rwin, LANE0, pend, 16);
                            pend = 0x40000000u;
                        }
                    }
                }
                if (more && !early) { wait_rdy(s + 2); fetch2(s + 2); }
                RT(2);
                RTS();
                if (s < 8) SST(8 + s);
                if (s >= 56) SST(s - 40);
            }
        }
    };
    auto a1_units = [&](int u0, int u1) INL {
        const int tid = mytid();
        RowTw tw;
        row_tw(tw, tid);
        for (int u = u0; u < u1; u += 40) {
            if (w == 4) {
                for (unsigned spins = 0;;) {
                    asm volatile("" ::: "memory");
                    const unsigned f = __builtin_amdgcn_raw_buffer_load_b32(rwin, min(fresh_lane(), (unsigned)NOWN - 1u) * 4u, W_OUT + (unsigned)(u >> 2) * 256u, 16);
                    if (!__any(f != 0u)) break;
                    if (spin_fail(spins, rwin)) break;
                    if (u < 216) __builtin_amdgcn_s_sleep(WSLP); else __builtin_amdgcn_s_sleep(4);
                }
            }
            BAR();
            SST(24);
            a1_unit<4>(mkrsrc(ws + OFF_X2, S_ * D_ * 2), u * 8, rws, tw, tid, fs);
            VMWAIT();
            FST(8);
            BAR0();
            FST(9);
            if (w == 0) __builtin_amdgcn_raw_buffer_store_b64(__builtin_bit_cast(u32x2, tag + 9ull), rwin, LANE0, W_A1 + (unsigned)u * 128u, 16);
            SST(25);
            FSW(7);
        }
    };
    if (blockIdx.x < 64) {
        u32x8 wt[2][10];
        for (;;) {
            load_qv(wt, bid & 31, bid >= 32);
            const unsigned x = get_rot();
            if (__builtin_expect(x == vx, 1)) break;
            set_role(x);
        }
        SST(1);
        if (w == 0) wait_tags(rwin, 1, 2, 64u, tag + 8ull, true);
        BAR();
        if (bid < 32) qk_role(wt);
        else for (; layer < 2; ++layer) { new_layer(); vt_role(wt, bid & 31, ~MXV); SST(31); }
    } else if (bid < 64) vo_role();
    else for (; layer < 2; ++layer) {
        new_layer();
        BAR();
        SST(26);
        a2_unit(bid - 96, rws, mytid(), [&](int k) INL {
            if (layer ? k == 0 : k == 2) return;
            if (k == 2 && bid >= NQT + NOWN) a1_units(bid >= 240 ? bid - 16 : bid + 24, 256);
            if (w == 0) {
                if (layer == 0) wait_tags(rwin, 1 + k, 2 + 2 * k, 64u, tag + 8ull, true);
                else if (k == 1) wait_tags(rwin, 0, 4, 32u, tag + 9ull, false);
                else wait_tags(rwin, 3, 4, 64u, tag + 9ull, false);
            }
            BAR0();
            SST(27 + k - layer);
        }, fs);
        SST(29);
        VMWAIT();
        FST(8);
        BAR0();
        FST(9);
        if (w == 0) {
            __builtin_amdgcn_raw_buffer_store_b32(0u, rwin, LANE0, W_COL + fo + (unsigned)(bid - 96) * 128u, 16);
            if (bid == 255 && layer == 1) __builtin_amdgcn_raw_buffer_store_b64(__builtin_bit_cast(u32x2, tag), rws, LANE0, 0u, 16);
        }
        SST(30);
        FSW(1 + layer);
        if (bid < NQT || bid >= NWRK) {
            const int fi = bid >= NWRK ? 255 - bid : NQT + 15 - bid, tid = mytid();
            RowTw tw;
            row_tw(tw, tid);
            for (int u = fi; u < 256; u += 56) {
                a3_unit(mkrsrc(layer ? (const u16*)(ws + OFF_X2) : arg(A_X), S_ * D_ * 2), u * 8, rws, tw, tid, [&]() INL {
                    if (u != fi) return;
                    if (w == 7) {
                        const unsigned l = fresh_lane();
                        unsigned o[3] = {l * 128u, l * 128u + 8192u, l < 32u ? l * 128u + 16384u : 0x40000000u};
                        for (unsigned spins = 0;;) {
                            asm volatile("" ::: "memory");
                            unsigned v[3];
#pragma unroll
                            for (int g = 0; g < 3; ++g) v[g] = __builtin_amdgcn_raw_buffer_load_b32(rwin, o[g], W_COL + fo, 16);
#pragma unroll
                            for (int g = 0; g < 3; ++g) if (v[g] == 0u) o[g] = 0x40000000u;
                            if (!__any((v[0] | v[1] | v[2]) != 0u) || spin_fail(spins, rwin)) break;
                            __builtin_amdgcn_s_sleep(CSLP);
                        }
                    }
                    BAR0();
                    SST(6);
                }, fs);
                VMWAIT();
                FST(8);
                BAR0();
                FST(9);
                if (w == 0) __builtin_amdgcn_raw_buffer_store_b32(0u, rwin, LANE0, W_H + fo + (unsigned)((u >> 2) * 128 + (u & 3) * 4), 16);
                if (u < 96) SST(7);
                FSW(u < 96 ? 3 + layer : 5 + layer);
                if (fi >= 56) break;
                if (u < 96) u += 40;
            }
            if (bid < NREC) own_role();
            else if (bid < NQT) out_role(0);
        } else {
            u32x8 wt[2][10];
            if (xt) {
                load_qv(wt, bid - NXT, true);
                SST(1);
                vt_role(wt, bid - NXT, MXV);
            } else if (DQT) wait_until(__builtin_amdgcn_s_memtime() + DQT);
            load_qv(wt, (bid - NQT) & 31, false);
            if (!xt) SST(1);
            qt_role(wt, (bid - NQT) & 31, IC<1>());
            if (bid < NQT + NOWN) out_role(1);
        }
        if (layer == 0 && bid >= NQT + NOWN) a1_units(bid - (NQT + NOWN), 224);
        SST(31);
    }
}

constexpr int L_DBG = 0;
extern "C" size_t dbg_offset(int i) {
    const size_t o[] = {OFF_WIN, W_TMO, W_NEAR, OFF_F, OFF_X1, OFF_X2, OFF_H, OFF_WFT};
    return o[i];
}

extern "C" void kernel_launch(void* const* d_in, const int* in_sizes, int n_in, void* d_out, int out_size, void* d_ws, size_t ws_size,
                              hipStream_t stream) {
    char* ws = (char*)d_ws;
    hipFuncSetAttribute((const void*)mega_kernel, hipFuncAttributeMaxDynamicSharedMemorySize, L_END + L_DBG);
    char* win = (char*)(((uintptr_t)(ws + OFF_WIN) + 0x1FFFFFu) & ~(uintptr_t)0x1FFFFFu);
    Args a{ws, win, (const u16*)d_in[0], (const u16*)d_in[1], (const u16*)d_in[2], (const u16*)d_in[4], (const u16*)d_in[5], (const u16*)d_in[6], (const u16*)d_in[7], (const u16*)d_in[8], (u16*)d_out};
    mega_kernel<<<256, 512, L_END + L_DBG, stream>>>(a);
}
```

```cpp
#include <hip/hip_runtime.h>
#include <hip/hip_bf16.h>
#include <stdint.h>

typedef unsigned short u16;
typedef unsigned long long u64;
typedef __attribute__((ext_vector_type(8))) __bf16 bf16x8;
typedef __attribute__((ext_vector_type(4))) short s16x4;
typedef __attribute__((ext_vector_type(4))) float f32x4;
typedef __attribute__((ext_vector_type(8))) float f32x8;
typedef __attribute__((ext_vector_type(16))) float f32x16;
typedef __attribute__((ext_vector_type(4))) unsigned u32x4;
typedef __attribute__((ext_vector_type(2))) unsigned u32x2;
typedef __attribute__((ext_vector_type(8))) unsigned u32x8;
typedef __attribute__((ext_vector_type(16))) __bf16 bf16x16;
typedef __attribute__((ext_vector_type(2))) unsigned short u16x2;
#define LAS __attribute__((address_space(3)))
#define INL __attribute__((always_inline))

constexpr int S_ = 2048, D_ = 640, DF = 321, H_ = 32, DH = 128, HD = 4096, NM = 8, SEG = 32, NSEG = 64;
constexpr int NOWN = 40;
constexpr int NCOL = 160;
constexpr size_t ZTSEG = (size_t)NOWN * H_ * 512;
constexpr unsigned PAIR = 16384, HALF = 8192;
constexpr unsigned W_MEM = 0, W_T = W_MEM + 24 * PAIR, W_P = 33 * PAIR, W_PROBE = 74 * PAIR, W_GO = W_PROBE + 2 * PAIR, W_FZ = W_GO + 5120, W_FPT = W_FZ + 8192, W_FZ2 = W_FPT + 4096,
                   W_FQ = W_FZ2 + 4096, W_H = W_FQ + 8192, W_COL = W_H + HALF, W_PL = W_COL + 20480, W_LAYER = W_PL + 2 * 81920 - W_GO, W_OUT = 32 * PAIR, W_MISC = W_OUT + 14336,
                   W_ARM = W_GO + 2 * W_LAYER, W_A1 = 106 * PAIR, W_ZM = 2u * 1024 * 1024, W_END = W_ZM + 120 * PAIR;
static_assert(W_ARM <= W_A1 && W_ARM % 1024 == 0, "window");
__device__ __forceinline__ unsigned p_slot(int s) { return W_P + (unsigned)((s & 3) * 10 + (((s & 3) + 1) >> 2)) * PAIR; }
constexpr unsigned W_TMO = W_MISC, W_NEAR = W_MISC + 128;
constexpr int PTSEG = 32768 + 8192;

#define FPAD 256
constexpr unsigned FST_ = S_ * 16 + FPAD, GST = 4096 + FPAD, WST = S_ * 8 + FPAD;
constexpr size_t al(size_t x) { return (x + 255) & ~(size_t)255; }
constexpr size_t OFF_CTL  = 0;
constexpr size_t OFF_WFT  = 4096 + 4 * 1024 * 1024;
constexpr size_t OFF_F    = OFF_WFT + al((size_t)(NCOL + 2) * WST);
constexpr size_t OFF_X1   = OFF_F + al((size_t)NCOL * FST_);
constexpr size_t OFF_X2   = OFF_X1 + al((size_t)S_ * D_ * 2);
constexpr size_t OFF_FL   = OFF_X2 + al((size_t)S_ * D_ * 2);
constexpr int NHF = 5;
constexpr size_t OFF_H    = OFF_FL + 3 * (size_t)NCOL * GST + NHF * 40960;
constexpr size_t OFF_Z    = OFF_H + al((size_t)S_ * D_ * 2);
constexpr size_t OFF_Z2   = OFF_Z + al(ZTSEG * NSEG * 2);
constexpr size_t OFF_PT   = OFF_Z2 + al((size_t)NSEG * NOWN * 8192);
constexpr size_t OFF_QK   = OFF_PT + al((size_t)NSEG * PTSEG * 2);
constexpr size_t OFF_WIN  = OFF_QK + (size_t)NSEG * H_ * 16384;
constexpr size_t OFF_END  = OFF_WIN + (size_t)6 * 1024 * 1024;
static_assert(OFF_END <= (size_t)256 * 1024 * 1024, "workspace");
constexpr unsigned O_FL = (unsigned)OFF_FL, O_ARM = O_FL - 256 * D_ * 2, O_WFT = (unsigned)OFF_WFT, O_F = (unsigned)OFF_F, O_H = (unsigned)OFF_H, O_X1 = (unsigned)OFF_X1, O_Z = (unsigned)OFF_Z, O_Z2 = (unsigned)OFF_Z2, O_PT = (unsigned)OFF_PT, O_QK = (unsigned)OFF_QK;

constexpr double PI_ = 3.14159265358979323846;
constexpr double tay(double x, int s) {
    double t = s ? x : 1.0, r = t;
    for (int i = s + 1; i < 18; i += 2) { t *= -x * x / (double)(i * (i + 1)); r += t; }
    return r;
}
struct Cs { float c, s; };
constexpr Cs cis(int m, int n) {
    m %= n;
    const int q = (m * 8 / n + 1) / 2;
    const double x = 2.0 * PI_ * (double)(4 * m - q * n) / (double)(4 * n), c = tay(x, 0), s = tay(x, 1);
    return (q & 3) == 0 ? Cs{(float)c, (float)-s} : (q & 3) == 1 ? Cs{(float)-s, (float)-c} : (q & 3) == 2 ? Cs{(float)-c, (float)s} : Cs{(float)s, (float)c};
}
template <int NI> struct alignas(16) Tw7 { Cs v[4][NI][2]; };
template <int NI, int N, int KIND> constexpr Tw7<NI> mk_tw() {
    Tw7<NI> r{};
    for (int i = 0; i < NI; ++i)
        for (int q = 1; q <= 8; ++q) r.v[(q - 1) >> 1][i][(q - 1) & 1] = cis((KIND == 0 ? i : KIND == 1 ? 8 * (i >> 3) : (i >> 3) + 10 * (i & 7)) * q, N);
    return r;
}
struct TwCol { Tw7<256> a; Tw7<32> b; Tw7<4> c; };
static __device__ const TwCol TWC = {mk_tw<256, 2048, 0>(), mk_tw<32, 256, 0>(), mk_tw<4, 32, 0>()};
static __device__ const Tw7<80> TWR2 = mk_tw<80, 640, 1>();
static __device__ const Tw7<80> TWR3 = mk_tw<80, 640, 2>();

#define st128(v, r, vo, so, aux) do { u32x4 d_ = (v); asm volatile("" : "+v"(d_)); __builtin_amdgcn_raw_buffer_store_b128(d_, r, vo, so, aux); asm volatile("s_nop 1" :: "v"(d_)); } while (0)
__device__ __forceinline__ float bf2f(u16 v) { return __uint_as_float((unsigned)v << 16); }
__device__ __forceinline__ u16 f2bf(float f) { __bf16 b = (__bf16)f; return __builtin_bit_cast(u16, b); }
__device__ __forceinline__ float bfr(float f) { return (float)(__bf16)f; }
typedef __attribute__((ext_vector_type(2))) float f32x2;
typedef __attribute__((ext_vector_type(2))) __bf16 bf16x2;
__device__ __forceinline__ unsigned pack2(float a, float b) { return __builtin_bit_cast(unsigned, __builtin_convertvector((f32x2){a, b}, bf16x2)); }
__device__ __forceinline__ float lo2f(unsigned v) { return __uint_as_float(v << 16); }
__device__ __forceinline__ float hi2f(unsigned v) { return __uint_as_float(v & 0xFFFF0000u); }
__device__ __forceinline__ float fmx(float a, float b) { return __builtin_elementwise_maximum(a, b); }
__device__ __forceinline__ f32x4 mfma32(u32x4 a, u32x4 b, f32x4 c) {
    return __builtin_amdgcn_mfma_f32_16x16x32_bf16(__builtin_bit_cast(bf16x8, a), __builtin_bit_cast(bf16x8, b), c, 0, 0, 0);
}
__device__ __forceinline__ f32x4 mfma16(u32x2 a, u32x2 b, f32x4 c) {
    return __builtin_amdgcn_mfma_f32_16x16x16bf16_1k(__builtin_bit_cast(s16x4, a), __builtin_bit_cast(s16x4, b), c, 0, 0, 0);
}
__device__ __forceinline__ f32x4 smfmac(u32x4 a, u32x8 b, f32x4 c, int idx) {
    return __builtin_amdgcn_smfmac_f32_16x16x64_bf16(__builtin_bit_cast(bf16x8, a), __builtin_bit_cast(bf16x16, b), c, idx, 0, 0);
}
__device__ __forceinline__ int pi16(int k) { return ((k & 3) << 2) | ((k >> 1) & 2) | (k >> 3); }
__device__ __forceinline__ float addswap32(float a, float b) {
    auto r = __builtin_amdgcn_permlane32_swap(__float_as_uint(a), __float_as_uint(b), false, false);
    return __uint_as_float(r[0]) + __uint_as_float(r[1]);
}
__device__ __forceinline__ float xmax3216(float v) {
    auto r = __builtin_amdgcn_permlane32_swap(__float_as_uint(v), __float_as_uint(v), false, false);
    v = fmx(__uint_as_float(r[0]), __uint_as_float(r[1]));
    auto q = __builtin_amdgcn_permlane16_swap(__float_as_uint(v), __float_as_uint(v), false, false);
    return fmx(__uint_as_float(q[0]), __uint_as_float(q[1]));
}
__device__ __forceinline__ float xsum3216(float v) {
    auto r = __builtin_amdgcn_permlane32_swap(__float_as_uint(v), __float_as_uint(v), false, false);
    v = __uint_as_float(r[0]) + __uint_as_float(r[1]);
    auto q = __builtin_amdgcn_permlane16_swap(__float_as_uint(v), __float_as_uint(v), false, false);
    return __uint_as_float(q[0]) + __uint_as_float(q[1]);
}
template <int CTRL> __device__ __forceinline__ float dppf(float v) { return __uint_as_float((unsigned)__builtin_amdgcn_update_dpp(0, (int)__float_as_uint(v), CTRL, 0xF, 0xF, false)); }
__device__ __forceinline__ float wsum(float v) {
    v += dppf<0xB1>(v); v += dppf<0x4E>(v); v += dppf<0x141>(v); v += dppf<0x140>(v);
    return xsum3216(v);
}
__device__ __forceinline__ unsigned ror8(unsigned v) { return (unsigned)__builtin_amdgcn_update_dpp(0, (int)v, 0x128, 0xF, 0xF, false); }
__device__ __forceinline__ float ror8f(float v) { return __uint_as_float(ror8(__float_as_uint(v))); }
__device__ __forceinline__ unsigned nosent(unsigned v) {
    u16x2 lim = {0xFFFE, 0xFFFE};
    return __builtin_bit_cast(unsigned, __builtin_elementwise_min(__builtin_bit_cast(u16x2, v), lim));
}
__device__ __forceinline__ unsigned pkmax(unsigned a, unsigned b) {
    return __builtin_bit_cast(unsigned, __builtin_elementwise_max(__builtin_bit_cast(u16x2, a), __builtin_bit_cast(u16x2, b)));
}
__device__ __forceinline__ bool has_sent(u32x4 v) {
    unsigned m = pkmax(pkmax(v.x, v.y), pkmax(v.z, v.w));
    return (m & 0xFFFFu) == 0xFFFFu || (m >> 16) == 0xFFFFu;
}

__device__ __forceinline__ unsigned nosent1(unsigned v) { return min(v, 0xFFFFFFFEu); }
__device__ __forceinline__ unsigned smax(u32x4 v) { return max(v[1], v[3]); }
__device__ __forceinline__ u32x4 sent4() { unsigned x; asm volatile("v_mov_b32 %0, -1" : "=v"(x)); return (u32x4){x, x, x, x}; }
#define SENT sent4()
__device__ __forceinline__ f32x4 zero4() { f32x2 x, y; asm volatile("v_mov_b64 %0, 0\n\tv_mov_b64 %1, 0" : "=v"(x), "=v"(y)); return (f32x4){x[0], x[1], y[0], y[1]}; }
__device__ __forceinline__ u32x4 nosent4(u32x4 v) { return (u32x4){nosent1(v[0]), nosent1(v[1]), nosent1(v[2]), nosent1(v[3])}; }
#define SPIN_CAP 400000u
__device__ __forceinline__ void set_tmo(__amdgpu_buffer_rsrc_t rwin) { __builtin_amdgcn_raw_buffer_store_b32(0u, rwin, 0u, W_TMO, 16); }
__device__ __forceinline__ bool spin_fail(unsigned& spins, __amdgpu_buffer_rsrc_t rwin) {
    if (++spins > SPIN_CAP) { set_tmo(rwin); return true; }
    return (spins & 1023u) == 0u && __builtin_amdgcn_readfirstlane(__builtin_amdgcn_raw_buffer_load_b32(rwin, 0u, W_TMO, 16)) == 0u;
}
#define PS1 3
#define PS2 3
#define PS3 2

typedef f32x2 c2;
__device__ __forceinline__ c2 swp(c2 a) { return __builtin_shufflevector(a, a, 1, 0); }
__device__ __forceinline__ c2 fm(c2 a, c2 b, c2 c) { return __builtin_elementwise_fma(a, b, c); }
__device__ __forceinline__ c2 bc(float x) { return (c2){x, x}; }
__device__ __forceinline__ c2 sgn() { c2 s = {-1.f, 1.f}; asm("" : "+s"(s)); return s; }
template <int SG> __device__ __forceinline__ c2 addi(c2 a, c2 b) { return fm(swp(b), SG > 0 ? sgn() : swp(sgn()), a); }
template <int SG> __device__ __forceinline__ c2 cfin(c2 a, c2 w, c2 t) {
    c2 d;
    if (SG < 0) asm("v_pk_fma_f32 %0, %1, %2, %3 op_sel:[1,1,0] op_sel_hi:[0,1,1] neg_lo:[1,0,0]" : "=v"(d) : "v"(a), "v"(w), "v"(t));
    else asm("v_pk_fma_f32 %0, %1, %2, %3 op_sel:[1,1,0] op_sel_hi:[0,1,1] neg_hi:[1,0,0]" : "=v"(d) : "v"(a), "v"(w), "v"(t));
    return d;
}
struct Tw { f32x4 v[4]; };
template <int NI> __device__ __forceinline__ void ldtw(Tw& t, const Tw7<NI>& tab, int i) {
#pragma unroll
    for (int j = 0; j < 4; ++j) t.v[j] = *(const f32x4*)tab.v[j][i];
}
template <int NI> __device__ __forceinline__ void ldtw(Tw& t, const LAS f32x4* p) {
#pragma unroll
    for (int j = 0; j < 4; ++j) t.v[j] = p[j * NI];
}
template <int SG> __device__ __forceinline__ void twid(c2 (&u)[8], const Tw& t) {
#pragma unroll
    for (int r = 1; r < 8; ++r) {
        const f32x4 x = t.v[(r - 1) >> 1];
        const c2 w = r & 1 ? (c2){x[0], x[1]} : (c2){x[2], x[3]};
        u[r] = cfin<SG>(u[r], w, u[r] * bc(w.x));
    }
}
template <int SG> __device__ __forceinline__ void dft4(c2& a, c2& b, c2& c, c2& d) {
    const c2 t0 = a + c, t1 = a - c, t2 = b + d, t3 = b - d;
    a = t0 + t2; c = t0 - t2; b = addi<SG>(t1, t3); d = addi<-SG>(t1, t3);
}
template <int SG> __device__ __forceinline__ void dft8(c2 (&u)[8]) {
    c2 e0 = u[0], e1 = u[2], e2 = u[4], e3 = u[6], o0 = u[1], o1 = u[3], o2 = u[5], o3 = u[7];
    dft4<SG>(e0, e1, e2, e3); dft4<SG>(o0, o1, o2, o3);
    const c2 r = bc(0.70710678118654752440f);
    o1 = addi<SG>(o1, o1); o3 = addi<-SG>(o3, o3);
    u[0] = e0 + o0; u[4] = e0 - o0;
    u[1] = fm(o1, r, e1); u[5] = fm(o1, -r, e1);
    u[2] = addi<SG>(e2, o2); u[6] = addi<-SG>(e2, o2);
    u[3] = fm(o3, -r, e3); u[7] = fm(o3, r, e3);
}
template <int SG> __device__ __forceinline__ void dft5(c2& x0, c2& x1, c2& x2, c2& x3, c2& x4) {
    const c2 c1 = bc(0.30901699437494742410f), c2_ = bc(-0.80901699437494742410f), s1 = bc(0.95105651629515357212f), s2 = bc(0.58778525229247312917f);
    const c2 t1 = x1 + x4, t2 = x2 + x3, t3 = x1 - x4, t4 = x2 - x3;
    const c2 a1 = fm(c2_, t2, fm(c1, t1, x0)), a2 = fm(c1, t2, fm(c2_, t1, x0));
    const c2 b1 = fm(s2, t4, s1 * t3), b2 = fm(-s1, t4, s2 * t3);
    x0 = x0 + t1 + t2;
    x1 = addi<SG>(a1, b1); x4 = addi<-SG>(a1, b1); x2 = addi<SG>(a2, b2); x3 = addi<-SG>(a2, b2);
}
template <int SG> __device__ __forceinline__ void dft10(c2 (&u)[10]) {
    c2 e0 = u[0], e1 = u[2], e2 = u[4], e3 = u[6], e4 = u[8], o0 = u[5], o1 = u[7], o2 = u[9], o3 = u[1], o4 = u[3];
    dft5<SG>(e0, e1, e2, e3, e4); dft5<SG>(o0, o1, o2, o3, o4);
    u[0] = e0 + o0; u[5] = e0 - o0; u[6] = e1 + o1; u[1] = e1 - o1; u[2] = e2 + o2; u[7] = e2 - o2; u[8] = e3 + o3; u[3] = e3 - o3; u[4] = e4 + o4; u[9] = e4 - o4;
}
__device__ __forceinline__ void swap32(c2& v) { auto r = __builtin_amdgcn_permlane32_swap(__float_as_uint(v.x), __float_as_uint(v.y), false, false); v = (c2){__uint_as_float(r[0]), __uint_as_float(r[1])}; }
template <int B> __device__ __forceinline__ void swapl(c2& a, c2& b) {
#pragma unroll
    for (int i = 0; i < 2; ++i) {
        auto r = B == 5 ? __builtin_amdgcn_permlane32_swap(__float_as_uint(a[i]), __float_as_uint(b[i]), false, false) : __builtin_amdgcn_permlane16_swap(__float_as_uint(a[i]), __float_as_uint(b[i]), false, false);
        a[i] = __uint_as_float(r[0]); b[i] = __uint_as_float(r[1]);
    }
}
__device__ __forceinline__ c2 unpk(unsigned v) { return (c2){lo2f(v), hi2f(v)}; }
__device__ __forceinline__ float hsum32(c2 s) {
    float v = s.x + s.y;
    v += dppf<0xB1>(v); v += dppf<0x4E>(v); v += dppf<0x141>(v); v += dppf<0x140>(v);
    auto q = __builtin_amdgcn_permlane16_swap(__float_as_uint(v), __float_as_uint(v), false, false);
    return __uint_as_float(q[0]) + __uint_as_float(q[1]);
}
__device__ __forceinline__ void ln_row(const c2 (&v)[10], unsigned (&o)[10]) {
    const c2 s = ((v[0] + v[1]) + (v[2] + v[3])) + ((v[4] + v[5]) + (v[6] + v[7])) + (v[8] + v[9]);
    const float mean = hsum32(s) * (1.f / 640.f);
    c2 q0 = bc(0.f), q1 = bc(0.f);
#pragma unroll
    for (int r = 0; r < 10; r += 2) { const c2 d = v[r] - bc(mean), e = v[r + 1] - bc(mean); q0 = fm(d, d, q0); q1 = fm(e, e, q1); }
    const float mb = bfr(mean);
    c2 d[10];
#pragma unroll
    for (int r = 0; r < 10; ++r) { const c2 x = v[r] - bc(mb); d[r] = unpk(pack2(x.x, x.y)); }
    const float var = hsum32(q0 + q1) * (1.f / 640.f);
    const float rs = __builtin_amdgcn_rcpf(bfr(sqrtf(bfr(bfr(var) + 1e-5f))));
#pragma unroll
    for (int r = 0; r < 10; ++r) { const c2 x = d[r] * bc(rs); o[r] = pack2(x.x, x.y); }
}

__device__ __forceinline__ __amdgpu_buffer_rsrc_t mkrsrc(const void* p, unsigned bytes) {
    return __builtin_amdgcn_make_buffer_rsrc((void*)p, 0, bytes, 0x00020000);
}
__device__ __forceinline__ unsigned xcc_id() { unsigned x; asm volatile("s_getreg_b32 %0, hwreg(HW_REG_XCC_ID)" : "=s"(x)); return x & 15u; }
#define VMWAIT() do { asm volatile("" ::: "memory"); __builtin_amdgcn_s_waitcnt(0x0F70); asm volatile("" ::: "memory"); } while (0)
#define BAR() asm volatile("s_waitcnt lgkmcnt(0)\n\ts_barrier" ::: "memory")
#define BAR0() asm volatile("s_barrier" ::: "memory")

#define FST(k) do {} while (0)
#define FSW(k) do {} while (0)
extern __shared__ __attribute__((aligned(16))) char smem[];
#define RST(k, lay, wv, sel) do {} while (0)
constexpr int R_C = 8 * 10 * 72 * 8;
__device__ __forceinline__ c2 ldv(const LAS void* p) { return *(const volatile LAS c2*)p; }
struct RowTw { Tw a, b; };
__device__ __forceinline__ void row_tw(RowTw& t, int tid) { const int i = tid % 80; ldtw(t.a, TWR2, i); ldtw(t.b, TWR3, i); }
template <int NF, bool LATE> __device__ __forceinline__ void a1_unit(__amdgpu_buffer_rsrc_t rx, int row0, __amdgpu_buffer_rsrc_t rws, __amdgpu_buffer_rsrc_t rwin, const RowTw& tw, int tid, unsigned (&fs)[12]) {
    const int wave = __builtin_amdgcn_readfirstlane(tid >> 6), lane = tid & 63;
    LAS c2* A = (LAS c2*)smem; LAS c2* C = (LAS c2*)(smem + R_C);
    FST(0);
    if (wave < NF) {
        unsigned d[10];
        for (unsigned spins = 0;;) {
            asm volatile("" ::: "memory");
#pragma unroll
            for (int r = 0; r < 10; ++r) d[r] = __builtin_amdgcn_raw_buffer_load_b32(rx, (lane & 31) * 4u + (lane >> 5) * (D_ * 2u) + r * 128u, (unsigned)(row0 + 2 * wave) * (D_ * 2), 16);
            if (!LATE) break;
            unsigned m = d[0];
#pragma unroll
            for (int r = 1; r < 10; ++r) m = max(m, d[r]);
            if (!__any(m == ~0u) || spin_fail(spins, rwin)) break;
            __builtin_amdgcn_s_sleep(PS1);
        }
        c2 u[10];
#pragma unroll
        for (int r = 0; r < 10; ++r) u[r] = unpk(d[r]);
        FST(1);
        if (LATE) RST(16, 0, wave, 0);
        ln_row(u, d);
        FST(2);
#pragma unroll
        for (int r = 0; r < 10; ++r) { u[r] = unpk(d[r]); swap32(u[r]); }
        dft10<-1>(u);
#pragma unroll
        for (int r = 0; r < 10; ++r) A[(wave * 10 + r) * 72 + (lane & 31) + 36 * (lane >> 5)] = u[r];
        FST(3);
    }
    BAR();
    FST(4);
#pragma unroll
    for (int c = 0; c < (NF > 4 ? 2 : 1); ++c) {
        if (tid < (NF > 4 ? (c ? 160 : 480) : 80 * NF)) {
            const int K = (tid >> 3) + 60 * c, m = tid & 7;
            LAS c2* a = A + K * 72;
            c2 u[8];
#pragma unroll
            for (int q = 0; q < 8; ++q) u[q] = ldv(a + (m >> 1) + 36 * (m & 1) + 4 * q);
            twid<-1>(u, tw.a);
            dft8<-1>(u);
#pragma unroll
            for (int p = 0; p < 8; ++p) a[9 * p + m] = u[p];
            asm volatile("" ::: "memory");
#pragma unroll
            for (int q = 0; q < 8; ++q) u[q] = ldv(a + 9 * m + q);
            twid<-1>(u, tw.b);
            dft8<-1>(u);
            LAS c2* o = C + (K / 10) * 638 + K + 10 * m;
#pragma unroll
            for (int p = 0; p < 8; ++p) o[80 * p] = u[p];
        }
    }
    FST(5);
    BAR();
    FST(6);
#pragma unroll
    for (int c = 0; c < (NF > 4 ? 5 : 3); ++c) {
        const int it = tid + 512 * c, kp = it / (2 * NF), row = it % (2 * NF);
        if (NF > 4 || c < 2 || tid < 256) {
            const LAS c2* z = C + (row >> 1) * 648;
            const f32x4 zk = *(const LAS f32x4*)(z + 2 * kp);
            const c2 n0 = ldv(z + (kp ? 640 - 2 * kp : 320)), n1 = ldv(z + 639 - 2 * kp);
            const bool odd = it & 1;
            f32x4 o = odd ? (f32x4){zk[1] + n0.y, n0.x - zk[0], zk[3] + n1.y, n1.x - zk[2]} : (f32x4){zk[0] + n0.x, zk[1] - n0.y, zk[2] + n1.x, zk[3] - n1.y};
            if (c == 0 && kp == 0) { o[0] = 2.f * (odd ? zk[1] : zk[0]); o[1] = 2.f * (odd ? n0.y : n0.x); }
            u32x4 ob = __builtin_bit_cast(u32x4, o);
            if (LATE) { ob[1] = nosent1(ob[1]); ob[3] = nosent1(ob[3]); }
            st128(ob, rws, kp * (LATE ? GST : FST_) + row * 16u, (LATE ? O_FL - (S_ - 256) * 16u : O_F) + (unsigned)row0 * 16u, 16);
        }
    }
    FST(7);
}
template <class FG> __device__ __forceinline__ void a3_unit(__amdgpu_buffer_rsrc_t rx, int row0, int layer, __amdgpu_buffer_rsrc_t rws, __amdgpu_buffer_rsrc_t rwin, const RowTw& tw, int tid, FG go, unsigned (&fs)[12]) {
    const int wave = __builtin_amdgcn_readfirstlane(tid >> 6), lane = tid & 63;
    LAS c2* A = (LAS c2*)smem; LAS c2* C = (LAS c2*)(smem + R_C);
    const unsigned n0 = (lane & 7) | ((lane & 24) << 1);
    c2 x[10];
    if (wave < 4) {
        const unsigned base = (unsigned)(row0 + 2 * wave) * (D_ * 2), vo = n0 * 2u + (lane >> 5) * (D_ * 2u);
#pragma unroll
        for (int r = 0; r < 10; ++r) x[r] = (c2){bf2f(__builtin_amdgcn_raw_buffer_load_b16(rx, vo + 128u * r, base, 16)), bf2f(__builtin_amdgcn_raw_buffer_load_b16(rx, vo + 16u + 128u * r, base, 16))};
    }
    const bool early = row0 < 256;
    if (!early) go();
    FST(0);
    u32x4 fv[3];
    {
        const unsigned vo = (tid >> 3) * (early ? GST : FST_) + (tid & 7) * 16u, st = (early ? GST : FST_) * 64u, so = (early ? O_FL + (unsigned)(1 + layer) * (NCOL * GST) : O_F) + (unsigned)row0 * 16u;
#pragma unroll
        for (int c = 0; c < 3; ++c) fv[c] = __builtin_amdgcn_raw_buffer_load_b128(rws, vo, c < 2 || wave < 4 ? so + c * st : 0x40000000u, 16);
        if (early) for (unsigned spins = 0;;) {
            if (!__any(max(max(smax(fv[0]), smax(fv[1])), smax(fv[2])) == ~0u) || spin_fail(spins, rwin)) break;
            __builtin_amdgcn_s_sleep(PS3);
            asm volatile("" ::: "memory");
            u32x4 x[3];
#pragma unroll
            for (int c = 0; c < 3; ++c) x[c] = __builtin_amdgcn_raw_buffer_load_b128(rws, smax(fv[c]) == ~0u ? vo : 0x40000000u, so + c * st, 16);
#pragma unroll
            for (int c = 0; c < 3; ++c) if (smax(fv[c]) == ~0u) fv[c] = x[c];
        }
    }
#pragma unroll
    for (int c = 0; c < 3; ++c) {
        const int it = tid + 512 * c, kp = it >> 3;
        if (c < 2 || tid < 256) {
            const bool odd = it & 1;
            const f32x4 v = __builtin_bit_cast(f32x4, fv[c]), p = {dppf<0xB1>(v[0]), dppf<0xB1>(v[1]), dppf<0xB1>(v[2]), dppf<0xB1>(v[3])};
            c2 z0 = odd ? (c2){p[0] + v[1], v[0] - p[1]} : (c2){v[0] - p[1], p[0] + v[1]};
            const c2 z1 = odd ? (c2){p[2] + v[3], v[2] - p[3]} : (c2){v[2] - p[3], p[2] + v[3]};
            int i0 = odd ? 640 - 2 * kp : 2 * kp;
            if (c == 0 && kp == 0) { z0 = odd ? (c2){p[1], v[1]} : (c2){v[0], p[0]}; i0 >>= 1; }
            LAS c2* z = C + ((it >> 1) & 3) * 648;
            z[i0] = z0; z[odd ? 639 - 2 * kp : 2 * kp + 1] = z1;
        }
    }
    FST(1);
    BAR();
    if (early) RST(3, layer, wave, 0);
    FST(2);
    if (tid < 320) {
        const int K = tid >> 3, m = tid & 7;
        LAS c2* a = A + K * 72;
        const LAS c2* o = C + (K / 10) * 638 + K + 10 * m;
        c2 u[8];
#pragma unroll
        for (int p = 0; p < 8; ++p) u[p] = ldv(o + 80 * p);
        dft8<1>(u);
        twid<1>(u, tw.b);
#pragma unroll
        for (int q = 0; q < 8; ++q) a[9 * q + m] = u[q];
        asm volatile("" ::: "memory");
#pragma unroll
        for (int p = 0; p < 8; ++p) u[p] = ldv(a + 9 * m + p);
        dft8<1>(u);
        twid<1>(u, tw.a);
#pragma unroll
        for (int q = 0; q < 8; ++q) a[m + 8 * (q >> 1) + 32 * (q & 1)] = u[q];
    }
    FST(3);
    BAR();
    FST(4);
    if (wave < 4) {
        c2 u[10];
#pragma unroll
        for (int r = 0; r < 10; ++r) u[r] = ldv(A + (wave * 10 + r) * 72 + lane);
        dft10<1>(u);
        unsigned x1[10], h[10];
#pragma unroll
        for (int r = 0; r < 10; ++r) {
            swap32(u[r]);
            const c2 y = x[r] + unpk(pack2(u[r].x, u[r].y));
            x1[r] = pack2(y.x, y.y);
            u[r] = unpk(x1[r]);
        }
        FST(5);
        ln_row(u, h);
        FST(6);
        LAS char* st = (LAS char*)C + wave * 5120;
        LAS char* hp = st + (lane >> 5) * 1280 + (n0 & 48) * 2 + pi16(n0 & 15) * 2;
#pragma unroll
        for (int r = 0; r < 10; ++r) *(LAS unsigned*)(hp + 128 * r) = nosent1(h[r]);
#pragma unroll
        for (int c = 0; c < 3; ++c) {
            if (c < 2 || lane < 32) {
                const int id = c * 64 + lane, rr = id >= 80, ch = id - 80 * rr, row = (row0 + 2 * wave + rr) & 31;
                st128(*(const LAS u32x4*)(st + id * 16), rws, (unsigned)((ch >> 3) * 4096 + row * 128 + (((ch & 7) ^ (row & 7)) << 4)), O_H + (unsigned)((row0 >> 5) - NHF * layer) * 40960u, 16);
            }
        }
        if (early) RST(4, layer, wave, 0);
        LAS char* xp = st + 2560 + (lane >> 5) * 1280 + n0 * 2;
#pragma unroll
        for (int r = 0; r < 10; ++r) { *(LAS u16*)(xp + 128 * r) = (u16)x1[r]; *(LAS u16*)(xp + 128 * r + 16) = (u16)(x1[r] >> 16); }
#pragma unroll
        for (int c = 0; c < 3; ++c)
            if (c < 2 || lane < 32) st128(*(const LAS u32x4*)(st + 2560 + (c * 64 + lane) * 16), rws, (c * 64 + lane) * 16u, O_X1 + (unsigned)(row0 + 2 * wave) * (D_ * 2), 16);
        FST(7);
    }
}

constexpr int C_T = 8 * 544 * 8, C_A = C_T + (int)sizeof(TwCol), C_B = C_A + 32768, C_W = C_B + 16384, C_END = C_W + 8 * 516 * 4;
constexpr float FSC = 0.5f / ((float)S_ * (float)D_);
template <class FG> __device__ __forceinline__ void a2_unit(int kp, int late, __amdgpu_buffer_rsrc_t rws, __amdgpu_buffer_rsrc_t rwin, int tid, FG go, unsigned (&fs)[12]) {
    const int w = __builtin_amdgcn_readfirstlane(tid >> 6), l = tid & 63;
    const unsigned fb = O_F + (unsigned)kp * FST_, wb = O_WFT + (unsigned)kp * WST;
    const int b = (l >> 1) & 7;
    const int k0 = w + 8 * b + 128 * (l >> 4);
    LAS c2* fa = (LAS c2*)(smem + C_A) + tid;
    LAS c2* fb_ = (LAS c2*)(smem + C_B) + (tid >> 1);
    c2 u[8];
    {
        f32x4 tv[3];
#pragma unroll
        for (int i = 0; i < 3; ++i) tv[i] = ((const f32x4*)&TWC)[min(tid + 512 * i, (int)sizeof(TwCol) / 16 - 1)];
        go(0);
#pragma unroll
        for (int i = 0; i < 3; ++i) ((LAS f32x4*)(smem + C_T))[min(tid + 512 * i, (int)sizeof(TwCol) / 16 - 1)] = tv[i];
        BAR0();
        FST(0);
        u32x4 wv[2], ab[8];
#pragma unroll
        for (int r = 0; r < 2; ++r) wv[r] = __builtin_amdgcn_raw_buffer_load_b128(rws, tid * 16u, wb + r * 8192u, 16);
        if (kp == 0) {
#pragma unroll
            for (int r = 0; r < 8; ++r) ab[r] = __builtin_amdgcn_raw_buffer_load_b128(rws, (tid >> 1) * 16u, O_WFT + NCOL * WST + r * 4096u, 16);
        }
#pragma unroll
        for (int r = 0; r < 7; ++r) u[r] = __builtin_bit_cast(c2, __builtin_amdgcn_raw_buffer_load_b64(rws, tid * 8u, fb + r * 4096u, 16));
        if (!late) u[7] = __builtin_bit_cast(c2, __builtin_amdgcn_raw_buffer_load_b64(rws, tid * 8u, fb + 7 * 4096u, 16));
        LAS u32x2* wl = (LAS u32x2*)(smem + C_W) + (tid & 3) * 516 + (tid >> 2);
#pragma unroll
        for (int r = 0; r < 2; ++r) { wl[r * 128] = (u32x2){wv[r][0], wv[r][1]}; wl[r * 128 + 258] = (u32x2){wv[r][2], wv[r][3]}; }
        BAR();
        const LAS unsigned* wr = (const LAS unsigned*)(smem + C_W) + w * 516 + (l & 15) + 32 * (l >> 4);
#pragma unroll
        for (int r = 0; r < 8; ++r) fa[512 * r] = unpk(wr[16 * (r & 1) + 128 * (r >> 1)]) * bc(FSC);
        if (kp == 0 && !(l & 1)) {
#pragma unroll
            for (int r = 0; r < 8; ++r) { *(LAS u32x2*)(fa + 512 * r) = (u32x2){ab[r][0], ab[r][1]}; *(LAS u32x2*)(fb_ + 256 * r) = (u32x2){ab[r][2], ab[r][3]}; }
        }
    }
    const LAS f32x4* t1 = (const LAS f32x4*)(smem + C_T) + (tid >> 1);
    const LAS f32x4* t2 = (const LAS f32x4*)(smem + C_T + sizeof(Tw7<256>)) + (l >> 1);
    const LAS f32x4* t3 = (const LAS f32x4*)(smem + C_T + sizeof(Tw7<256>) + sizeof(Tw7<32>)) + (l >> 4);
    LAS c2* e0 = (LAS c2*)smem + tid;
    LAS c2* e1 = (LAS c2*)smem + w * 544 + l;
    LAS c2* e2 = (LAS c2*)smem + w * 544 + 2 * (l >> 4) + (l & 1);
    Tw tw;
    ldtw<256>(tw, t1);
    if (late) {
        go(2);
        FST(1);
        for (unsigned spins = 0;;) {
            asm volatile("" ::: "memory");
            const u32x2 x = __builtin_amdgcn_raw_buffer_load_b64(rws, tid * 8u, O_FL + (unsigned)kp * GST, 16);
            u[7] = __builtin_bit_cast(c2, x);
            if (!__any(x[1] == ~0u) || spin_fail(spins, rwin)) break;
            if (w < 6) __builtin_amdgcn_s_sleep(24); else __builtin_amdgcn_s_sleep(PS2);
        }
    }
    FST(2);
    dft8<-1>(u);
    twid<-1>(u, tw);
#pragma unroll
    for (int r = 0; r < 8; ++r) e0[544 * r] = u[r];
    ldtw<32>(tw, t2);
    BAR();
#pragma unroll
    for (int r = 0; r < 8; ++r) u[r] = ldv(e1 + 64 * r);
    dft8<-1>(u);
    twid<-1>(u, tw);
#pragma unroll
    for (int r = 0; r < 8; ++r) e1[68 * r] = u[r];
    asm volatile("" ::: "memory");
#pragma unroll
    for (int r = 0; r < 8; ++r) u[r] = ldv(e2 + 68 * b + 8 * r);
    ldtw<4>(tw, t3);
    dft8<-1>(u);
    twid<-1>(u, tw);
    c2 f[8];
#pragma unroll
    for (int r = 0; r < 8; ++r) f[r] = ldv(fa + 512 * r);
    asm volatile("s_nop 1" : "+v"(u[1]), "+v"(u[2]), "+v"(u[3]), "+v"(u[4]), "+v"(u[5]), "+v"(u[6]), "+v"(u[7]));
#pragma unroll
    for (int r = 0; r < 4; ++r) swapl<5>(u[r], u[r + 4]);
#pragma unroll
    for (int r = 0; r < 4; ++r) swapl<4>(u[(r & 1) + 2 * (r & 2)], u[(r & 1) + 2 * (r & 2) + 2]);
    dft4<-1>(u[0], u[2], u[4], u[6]); dft4<-1>(u[1], u[3], u[5], u[7]);
    FST(3);
    if (kp == 0) {
        LAS c2* z = (LAS c2*)(smem + C_W);
#pragma unroll
        for (int r = 0; r < 8; ++r) if (!(l & 1)) z[w * 258 + b + 16 * (l >> 4) + 8 * (r & 1) + 64 * (r >> 1)] = u[r];
        BAR();
#pragma unroll
        for (int r = 0; r < 8; ++r) {
            const int kn = (S_ - k0 - 64 * (r & 1) - 512 * (r >> 1)) & (S_ - 1);
            const c2 zn = z[(kn & 7) * 258 + (kn >> 3)], g = fb_[256 * r];
            const c2 y = cfin<1>(g, zn, g * bc(zn.x));
            u[r] = cfin<-1>(u[r], f[r], fm(u[r], bc(f[r].x), l & 1 ? bc(0.f) : y));
        }
    } else {
#pragma unroll
        for (int r = 0; r < 8; ++r) u[r] = cfin<-1>(u[r], f[r], u[r] * bc(f[r].x));
    }
    FST(4);
    dft4<1>(u[0], u[2], u[4], u[6]); dft4<1>(u[1], u[3], u[5], u[7]);
#pragma unroll
    for (int r = 0; r < 4; ++r) swapl<4>(u[(r & 1) + 2 * (r & 2)], u[(r & 1) + 2 * (r & 2) + 2]);
#pragma unroll
    for (int r = 0; r < 4; ++r) swapl<5>(u[r], u[r + 4]);
    twid<1>(u, tw);
    dft8<1>(u);
#pragma unroll
    for (int r = 0; r < 8; ++r) e2[66 * b + 8 * r] = u[r];
    asm volatile("" ::: "memory");
    ldtw<32>(tw, t2);
#pragma unroll
    for (int r = 0; r < 8; ++r) u[r] = ldv(e1 + 66 * r);
    twid<1>(u, tw);
    dft8<1>(u);
#pragma unroll
    for (int r = 0; r < 8; ++r) e1[64 * r] = u[r];
    ldtw<256>(tw, t1);
    BAR();
#pragma unroll
    for (int r = 0; r < 8; ++r) u[r] = ldv(e0 + 544 * r);
    twid<1>(u, tw);
    dft8<1>(u);
    FST(5);
    __builtin_amdgcn_raw_buffer_store_b64((u32x2){__float_as_uint(u[0].x), nosent1(__float_as_uint(u[0].y))}, rws, tid * 8u, O_FL + (unsigned)((1 + late) * NCOL + kp) * GST, 16);
#pragma unroll
    for (int r = 1; r < 8; ++r) __builtin_amdgcn_raw_buffer_store_b64(__builtin_bit_cast(u32x2, u[r]), rws, tid * 8u, fb + r * 4096u, 16);
    FST(6);
}

struct Args { char* ws; char* win; const u16 *x, *wr, *wi, *mem0, *wq, *wk, *wv, *wo; u16* out; };
__device__ __forceinline__ const u16* arg(int i) {
    const __attribute__((address_space(4))) u64* p = (const __attribute__((address_space(4))) u64*)__builtin_amdgcn_kernarg_segment_ptr();
    asm volatile("" : "+s"(p));
    return (const u16*)p[i];
}
enum { A_X = 2, A_WR, A_WI, A_MEM0, A_WQ, A_WK, A_WV, A_WO, A_OUT };
#define MXV 0x1556ull
#define MXQ 0x1554ull
#define NTAIL 7
static_assert(NTAIL >= 3, "the owners release segments up to NSEG - 4");
constexpr int TAILS = NSEG - NTAIL;
static_assert((MXV & 31) == 22 && (MXQ & 31) == 20, "segment sets");
constexpr int NREC = 96 + NOWN, NQT = NREC + NOWN, NXT = NQT + 32, NWRK = NXT + 32;
#define SST(k) do {} while (0)
#define LSEL 0
#define RT(k) do {} while (0)
#define RTS() do {} while (0)
#define B3ATTR
#define MTA(k)
#define MTRT() do {} while (0)
#define MT(k) do {} while (0)
#define MTS() do {} while (0)
#define FC(x) do {} while (0)
#define DQC 1200
#define DVC 1200
#define M1 44
#define M2 226
#define PEN 280
#define DZP 55
#define LT0 342
#define SAD 1
#define PEN1 50
#define PEN0 150
#define TSLP 1
#define OPR 2
#define BSPARSE 0
#define ZLAG 3
#define TKP 0
#define DPS 12
#define DXT 3000
#define LTM 12
#define XS0 10
#define XSU 8
#define XSM 32
#define LTU 4
#define LTS 3
#define XSS 3
#define TKS 3
#define LT1 450
#define FDL 2
#define ZLA 3
#define NORND 0
#define LTX 40
#define TKE 200
#define DVO 6000
#define ZPS 2
#define HLA 8
#define QB2 2
#define NB2 6
#define DZR 24
#define ML 150
#define NB1 3
#define DPT 3
#define DDMA 8
#define DZ2 800
#define QEARLY 4
#define OSLP 32
#define OSLP2 1
#define WSLP 64
#define DO0 5500
#define DW0 1200
#define DQT 2000
#define ZEARLY 4
#define HSLP 4
#define ASLP 4
#define CSLP 4
#define NPR 3
#define OSLP3 12
#define DVT 1100
#define VSL 0
#define WARM 1
constexpr unsigned CODE_BYTES = 96000;
#define DTK 4
#define OSL4 10
#define OAUX 0
#define TSL0 6
#define SE 6
#define XE 20
#define PENS 20
#define CF 190
#define HSL2 16
#define DH2 40
#define DH1 10
#define DIE 0
#define SB() __builtin_amdgcn_sched_barrier(0)
#define PIN1(a) asm volatile("" : "+v"(a) :: "memory")
#define PIN2(a, b) asm volatile("" : "+v"(a), "+v"(b) :: "memory")
__device__ __forceinline__ unsigned fresh_lane() { unsigned l; asm volatile("v_mbcnt_lo_u32_b32 %0, -1, 0\n\tv_mbcnt_hi_u32_b32 %0, -1, %0" : "=v"(l)); return l; }
#define LANE0 (min(fresh_lane(), 1u) << 30)
__device__ __forceinline__ void wait_until(unsigned long long t) { while ((long long)(__builtin_amdgcn_s_memtime() - t) < 0) __builtin_amdgcn_s_sleep(1); }
struct Clk { unsigned ta, ma, cpq, qpc; };
__device__ __forceinline__ int sx24(unsigned v) { return (int)(v << 8) >> 8; }
#define WAIT_FINE \
        "s_memrealtime vcc\n\t" \
        "s_memtime s[100:101]\n\t" \
        "s_brev_b32 %[fi], -2\n\t" \
        "s_waitcnt lgkmcnt(0)\n\t" \
        "s_mov_b32 %[rp], vcc_lo\n\t" \
        "s_lshl_b32 vcc_hi, vcc_lo, 2\n\t" \
        "s_sub_u32 %[x], %[t], vcc_hi\n\t" \
        "s_bfe_i32 %[x], %[x], 0x180000\n\t" \
        "s_min_i32 %[x], %[x], 4000\n\t" \
        "s_max_i32 %[x], %[x], -4000\n\t" \
        "s_add_u32 %[t], vcc_hi, %[x]\n\t" \
        "s_mul_i32 %[x], %[x], %[cq]\n\t" \
        "s_ashr_i32 %[x], %[x], 8\n\t" \
        "s_sub_u32 %[tm], %[t], %[ta]\n\t" \
        "s_bfe_i32 %[tm], %[tm], 0x180000\n\t" \
        "s_min_i32 %[tm], %[tm], 1000000\n\t" \
        "s_max_i32 %[tm], %[tm], -1000000\n\t" \
        "s_mul_i32 %[tm], %[tm], %[cq]\n\t" \
        "s_ashr_i32 %[tm], %[tm], 8\n\t" \
        "s_add_u32 %[tm], %[tm], %[ma]\n\t" \
        "s_sub_u32 %[tm], %[tm], s100\n\t" \
        "s_lshr_b32 vcc_hi, %[cq], 4\n\t" \
        "s_sub_u32 vcc_hi, %[x], vcc_hi\n\t" \
        "s_max_i32 %[tm], %[tm], vcc_hi\n\t" \
        "s_min_i32 %[tm], %[tm], %[x]\n\t" \
        "s_add_u32 %[tm], %[tm], s100\n" \
        "1:\n\t" \
        "s_memrealtime vcc\n\t" \
        "s_memtime s[100:101]\n\t" \
        "s_waitcnt lgkmcnt(0)\n\t" \
        "s_cmp_eq_u32 vcc_lo, %[rp]\n\t" \
        "s_cbranch_scc1 2f\n\t" \
        "s_mov_b32 %[rp], vcc_lo\n\t" \
        "s_lshl_b32 %[x], vcc_lo, 2\n\t" \
        "s_sub_u32 %[x], %[t], %[x]\n\t" \
        "s_bfe_i32 %[x], %[x], 0x180000\n\t" \
        "s_mul_i32 %[x], %[x], %[cq]\n\t" \
        "s_ashr_i32 %[x], %[x], 8\n\t" \
        "s_add_u32 %[x], %[x], s100\n\t" \
        "s_sub_u32 s101, %[x], %[tm]\n\t" \
        "s_cmp_lt_i32 s101, %[fi]\n\t" \
        "s_cselect_b32 %[tm], %[x], %[tm]\n\t" \
        "s_mov_b32 %[fi], 0\n" \
        "2:\n\t" \
        "s_sub_u32 %[x], %[tm], s100\n\t" \
        "s_cmp_gt_i32 %[x], 260\n\t" \
        "s_cbranch_scc1 1b\n\t" \
        "s_cmp_eq_u32 %[fi], 0\n\t" \
        "s_cselect_b32 %[ta], %[t], %[ta]\n\t" \
        "s_cselect_b32 %[ma], %[tm], %[ma]\n" \
        "3:\n\t" \
        "s_memtime s[100:101]\n\t" \
        "s_waitcnt lgkmcnt(0)\n\t" \
        "s_sub_u32 %[x], %[tm], s100\n\t" \
        "s_cmp_gt_i32 %[x], 92\n\t" \
        "s_cbranch_scc1 3b\n\t" \
        "s_max_i32 %[x], %[x], 0\n\t" \
        "s_andn2_b32 %[x], %[x], 3\n\t" \
        "s_sub_u32 %[x], 104, %[x]\n\t" \
        "s_getpc_b64 vcc\n\t" \
        "s_add_u32 vcc_lo, vcc_lo, %[x]\n\t" \
        "s_addc_u32 vcc_hi, vcc_hi, 0\n\t" \
        "s_setpc_b64 vcc\n\t" \
        ".rept 23\n\t" \
        "s_nop 0\n\t" \
        ".endr\n\t"
#define WAIT_FINE_OUT [tm] "=&s"(tm), [rp] "=&s"(rp), [x] "=&s"(x), [fi] "=&s"(fi), [t] "+s"(t), [ta] "+s"(c.ta), [ma] "+s"(c.ma)
__device__ __forceinline__ void wait_fine(unsigned t, Clk& c) {
    unsigned tm, rp, x, fi;
    asm volatile(WAIT_FINE : WAIT_FINE_OUT : [cq] "s"(c.cpq) : "vcc", "scc", "s100", "s101", "memory");
}
__device__ __forceinline__ void wait_fine_bar(unsigned t, Clk& c) {
    unsigned tm, rp, x, fi;
    asm volatile(WAIT_FINE "s_barrier" : WAIT_FINE_OUT : [cq] "s"(c.cpq) : "vcc", "scc", "s100", "s101", "memory");
}
__device__ __forceinline__ void wait_cyc_bar(unsigned tm) {
    unsigned x;
    asm volatile(
        "3:\n\t"
        "s_memtime s[100:101]\n\t"
        "s_waitcnt lgkmcnt(0)\n\t"
        "s_sub_u32 %0, %1, s100\n\t"
        "s_cmp_gt_i32 %0, 92\n\t"
        "s_cbranch_scc1 3b\n\t"
        "s_max_i32 %0, %0, 0\n\t"
        "s_andn2_b32 %0, %0, 3\n\t"
        "s_sub_u32 %0, 104, %0\n\t"
        "s_getpc_b64 vcc\n\t"
        "s_add_u32 vcc_lo, vcc_lo, %0\n\t"
        "s_addc_u32 vcc_hi, vcc_hi, 0\n\t"
        "s_setpc_b64 vcc\n\t"
        ".rept 23\n\t"
        "s_nop 0\n\t"
        ".endr\n\t"
        "s_barrier"
        : "=&s"(x) : "s"(tm) : "vcc", "scc", "s100", "s101", "memory");
}
__device__ __forceinline__ unsigned cyc_at(const Clk& c, int d) {
    d = min(d, 20000);
    asm volatile("" : "+s"(d));
    return c.ma + (unsigned)((max(d, -4000) * (int)c.cpq) >> 8);
}
__device__ __forceinline__ void clk_rate(Clk& c, unsigned ta0, unsigned ma0) {
    const int dq = sx24(c.ta - ta0);
    if (dq > 200) {
        const float r = fminf(fmaxf((float)(c.ma - ma0) * __builtin_amdgcn_rcpf((float)dq), 1.f), 8.f);
        asm volatile("s_nop 1\n\tv_readfirstlane_b32 %0, %2\n\tv_readfirstlane_b32 %1, %3\n\ts_nop 3" : "=s"(c.cpq), "=s"(c.qpc)
                     : "v"((unsigned)(256.f * r + 0.5f)), "v"((unsigned)(65536.f * __builtin_amdgcn_rcpf(r))));
    }
}
__device__ __forceinline__ void clk_init(Clk& c) {
    c.ta = 4u * (unsigned)__builtin_amdgcn_s_memrealtime(); c.ma = (unsigned)__builtin_amdgcn_s_memtime();
    for (int i = 0; i < 2; ++i) {
        const unsigned ta0 = c.ta, ma0 = c.ma;
        wait_fine(c.ta + (i ? 360u : 60u), c);
        clk_rate(c, ta0, ma0);
    }
}
__device__ __forceinline__ unsigned sched_lane(int s) { const unsigned l = fresh_lane(); return (l & 31u) * 128u + (l >> 5) * 4u; }
__device__ __forceinline__ int read_sched(__amdgpu_buffer_rsrc_t rwin, int s, unsigned vo, unsigned to, unsigned f, unsigned g0, int& lt, int& xs, unsigned& fc) {
    int d = (int)((f - g0) & 0xFFFFFFu);
    if (__builtin_expect(__any((f >> 24) != (unsigned)s) || s == 0, 0)) {
        for (unsigned spins = 0; __any((f >> 24) != (unsigned)s);) {
            FC(fc);
            if (spin_fail(spins, rwin)) break;
            if (s == 0) __builtin_amdgcn_s_sleep(TSL0); else __builtin_amdgcn_s_sleep(TSLP);
            asm volatile("" ::: "memory");
            if ((f >> 24) != (unsigned)s) f = __builtin_amdgcn_raw_buffer_load_b32(rwin, vo, to, 16);
        }
        const unsigned f0 = __builtin_amdgcn_readfirstlane(f) - 0x100000u;
        d = (int)((f - f0) & 0xFFFFFFu) + (int)((f0 - g0) & 0xFFFFFFu);
    }
    d = max(d, __builtin_amdgcn_update_dpp(0, d, 0xB1, 0xF, 0xF, true));
    d = max(d, __builtin_amdgcn_update_dpp(0, d, 0x4E, 0xF, 0xF, true));
    d = max(d, __builtin_amdgcn_update_dpp(0, d, 0x141, 0xF, 0xF, true));
    d = max(d, __builtin_amdgcn_update_dpp(0, d, 0x140, 0xF, 0xF, true));
    d = max(d, __builtin_amdgcn_update_dpp(0, d, 0x142, 0xA, 0xF, true));
    d = max(__builtin_amdgcn_readlane(d, 16), __builtin_amdgcn_readlane(d, 48));
    const int l0 = lt, x0 = xs;
    d -= 0x400000;
    const bool miss = d > l0 + min(x0 + LTM, LTX);
    if (s >= SAD) { lt = miss ? l0 : min(max(d, l0 - 1 - ((l0 - d) >> LTS)), l0 + LTU); xs = miss ? min(x0 + XSU, XSM) : max(x0 - 1 - (x0 >> XSS), 0); }
    if (s == 1) lt = LT0;
    if (s < SE) xs = max(xs, XE);
    return 0x400000 + (s < SAD ? d + (s ? PENS : PEN0) : miss ? d + PEN : l0 + x0) + M1 + M2 + x0;
}
constexpr int QS = 272;
constexpr int TQS = 1152;
constexpr int L_Q = 10240, L_K = L_Q + 2 * TQS, L_S = L_K + 2 * TQS, L_TOK = L_S + 256, L_H = L_TOK + 49152, L_END = L_H + 2 * 40960;
static_assert(C_END <= L_END && L_END == 146176, "LDS");
typedef __attribute__((ext_vector_type(4))) short v4i16_t;
__device__ __forceinline__ u32x2 ldtr(const char* p) { return __builtin_bit_cast(u32x2, __builtin_amdgcn_ds_read_tr16_b64_v4i16((LAS v4i16_t*)p)); }
__device__ __forceinline__ u32x2 ldtr1(const LAS char* p) { return __builtin_bit_cast(u32x2, __builtin_amdgcn_ds_read_tr16_b64_v4i16((LAS v4i16_t*)p)); }
__device__ __forceinline__ u32x4 ldtr2(const LAS char* p0, const LAS char* p1) {
    const u32x2 x = __builtin_bit_cast(u32x2, __builtin_amdgcn_ds_read_tr16_b64_v4i16((LAS v4i16_t*)p0)), y = __builtin_bit_cast(u32x2, __builtin_amdgcn_ds_read_tr16_b64_v4i16((LAS v4i16_t*)p1));
    return (u32x4){x[0], x[1], y[0], y[1]};
}
__device__ __forceinline__ u32x4 ldtr2(const char* p0, const char* p1) { const u32x2 x = ldtr(p0), y = ldtr(p1); return (u32x4){x[0], x[1], y[0], y[1]}; }

__device__ __forceinline__ unsigned poll_lim(int& budget) {
    --budget;
    unsigned lim = budget > 0 ? 0xFFFFFFFEu : 0xFFFFFFFFu;
    asm volatile("" : "+s"(lim));
    return lim;
}
#define GATHER \
        "buffer_load_dwordx4 %[vo], %[rw], %[s0] offen sc1 lds\n\t" \
        "buffer_load_dwordx4 %[vo], %[rw], %[s0] offen offset:1024 sc1 lds\n\t" \
        "buffer_load_dwordx4 %[vo], %[rw], %[s0] offen offset:2048 sc1 lds\n\t" \
        "buffer_load_dwordx4 %[vo], %[rw], %[s0] offen offset:3072 sc1 lds\n\t" \
        "s_movk_i32 m0, 0x1000\n\t" \
        "s_nop 0\n\t" \
        "buffer_load_dwordx4 %[vo], %[rw], %[s4] offen sc1 lds\n\t" \
        "buffer_load_dwordx4 %[vo], %[rw], %[s4] offen offset:1024 sc1 lds\n\t" \
        "buffer_load_dwordx4 %[vo], %[rw], %[s4] offen offset:2048 sc1 lds\n\t" \
        "buffer_load_dwordx4 %[vo], %[rw], %[s4] offen offset:3072 sc1 lds\n\t" \
        "s_movk_i32 m0, 0x2000\n\t" \
        "s_nop 0\n\t" \
        "buffer_load_dwordx4 %[vo], %[rw], %[s1] offen sc1 lds\n\t" \
        "buffer_load_dwordx4 %[vo], %[rw], %[s1] offen offset:1024 sc1 lds"
#define GATHER_IN [vo] "v"(vo), [rw] "s"(rwin), [s0] "s"(so0), [s4] "s"(so0 + 4096u), [s1] "s"(so1)
__device__ __forceinline__ void gather_dma(__amdgpu_buffer_rsrc_t rwin, unsigned vo, unsigned so0, unsigned so1) {
    asm volatile("s_mov_b32 m0, 0\n\ts_nop 0\n\t" GATHER :: GATHER_IN : "m0", "memory");
}
__device__ __forceinline__ void gather_at(unsigned t, Clk& c, __amdgpu_buffer_rsrc_t rwin, unsigned vo, unsigned so0, unsigned so1) {
    unsigned tm, rp, x, fi;
    asm volatile("s_mov_b32 m0, 0\n\t" WAIT_FINE GATHER : WAIT_FINE_OUT : [cq] "s"(c.cpq), GATHER_IN : "vcc", "scc", "s100", "s101", "m0", "memory");
}
__device__ __forceinline__ void dma4k(__amdgpu_buffer_rsrc_t r, unsigned vo, unsigned so, unsigned lds) {
    asm volatile(
        "s_mov_b32 m0, %3\n\t"
        "s_nop 0\n\t"
        "buffer_load_dwordx4 %0, %1, %2 offen sc1 lds\n\t"
        "buffer_load_dwordx4 %0, %1, %2 offen offset:1024 sc1 lds\n\t"
        "buffer_load_dwordx4 %0, %1, %2 offen offset:2048 sc1 lds\n\t"
        "buffer_load_dwordx4 %0, %1, %2 offen offset:3072 sc1 lds"
        :: "v"(vo), "s"(r), "s"(so), "s"(lds) : "m0", "memory");
}
__device__ __forceinline__ int bs2(int c) { return (c & ~3) | ((c & 1) << 1) | ((c >> 1) & 1); }
__device__ __forceinline__ void gather_mem0(char* memL, int tid) {
    const u16* mem0 = arg(A_MEM0);
    for (int c = tid; c < 640; c += 512) {
        const u32x2* src = (const u32x2*)(mem0 + ((c & 6) + ((c >> 3) & 1)) * D_ + (c >> 4) * 16 + (c & 1) * 4);
        const u32x2 x = src[0], y = src[2];
        *(u32x4*)(memL + c * 16) = (u32x4){(x[0] & 0xFFFFu) | (y[0] << 16), (x[0] >> 16) | (y[0] & 0xFFFF0000u), (x[1] & 0xFFFFu) | (y[1] << 16), (x[1] >> 16) | (y[1] & 0xFFFF0000u)};
    }
}
__device__ __forceinline__ f32x2 hadd(f32x4 c) { return (f32x2){c[0], c[1]} + (f32x2){c[2], c[3]}; }
__device__ __forceinline__ unsigned hpack(f32x4 c) { const f32x2 y = hadd(c); return pack2(y[0], y[1]); }
__device__ __forceinline__ u32x8 cat8(u32x4 lo, u32x4 hi) { return (u32x8){lo[0], lo[1], lo[2], lo[3], hi[0], hi[1], hi[2], hi[3]}; }
__device__ __forceinline__ u32x4 half8(const u32x8& v, int u) { return u ? (u32x4){v[4], v[5], v[6], v[7]} : (u32x4){v[0], v[1], v[2], v[3]}; }
__device__ __forceinline__ void dma1k(__amdgpu_buffer_rsrc_t r, unsigned vo, unsigned so, unsigned lds) {
    asm volatile("s_mov_b32 m0, %3\n\ts_nop 0\n\tbuffer_load_dwordx4 %0, %1, %2 offen sc1 lds" :: "v"(vo), "s"(r), "s"(so), "s"(lds) : "m0", "memory");
}
__device__ __forceinline__ void load_w(u32x8 (&wt)[2][10], const u16* src, unsigned rs, bool perm, unsigned b, unsigned c, int w) {
    const __amdgpu_buffer_rsrc_t r = mkrsrc(src, 0x7FFFFFFFu);
    const unsigned l = fresh_lane(), q = l >> 4, lds = (unsigned)(w >> 2) * 65536u, v = w & 3;
    const unsigned vo0 = (perm ? ((q & 1u) << 3) | ((q & 2u) << 1) : q) * rs + ((l & 15u) ^ (q << 1)) * 16u, vo1 = vo0 ^ 128u, a = perm ? rs : 4u * rs;
    const LAS char* p0 = (const LAS char*)(size_t)(lds + q * 2048u + ((l >> 2) & 3u) * 256u + ((2u * v) ^ ((l >> 2) & 7u)) * 32u + (l & 3u) * 8u);
    const LAS char* p1 = (const LAS char*)((size_t)p0 ^ 32u);
    auto issue = [&](int g) {
#pragma unroll
        for (int i = 0; i < 4; ++i)
            asm volatile("s_mov_b32 m0, %3\n\ts_nop 3\n\tbuffer_load_dwordx4 %0, %1, %2 offen lds" :: "v"(i < 2 ? vo0 : vo1), "s"(r), "s"(i * a + (g & 1) * b + (g >> 1) * c), "s"(lds + (g & 3) * 16384u + v * 4096u + i * 1024u) : "m0", "memory");
    };
#pragma unroll
    for (int g = 0; g < 3; ++g) issue(g);
#pragma unroll
    for (int g = 0; g < 10; ++g) {
        asm volatile("s_waitcnt vmcnt(%0)\n\ts_barrier" :: "n"(g < 8 ? 8 : 4 * (9 - g)) : "memory");
        if (g + 3 < 10) issue(g + 3);
        const int o = (g & 3) * 16384;
        wt[0][g] = cat8(ldtr2(p0 + o, p0 + o + 1024), ldtr2(p0 + o + 8192, p0 + o + 9216));
        wt[1][g] = cat8(ldtr2(p1 + o, p1 + o + 1024), ldtr2(p1 + o + 8192, p1 + o + 9216));
        asm volatile("s_waitcnt lgkmcnt(0)" : "+v"(wt[0][g]), "+v"(wt[1][g]) :: "memory");
    }
    BAR0();
}
__device__ __forceinline__ int tile_of(int o) { return 8 * ((o % 10) >> 1) + 2 * (o / 10) + (o & 1); }
__device__ __forceinline__ void wait_tags(__amdgpu_buffer_rsrc_t rwin, int g0, int g1, unsigned n1, u64 val, bool boot) {
    const unsigned l = fresh_lane();
    unsigned o[4];
#pragma unroll
    for (int g = 0; g < 4; ++g) o[g] = g >= g0 && g < g1 && (g + 1 < g1 || l < n1) ? l * 128u + g * 8192u : 0x40000000u;
    for (unsigned spins = 0;;) {
        asm volatile("" ::: "memory");
        u32x2 v[4];
#pragma unroll
        for (int g = 0; g < 4; ++g) v[g] = __builtin_amdgcn_raw_buffer_load_b64(rwin, o[g], W_A1, 16);
#pragma unroll
        for (int g = 0; g < 4; ++g) if (__builtin_bit_cast(u64, v[g]) == val) o[g] = 0x40000000u;
        if (!__any((o[0] & o[1] & o[2] & o[3]) != 0x40000000u)) break;
        if (boot ? ++spins > SPIN_CAP : spin_fail(spins, rwin)) break;
        __builtin_amdgcn_s_sleep(ASLP);
    }
}
template <int N> __device__ __forceinline__ void stage_h(__amdgpu_buffer_rsrc_t rws, unsigned src, unsigned lds, unsigned st) {
    const unsigned l = fresh_lane() * 16u;
#pragma unroll
    for (int i = 0; i < N; ++i) dma1k(rws, l, src + i * st, lds + i * st);
}
template <int N> __device__ __forceinline__ void check_h(__amdgpu_buffer_rsrc_t rws, __amdgpu_buffer_rsrc_t rwin, unsigned src, unsigned lds, unsigned st) {
    const LAS char* p = (const LAS char*)smem + lds + fresh_lane() * 16u;
    for (unsigned spins = 0;;) {
        unsigned m = 0;
#pragma unroll
        for (int i = 0; i < N; ++i) { const u32x4 x = *(const volatile LAS u32x4*)(p + i * st); m = max(max(m, max(x[0], x[1])), max(x[2], x[3])); }
        if (__builtin_expect(!__any(m == ~0u), 1) || spin_fail(spins, rwin)) break;
        stage_h<N>(rws, src, lds, st);
        VMWAIT();
    }
}
__device__ __forceinline__ void wait_h(__amdgpu_buffer_rsrc_t rwin, unsigned fo, int seg, int& hrdy) {
    unsigned lim = 63u;
    if (__builtin_expect(hrdy <= seg, 0)) for (unsigned spins = 0;;) {
        asm volatile("" ::: "memory");
        const u32x4 f = __builtin_amdgcn_raw_buffer_load_b128(rwin, min(fresh_lane(), lim) * 128u, W_H + fo, 16);
        const unsigned long long miss = __ballot((f[0] | f[1] | f[2] | f[3]) != 0u);
        hrdy = miss ? __builtin_ctzll(miss) : (int)lim + 1;
        if (hrdy > seg) break;
        if (spin_fail(spins, rwin)) break;
        lim = (unsigned)min(seg + HLA, 63);
        if (seg < 8) __builtin_amdgcn_s_sleep(HSLP); else __builtin_amdgcn_s_sleep(HSL2);
    }
}
template <int CTRL> __device__ __forceinline__ unsigned dppu(unsigned v) { return (unsigned)__builtin_amdgcn_mov_dpp((int)v, CTRL, 0xF, 0xF, true); }
template <int CTRL> __device__ __forceinline__ u32x4 ror4(u32x4 v) { return (u32x4){dppu<CTRL>(v[0]), dppu<CTRL>(v[1]), dppu<CTRL>(v[2]), dppu<CTRL>(v[3])}; }
template <int N> struct IC { static constexpr int v = N; };
__device__ __forceinline__ float xmax16(float v) {
    auto q = __builtin_amdgcn_permlane16_swap(__float_as_uint(v), __float_as_uint(v), false, false);
    return fmx(__uint_as_float(q[0]), __uint_as_float(q[1]));
}
__device__ __forceinline__ float xsum16(float v) {
    auto q = __builtin_amdgcn_permlane16_swap(__float_as_uint(v), __float_as_uint(v), false, false);
    return __uint_as_float(q[0]) + __uint_as_float(q[1]);
}
__device__ __forceinline__ float addswap16(float a, float b) {
    auto r = __builtin_amdgcn_permlane16_swap(__float_as_uint(a), __float_as_uint(b), false, false);
    return __uint_as_float(r[0]) + __uint_as_float(r[1]);
}
#define LANES() \
    const int lane = (int)fresh_lane(), tid = w * 64 + lane, fr = lane & 15, fq = lane >> 4; \
    const int sidx = (lane & 2) ? 0xEEEE : 0x4444; \
    const int ma0 = fq * 256 + (fr & 1) * 128 + (fr >> 1) * 16; \
    const int prow = 16 * ((fr >> 2) & 1) + 4 * (fr >> 3) + (fr & 3), hoff = fr * 128; \
    (void)tid; (void)sidx; (void)ma0; (void)prow; (void)hoff

__device__ __forceinline__ int seg_after(unsigned long long m, int j) { m = j >= 63 ? 0ull : m >> (j + 1); return m ? j + 1 + __builtin_ctzll(m) : NSEG; }
__device__ __forceinline__ int seg_done(unsigned long long m, int f0, int f1) { return min(seg_after(~m, f0), seg_after(m, f1)) - 1; }
template <int OFF> __device__ __forceinline__ void ldfrag(u32x4& d, const LAS char* p) { asm volatile("ds_read_b128 %0, %1 offset:%2" : "=v"(d) : "v"(p), "n"(OFF) : "memory"); }
template <int U, class FM, class FV> __device__ __forceinline__ void project(const u32x8 (&wt)[2][10], f32x4 (&tc)[2][2], u32x4 (&bb)[8], const LAS char* c0, const LAS char* c1, const LAS char* n0, const LAS char* n1, FM mid, FV vm) {
    constexpr int t = U >> 2, k = U & 3, v = (U + 8) % 40;
    if (U == 32) mid();
    vm(IC<U>());
    const LAS char* p = U < 32 ? (k >> 1 ? c1 : c0) : (k >> 1 ? n1 : n0);
    if (U < 2) asm volatile("s_nop 0\n\ts_waitcnt lgkmcnt(7)\n\tv_mfma_f32_16x16x32_bf16 %0, %3, %2, 0\n\tv_mfma_f32_16x16x32_bf16 %1, %4, %2, 0\n\tds_read_b128 %2, %5 offset:%6"
                            : "=&v"(tc[0][k & 1]), "=&v"(tc[1][k & 1]), "+v"(bb[U & 7]) : "v"(half8(wt[0][t], k >> 1)), "v"(half8(wt[1][t], k >> 1)), "v"(p), "n"((v >> 2) * 4096 + (k & 1) * 2048) : "memory");
    else asm volatile(".rept %7\n\ts_nop 0\n\t.endr\n\ts_waitcnt lgkmcnt(7)\n\tv_mfma_f32_16x16x32_bf16 %0, %3, %2, %0\n\tv_mfma_f32_16x16x32_bf16 %1, %4, %2, %1\n\tds_read_b128 %2, %5 offset:%6"
                      : "+v"(tc[0][k & 1]), "+v"(tc[1][k & 1]), "+v"(bb[U & 7]) : "v"(half8(wt[0][t], k >> 1)), "v"(half8(wt[1][t], k >> 1)), "v"(p), "n"((v >> 2) * 4096 + (k & 1) * 2048), "n"(U < 8 ? 1 : 0) : "memory");
    if constexpr (U < 39) project<U + 1>(wt, tc, bb, c0, c1, n0, n1, mid, vm);
    else asm volatile("s_nop 7" : "+v"(tc[0][0]), "+v"(tc[0][1]), "+v"(tc[1][0]), "+v"(tc[1][1]));
}
__device__ __forceinline__ void project_end(u32x4 (&bb)[8]) {
    asm volatile("s_waitcnt lgkmcnt(0)" : "+v"(bb[0]), "+v"(bb[1]), "+v"(bb[2]), "+v"(bb[3]), "+v"(bb[4]), "+v"(bb[5]), "+v"(bb[6]), "+v"(bb[7]) :: "memory");
}
__device__ __forceinline__ void project0(u32x4 (&bb)[8], const LAS char* c0, const LAS char* c1) {
    ldfrag<0>(bb[0], c0); ldfrag<2048>(bb[1], c0); ldfrag<0>(bb[2], c1); ldfrag<2048>(bb[3], c1);
    ldfrag<4096>(bb[4], c0); ldfrag<6144>(bb[5], c0); ldfrag<4096>(bb[6], c1); ldfrag<6144>(bb[7], c1);
    project_end(bb);
}

__global__ __launch_bounds__(512, 2) B3ATTR void mega_kernel(Args a) {
    char* memL = smem;
    char* tokL = smem + L_TOK;
    char* hL = smem + L_H;
    int w = __builtin_amdgcn_readfirstlane(threadIdx.x >> 6);
    __amdgpu_buffer_rsrc_t rwin = mkrsrc(a.win, W_END);
    char* ws = a.ws;
    __amdgpu_buffer_rsrc_t rws = mkrsrc(ws, (unsigned)OFF_WIN);
    auto fresh_rsrc = [&]() INL { ws = (char*)arg(0); rws = mkrsrc(ws, (unsigned)OFF_WIN); rwin = mkrsrc(arg(1), W_END); };
    const char* pc;
    asm volatile("s_getpc_b64 %0" : "=s"(pc));
    unsigned fs[12] = {};
    const u32x2 tg0 = __builtin_amdgcn_raw_buffer_load_b64(rws, 0u, 0u, 16);
    u64 tag;
    u32x2 rot0;
    {
        const int tid = threadIdx.x;
        if (blockIdx.x < 64) {
            const unsigned j = blockIdx.x;
            if (j == 0 && w == 0) __builtin_amdgcn_raw_buffer_store_b64(__builtin_bit_cast(u32x2, (__builtin_bit_cast(u64, tg0) & ~15ull) + 16ull + (xcc_id() & 7u)), rws, LANE0, 128u, 16);
#pragma unroll
            for (int i = 0; i < 6; ++i) st128(SENT, mkrsrc(ws + O_ARM, O_H + NHF * 40960u - O_ARM), (j * 512u + tid) * 16u + i * 524288u, 0, 16);
#pragma unroll
            for (int i = 0; i < 7; ++i) {
                const unsigned g = (j * 512u + tid + i * 32768u) * 16u;
                st128(SENT, rwin, g + (g < W_ARM ? 0u : W_ZM - W_ARM), 0, 16);
            }
            static_assert(W_ARM + W_END - W_ZM <= 7u * 32768u * 16u && O_ARM + 6u * 524288u >= O_H + NHF * 40960u, "arming");
        } else if (blockIdx.x >= 128) {
            RowTw tw;
            row_tw(tw, tid);
            a1_unit<8, false>(mkrsrc(a.x, S_ * D_ * 2), (blockIdx.x - 128) * 16, rws, rwin, tw, tid, fs);
        } else {
            const unsigned j = blockIdx.x - 64;
            u32x4 v[6];
#pragma unroll
            for (int i = 0; i < 6; ++i) {
                const int id = min(tid + 512 * i, 8 * DF - 1);
                v[i] = ((const u32x4*)((id < 4 * DF ? a.wr : a.wi) + (size_t)j * 32 * DF))[id < 4 * DF ? id : id - 4 * DF];
            }
            u16 n[4];
            if (w == 7) {
                const size_t kn = (size_t)((S_ - j * 32 - (tid & 31)) & (S_ - 1)) * DF;
                n[0] = a.wr[kn]; n[1] = a.wi[kn]; n[2] = a.wr[kn + 320]; n[3] = a.wi[kn + 320];
            }
            const unsigned wo = WARM ? (w * 8u + (j >> 3)) * 8192u : 0x40000000u;
            const unsigned x0 = __builtin_amdgcn_raw_buffer_load_b32(mkrsrc(pc, CODE_BYTES), (tid & 63) * 128u, wo, 0), x1 = __builtin_amdgcn_raw_buffer_load_b32(mkrsrc(&TWC, sizeof(TwCol)), (tid & 63) * 128u, wo, 0);
#pragma unroll
            for (int i = 0; i < 6; ++i) ((u32x4*)smem)[min(tid + 512 * i, 8 * DF - 1)] = v[i];
            BAR();
#pragma unroll
            for (int i = 0; i < 5; ++i) {
                const int id = tid + 512 * i, kp = id >> 4;
                const u16* q = (const u16*)smem + (id & 15) * 2 * DF + 2 * kp;
                u32x4 o;
#pragma unroll
                for (int e = 0; e < 4; ++e) o[e] = (unsigned)q[(e >> 1) * DF + (e & 1)] | ((unsigned)q[(32 + (e >> 1)) * DF + (e & 1)] << 16);
                st128(o, rws, kp * WST + (id & 15) * 16u, O_WFT + j * 256u, 16);
            }
            if (w == 7) {
                const u16* q = (const u16*)smem + (tid & 31) * DF;
                const unsigned k = j * 32u + (tid & 31);
                const c2 h0 = (c2){bf2f(q[0]) + bf2f(n[0]), bf2f(q[32 * DF]) - bf2f(n[1])} * bc(0.25f * FSC), h1 = (c2){bf2f(q[320]) + bf2f(n[2]), bf2f(q[32 * DF + 320]) - bf2f(n[3])} * bc(0.25f * FSC);
                st128(__builtin_bit_cast(u32x4, (f32x4){h0.x + h1.x, h0.y + h1.y, h0.x - h1.x, h0.y - h1.y}), rws, (((k >> 8) & 6u) * 256u + (k & 64u) * 4u + (k & 7u) * 32u + ((k >> 4) & 24u) + ((k >> 3) & 7u)) * 16u, O_WFT + NCOL * WST, 16);
            }
            asm volatile("" :: "v"(x0), "v"(x1));
        }
        rot0 = __builtin_amdgcn_raw_buffer_load_b64(rws, 0u, 128u, 16);
        VMWAIT();
        FST(8);
        BAR0();
        FST(9);
    }
    tag = (((u64)__builtin_amdgcn_readfirstlane(tg0[1]) << 32) | (__builtin_amdgcn_readfirstlane(tg0[0]) & ~15u)) + 16ull;
    if (w == 0) __builtin_amdgcn_raw_buffer_store_b64(__builtin_bit_cast(u32x2, tag + 8ull), rwin, LANE0, W_A1 + blockIdx.x * 128u, 16);
    if (blockIdx.x >= 128) FSW(0);
    unsigned vx;
    int bid;
    auto set_role = [&](unsigned x0) INL {
        vx = x0;
        const int j = blockIdx.x >> 3, x = vx & 3;
        if (!(vx & 4)) bid = j < 8 ? 8 * x + j : j < 16 ? 24 + 8 * x + j : j < 26 ? 80 + 10 * x + j : j < 30 ? NWRK + 4 * (j - 26) + x : NXT - 24 + 8 * x + j;
        else bid = j < 8 ? 64 + 8 * x + j : j < 16 ? NQT - 8 + 8 * x + j : j < 26 ? NREC - 16 + 10 * x + j : NXT - 26 + 8 * x + j;
    };
    auto get_rot = [&]() INL {
        unsigned r = 0;
        for (unsigned spins = 0; spins < SPIN_CAP; ++spins) {
            const u64 x = __builtin_bit_cast(u64, rot0) - tag;
            r = __builtin_amdgcn_readfirstlane((unsigned)x);
            if (__all(x < 8ull)) break;
            __builtin_amdgcn_s_sleep(2);
            asm volatile("" ::: "memory");
            rot0 = __builtin_amdgcn_raw_buffer_load_b64(rws, 0u, 128u, 16);
        }
        return (r + blockIdx.x + 4u * DIE) & 7u;
    };
    set_role(blockIdx.x < 64 ? (xcc_id() ^ (4u * DIE)) & 7u : get_rot());
    unsigned long long tref = 0;
    int layer = 0;
    unsigned fo = 0;
    unsigned nh = 0, nz = 0, mc = 0, qz = 0, mo = 0, mo1 = 0, tro = 0;
    auto near_init = [&]() INL {
        unsigned f;
        for (unsigned spins = 0;;) {
            asm volatile("" ::: "memory");
            f = __builtin_amdgcn_raw_buffer_load_b32(rwin, (fresh_lane() & 7u) * 4u, W_NEAR, 16);
            if (!__any(f == ~0u) || spin_fail(spins, rwin)) break;
            __builtin_amdgcn_s_sleep(8);
        }
        const unsigned h = (unsigned)__ballot(f & 1u), q = (unsigned)__ballot(f & 2u) ^ 0xAAu;
        auto quad = [&](unsigned m) { return (3u * (m & 1u) + 2u * __builtin_popcount(m & 14u) + 3u) >> 3; };
        nh = (h & 1u) * HALF; nz = ((h >> 4) & 1u) * HALF;
        mc = (vx & 3u) ^ quad(q);
        qz = quad(q >> 4);
        mo = W_MEM + mc * PAIR + nh; mo1 = W_MEM + (4u + (mc & 1u)) * PAIR + nh + (mc >> 1) * 2048u;
        tro = W_T + (mc & 1u) * PAIR + nh + (mc >> 1) * 4096u;
    };
    auto zpair = [&](unsigned x, unsigned k) { return (((x >> 1) * 10u + k) * 2u + ((x & 1u) ^ qz)) * PAIR + nz; };
    auto two = [&]() { const unsigned l = fresh_lane(); return l < 4 ? (l >> 1) * PAIR + (l & 1) * 4096u + nh : 0x40000000u; };
    const f32x4 z4 = {0.f, 0.f, 0.f, 0.f};
    int hrdy = 0, budget = (int)SPIN_CAP;
    unsigned fc0 = 0, fc1 = 0, fc2 = 0;
    Clk ck = {0u, 0u, 1600u, 10486u};
    SST(5);
#define xt (bid >= NXT && bid < NWRK)
#define qx (bid >= NQT && bid < NWRK)
#define xq (bid >= NREC && bid < NQT && (bid - NREC) % 10 < 8)
    unsigned tpp = 0;
    int lt = LT1, xs = XS0;
    struct Tk { unsigned vo, tw, sl; const LAS char* sp; };
    auto tk_init = [&]() {
        const unsigned l = fresh_lane();
        Tk k = {l * 16u, two(), (l & 31u) * 128u + (l >> 5) * 4u, (const LAS char*)(memL + l * 64 + 4 + ((l >> 1) & 7) * 8)};
        PIN2(k.vo, k.tw); PIN2(k.sl, k.sp);
        return k;
    };
    auto sched2 = [&](Tk& k, int s, unsigned f) INL {
        const unsigned g0 = tpp - 0x400000u;
        tpp = g0 + (unsigned)read_sched(rwin, s, k.sl, tro + (unsigned)(s & 3) * 2u * PAIR, f, g0, lt, xs, fc1);
        k.sl &= ~4u;
        RT(6);
    };
    auto sched = [&](Tk& k, int s) INL { sched2(k, s, __builtin_amdgcn_raw_buffer_load_b32(rwin, k.sl, tro + (unsigned)(s & 3) * 2u * PAIR, 16)); };
    unsigned it = 0;
    auto fetch = [&](const Tk& k, int s, bool first, unsigned lim, unsigned dc, unsigned fo, bool tell) INL {
        const unsigned sb = (unsigned)(s & 3) * 6u * PAIR, ta0 = ck.ta, mm0 = ck.ma;
        if (__builtin_expect(s > 0, 1)) {
            if (__builtin_expect(first, 1)) { const unsigned te = cyc_at(ck, sx24(tpp - TKE - ck.ta)); while ((int)((unsigned)__builtin_amdgcn_s_memtime() - te) < 0) __builtin_amdgcn_s_sleep(1); }
            if (__builtin_expect(first, 1)) gather_at(tpp, ck, rwin, k.vo, mo + sb, mo1 + sb); else { const unsigned c = (mc + 1u + (it & 1u)) & 3u; gather_dma(rwin, k.vo, W_MEM + c * PAIR + nh + sb, W_MEM + (4u + (c & 1u)) * PAIR + nh + (c >> 1) * 2048u + sb); }
        }
        __builtin_amdgcn_s_setprio(0);
        RT(1);
        clk_rate(ck, ta0, mm0);
        unsigned tb = ck.ma - dc, tg = (unsigned)s << 24, so = W_T + (unsigned)(s & 3) * 2u * PAIR + fo;
        asm volatile("" : "+s"(tb), "+s"(tg), "+s"(so));
        asm volatile("s_waitcnt vmcnt(%1) lgkmcnt(0)\n\ts_barrier\n\ts_waitcnt vmcnt(%2)\n\ts_barrier\n\ts_waitcnt vmcnt(0)\n\ts_memtime %0\n\ts_barrier"
                     : "=s"(tref) : "n"(10 - NB1), "n"(10 - NB2) : "memory");
        MT(1);
        if (!tell) { RT(3); return; }
        unsigned x0 = *(const LAS unsigned*)k.sp, x1 = *(const LAS unsigned*)(k.sp + 4096), x2 = *(const LAS unsigned*)(k.sp + 6144);
        __builtin_amdgcn_s_waitcnt(0xC07F);
        asm volatile("" : "+s"(tref) :: "memory");
        const unsigned fw = ((ck.ta + ((((unsigned)tref - tb) * ck.qpc) >> 16)) & 0xFFFFFFu) | tg;
        __builtin_amdgcn_raw_buffer_store_b32(fw, rwin, k.tw, __any(max(max(x0, x1), x2) > lim) ? 0x40000000u : so, 16);
        RT(3);
    };
    auto rout = [&]() { return mkrsrc(layer ? arg(A_OUT) : (const u16*)(ws + OFF_X2), S_ * D_ * 2); };
    auto mytid = [&]() { return w * 64 + (int)fresh_lane(); };
    auto new_layer = [&]() { fo = (unsigned)layer * W_LAYER; asm volatile("" : "+s"(fo), "+s"(w), "+s"(bid), "+s"(nh), "+s"(nz), "+s"(mc), "+s"(qz), "+s"(tag)); hrdy = 0; tpp = 0; lt = LT1; xs = XS0; };
    auto load_qv = [&](u32x8 (&wt)[2][10], int hh, bool v) INL {
        const bool o = v && w >= 4;
        load_w(wt, arg(A_WQ + 2 * v + (w >> 2)) + (o ? (hh * 128 + (w & 3) * 32) * D_ : hh * 128 + (w & 3) * 16 * HD), o ? D_ * 2 : HD * 2, !o, o ? 16 * D_ * 2 : 64 * HD * 2, o ? 256 : 128 * HD * 2, w);
    };
    auto hseg = [&](int j) { return O_H + (unsigned)(j - NHF * layer) * 40960u; };
    auto stage2 = [&](int j0, int j1, bool vt) INL {
        if (w == 0) {
            if (j1 >= NHF) wait_h(rwin, fo, j1, hrdy);
            else for (unsigned spins = 0;;) {
                asm volatile("" ::: "memory");
                const unsigned l = fresh_lane();
                if (!__any(__builtin_amdgcn_raw_buffer_load_b32(rws, (l & 31u) * 128u + (l >> 5) * (unsigned)(j1 - j0) * 40960u, hseg(j0) + 36864u, 16) == ~0u) || spin_fail(spins, rwin)) break;
                __builtin_amdgcn_s_sleep(HSLP);
            }
            *(volatile LAS int*)(smem + L_S + 32) = hrdy;
        }
        BAR();
        hrdy = __builtin_amdgcn_readfirstlane(*(volatile LAS int*)(smem + L_S + 32));
        SST(2);
        if (j0 == 2) __builtin_amdgcn_s_sleep(DH2);
        if (j0 == 1 && vt) __builtin_amdgcn_s_sleep(DH1);
        stage_h<5>(rws, hseg(j0) + w * 1024u, L_H + w * 1024u, 8192u);
        if (!vt) {
            stage_h<5>(rws, hseg(j1) + w * 1024u, L_H + 40960u + w * 1024u, 8192u);
            asm volatile("s_waitcnt vmcnt(5)" ::: "memory");
        } else if (w >= 4 && j1 != j0) {
            stage_h<10>(rws, hseg(j1) + (w - 4) * 1024u, L_H + 40960u + (w - 4) * 1024u, 4096u);
            asm volatile("s_waitcnt vmcnt(10)" ::: "memory");
        } else VMWAIT();
        check_h<5>(rws, rwin, hseg(j0) + w * 1024u, L_H + w * 1024u, 8192u);
        BAR0();
        SST(3);
        if (vt && w >= 4 && j1 != j0) {
            VMWAIT();
            check_h<10>(rws, rwin, hseg(j1) + (w - 4) * 1024u, L_H + 40960u + (w - 4) * 1024u, 4096u);
        }
    };
#define WO(eb, nt) wt[(nt) & 1][((nt) & ~1) + (eb)]
    unsigned zb = W_ZM;
    auto vo_start = [&](u32x8 (&wt)[2][10]) INL {
        const int hh = bid & 31;
        if ((bid & 7) < 2) {
            unsigned* best = (unsigned*)smem;
            const int tid = mytid();
            const __amdgpu_buffer_rsrc_t rw = mkrsrc(a.win + (bid & 1 ? W_END : W_PROBE), 2 * PAIR);
            const unsigned base = (unsigned)(hh >> 3) * 128u + w * 896u;
            if (tid < 4) best[tid] = 0u;
            BAR();
            unsigned mn[4] = {~0u, ~0u, ~0u, ~0u};
            for (int r = 0; r < NPR; ++r) {
#pragma unroll
                for (int i = 0; i < 4; ++i) __builtin_amdgcn_raw_buffer_store_b32(0u, rw, base + i * HALF, 0, 16);
                asm volatile("s_waitcnt vmcnt(0)" ::: "memory");
#pragma unroll
                for (int i = 0; i < 4; ++i) {
                    const unsigned long long t1 = __builtin_amdgcn_s_memtime();
                    asm volatile("s_waitcnt lgkmcnt(0)" ::: "memory");
                    unsigned v = __builtin_amdgcn_raw_buffer_load_b32(rw, base + i * HALF, 0, 16);
                    asm volatile("s_waitcnt vmcnt(0)" : "+v"(v) :: "memory");
                    const unsigned long long t2 = __builtin_amdgcn_s_memtime();
                    mn[i] = min(mn[i], (unsigned)(t2 - t1) + v);
                }
            }
            if ((tid & 63) == 0) {
#pragma unroll
                for (int i = 0; i < 4; ++i) atomicAdd(&best[i], min(mn[i], 1000u));
            }
            if (w == 0) wait_tags(rwin, 0, 1, 64u, tag + 8ull, true);
            BAR();
            if (w == 0) __builtin_amdgcn_raw_buffer_store_b32(min(best[1], best[3]) < min(best[0], best[2]) ? (best[3] < best[1] ? 3u : 1u) : (best[2] < best[0] ? 2u : 0u), rwin, LANE0, W_NEAR + (unsigned)((hh >> 3) + (bid & 1) * 4) * 4u, 16);
            BAR();
        } else wait_until(__builtin_amdgcn_s_memtime() + DVO);
        load_qv(wt, hh, true);
        SST(1);
        if (w == 0) wait_tags(rwin, 0, 1, 64u, tag + 8ull, true);
        BAR();
        near_init();
    };
    auto vo_role = [&](const u32x8 (&wt)[2][10], int ph) INL {
        const int hh = bid & 31;
        auto steps = [&](auto cc) INL {
            constexpr int C = decltype(cc)::v & 1;
            constexpr bool TK = decltype(cc)::v == 3;
            LANES();
            Tk tk = {0u, 0u, 0x40000000u, nullptr};
            if (TK) { tk = tk_init(); __builtin_amdgcn_s_setprio(TKP); }
            const unsigned zi = (unsigned)(hh * 128 + fr * 8) * 2u;
            unsigned zo0 = zpair(w & 3, fq) + zi, zo1 = zo0 + 8 * PAIR, zo2 = zpair(w & 3, 8 + (fq >> 1)) + zi + (fq & 1) * 8;
            const LAS char* va = (const LAS char*)(smem + L_Q + (fr & 1) * 544 + ((fr >> 1) & 1) * 1152 + (16 * fq + 2 * (fr >> 2)) * 4);
            LAS char* vw = (LAS char*)(smem + L_Q + (fq & 1) * 544 + (fq >> 1) * 1152 + (16 * w + bs2(fr)) * 4);
            const LAS char* mp0 = (const LAS char*)(memL + ma0);
            PIN2(zo0, zo1); PIN1(zo2);
            if (C == 0) { PIN2(mp0, vw); } else PIN1(va);
            u32x4 za = SENT, zc = SENT;
            u32x2 ze = {0u, 0u};
            auto put_z2 = [&](int s) {
                const unsigned z2b = s < NSEG ? O_Z2 + (unsigned)s * (NOWN * 8192u) + (unsigned)(10 * (w & 3) * 8192 + hh * 256) : 0x40000000u, l2 = fresh_lane();
                st128(za, rws, (l2 >> 4) * 8192u + (l2 & 15u) * 16u, z2b, 16);
                st128(zc, rws, (l2 >> 4) * 8192u + (l2 & 15u) * 16u, z2b + 4u * 8192u, 16);
                __builtin_amdgcn_raw_buffer_store_b64(ze, rws, (l2 >> 5) * 8192u + (l2 & 15u) * 16u + ((l2 >> 4) & 1u) * 8u, z2b + 8u * 8192u, 16);
            };
            if (TK && ph) sched(tk, 0);
            for (int s = ph; s < (ph ? NSEG : 1); ++s) {
                RT(0);
                bool redo = false;
                unsigned sf = 0;
                do {
                    ++it;
                    if (C == 0) {
                        const unsigned lim = s > 0 ? poll_lim(budget) : ~0u;
                        asm volatile(
                            "v_mov_b64 v[200:201], 0\n\t"
                            "v_mov_b64 v[202:203], 0\n\t"
                            "v_mov_b64 v[204:205], 0\n\t"
                            "v_mov_b64 v[206:207], 0\n\t"
                            "v_mov_b32 v208, 0\n\t"
                            "s_setprio 2\n\t"
                            "s_waitcnt lgkmcnt(0)\n\t"
                            "s_barrier\n\t"
                            MTA(0)
                            "ds_read_b128 v[212:215], %[mp] offset:0\n\t"
                            "ds_read_b128 v[216:219], %[mp] offset:1024\n\t"
                            "ds_read_b128 v[220:223], %[mp] offset:2048\n\t"
                            "s_barrier\n\t"
                            "ds_read_b128 v[224:227], %[mp] offset:3072\n\t"
                            "ds_read_b128 v[228:231], %[mp] offset:4096\n\t"
                            "ds_read_b128 v[232:235], %[mp] offset:5120\n\t"
                            "s_waitcnt lgkmcnt(5)\n\t"
                            "v_smfmac_f32_16x16x64_bf16 v[200:203], v[212:215], %[w00], %[ix]\n\t"
                            "s_waitcnt lgkmcnt(4)\n\t"
                            "v_smfmac_f32_16x16x64_bf16 v[200:203], v[216:219], %[w01], %[ix]\n\t"
                            "s_barrier\n\t"
                            MTA(1)
                            "ds_read_b128 v[236:239], %[mp] offset:6144\n\t"
                            "s_waitcnt lgkmcnt(4)\n\t"
                            "v_smfmac_f32_16x16x64_bf16 v[200:203], v[220:223], %[w02], %[ix]\n\t"
                            "ds_read_b128 v[240:243], %[mp] offset:7168\n\t"
                            "s_waitcnt lgkmcnt(4)\n\t"
                            "v_smfmac_f32_16x16x64_bf16 v[200:203], v[224:227], %[w03], %[ix]\n\t"
                            "ds_read_b128 v[244:247], %[mp] offset:8192\n\t"
                            "s_waitcnt lgkmcnt(4)\n\t"
                            "v_smfmac_f32_16x16x64_bf16 v[200:203], v[228:231], %[w04], %[ix]\n\t"
                            "ds_read_b128 v[248:251], %[mp] offset:9216\n\t"
                            "s_waitcnt lgkmcnt(4)\n\t"
                            "v_smfmac_f32_16x16x64_bf16 v[200:203], v[232:235], %[w05], %[ix]\n\t"
                            "s_waitcnt lgkmcnt(3)\n\t"
                            "v_smfmac_f32_16x16x64_bf16 v[200:203], v[236:239], %[w06], %[ix]\n\t"
                            "s_waitcnt lgkmcnt(2)\n\t"
                            "v_smfmac_f32_16x16x64_bf16 v[200:203], v[240:243], %[w07], %[ix]\n\t"
                            "s_waitcnt lgkmcnt(1)\n\t"
                            "v_smfmac_f32_16x16x64_bf16 v[200:203], v[244:247], %[w08], %[ix]\n\t"
                            "s_waitcnt lgkmcnt(0)\n\t"
                            "v_smfmac_f32_16x16x64_bf16 v[200:203], v[248:251], %[w09], %[ix]\n\t"
                            "s_nop 2\n\t"
                            "v_smfmac_f32_16x16x64_bf16 v[204:207], v[212:215], %[w10], %[ix]\n\t"
                            "s_nop 2\n\t"
                            "v_smfmac_f32_16x16x64_bf16 v[204:207], v[216:219], %[w11], %[ix]\n\t"
                            "v_add_f32 v200, v200, v202\n\t"
                            "v_add_f32 v201, v201, v203\n\t"
                            "v_cvt_pk_bf16_f32 v200, v200, v201\n\t"
                            "ds_write_b32 %[vw], v200\n\t"
                            MTA(2)
                            "v_smfmac_f32_16x16x64_bf16 v[204:207], v[220:223], %[w12], %[ix]\n\t"
                            "v_max3_u32 v208, v208, v212, v213\n\t"
                            "v_max3_u32 v208, v208, v214, v215\n\t"
                            "v_smfmac_f32_16x16x64_bf16 v[204:207], v[224:227], %[w13], %[ix]\n\t"
                            "v_max3_u32 v208, v208, v216, v217\n\t"
                            "v_max3_u32 v208, v208, v218, v219\n\t"
                            "v_smfmac_f32_16x16x64_bf16 v[204:207], v[228:231], %[w14], %[ix]\n\t"
                            "v_max3_u32 v208, v208, v220, v221\n\t"
                            "v_max3_u32 v208, v208, v222, v223\n\t"
                            "v_smfmac_f32_16x16x64_bf16 v[204:207], v[232:235], %[w15], %[ix]\n\t"
                            "v_max3_u32 v208, v208, v224, v225\n\t"
                            "v_max3_u32 v208, v208, v226, v227\n\t"
                            "v_smfmac_f32_16x16x64_bf16 v[204:207], v[236:239], %[w16], %[ix]\n\t"
                            "v_max3_u32 v208, v208, v228, v229\n\t"
                            "v_max3_u32 v208, v208, v230, v231\n\t"
                            "v_smfmac_f32_16x16x64_bf16 v[204:207], v[240:243], %[w17], %[ix]\n\t"
                            "s_waitcnt lgkmcnt(0)\n\t"
                            "s_barrier\n\t"
                            MTA(5)
                            "v_smfmac_f32_16x16x64_bf16 v[204:207], v[244:247], %[w18], %[ix]\n\t"
                            "v_max3_u32 v208, v208, v232, v233\n\t"
                            "v_max3_u32 v208, v208, v234, v235\n\t"
                            "v_smfmac_f32_16x16x64_bf16 v[204:207], v[248:251], %[w19], %[ix]\n\t"
                            "v_max3_u32 v208, v208, v236, v237\n\t"
                            "v_max3_u32 v208, v208, v238, v239\n\t"
                            "v_max3_u32 v208, v208, v240, v241\n\t"
                            "v_max3_u32 v208, v208, v242, v243\n\t"
                            "v_max3_u32 v208, v208, v244, v245\n\t"
                            "v_max3_u32 v208, v208, v246, v247\n\t"
                            "v_max3_u32 v208, v208, v248, v249\n\t"
                            "v_max3_u32 v208, v208, v250, v251\n\t"
                            "v_cmp_lt_u32 vcc, %[lim], v208\n\t"
                            "v_add_f32 v204, v204, v206\n\t"
                            "v_add_f32 v205, v205, v207\n\t"
                            "v_cvt_pk_bf16_f32 v204, v204, v205\n\t"
                            "ds_write_b32 %[vw], v204 offset:256\n\t"
                            MTA(3)
                            "s_cbranch_vccz 1f\n\t"
                            "v_mov_b32 v208, %[it]\n\t"
                            "v_mov_b32 v209, 0\n\t"
                            "ds_write_b32 v209, v208 offset:%[ls]\n\t"
                            "1:\n\t"
                            "s_waitcnt vmcnt(0) lgkmcnt(0)\n\t"
                            "s_barrier\n\t"
                            "s_setprio 0\n\t"
                            :: [mp] "v"(mp0), [vw] "v"(vw), [ix] "v"(sidx), [lim] "s"(lim), [it] "s"(it), [ls] "n"(L_S),
                              [w00] "v"(wt[0][0]), [w01] "v"(wt[0][1]), [w02] "v"(wt[0][2]), [w03] "v"(wt[0][3]), [w04] "v"(wt[0][4]), [w05] "v"(wt[0][5]), [w06] "v"(wt[0][6]), [w07] "v"(wt[0][7]), [w08] "v"(wt[0][8]), [w09] "v"(wt[0][9]), [w10] "v"(wt[1][0]), [w11] "v"(wt[1][1]), [w12] "v"(wt[1][2]), [w13] "v"(wt[1][3]), [w14] "v"(wt[1][4]), [w15] "v"(wt[1][5]), [w16] "v"(wt[1][6]), [w17] "v"(wt[1][7]), [w18] "v"(wt[1][8]), [w19] "v"(wt[1][9])
                            : "v200", "v201", "v202", "v203", "v204", "v205", "v206", "v207", "v208", "v209", "v212", "v213", "v214", "v215", "v216", "v217", "v218", "v219", "v220", "v221", "v222", "v223", "v224", "v225", "v226", "v227", "v228", "v229", "v230", "v231", "v232", "v233", "v234", "v235", "v236", "v237", "v238", "v239", "v240", "v241", "v242", "v243", "v244", "v245", "v246", "v247", "v248", "v249", "v250", "v251", "vcc", "memory");
                        MT(4);
                        redo = __builtin_amdgcn_readfirstlane(*(volatile LAS unsigned*)(smem + L_S)) == it;
                        if (redo) FC(fc0);
                    } else {
                        f32x16 ca, cb;
                        f32x8 cc;
                        asm volatile("v_mov_b64 v[212:213], 0\n\tv_mov_b64 v[214:215], 0\n\tv_mov_b64 v[216:217], 0\n\tv_mov_b64 v[218:219], 0\n\tv_mov_b64 v[220:221], 0\n\tv_mov_b64 v[222:223], 0\n\tv_mov_b64 v[224:225], 0\n\tv_mov_b64 v[226:227], 0\n\tv_mov_b64 v[228:229], 0\n\tv_mov_b64 v[230:231], 0\n\tv_mov_b64 v[232:233], 0\n\tv_mov_b64 v[234:235], 0\n\tv_mov_b64 v[236:237], 0\n\tv_mov_b64 v[238:239], 0\n\tv_mov_b64 v[240:241], 0\n\tv_mov_b64 v[242:243], 0\n\tv_mov_b64 v[244:245], 0\n\tv_mov_b64 v[246:247], 0\n\tv_mov_b64 v[248:249], 0\n\tv_mov_b64 v[250:251], 0\n\t" : "={v[212:227]}"(ca), "={v[228:243]}"(cb), "={v[244:251]}"(cc));
                        unsigned sr = tro + (unsigned)(s & 3) * 2u * PAIR;
                        asm volatile("" : "+s"(sr));
                        if (TK) fetch(tk, s, !redo, s > 0 ? poll_lim(budget) : ~0u, 0u, 0u, false);
                        else asm volatile("s_waitcnt lgkmcnt(0)\n\ts_barrier\n\ts_barrier\n\ts_barrier" ::: "memory");
                        unsigned vd, vv, zs = zb;
                        u32x4 af1;
                        asm volatile(
                            "s_setprio 1\n\t"
                            "s_waitcnt lgkmcnt(0)\n\t"
                            "s_barrier\n\t"
                            "ds_read_b64_tr_b16 v[192:193], %[va]\n\t"
                            "ds_read_b64_tr_b16 v[194:195], %[va] offset:32\n\t"
                            "s_waitcnt lgkmcnt(0)\n\t"
                            "v_smfmac_f32_16x16x64_bf16 v[212:215], v[192:195], %[w0], %[ix]\n\t"
                            "v_smfmac_f32_16x16x64_bf16 v[220:223], v[192:195], %[w2], %[ix]\n\t"
                            "v_smfmac_f32_16x16x64_bf16 v[216:219], v[192:195], %[w1], %[ix]\n\t"
                            "v_smfmac_f32_16x16x64_bf16 v[224:227], v[192:195], %[w3], %[ix]\n\t"
                            "s_barrier\n\t"
                            MTA(4)
                            "ds_read_b64_tr_b16 v[196:197], %[va] offset:256\n\t"
                            "v_smfmac_f32_16x16x64_bf16 v[228:231], v[192:195], %[w4], %[ix]\n\t"
                            "ds_read_b64_tr_b16 v[198:199], %[va] offset:288\n\t"
                            "v_smfmac_f32_16x16x64_bf16 v[236:239], v[192:195], %[w6], %[ix]\n\t"
                            "ds_read_b32 v210, %[vz] offset:%[ls]\n\t"
                            "v_smfmac_f32_16x16x64_bf16 v[232:235], v[192:195], %[w5], %[ix]\n\t"
                            "v_smfmac_f32_16x16x64_bf16 v[240:243], v[192:195], %[w7], %[ix]\n\t"
                            "v_smfmac_f32_16x16x64_bf16 v[244:247], v[192:195], %[w8], %[ix]\n\t"
                            "v_smfmac_f32_16x16x64_bf16 v[248:251], v[192:195], %[w9], %[ix]\n\t"
                            "buffer_load_dword %[sf], %[sl], %[rw], %[sr] offen sc1\n\t"
                            : [sf] "=&v"(sf), "+{v[212:227]}"(ca), "+{v[228:243]}"(cb), "+{v[244:251]}"(cc), "=&{v[196:199]}"(af1), "=&{v210}"(vv)
                            : [va] "v"(va), [vz] "v"(0u), [ls] "n"(L_S), [ix] "v"(sidx), [sl] "v"(tk.sl), [sr] "s"(sr), [rw] "s"(rwin),
                              [w0] "v"(WO(0, 0)), [w1] "v"(WO(0, 1)), [w2] "v"(WO(0, 2)), [w3] "v"(WO(0, 3)), [w4] "v"(WO(0, 4)), [w5] "v"(WO(0, 5)), [w6] "v"(WO(0, 6)), [w7] "v"(WO(0, 7)), [w8] "v"(WO(0, 8)), [w9] "v"(WO(0, 9))
                            : "v192", "v193", "v194", "v195", "memory");
                        asm volatile(
                            "s_waitcnt lgkmcnt(0)\n\t"
                            "v_smfmac_f32_16x16x64_bf16 v[212:215], v[196:199], %[w0], %[ix]\n\t"
                            "v_readfirstlane_b32 %[vd], v210\n\t"
                            "v_smfmac_f32_16x16x64_bf16 v[220:223], v[196:199], %[w2], %[ix]\n\t"
                            "s_cmp_eq_u32 %[vd], %[it]\n\t"
                            "s_cselect_b32 %[zs], 2.0, %[zs]\n\t"
                            "v_smfmac_f32_16x16x64_bf16 v[216:219], v[196:199], %[w1], %[ix]\n\t"
                            "s_nop 1\n\t"
                            "v_smfmac_f32_16x16x64_bf16 v[224:227], v[196:199], %[w3], %[ix]\n\t"
                            "v_add_f32 v212, v212, v214\n\t"
                            "v_smfmac_f32_16x16x64_bf16 v[228:231], v[196:199], %[w4], %[ix]\n\t"
                            "v_add_f32 v213, v213, v215\n\t"
                            "v_smfmac_f32_16x16x64_bf16 v[236:239], v[196:199], %[w6], %[ix]\n\t"
                            "v_add_f32 v220, v220, v222\n\t"
                            "v_smfmac_f32_16x16x64_bf16 v[232:235], v[196:199], %[w5], %[ix]\n\t"
                            "v_add_f32 v221, v221, v223\n\t"
                            "v_smfmac_f32_16x16x64_bf16 v[240:243], v[196:199], %[w7], %[ix]\n\t"
                            "v_cvt_pk_bf16_f32 v200, v212, v213\n\t"
                            "v_smfmac_f32_16x16x64_bf16 v[244:247], v[196:199], %[w8], %[ix]\n\t"
                            "v_cvt_pk_bf16_f32 v201, v220, v221\n\t"
                            "v_smfmac_f32_16x16x64_bf16 v[248:251], v[196:199], %[w9], %[ix]\n\t"
                            "v_add_f32 v216, v216, v218\n\t"
                            "v_add_f32 v217, v217, v219\n\t"
                            "v_add_f32 v224, v224, v226\n\t"
                            "v_add_f32 v225, v225, v227\n\t"
                            "v_cvt_pk_bf16_f32 v202, v216, v217\n\t"
                            "v_cvt_pk_bf16_f32 v203, v224, v225\n\t"
                            "v_permlane32_swap_b32 v200, v201\n\t"
                            "v_pk_add_f32 v[228:229], v[228:229], v[230:231]\n\t"
                            "v_permlane32_swap_b32 v202, v203\n\t"
                            "v_min_u32 v201, -2, v201\n\t"
                            "v_pk_add_f32 v[236:237], v[236:237], v[238:239]\n\t"
                            "v_min_u32 v203, -2, v203\n\t"
                            "v_permlane16_swap_b32 v200, v202\n\t"
                            "v_pk_add_f32 v[232:233], v[232:233], v[234:235]\n\t"
                            "v_permlane16_swap_b32 v201, v203\n\t"
                            "v_pk_add_f32 v[240:241], v[240:241], v[242:243]\n\t"
                            MTA(5)
                            "buffer_store_dwordx4 v[200:203], %[z0], %[rw], %[zs] offen sc1\n\t"
                            "v_cvt_pk_bf16_f32 v204, v228, v229\n\t"
                            "v_cvt_pk_bf16_f32 v205, v236, v237\n\t"
                            "v_cvt_pk_bf16_f32 v206, v232, v233\n\t"
                            "v_cvt_pk_bf16_f32 v207, v240, v241\n\t"
                            "v_permlane32_swap_b32 v204, v205\n\t"
                            "v_pk_add_f32 v[244:245], v[244:245], v[246:247]\n\t"
                            "v_permlane32_swap_b32 v206, v207\n\t"
                            "v_min_u32 v205, -2, v205\n\t"
                            "v_pk_add_f32 v[248:249], v[248:249], v[250:251]\n\t"
                            "v_min_u32 v207, -2, v207\n\t"
                            "v_permlane16_swap_b32 v204, v206\n\t"
                            "v_cvt_pk_bf16_f32 v208, v244, v245\n\t"
                            "v_permlane16_swap_b32 v205, v207\n\t"
                            "v_cvt_pk_bf16_f32 v209, v248, v249\n\t"
                            "buffer_store_dwordx4 v[204:207], %[z1], %[rw], %[zs] offen sc1\n\t"
                            "s_nop 0\n\t"
                            "v_permlane32_swap_b32 v208, v209\n\t"
                            "v_min_u32 v209, -2, v209\n\t"
                            "buffer_store_dwordx2 v[208:209], %[z2], %[rw], %[zs] offen sc1\n\t"
                            : [vd] "=&s"(vd), [zs] "+s"(zs), "=&{v[200:203]}"(za), "=&{v[204:207]}"(zc), "=&{v[208:209]}"(ze), "+{v[212:227]}"(ca), "+{v[228:243]}"(cb), "+{v[244:251]}"(cc), "+v"(sf)
                            : [ix] "v"(sidx), [it] "s"(it), [rw] "s"(rwin), [z0] "v"(zo0), [z1] "v"(zo1), [z2] "v"(zo2), "{v[196:199]}"(af1), "{v210}"(vv),
                              [w0] "v"(WO(1, 0)), [w1] "v"(WO(1, 1)), [w2] "v"(WO(1, 2)), [w3] "v"(WO(1, 3)), [w4] "v"(WO(1, 4)), [w5] "v"(WO(1, 5)), [w6] "v"(WO(1, 6)), [w7] "v"(WO(1, 7)), [w8] "v"(WO(1, 8)), [w9] "v"(WO(1, 9))
                            : "scc", "memory");
                        redo = vd == it;
                        RT(4);
                        MT(6);
                        tref = __builtin_amdgcn_s_memtime();
                        __builtin_amdgcn_s_setprio(TKP);
                        asm volatile("s_waitcnt vmcnt(3)" : "+v"(sf) :: "memory");
                    }
                } while (__builtin_expect(redo, 0));
                if (s < 8) SST(8 + s);
                if (C == 0) {
                    if (s > 0 && s + 1 < NSEG) {
                        __builtin_amdgcn_s_sleep(DZR);
                        const unsigned zq = zb == W_ZM ? W_ZM + 2u * NOWN * PAIR : zb - NOWN * PAIR;
                        unsigned s1;
                        asm volatile("v_mov_b32 %0, -1" : "=v"(s1));
                        st128(((u32x4){s1, s1, s1, s1}), rwin, zo0, zq, 16);
                        st128(((u32x4){s1, s1, s1, s1}), rwin, zo1, zq, 16);
                        __builtin_amdgcn_raw_buffer_store_b64((u32x2){s1, s1}, rwin, zo2, zq, 16);
                    }
                } else {
                    if (s > TAILS) __builtin_amdgcn_raw_buffer_store_b32((unsigned)s - 1u, rwin, 0u, W_FZ2 + fo + (unsigned)(hh * 128 + (w & 3) * 4), 16);
                    if (TK && s + 1 < NSEG && s > 0) sched2(tk, s, sf);
                    if (!TK) wait_until(tref + DZ2);
                    if (s + 2 < NSEG) put_z2(s);
                }
                if (budget <= 0) set_tmo(rwin);
                zb = zb == W_ZM + 2u * NOWN * PAIR ? W_ZM : zb + NOWN * PAIR;
                RT(5);
                RTS();
                MT(7);
                MTS();
            }
        };
        const int tid = mytid();
        if (ph == 0) {
            gather_mem0(memL, tid);
            if (tid < 8) *(unsigned*)(smem + L_S + tid * 4) = 0u;
        }
        if (w < 4) steps(IC<0>()); else if (w < 7) steps(IC<1>()); else steps(IC<3>());
        if (ph == 0) {
            if (w == 7) clk_init(ck);
            __builtin_amdgcn_s_setprio(0);
            BAR();
        }
    };
    auto vt_role = [&](const u32x8 (&wt)[2][10], int hh, unsigned long long mv) INL {
        constexpr bool vt = true;
        const int jl = 63 - __builtin_clzll(mv);
        auto nextv = [&](int j) { return min(seg_after(mv, j), jl); };
        const int jv1 = nextv(-1), jv2 = nextv(jv1);
        if (w == 0) *(unsigned*)(smem + L_S + (fresh_lane() & 7u) * 4) = 0u;
        stage2(jv1, jv2, true);
        {
            LANES();
            const int ni = __builtin_popcountll(mv) + 1;
            if (w < 4) {
                const LAS char* c0 = (const LAS char*)(hL + hoff + ((fq ^ (fr & 7)) << 4));
                const LAS char* c1 = (const LAS char*)(hL + hoff + (((4 + fq) ^ (fr & 7)) << 4));
                const LAS char* n0 = c0 + 40960; const LAS char* n1 = c1 + 40960;
                u32x4 bb[8];
                project0(bb, c0, c1);
                for (int i = 1; i < ni; ++i) {
                    if (vt) MT(0);
                    f32x4 tc[2][2];
                    project<0>(wt, tc, bb, c0, c1, n0, n1,
                        [&]() {
                            MT(1);
                            if (i == 1) SST(16);
                            asm volatile("s_waitcnt vmcnt(0) lgkmcnt(0)\n\ts_barrier" ::: "memory");
                            if (i == 1) SST(17);
                            MT(2);
                        }, [](auto) {});
                    { const LAS char* x = c0; c0 = n0; n0 = x; x = c1; c1 = n1; n1 = x; }
                    char* tb = tokL + (i & 1) * 32 * QS + (16 * w + 4 * fq) * 2;
#pragma unroll
                    for (int g = 0; g < 2; ++g)
#pragma unroll
                        for (int tt = 0; tt < 2; ++tt) *(u32x2*)(tb + (prow + 8 * tt) * QS + g * 128) = (u32x2){pack2(tc[g][tt][0], tc[g][tt][1]), pack2(tc[g][tt][2], tc[g][tt][3])};
                    asm volatile("" ::: "memory");
                    *(volatile LAS int*)(smem + L_S + 16 + w * 4) = i;
                    if (i == 1) SST(18);
                    if (__builtin_expect(i == 1, 0)) __builtin_amdgcn_s_sleep(VSL);
                    if (vt) { MT(3); const int s = i & 63; MTRT(); MTS(); }
                }
                project_end(bb);
            } else {
                __builtin_amdgcn_s_setprio(OPR);
                const unsigned hv = lane * 16u, fz_ = W_FZ + fo + (unsigned)(hh * 128 + (jv1 ? 4096 : 0) + (w - 4) * 4);
                unsigned fp = ~0u;
                unsigned fzp = 0x40000000u;
                int jo = jv1, jh = jv2;
                for (int i = 1; i < ni; ++i, jo = nextv(jo)) {
                    const int b = i + (i >= ZLAG);
                    if (__builtin_expect(i == 1 || i == ZLAG, 0)) {
                        VMWAIT();
                        if (i == ZLAG) {
                            jh = nextv(jh);
                            wait_h(rwin, fo, jh, hrdy);
                            BAR0();
                            for (int t = 0; t < 10; ++t) dma1k(rws, hv, hseg(jh) + (unsigned)((w - 4) * 1024 + t * 4096), (unsigned)(L_H + ((i + 1) & 1) * 40960 + (w - 4) * 1024 + t * 4096));
                            VMWAIT();
                        }
                    }
                    jh = nextv(jh);
                    if (b < ni) BAR0();
                    wait_h(rwin, fo, jh, hrdy);
                    if (i == 1) SST(19);
                    if (vt) MT(1);
                    for (;;) {
                        const u32x4 f = *(volatile LAS u32x4*)(smem + L_S + 16);
                        if (__builtin_amdgcn_readfirstlane(min(min(f[0], f[1]), min(f[2], f[3]))) >= (unsigned)i) break;
                        __builtin_amdgcn_s_sleep(1);
                    }
                    asm volatile("" ::: "memory");
                    if (i == 1) SST(20);
                    if (__builtin_expect(jo == 0 && w == 7, 0)) __builtin_amdgcn_raw_buffer_store_b32((4u * (unsigned)__builtin_amdgcn_s_memrealtime() + DVT) & 0xFFFFFFu, rwin, two(), W_T + (unsigned)hh * 128u + 4u, 16);
                    if (vt) MT(0);
                    u32x4 vf[2][4];
#pragma unroll
                    for (int t = 0; t < 2; ++t)
#pragma unroll
                        for (int ks = 0; ks < 4; ++ks) vf[t][ks] = *(const u32x4*)(tokL + (i & 1) * 32 * QS + (16 * t + fr) * QS + ks * 64 + fq * 16);
                    const unsigned ztb = O_Z + (unsigned)(jo * ZTSEG) * 2u + (unsigned)(10 * (w - 4) * H_ + hh) * 1024u;
                    f32x4 c[2][2];
                    u32x4 r;
                    unsigned hs = hseg(jh) + (unsigned)((w - 4) * 1024), hd = (unsigned)(L_H + ((b + 1) & 1) * 40960 + (w - 4) * 1024);
                    asm volatile("" : "+s"(hs), "+s"(hd));
#pragma unroll
                    for (int nt = 0; nt < 11; ++nt) {
                        auto& cc = c[nt & 1]; auto& cp = c[(nt + 1) & 1];
#pragma unroll
                        for (int ks = 0; ks < 4; ++ks) {
                            if (nt < 10) { cc[0] = mfma32(vf[0][ks], half8(WO(ks >> 1, nt < 10 ? nt : 0), ks & 1), ks ? cc[0] : z4); PIN1(cc[0]); }
                            if (nt > 0 && ks >= 1 && ks < 3) { r[2 * ks - 2] = pack2(cp[ks - 1][0], cp[ks - 1][1]); PIN1(r); }
                            if (nt < 10) { cc[1] = mfma32(vf[1][ks], half8(WO(ks >> 1, nt < 10 ? nt : 0), ks & 1), ks ? cc[1] : z4); PIN1(cc[1]); }
                            if (nt > 0 && ks >= 1 && ks < 3) { r[2 * ks - 1] = pack2(cp[ks - 1][2], cp[ks - 1][3]); PIN1(r); }
                            if (nt > 0 && ks == 2) st128(r, rws, lane * 16u, ztb + (unsigned)((nt - 1) * H_) * 1024u, 16);
                            if (nt < 5 && !(ks & 1)) dma1k(rws, hv, hs + (2 * nt + (ks >> 1)) * 4096u, hd + (2 * nt + (ks >> 1)) * 4096u);
                            if (nt == 5 && ks == 0) {
                                asm volatile("s_waitcnt vmcnt(14)" ::: "memory");
                                __builtin_amdgcn_raw_buffer_store_b32(fp, rwin, 0u, fzp, 16);
                            }
                            SB();
                        }
                    }
                    if (vt) MT(2);
                    if (i == 1) SST(21);
                    fp = (unsigned)jo;
                    fzp = fz_;
                    if (__builtin_expect(i == 1 && xt, 0)) for (unsigned spins = 0;;) {
                        asm volatile("" ::: "memory");
                        if (__builtin_amdgcn_readfirstlane(__builtin_amdgcn_raw_buffer_load_b32(rwin, 0u, fz_, 16)) != ~0u || spin_fail(spins, rwin)) break;
                        __builtin_amdgcn_s_sleep(4);
                    }
                    if (__builtin_expect(jo <= ZEARLY || i + 1 == ni, 0)) {
                        VMWAIT();
                        __builtin_amdgcn_raw_buffer_store_b32(fp, rwin, 0u, fz_, 16);
                    } else asm volatile("s_waitcnt vmcnt(7)" ::: "memory");
                    if (jo < 8) SST(8 + jo);
                    if (vt && (!BSPARSE || jo % 6 == 2)) { const int s = jo; RT(0); RTS(); }
                    if (vt) { MT(3); const int s = i & 63; MTRT(); MTS(); }
                }
            }
            __builtin_amdgcn_s_setprio(0);
            VMWAIT();
            BAR();
        }
    };
    auto qt_role = [&](const u32x8 (&wt)[2][10], int hh, auto qq) INL {
        constexpr bool qt = decltype(qq)::v;
        const unsigned long long mq = bid < NQT ? MXQ : ~MXQ;
        const int jl = 63 - __builtin_clzll(mq);
        auto nextq = [&](int j) { return min(seg_after(mq, j), jl); };
        const int jq0 = qt ? nextq(0) : 0, jq1 = qt ? nextq(jq0) : 0;
        stage2(jq0, jq1, !qt);
        auto check2 = [&]() INL { VMWAIT(); check_h<5>(rws, rwin, hseg(jq1) + w * 1024u, L_H + 40960u + w * 1024u, 8192u); };
        {
            LANES();
            const unsigned to0 = (unsigned)((w ^ 4) >> 2) * 8192u + (unsigned)((w & 3) * 64 + (lane ^ ((fq & 1) * (w < 4 ? 8 : 5)))) * 16u, to1 = to0 + 4096u, hv = lane * 16u;
            const LAS char* c0 = (const LAS char*)(hL + hoff + ((fq ^ (fr & 7)) << 4));
            const LAS char* c1 = (const LAS char*)(hL + hoff + (((4 + fq) ^ (fr & 7)) << 4));
            u32x4 bb[8], g0 = SENT, g1 = SENT;
            f32x4 tc[2][2];
            auto pack = [&]() {
                g0 = (u32x4){pack2(tc[0][0][0], tc[0][0][1]), pack2(tc[0][0][2], tc[0][0][3]), pack2(tc[1][0][0], tc[1][0][1]), pack2(tc[1][0][2], tc[1][0][3])};
                g1 = (u32x4){pack2(tc[0][1][0], tc[0][1][1]), pack2(tc[0][1][2], tc[0][1][3]), pack2(tc[1][1][0], tc[1][1][1]), pack2(tc[1][1][2], tc[1][1][3])};
            };
            const LAS char* n0 = c0 + 40960; const LAS char* n1 = c1 + 40960;
            if (!qt) {
                project0(bb, c0, c1);
                project<0>(wt, tc, bb, c0, c1, c0, c1, []() {}, [](auto) {});
                pack();
                *(u32x4*)(tokL + to0) = g0; *(u32x4*)(tokL + to1) = g1;
                project_end(bb);
            } else {
                project0(bb, c0, c1);
                unsigned hs = hseg(jq1) + (unsigned)(w * 1024), hd = (unsigned)(L_H + 40960 + w * 1024), fv = tid == 0 ? 0u : 0x40000000u;
                const unsigned fq_ = W_FQ + fo + (unsigned)(hh * 128 + (jq0 > 1 ? 4096 : 0));
                for (int jp = 0, j = jq0, jn = jq1;; jp = j, j = jn, jn = nextq(jn)) {
                    MT(0);
                    project<0>(wt, tc, bb, c0, c1, n0, n1,
                        [&]() {
                            if (__builtin_expect(j == jq0, 0)) check2();
                            wait_h(rwin, fo, nextq(jn), hrdy);
                            hs = hseg(nextq(jn)) + (unsigned)(w * 1024); hd = (unsigned)(2 * L_H + 40960 + 2 * w * 1024) - hd;
                            unsigned fj = (unsigned)jp;
                            asm volatile("" : "+s"(hs), "+s"(hd));
                            PIN1(fj);
                            MT(1);
                            VMWAIT();
                            BAR();
                            MT(2);
                            if (w == 0) __builtin_amdgcn_raw_buffer_store_b32(fj, rwin, fv, fq_, 16);
                        },
                        [&](auto uu) {
                            constexpr int u = decltype(uu)::v;
                            if (u == 34 || u == 38 || u == 2 || u == 6 || u == 10) { constexpr int t = ((u + 6) % 40) / 4; dma1k(rws, hv, hs + t * 8192u, hd + t * 8192u); }
                        });
                    pack();
                    const unsigned qs = O_QK + (unsigned)(j * H_ + hh) * 16384u;
                    st128(g0, rws, to0, qs, 16);
                    st128(g1, rws, to1, qs, 16);
                    { const LAS char* x = c0; c0 = n0; n0 = x; x = c1; c1 = n1; n1 = x; }
                    if (__builtin_expect(j <= QEARLY || j == jl, 0)) {
                        VMWAIT();
                        BAR0();
                        if (w == 0) __builtin_amdgcn_raw_buffer_store_b32((unsigned)j, rwin, fv, fq_, 16);
                        if (j == jl) break;
                    }
                    MT(3);
                    if (j < 8) SST(8 + j);
                    if (!BSPARSE || j % 6 == 2) { const int s = j; RT(0); RTS(); MTRT(); MTS(); }
                }
                project_end(bb);
                VMWAIT();
                BAR0();
            }
        }
        SST(4);
    };
    auto qk_role = [&](const u32x8 (&wt)[2][10]) INL {
        const int hh = bid;
        int qrdy = 0;
        auto steps = [&](auto cc) INL {
        constexpr int C = decltype(cc)::v >> 1, R = decltype(cc)::v & 1;
        LANES();
        const LAS char* ka = (const LAS char*)(smem + L_K + (fr & 1) * TQS + (4 * fq + (fr >> 2)) * 8);
        const LAS char* qa = (const LAS char*)(smem + L_Q + (fr & 1) * 544 + ((fr >> 1) & 1) * 1152 + (16 * fq + 2 * (fr >> 2)) * 4);
        LAS char* qw = (LAS char*)(C < 2 ? smem + L_Q + (fq & 1) * 544 + (fq >> 1) * 1152 + (32 * w + 8 * (fr >> 2) + bs2(fr & 3)) * 4
                                         : smem + L_K + (fq >> 1) * TQS + (32 * (w & 3) + fr) * 8 + (fq & 1) * 4);
        const LAS char* mp0 = (const LAS char*)(memL + ma0);
        const int z = 16 * (fr >> 3) + 4 * (fr & 3) + 2 * ((fr >> 2) & 1);
        const int ko = ((z >> 4) * 256 + fq * 16 + ((z & 15) ^ ((fq & 1) * 5))) * 16;
        unsigned pa0 = (4u * C + 2u * (hh >> 4)) * PAIR + (unsigned)((hh & 15) * 512 + R * 256 + lane * 4);
        unsigned pa2 = ((8u + (fr >> 3)) * PAIR + C * 4096u + (unsigned)((hh >> 1) * 256 + R * 128 + (hh & 1) * 64 + fq * 16 + (fr & 6) * 2)) | (fr & 1) << 30;
        PIN2(ka, qw); PIN1(mp0);
        constexpr bool TK = C == 2 && R == 1;
        Tk tk = {0u, 0u, 0x40000000u, nullptr};
        if (TK) { tk = tk_init(); __builtin_amdgcn_s_setprio(TKP); }
        if (C < 2) { PIN2(qa, pa0); PIN1(pa2); }
        int qf_ = 0;
        int tbo = 0;
        for (int s = 0; s < NSEG; ++s) {
            RT(0);
            const char* tb = tokL + tbo;
            unsigned pb = __builtin_amdgcn_readfirstlane(s + 1 < NSEG ? p_slot(s) + nh : 0x40000000u);
            asm volatile("" : "+s"(pb));
            bool redo = false;
            unsigned sf = 0;
            if (s <= 2) BAR();
            u32x8 kx[2][2];
            if (C < 2) {
#pragma unroll
                for (int u = 0; u < 4; ++u) kx[u >> 1][u & 1] = cat8(*(const u32x4*)(tb + (ko ^ ((u >> 1) * 16)) + (u & 1) * 2048), *(const u32x4*)(tb + (ko ^ ((u >> 1) * 16)) + (u & 1) * 2048 + 1024));
                PIN2(kx[0][0], kx[0][1]); PIN2(kx[1][0], kx[1][1]);
            }
            do {
                ++it;
                f32x4 a0 = zero4(), a1 = zero4();
                PIN2(a0, a1);
                u32x4 m[10];
                if (C < 2) {
                    __builtin_amdgcn_s_setprio(2);
                    BAR();
                    MT(0);
#pragma unroll
                    for (int t = 0; t < NB1; ++t) { m[t] = *(const LAS u32x4*)(mp0 + t * 1024); SB(); }
                    BAR0();
#pragma unroll
                    for (int t = 0; t < 10; ++t) {
                        if (t == QB2) { BAR0(); MT(1); }
                        if (t + NB1 < 10) m[t + NB1] = *(const LAS u32x4*)(mp0 + (t + NB1) * 1024);
                        SB();
                        a0 = smfmac(m[t], wt[0][t], a0, sidx); a1 = smfmac(m[t], wt[1][t], a1, sidx);
                        PIN2(a0, a1);
                    }
                } else {
                    unsigned mx = 0;
                    asm volatile("" : "+{v[200:203]}"(a0), "+{v[204:207]}"(a1), "+{v208}"(mx));
                    unsigned lim = s > 0 ? poll_lim(budget) : ~0u, sr = tro + (unsigned)(s & 3) * 2u * PAIR;
                    asm volatile("" : "+s"(lim), "+s"(sr));
                    if (TK) fetch(tk, s, !redo, lim, DQC, (unsigned)hh * 128u, true);
                    else asm volatile("s_waitcnt lgkmcnt(0)\n\ts_barrier\n\ts_barrier\n\ts_barrier" ::: "memory");
                    asm volatile(
                        "ds_read_b128 v[212:215], %[mp] offset:0\n\t"
                        "ds_read_b128 v[216:219], %[mp] offset:1024\n\t"
                        "ds_read_b128 v[220:223], %[mp] offset:2048\n\t"
                        "ds_read_b128 v[224:227], %[mp] offset:3072\n\t"
                        "ds_read_b128 v[228:231], %[mp] offset:4096\n\t"
                        "ds_read_b128 v[232:235], %[mp] offset:5120\n\t"
                        "ds_read_b128 v[236:239], %[mp] offset:6144\n\t"
                        "ds_read_b128 v[240:243], %[mp] offset:7168\n\t"
                        "ds_read_b128 v[244:247], %[mp] offset:8192\n\t"
                        "ds_read_b128 v[248:251], %[mp] offset:9216\n\t"
                        MTA(4)
                        "s_waitcnt lgkmcnt(9)\n\t"
                        "v_smfmac_f32_16x16x64_bf16 v[200:203], v[212:215], %[w00], %[ix]\n\t"
                        "v_max3_u32 v208, v208, v212, v213\n\t"
                        "v_smfmac_f32_16x16x64_bf16 v[204:207], v[212:215], %[w10], %[ix]\n\t"
                        "s_waitcnt lgkmcnt(8)\n\t"
                        "v_max3_u32 v208, v208, v214, v215\n\t"
                        "v_smfmac_f32_16x16x64_bf16 v[200:203], v[216:219], %[w01], %[ix]\n\t"
                        "v_max3_u32 v208, v208, v216, v217\n\t"
                        "v_smfmac_f32_16x16x64_bf16 v[204:207], v[216:219], %[w11], %[ix]\n\t"
                        "s_waitcnt lgkmcnt(7)\n\t"
                        "v_max3_u32 v208, v208, v218, v219\n\t"
                        "v_smfmac_f32_16x16x64_bf16 v[200:203], v[220:223], %[w02], %[ix]\n\t"
                        "v_max3_u32 v208, v208, v220, v221\n\t"
                        "v_smfmac_f32_16x16x64_bf16 v[204:207], v[220:223], %[w12], %[ix]\n\t"
                        "s_waitcnt lgkmcnt(6)\n\t"
                        "v_max3_u32 v208, v208, v222, v223\n\t"
                        "v_smfmac_f32_16x16x64_bf16 v[200:203], v[224:227], %[w03], %[ix]\n\t"
                        "v_max3_u32 v208, v208, v224, v225\n\t"
                        "v_smfmac_f32_16x16x64_bf16 v[204:207], v[224:227], %[w13], %[ix]\n\t"
                        "s_waitcnt lgkmcnt(0)\n\t"
                        "v_max3_u32 v208, v208, v226, v227\n\t"
                        "v_smfmac_f32_16x16x64_bf16 v[200:203], v[228:231], %[w04], %[ix]\n\t"
                        "v_max3_u32 v208, v208, v228, v229\n\t"
                        "v_smfmac_f32_16x16x64_bf16 v[204:207], v[228:231], %[w14], %[ix]\n\t"
                        "v_max3_u32 v208, v208, v230, v231\n\t"
                        "v_smfmac_f32_16x16x64_bf16 v[200:203], v[232:235], %[w05], %[ix]\n\t"
                        "v_max3_u32 v208, v208, v232, v233\n\t"
                        "v_smfmac_f32_16x16x64_bf16 v[204:207], v[232:235], %[w15], %[ix]\n\t"
                        "v_max3_u32 v208, v208, v234, v235\n\t"
                        "v_smfmac_f32_16x16x64_bf16 v[200:203], v[236:239], %[w06], %[ix]\n\t"
                        "v_max3_u32 v208, v208, v236, v237\n\t"
                        "v_smfmac_f32_16x16x64_bf16 v[204:207], v[236:239], %[w16], %[ix]\n\t"
                        "v_max3_u32 v208, v208, v238, v239\n\t"
                        "v_smfmac_f32_16x16x64_bf16 v[200:203], v[240:243], %[w07], %[ix]\n\t"
                        "v_max3_u32 v208, v208, v240, v241\n\t"
                        "v_smfmac_f32_16x16x64_bf16 v[204:207], v[240:243], %[w17], %[ix]\n\t"
                        "v_max3_u32 v208, v208, v242, v243\n\t"
                        "v_smfmac_f32_16x16x64_bf16 v[200:203], v[244:247], %[w08], %[ix]\n\t"
                        "v_max3_u32 v208, v208, v244, v245\n\t"
                        "v_smfmac_f32_16x16x64_bf16 v[204:207], v[244:247], %[w18], %[ix]\n\t"
                        "v_max3_u32 v208, v208, v246, v247\n\t"
                        "v_smfmac_f32_16x16x64_bf16 v[200:203], v[248:251], %[w09], %[ix]\n\t"
                        "v_max3_u32 v208, v208, v248, v249\n\t"
                        "v_smfmac_f32_16x16x64_bf16 v[204:207], v[248:251], %[w19], %[ix]\n\t"
                        "v_max3_u32 v208, v208, v250, v251\n\t"
                        "buffer_load_dword %[sf], %[sl], %[rw], %[sr] offen sc1\n\t"
                        "v_cmp_lt_u32 vcc, %[lim], v208\n\t"
                        "s_nop 3\n\t"
                        "v_pk_add_f32 v[200:201], v[200:201], v[202:203]\n\t"
                        "v_pk_add_f32 v[204:205], v[204:205], v[206:207]\n\t"
                        "v_cvt_pk_bf16_f32 v200, v200, v201\n\t"
                        "v_cvt_pk_bf16_f32 v204, v204, v205\n\t"
                        "ds_write2_b32 %[qw], v200, v204 offset1:32\n\t"
                        MTA(2)
                        "s_cbranch_vccz 1f\n\t"
                        "v_mov_b32 v208, %[it]\n\t"
                        "v_mov_b32 v209, 0\n\t"
                        "ds_write_b32 v209, v208 offset:%[ls]\n\t"
                        "1:\n\t"
                        "s_waitcnt vmcnt(%[nv]) lgkmcnt(0)\n\t"
                        "s_barrier\n\t"
                        "s_waitcnt vmcnt(0)\n\t"
                        : [sf] "=&v"(sf), "+v"(qf_), "+{v[200:203]}"(a0), "+{v[204:207]}"(a1), "+{v208}"(mx)
                        : [mp] "v"(mp0), [qw] "v"(qw), [ix] "v"(sidx), [lim] "s"(lim), [it] "s"(it), [ls] "n"(L_S), [sl] "v"(tk.sl), [sr] "s"(sr), [rw] "s"(rwin), [nv] "n"(TK ? 2 : 1),
                          [w00] "v"(wt[0][0]), [w01] "v"(wt[0][1]), [w02] "v"(wt[0][2]), [w03] "v"(wt[0][3]), [w04] "v"(wt[0][4]), [w05] "v"(wt[0][5]), [w06] "v"(wt[0][6]), [w07] "v"(wt[0][7]), [w08] "v"(wt[0][8]), [w09] "v"(wt[0][9]), [w10] "v"(wt[1][0]), [w11] "v"(wt[1][1]), [w12] "v"(wt[1][2]), [w13] "v"(wt[1][3]), [w14] "v"(wt[1][4]), [w15] "v"(wt[1][5]), [w16] "v"(wt[1][6]), [w17] "v"(wt[1][7]), [w18] "v"(wt[1][8]), [w19] "v"(wt[1][9])
                        : "v209", "v212", "v213", "v214", "v215", "v216", "v217", "v218", "v219", "v220", "v221", "v222", "v223", "v224", "v225", "v226", "v227", "v228", "v229", "v230", "v231", "v232", "v233", "v234", "v235", "v236", "v237", "v238", "v239", "v240", "v241", "v242", "v243", "v244", "v245", "v246", "v247", "v248", "v249", "v250", "v251", "vcc", "memory");
                }
                if (C < 2) {
                    *(LAS unsigned*)qw = hpack(a0); *(LAS unsigned*)(qw + 16) = hpack(a1);
                    SB();
                    MT(2);
                    unsigned vd;
#define SC0 "v[212:215]"
#define SC1 "v[216:219]"
#define SC2 "v[220:223]"
#define DPP(op, d, ctl) op " " d ", " d ", " d " " ctl " row_mask:0xf bank_mask:0xf\n\t"
#define TAIL(A0, A2, B0, B2, C0, C2) \
                        "v_mov_b64 v[212:213], 0\n\tv_mov_b64 v[214:215], 0\n\tv_mov_b64 v[216:217], 0\n\tv_mov_b64 v[218:219], 0\n\tv_mov_b64 v[220:221], 0\n\tv_mov_b64 v[222:223], 0\n\t" \
                        "s_setprio 3\n\t" \
                        "s_waitcnt lgkmcnt(0)\n\t" \
                        "s_barrier\n\t" MTA(3) \
                        "ds_read_b64_tr_b16 v[224:225], %[qa]\n\t" \
                        "ds_read_b64_tr_b16 v[226:227], %[qa] offset:32\n\t" \
                        "ds_read_b64_tr_b16 v[228:229], %[qa] offset:256\n\t" \
                        "ds_read_b64_tr_b16 v[230:231], %[qa] offset:288\n\t" \
                        "ds_read_b64_tr_b16 v[232:233], %[ka]\n\t" \
                        "ds_read_b64_tr_b16 v[234:235], %[ka] offset:128\n\t" \
                        "ds_read_b64_tr_b16 v[236:237], %[ka] offset:256\n\t" \
                        "ds_read_b64_tr_b16 v[238:239], %[ka] offset:384\n\t" \
                        "ds_read_b64_tr_b16 v[240:241], %[ka] offset:512\n\t" \
                        "ds_read_b64_tr_b16 v[242:243], %[ka] offset:640\n\t" \
                        "s_waitcnt lgkmcnt(8)\n\t" \
                        "v_smfmac_f32_16x16x64_bf16 " SC0 ", v[224:227], %[k00], %[ix]\n\t" \
                        "ds_read_b64_tr_b16 v[244:245], %[ka] offset:768\n\t" \
                        "v_smfmac_f32_16x16x64_bf16 " SC1 ", v[224:227], %[k10], %[ix]\n\t" \
                        "ds_read_b64_tr_b16 v[246:247], %[ka] offset:896\n\t" \
                        "s_waitcnt lgkmcnt(8)\n\t" \
                        "v_smfmac_f32_16x16x64_bf16 " SC0 ", v[228:231], %[k01], %[ix]\n\t" \
                        "ds_read_b32 v248, %[vz] offset:%[ls]\n\t" \
                        "v_smfmac_f32_16x16x64_bf16 " SC1 ", v[228:231], %[k11], %[ix]\n\t" \
                        "s_waitcnt lgkmcnt(5)\n\t" \
                        "v_smfmac_f32_16x16x64_bf16 " SC2 ", v[224:227], v[232:239], %[ix]\n\t" \
                        "s_waitcnt lgkmcnt(1)\n\t" \
                        "v_smfmac_f32_16x16x64_bf16 " SC2 ", v[228:231], v[240:247], %[ix]\n\t" \
                        "s_waitcnt lgkmcnt(0)\n\t" \
                        MTA(4) "v_readfirstlane_b32 %[vd], v248\n\t" \
                        "v_add_f32 v232, " A0 ", " A2 "\n\t" \
                        "s_nop 0\n\t" \
                        "v_add_f32 v233, " B0 ", " B2 "\n\t" \
                        "v_max_f32 v235, v232, v233\n\t" \
                        "s_cmp_eq_u32 %[vd], %[it]\n\t" \
                        "s_cselect_b32 %[p0], 2.0, %[p0]\n\t" \
                        DPP("v_max_f32_dpp", "v235", "row_ror:8") \
                        "v_add_f32 v234, " C0 ", " C2 "\n\t" \
                        "v_max_f32 v235, v235, v234\n\t" \
                        "s_add_u32 %[p1], %[p0], 0x4000\n\t" \
                        "s_nop 0\n\t" DPP("v_max_f32_dpp", "v235", "row_ror:4") \
                        "s_nop 1\n\t" DPP("v_max_f32_dpp", "v235", "row_ror:2") \
                        "s_nop 1\n\t" DPP("v_max_f32_dpp", "v235", "row_ror:1") \
                        "v_mul_f32 v235, 0xbe0293ee, v235\n\t" \
                        "v_fmamk_f32 v232, v232, 0x3e0293ee, v235\n\t" \
                        "v_fmamk_f32 v233, v233, 0x3e0293ee, v235\n\t" \
                        "v_exp_f32 v232, v232\n\t" \
                        "v_exp_f32 v233, v233\n\t" \
                        "v_fmamk_f32 v234, v234, 0x3e0293ee, v235\n\t" \
                        "v_add_f32 v235, v232, v233\n\t" \
                        "v_exp_f32 v234, v234\n\t" \
                        "s_nop 0\n\t" DPP("v_add_f32_dpp", "v235", "row_ror:8") \
                        "v_add_f32 v235, v235, v234\n\t" \
                        "v_mov_b32_dpp v236, v234 quad_perm:[1,1,3,3] row_mask:0xf bank_mask:0xf\n\t" \
                        "s_nop 0\n\t" DPP("v_add_f32_dpp", "v235", "row_ror:4") \
                        "s_nop 1\n\t" DPP("v_add_f32_dpp", "v235", "row_ror:2") \
                        "s_nop 1\n\t" DPP("v_add_f32_dpp", "v235", "row_ror:1") \
                        "v_rcp_f32 v235, v235\n\t" \
                        "s_nop 0\n\t" \
                        "v_mul_f32_e64 v232, v232, v235 clamp\n\t" \
                        "v_mul_f32_e64 v233, v233, v235 clamp\n\t" \
                        "v_cvt_pk_bf16_f32 v232, v232, v233\n\t" \
                        MTA(5) "buffer_store_dword v232, %[pa], %[rw], %[p0] offen sc1\n\t" \
                        "v_mul_f32_e64 v234, v234, v235 clamp\n\t" \
                        "v_mul_f32_e64 v236, v236, v235 clamp\n\t" \
                        "buffer_store_dword v232, %[pa], %[rw], %[p1] offen sc1\n\t" \
                        "v_cvt_pk_bf16_f32 v234, v234, v236\n\t" \
                        "buffer_store_dword v234, %[pm], %[rw], %[p0] offen sc1\n\t" \
                        "s_setprio 0"
#define TAILIO : [vd] "=&s"(vd), [p0] "+s"(ps), [p1] "=&s"(ps1) \
                        : [qa] "v"(qa), [ka] "v"(ka), [vz] "v"(0u), [ls] "n"(L_S), [ix] "v"(sidx), [it] "s"(it), [pa] "v"(pa0), [pm] "v"(pa2), [rw] "s"(rwin), \
                          [k00] "v"(kx[0][0]), [k01] "v"(kx[0][1]), [k10] "v"(kx[1][0]), [k11] "v"(kx[1][1]) \
                        : "v212", "v213", "v214", "v215", "v216", "v217", "v218", "v219", "v220", "v221", "v222", "v223", "v224", "v225", "v226", "v227", "v228", "v229", "v230", "v231", "v232", \
                          "v233", "v234", "v235", "v236", "v237", "v238", "v239", "v240", "v241", "v242", "v243", "v244", "v245", "v246", "v247", "v248", "scc", "memory"
                    unsigned ps = pb, ps1;
                    if (R == 0) asm volatile(TAIL("v212", "v214", "v216", "v218", "v220", "v222") TAILIO);
                    else asm volatile(TAIL("v213", "v215", "v217", "v219", "v221", "v223") TAILIO);
                    redo = vd == it;
                    RT(4);
                    MT(6);
                } else {
                    MT(3);
                    if (TK) { __builtin_amdgcn_s_setprio(TKP); if (TKS) __builtin_amdgcn_s_sleep(TKS); }
                    redo = __builtin_amdgcn_readfirstlane(*(volatile LAS unsigned*)(smem + L_S)) == it;
                    if (redo) FC(fc0);
                }
            } while (__builtin_expect(redo, 0));
            if (s < 8) SST(8 + s);
            if (s >= 56) SST(s - 40);
            if (C + R == 1) {
                if (s + layer > 0) {
                    __builtin_amdgcn_s_sleep(DDMA);
                    unsigned l2 = fresh_lane(), s1;
                    asm volatile("v_mov_b32 %0, -1" : "=v"(s1));
                    const unsigned base = p_slot(s - 1) + nh;
                    asm volatile("s_waitcnt vmcnt(3)" ::: "memory");
                    st128(((u32x4){s1, s1, s1, s1}), rwin, (l2 >> 5) * PAIR + (l2 & 31u) * 16u, base + (unsigned)(R * 4 + 2 * (hh >> 4)) * PAIR + (unsigned)(hh & 15) * 512u, 16);
                    if (R) __builtin_amdgcn_raw_buffer_store_b64((u32x2){s1, s1}, rwin, (l2 >> 5) * PAIR + (l2 & 16u) * 256u + (l2 & 8u) * 16u + (l2 & 7u) * 8u, base + 8u * PAIR + (unsigned)((hh >> 1) * 256 + (hh & 1) * 64), 16);
                    else __builtin_amdgcn_raw_buffer_store_b64((u32x2){s1, s1}, rwin, two(), W_T + (unsigned)((s + 2) & 3) * 2u * PAIR + (unsigned)hh * 128u, 16);
                }
            } else if (TK) {
                if (s + 1 < NSEG) sched2(tk, s, sf);
            } else if (C == 2 && w == 4) {
                if (s + 2 < NSEG) {
                    qrdy = max(qrdy, seg_done(MXQ, __builtin_amdgcn_readlane(qf_, 0), __builtin_amdgcn_readlane(qf_, 1)));
                    const int sq = s ? s + 2 : 1;
                    if (__builtin_expect(qrdy < sq, 0)) for (unsigned spins = 0;;) {
                        asm volatile("" ::: "memory");
                        const int f = (int)__builtin_amdgcn_raw_buffer_load_b32(rwin, (unsigned)(lane & 1) * 4096u, W_FQ + fo + (unsigned)hh * 128u, 16);
                        qrdy = seg_done(MXQ, __builtin_amdgcn_readlane(f, 0), __builtin_amdgcn_readlane(f, 1));
                        if (qrdy >= sq) break;
                        FC(fc2);
                        if (spin_fail(spins, rwin)) break;
                        __builtin_amdgcn_s_sleep(2);
                    }
                    const unsigned so = O_QK + (unsigned)(((s + 2) * H_ + hh) * 16384), lo = (unsigned)(L_TOK + (tbo ? tbo - 16384 : 32768)), l2 = fresh_lane();
                    if (__builtin_expect(s <= 1, 0)) {
                        for (int i = 0; i < 4; ++i) dma4k(rws, l2 * 16u, so - H_ * 16384u + i * 4096u, L_TOK + (s + 1) * 16384u + i * 4096u);
                        VMWAIT();
                    }
                    if (__builtin_expect(s > 0, 1)) {
#pragma unroll
                        for (int i = 0; i < 4; ++i) dma4k(rws, l2 * 16u, so + i * 4096u, lo + i * 4096u);
                    }
                    if (qrdy < min(s + 5, NSEG - 1))
                        asm volatile("buffer_load_dword %0, %1, %2, %3 offen sc1" : "+v"(qf_) : "v"((l2 & 1u) * 4096u), "s"(rwin), "s"(W_FQ + fo + (unsigned)hh * 128u) : "memory");
                }
            } else {
                asm volatile("s_waitcnt vmcnt(3)" ::: "memory");
                if (s > TAILS) __builtin_amdgcn_raw_buffer_store_b32((unsigned)s - 1u, rwin, 0u, W_FPT + fo + (unsigned)(hh * 128 + (w & 3) * 4), 16);
                if (C == 2 && s + 1 < NSEG) __builtin_amdgcn_s_sleep(DPT);
                const float scale = 0.08838834764831845f, l2e = 1.44269504088896340736f * (NORND ? scale : 1.f);
                const int tq = (w >> 1) & 1, qh = w & 1;
                f32x4 sc[3] = {z4, z4, z4};
                u32x4 kt[2][4], qf[4], kf[4];
                const unsigned l2 = fresh_lane(), kto = ((l2 & 8u) * 512u + (l2 & 51u) * 16u + (l2 & 4u) * 32u) ^ ((l2 & 16u) * 5u), qto = ((l2 & 55u) * 16u + qh * 128u) ^ ((l2 & 16u) * 8u);
#pragma unroll
                for (int ks = 0; ks < 4; ++ks) {
                    qf[ks] = *(const u32x4*)(tb + 8192 + (tq * 4 + ks) * 1024 + qto);
                    kt[0][ks] = *(const u32x4*)(tb + ks * 1024 + kto);
                    kt[1][ks] = *(const u32x4*)(tb + ks * 1024 + (kto ^ 64u));
                    SB();
                }
#pragma unroll
                for (int ks = 0; ks < 4; ++ks) {
                    sc[0] = mfma32(kt[0][ks], qf[ks], sc[0]); PIN1(sc[0]);
                    kf[ks] = ldtr2(ka + ks * 256, ka + ks * 256 + 128);
                    SB();
                    sc[1] = mfma32(kt[1][ks], qf[ks], sc[1]); PIN1(sc[1]);
                }
#pragma unroll
                for (int ks = 0; ks < 4; ++ks) sc[2] = mfma32(kf[ks], qf[ks], sc[2]);
                const bool hi = (l2 & 8u) != 0u;
                float v[6];
#pragma unroll
                for (int r = 0; r < 4; ++r) v[r] = hi ? sc[1][r] : sc[0][r];
                v[4] = hi ? sc[2][2] : sc[2][0]; v[5] = hi ? sc[2][3] : sc[2][1];
#pragma unroll
                for (int r = 0; r < 6; r += 2) {
                    if (NORND) break;
                    const unsigned b0 = pack2(v[r], v[r + 1]);
                    const unsigned b1 = pack2(lo2f(b0) * scale, hi2f(b0) * scale);
                    v[r] = lo2f(b1); v[r + 1] = hi2f(b1);
                }
                const int lim = 8 * (w & 3) + (int)(l2 & 7u) - 8 * (int)(l2 >> 4) - (int)((l2 & 8u) >> 1);
#pragma unroll
                for (int r = 0; r < 4; ++r) if (r > lim) v[r] = NORND ? -1000000000.0f / scale : bfr(-1000000000.0f);
                float mx = fmx(fmx(fmx(fmx(fmx(v[0], v[1]), v[2]), v[3]), v[4]), v[5]);
                mx = xmax3216(fmx(mx, ror8f(mx)));
                const float mb = -mx * l2e;
#pragma unroll
                for (int r = 0; r < 6; ++r) v[r] = __builtin_amdgcn_exp2f(fmaf(v[r], l2e, mb));
                float sum = fmaf(v[4] + v[5], 0.5f, (v[0] + v[1]) + (v[2] + v[3]));
                sum = xsum3216(sum + ror8f(sum));
                const float inv = __builtin_amdgcn_rcpf(sum);
                const unsigned p0 = pack2(v[0] * inv, v[1] * inv), p1 = nosent1(pack2(v[2] * inv, v[3] * inv)), p2 = nosent1(pack2(v[4] * inv, v[5] * inv));
                const unsigned base = (s + 2 < NSEG ? O_PT : W_PL + fo - (NSEG - 2) * PTSEG * 2u) + (unsigned)(s * PTSEG) * 2u;
                const __amdgpu_buffer_rsrc_t rp = s + 2 < NSEG ? rws : rwin;
                const unsigned oob = (l2 & 8u) << 27;
                st128(((u32x4){p0, p1, dppu<0x128>(p0), dppu<0x128>(p1)}), rp, (l2 * 16u) | oob, base + (unsigned)((hh * 2 + tq) * 1024 + qh * 128), 16);
                __builtin_amdgcn_raw_buffer_store_b64((u32x2){p2, dppu<0x128>(p2)}, rp, ((l2 & 7u) * 16u + (l2 >> 4) * 8u) | oob | (l2 & 32u) << 25, base + 65536u + (unsigned)((tq * 32 + hh) * 256 + qh * 128), 16);
            }
            tbo = tbo == 32768 ? 0 : tbo + 16384;
            if (C == 2 && __builtin_expect(budget <= 0, 0)) set_tmo(rwin);
            RT(5);
            RTS();
            MT(7);
            MTS();
        }
        };
        const int tid = mytid();
        gather_mem0(memL, tid);
        if (tid < 4) *(unsigned*)(smem + L_S + tid * 4) = 0u;
        if (layer == 0) near_init();
        if (w == 7) clk_init(ck);
        qt_role(wt, hh, IC<0>());
        if (w == 0) steps(IC<0>()); else if (w == 1) steps(IC<1>()); else if (w == 2) steps(IC<2>()); else if (w == 3) steps(IC<3>()); else if (w < 7) steps(IC<4>()); else steps(IC<5>());
    };
    auto own_role = [&]() INL {
        const unsigned long long tstart = __builtin_amdgcn_s_memtime();
        if (layer == 0) near_init();
        LANES();
        const int o = bid - 96, hg = w & 3, ot = tile_of(o);
        const unsigned ztoff = (unsigned)((o * H_ + 8 * hg) * 64 + lane) * 16u;
        const unsigned zoff = zpair(o / 10, o % 10) + (unsigned)(lane * 16);
        const unsigned poff = (4u * (mc >> 1) + 2u * (hg >> 1) + (mc & 1u)) * PAIR + nh + (unsigned)((hg & 1) * 4096 + (fq >> 1) * 512 + (fr >> 2) * 128 + (fr & 1) * 64 + (fq & 1) * 32 + (fr & 2) * 8);
        const unsigned pmoff = (8u + (mc & 1u)) * PAIR + nh + (mc >> 1) * 4096u + (unsigned)(fq * 256 + (fr >> 3) * 128 + (fr & 2) * 32 + (fr & 4) * 8 + (fr & 1) * 16);
        const unsigned moff = (ot < 32 ? (unsigned)ot * 256u : 4u * PAIR + (unsigned)(ot - 32) * 256u) + nh + (unsigned)(lane * 4);
        const unsigned mc2 = ot < 32 ? 2u * PAIR : 2048u;
        char* op = smem + 8192;
        if (w == 4 && lane < 2) *(volatile LAS int*)(smem + 4096 + lane * 4) = -1;
        BAR();
        SST(2);
        if (w == 7) clk_init(ck);
        wait_until(tstart + DO0);
        if (w < 4) {
            u32x8 zf[4];
            int zr = -1;
            unsigned zb = W_ZM + (unsigned)layer * (NOWN * PAIR), zso = O_Z;
            const unsigned ra = (ot < 32 ? (unsigned)fq * PAIR + (unsigned)ot * 256u : (4u + (fq & 1)) * PAIR + (unsigned)(fq >> 1) * 2048u + (unsigned)(ot - 32) * 256u) + nh + (unsigned)(fr * 16);
            u32x4 zn[8];
            static_assert(!(MXV >> (NSEG - 2)), "");
            for (int s = 0; s + 1 < NSEG; ++s) {
                const unsigned pb = p_slot(s);
                const int sz = s + (s + 2 == NSEG);
                while (__builtin_expect(zr < sz, 0)) {
                    __builtin_amdgcn_s_sleep(1);
                    zr = __builtin_amdgcn_readfirstlane(*(volatile LAS int*)(smem + 4096));
                }
                if (s == 0) SST(1);
                asm volatile(
                    "buffer_load_dwordx4 v[160:163], %[zt], %[rs], %[so] offen sc1\n\t"
                    "buffer_load_dwordx4 v[164:167], %[zt], %[rs], %[so] offen offset:1024 sc1\n\t"
                    "buffer_load_dwordx4 v[168:171], %[zt], %[rs], %[so] offen offset:2048 sc1\n\t"
                    "buffer_load_dwordx4 v[172:175], %[zt], %[rs], %[so] offen offset:3072 sc1\n\t"
                    "buffer_load_dwordx4 v[176:179], %[zt], %[rs], %[s4] offen sc1\n\t"
                    "buffer_load_dwordx4 v[180:183], %[zt], %[rs], %[s4] offen offset:1024 sc1\n\t"
                    "buffer_load_dwordx4 v[184:187], %[zt], %[rs], %[s4] offen offset:2048 sc1\n\t"
                    "buffer_load_dwordx4 v[188:191], %[zt], %[rs], %[s4] offen offset:3072 sc1"
                    : "=&{v[160:167]}"(zf[0]), "=&{v[168:175]}"(zf[1]), "=&{v[176:183]}"(zf[2]), "=&{v[184:191]}"(zf[3])
                    : [zt] "v"(ztoff), [rs] "s"(rws), [so] "s"(zso), [s4] "s"(zso + 4096u) : "memory");
                zso = O_Z + (unsigned)((s + 1) * ZTSEG) * 2u;
                if (__builtin_expect(s + 2 == NSEG, 0)) {
#pragma unroll
                    for (int q = 0; q < 8; ++q) asm volatile("s_nop 4\n\tbuffer_load_dwordx4 %0, %1, %2, %3 offen sc1" : "=v"(zn[q]) : "v"(ztoff), "s"(rws), "s"(zso + q * 1024u) : "memory");
                }
                if (__builtin_expect(budget <= 0, 0)) set_tmo(rwin);
                RT(6);
                const unsigned spa = (unsigned)((s & 1) * 2048 + (w < 3 ? w * 512 : 0) + lane * 8);
#define OST(r)
                unsigned long long t0_, t1_, t2_, t3_, t4_ = 0, bad;
                int tries = budget;
                unsigned po = poff, pm = pmoff, x0 = 4u * PAIR, x1 = 4096u;
                const unsigned mb = W_MEM + (unsigned)((s + 1) & 3) * 6u * PAIR;
                if (w < 3) asm volatile(
                    "s_barrier\n\t"
                    OST("%[t0]")
                    "v_mov_b64 v[96:97], 0\n\t"
                    "v_mov_b64 v[98:99], 0\n\t"
                    "v_mov_b32 v100, 0\n\t"
                    "s_barrier\n\t"
                    "1:\n\t"
                    "buffer_load_dwordx4 v[208:211], %[po], %[rw], %[pb] offen sc1\n\t"
                    "buffer_load_dwordx4 v[212:215], %[po], %[rw], %[pb] offen offset:1024 sc1\n\t"
                    "buffer_load_dwordx4 v[216:219], %[po], %[rw], %[pb] offen offset:2048 sc1\n\t"
                    "buffer_load_dwordx4 v[220:223], %[po], %[rw], %[pb] offen offset:3072 sc1\n\t"
                    OST("%[t1]")
                    "s_waitcnt vmcnt(3)\n\t"
                    OST("%[t3]")
                    "v_smfmac_f32_16x16x64_bf16 v[96:99], v[208:211], v[160:167], %[ix]\n\t"
                    "v_max3_u32 v100, v100, v208, v209\n\t"
                    "v_max3_u32 v100, v100, v210, v211\n\t"
                    "s_waitcnt vmcnt(2)\n\t"
                    "v_smfmac_f32_16x16x64_bf16 v[96:99], v[212:215], v[168:175], %[ix]\n\t"
                    "v_max3_u32 v100, v100, v212, v213\n\t"
                    "v_max3_u32 v100, v100, v214, v215\n\t"
                    "s_waitcnt vmcnt(1)\n\t"
                    "v_smfmac_f32_16x16x64_bf16 v[96:99], v[216:219], v[176:183], %[ix]\n\t"
                    "v_max3_u32 v100, v100, v216, v217\n\t"
                    "v_max3_u32 v100, v100, v218, v219\n\t"
                    "s_waitcnt vmcnt(0)\n\t"
                    "v_smfmac_f32_16x16x64_bf16 v[96:99], v[220:223], v[184:191], %[ix]\n\t"
                    "v_max3_u32 v100, v100, v220, v221\n\t"
                    "v_max3_u32 v100, v100, v222, v223\n\t"
                    "v_cmp_eq_u32 vcc, -1, v100\n\t"
                    OST("%[t2]")
                    "s_nop 4\n\t"
                    "v_pk_add_f32 v[96:97], v[96:97], v[98:99]\n\t"
                    "ds_write_b64 %[sp], v[96:97]\n\t"
                    "s_cbranch_vccz 2f\n\t"
                    "s_sub_i32 %[tr], %[tr], 1\n\t"
                    "s_cmp_lt_i32 %[tr], 1\n\t"
                    "s_cbranch_scc1 2f\n\t"
                    "v_xor_b32 %[po], %[x0], %[po]\n\t"
                    "s_xor_b32 %[x0], %[x0], 0x14000\n\t"
                    "s_waitcnt lgkmcnt(0)\n\t"
                    "v_mov_b64 v[96:97], 0\n\t"
                    "v_mov_b64 v[98:99], 0\n\t"
                    "v_mov_b32 v100, 0\n\t"
                    "s_branch 1b\n\t"
                    "2:\n\t"
                    "s_waitcnt lgkmcnt(0)\n\t"
                    "s_barrier\n\t"
                    "s_sleep 3\n\t"
                    : [tr] "+s"(tries), [po] "+v"(po), [x0] "+s"(x0), [t0] "=&s"(t0_), [t1] "=&s"(t1_), [t2] "=&s"(t2_), [t3] "=&s"(t3_)
                    : [sp] "v"(spa), [ix] "v"(sidx), [rw] "s"(rwin), [pb] "s"(pb), "{v[160:167]}"(zf[0]), "{v[168:175]}"(zf[1]), "{v[176:183]}"(zf[2]), "{v[184:191]}"(zf[3])
                    : "v96", "v97", "v98", "v99", "v100", "v208", "v209", "v210", "v211", "v212", "v213", "v214", "v215", "v216", "v217", "v218", "v219", "v220", "v221", "v222", "v223", "vcc", "scc", "memory");
                else asm volatile(
                    "s_barrier\n\t"
                    "buffer_load_dwordx4 v[128:131], %[zo], %[rw], %[zb] offen sc1\n\t"
                    "buffer_load_dwordx4 v[132:135], %[zo], %[rw], %[zb] offen offset:1024 sc1\n\t"
                    "buffer_load_dwordx4 v[136:139], %[zo], %[rw], %[zb] offen offset:2048 sc1\n\t"
                    "buffer_load_dwordx4 v[140:143], %[zo], %[rw], %[zb] offen offset:3072 sc1\n\t"
                    "buffer_load_dwordx4 v[144:147], %[zo], %[rw], %[z4] offen sc1\n\t"
                    "buffer_load_dwordx4 v[148:151], %[zo], %[rw], %[z4] offen offset:1024 sc1\n\t"
                    "buffer_load_dwordx4 v[152:155], %[zo], %[rw], %[z4] offen offset:2048 sc1\n\t"
                    "buffer_load_dwordx4 v[156:159], %[zo], %[rw], %[z4] offen offset:3072 sc1\n\t"
                    OST("%[t0]")
                    "v_mov_b64 v[96:97], 0\n\t"
                    "v_mov_b64 v[98:99], 0\n\t"
                    "v_mov_b32 v100, 0\n\t"
                    "s_barrier\n\t"
                    "s_sleep %[fd]\n\t"
                    "buffer_load_dwordx4 v[208:211], %[po], %[rw], %[pb] offen sc1\n\t"
                    "buffer_load_dwordx4 v[212:215], %[po], %[rw], %[pb] offen offset:1024 sc1\n\t"
                    "buffer_load_dwordx4 v[216:219], %[po], %[rw], %[pb] offen offset:2048 sc1\n\t"
                    "buffer_load_dwordx4 v[220:223], %[po], %[rw], %[pb] offen offset:3072 sc1\n\t"
                    "buffer_load_dwordx4 v[224:227], %[pm], %[rw], %[pb] offen sc1\n\t"
                    "buffer_load_dwordx4 v[228:231], %[pm], %[rw], %[pb] offen offset:1024 sc1\n\t"
                    "buffer_load_dwordx4 v[232:235], %[pm], %[rw], %[pb] offen offset:2048 sc1\n\t"
                    "buffer_load_dwordx4 v[236:239], %[pm], %[rw], %[pb] offen offset:3072 sc1\n\t"
                    OST("%[t1]")
                    "s_waitcnt vmcnt(8)\n\t"
                    "v_max3_u32 v101, v129, v131, v133\n\t"
                    "v_max3_u32 v101, v101, v135, v137\n\t"
                    "v_max3_u32 v101, v101, v139, v141\n\t"
                    "v_max3_u32 v101, v101, v143, v145\n\t"
                    "v_max3_u32 v101, v101, v147, v149\n\t"
                    "v_max3_u32 v101, v101, v151, v153\n\t"
                    "v_max3_u32 v101, v101, v155, v157\n\t"
                    "v_max_u32 v101, v101, v159\n\t"
                    "v_cmp_eq_u32 vcc, -1, v101\n\t"
                    "s_cbranch_vccz 3f\n\t"
                    "buffer_load_dwordx4 v[128:131], %[zo], %[rw], %[zb] offen sc1\n\t"
                    "buffer_load_dwordx4 v[132:135], %[zo], %[rw], %[zb] offen offset:1024 sc1\n\t"
                    "buffer_load_dwordx4 v[136:139], %[zo], %[rw], %[zb] offen offset:2048 sc1\n\t"
                    "buffer_load_dwordx4 v[140:143], %[zo], %[rw], %[zb] offen offset:3072 sc1\n\t"
                    "buffer_load_dwordx4 v[144:147], %[zo], %[rw], %[z4] offen sc1\n\t"
                    "buffer_load_dwordx4 v[148:151], %[zo], %[rw], %[z4] offen offset:1024 sc1\n\t"
                    "buffer_load_dwordx4 v[152:155], %[zo], %[rw], %[z4] offen offset:2048 sc1\n\t"
                    "buffer_load_dwordx4 v[156:159], %[zo], %[rw], %[z4] offen offset:3072 sc1\n\t"
                    "s_waitcnt vmcnt(0)\n\t"
                    "v_max3_u32 v101, v129, v131, v133\n\t"
                    "v_max3_u32 v101, v101, v135, v137\n\t"
                    "v_max3_u32 v101, v101, v139, v141\n\t"
                    "v_max3_u32 v101, v101, v143, v145\n\t"
                    "v_max3_u32 v101, v101, v147, v149\n\t"
                    "v_max3_u32 v101, v101, v151, v153\n\t"
                    "v_max3_u32 v101, v101, v155, v157\n\t"
                    "v_max_u32 v101, v101, v159\n\t"
                    "v_mov_b32 v100, v101\n\t"
                    "3:\n\t"
                    "v_permlane16_swap_b32 v128, v129\n\t"
                    "v_permlane16_swap_b32 v130, v131\n\t"
                    "v_permlane16_swap_b32 v132, v133\n\t"
                    "v_permlane16_swap_b32 v134, v135\n\t"
                    "v_permlane16_swap_b32 v136, v137\n\t"
                    "v_permlane16_swap_b32 v138, v139\n\t"
                    "v_permlane16_swap_b32 v140, v141\n\t"
                    "v_permlane16_swap_b32 v142, v143\n\t"
                    "v_permlane16_swap_b32 v144, v145\n\t"
                    "v_permlane16_swap_b32 v146, v147\n\t"
                    "v_permlane16_swap_b32 v148, v149\n\t"
                    "v_permlane16_swap_b32 v150, v151\n\t"
                    "v_permlane16_swap_b32 v152, v153\n\t"
                    "v_permlane16_swap_b32 v154, v155\n\t"
                    "v_permlane16_swap_b32 v156, v157\n\t"
                    "v_permlane16_swap_b32 v158, v159\n\t"
                    "s_mov_b32 vcc_lo, 0x00ff00ff\n\t"
                    "s_mov_b32 vcc_hi, 0x00ff00ff\n\t"
                    "s_waitcnt vmcnt(7)\n\t"
                    OST("%[t3]")
                    "v_smfmac_f32_16x16x64_bf16 v[96:99], v[208:211], v[160:167], %[ix]\n\t"
                    "v_max3_u32 v100, v100, v208, v209\n\t"
                    "v_max3_u32 v100, v100, v210, v211\n\t"
                    "s_waitcnt vmcnt(6)\n\t"
                    "v_smfmac_f32_16x16x64_bf16 v[96:99], v[212:215], v[168:175], %[ix]\n\t"
                    "v_max3_u32 v100, v100, v212, v213\n\t"
                    "v_max3_u32 v100, v100, v214, v215\n\t"
                    "s_waitcnt vmcnt(5)\n\t"
                    "v_smfmac_f32_16x16x64_bf16 v[96:99], v[216:219], v[176:183], %[ix]\n\t"
                    "v_max3_u32 v100, v100, v216, v217\n\t"
                    "v_max3_u32 v100, v100, v218, v219\n\t"
                    "s_waitcnt vmcnt(4)\n\t"
                    "v_smfmac_f32_16x16x64_bf16 v[96:99], v[220:223], v[184:191], %[ix]\n\t"
                    "v_max3_u32 v100, v100, v220, v221\n\t"
                    "v_max3_u32 v100, v100, v222, v223\n\t"
                    "s_waitcnt vmcnt(3)\n\t"
                    "v_smfmac_f32_16x16x64_bf16 v[96:99], v[224:227], v[128:135], %[ix]\n\t"
                    "v_max3_u32 v100, v100, v224, v225\n\t"
                    "v_max3_u32 v100, v100, v226, v227\n\t"
                    "s_waitcnt vmcnt(2)\n\t"
                    "v_smfmac_f32_16x16x64_bf16 v[96:99], v[228:231], v[136:143], %[ix]\n\t"
                    "s_barrier\n\t"
                    "ds_read_b64 v[102:103], %[sp]\n\t"
                    "ds_read_b64 v[104:105], %[sp] offset:512\n\t"
                    "ds_read_b64 v[106:107], %[sp] offset:1024\n\t"
                    "v_max3_u32 v100, v100, v228, v229\n\t"
                    "v_max3_u32 v100, v100, v230, v231\n\t"
                    "s_waitcnt vmcnt(1)\n\t"
                    "v_smfmac_f32_16x16x64_bf16 v[96:99], v[232:235], v[144:151], %[ix]\n\t"
                    "v_max3_u32 v100, v100, v232, v233\n\t"
                    "v_max3_u32 v100, v100, v234, v235\n\t"
                    "s_waitcnt vmcnt(0)\n\t"
                    "v_smfmac_f32_16x16x64_bf16 v[96:99], v[236:239], v[152:159], %[ix]\n\t"
                    "v_max3_u32 v100, v100, v236, v237\n\t"
                    "v_max3_u32 v100, v100, v238, v239\n\t"
                    OST("%[t2]")
                    "v_cmp_eq_u32 %[bad], -1, v100\n\t"
                    "s_waitcnt lgkmcnt(0)\n\t"
                    "v_add_f32 v102, v102, v104\n\t"
                    "v_add_f32 v103, v103, v105\n\t"
                    "v_add_f32 v102, v102, v106\n\t"
                    "v_add_f32 v103, v103, v107\n\t"
                    "v_add_f32 v96, v96, v102\n\t"
                    "v_add_f32 v97, v97, v103\n\t"
                    "v_add_f32 v96, v96, v98\n\t"
                    "v_add_f32 v97, v97, v99\n\t"
                    "s_cmp_lg_u64 %[bad], 0\n\t"
                    "s_cbranch_scc1 4f\n\t"
                    "2:\n\t"
                    "v_cndmask_b32_dpp v98, v97, v96, vcc row_ror:8 row_mask:0xf bank_mask:0xf\n\t"
                    "s_not_b64 vcc, vcc\n\t"
                    "v_cndmask_b32_dpp v99, v96, v97, vcc row_ror:8 row_mask:0xf bank_mask:0xf\n\t"
                    "v_cvt_pk_bf16_f32 v98, v98, v99\n\t"
                    "v_min_u32 v98, -2, v98\n\t"
                    "buffer_store_dword v98, %[mo], %[rw], %[m0] offen sc1\n\t"
                    "buffer_store_dword v98, %[mo], %[rw], %[m1] offen sc1\n\t"
                    "buffer_store_dword v98, %[mo], %[rw], %[m2] offen sc1\n\t"
                    "buffer_store_dword v98, %[mo], %[rw], %[m3] offen sc1\n\t"
                    OST("%[t4]")
                    "s_branch 5f\n\t"
                    "4:\n\t"
                    "s_sub_i32 %[tr], %[tr], 1\n\t"
                    "s_cmp_lt_i32 %[tr], 1\n\t"
                    "s_cbranch_scc1 2b\n\t"
                    "v_xor_b32 %[po], %[x0], %[po]\n\t"
                    "s_xor_b32 %[x0], %[x0], 0x14000\n\t"
                    "v_xor_b32 %[pm], %[x1], %[pm]\n\t"
                    "s_xor_b32 %[x1], %[x1], 0x5000\n\t"
                    "buffer_load_dwordx4 v[128:131], %[zo], %[rw], %[zb] offen sc1\n\t"
                    "buffer_load_dwordx4 v[132:135], %[zo], %[rw], %[zb] offen offset:1024 sc1\n\t"
                    "buffer_load_dwordx4 v[136:139], %[zo], %[rw], %[zb] offen offset:2048 sc1\n\t"
                    "buffer_load_dwordx4 v[140:143], %[zo], %[rw], %[zb] offen offset:3072 sc1\n\t"
                    "buffer_load_dwordx4 v[144:147], %[zo], %[rw], %[z4] offen sc1\n\t"
                    "buffer_load_dwordx4 v[148:151], %[zo], %[rw], %[z4] offen offset:1024 sc1\n\t"
                    "buffer_load_dwordx4 v[152:155], %[zo], %[rw], %[z4] offen offset:2048 sc1\n\t"
                    "buffer_load_dwordx4 v[156:159], %[zo], %[rw], %[z4] offen offset:3072 sc1\n\t"
                    "buffer_load_dwordx4 v[208:211], %[po], %[rw], %[pb] offen sc1\n\t"
                    "buffer_load_dwordx4 v[212:215], %[po], %[rw], %[pb] offen offset:1024 sc1\n\t"
                    "buffer_load_dwordx4 v[216:219], %[po], %[rw], %[pb] offen offset:2048 sc1\n\t"
                    "buffer_load_dwordx4 v[220:223], %[po], %[rw], %[pb] offen offset:3072 sc1\n\t"
                    "buffer_load_dwordx4 v[224:227], %[pm], %[rw], %[pb] offen sc1\n\t"
                    "buffer_load_dwordx4 v[228:231], %[pm], %[rw], %[pb] offen offset:1024 sc1\n\t"
                    "buffer_load_dwordx4 v[232:235], %[pm], %[rw], %[pb] offen offset:2048 sc1\n\t"
                    "buffer_load_dwordx4 v[236:239], %[pm], %[rw], %[pb] offen offset:3072 sc1\n\t"
                    "v_mov_b64 v[96:97], 0\n\t"
                    "v_mov_b64 v[98:99], 0\n\t"
                    "v_mov_b32 v100, 0\n\t"
                    "s_waitcnt vmcnt(0)\n\t"
                    "v_max3_u32 v101, v129, v131, v133\n\t"
                    "v_max3_u32 v101, v101, v135, v137\n\t"
                    "v_max3_u32 v101, v101, v139, v141\n\t"
                    "v_max3_u32 v101, v101, v143, v145\n\t"
                    "v_max3_u32 v101, v101, v147, v149\n\t"
                    "v_max3_u32 v101, v101, v151, v153\n\t"
                    "v_max3_u32 v101, v101, v155, v157\n\t"
                    "v_max_u32 v101, v101, v159\n\t"
                    "v_mov_b32 v100, v101\n\t"
                    "v_permlane16_swap_b32 v128, v129\n\t"
                    "v_permlane16_swap_b32 v130, v131\n\t"
                    "v_permlane16_swap_b32 v132, v133\n\t"
                    "v_permlane16_swap_b32 v134, v135\n\t"
                    "v_permlane16_swap_b32 v136, v137\n\t"
                    "v_permlane16_swap_b32 v138, v139\n\t"
                    "v_permlane16_swap_b32 v140, v141\n\t"
                    "v_permlane16_swap_b32 v142, v143\n\t"
                    "v_permlane16_swap_b32 v144, v145\n\t"
                    "v_permlane16_swap_b32 v146, v147\n\t"
                    "v_permlane16_swap_b32 v148, v149\n\t"
                    "v_permlane16_swap_b32 v150, v151\n\t"
                    "v_permlane16_swap_b32 v152, v153\n\t"
                    "v_permlane16_swap_b32 v154, v155\n\t"
                    "v_permlane16_swap_b32 v156, v157\n\t"
                    "v_permlane16_swap_b32 v158, v159\n\t"
                    "s_nop 1\n\t"
                    "v_smfmac_f32_16x16x64_bf16 v[96:99], v[208:211], v[160:167], %[ix]\n\t"
                    "v_max3_u32 v100, v100, v208, v209\n\t"
                    "v_max3_u32 v100, v100, v210, v211\n\t"
                    "v_smfmac_f32_16x16x64_bf16 v[96:99], v[212:215], v[168:175], %[ix]\n\t"
                    "v_max3_u32 v100, v100, v212, v213\n\t"
                    "v_max3_u32 v100, v100, v214, v215\n\t"
                    "v_smfmac_f32_16x16x64_bf16 v[96:99], v[216:219], v[176:183], %[ix]\n\t"
                    "v_max3_u32 v100, v100, v216, v217\n\t"
                    "v_max3_u32 v100, v100, v218, v219\n\t"
                    "v_smfmac_f32_16x16x64_bf16 v[96:99], v[220:223], v[184:191], %[ix]\n\t"
                    "v_max3_u32 v100, v100, v220, v221\n\t"
                    "v_max3_u32 v100, v100, v222, v223\n\t"
                    "v_smfmac_f32_16x16x64_bf16 v[96:99], v[224:227], v[128:135], %[ix]\n\t"
                    "v_max3_u32 v100, v100, v224, v225\n\t"
                    "v_max3_u32 v100, v100, v226, v227\n\t"
                    "v_smfmac_f32_16x16x64_bf16 v[96:99], v[228:231], v[136:143], %[ix]\n\t"
                    "v_max3_u32 v100, v100, v228, v229\n\t"
                    "v_max3_u32 v100, v100, v230, v231\n\t"
                    "v_smfmac_f32_16x16x64_bf16 v[96:99], v[232:235], v[144:151], %[ix]\n\t"
                    "v_max3_u32 v100, v100, v232, v233\n\t"
                    "v_max3_u32 v100, v100, v234, v235\n\t"
                    "v_smfmac_f32_16x16x64_bf16 v[96:99], v[236:239], v[152:159], %[ix]\n\t"
                    "v_max3_u32 v100, v100, v236, v237\n\t"
                    "v_max3_u32 v100, v100, v238, v239\n\t"
                    "v_cmp_eq_u32 %[bad], -1, v100\n\t"
                    "s_nop 5\n\t"
                    "v_add_f32 v96, v96, v102\n\t"
                    "v_add_f32 v97, v97, v103\n\t"
                    "v_add_f32 v96, v96, v98\n\t"
                    "v_add_f32 v97, v97, v99\n\t"
                    "s_cmp_lg_u64 %[bad], 0\n\t"
                    "s_cbranch_scc1 4b\n\t"
                    "s_branch 2b\n\t"
                    "5:\n\t"
                    : [tr] "+s"(tries), [po] "+v"(po), [pm] "+v"(pm), [x0] "+s"(x0), [x1] "+s"(x1), [bad] "=&s"(bad), [t0] "=&s"(t0_), [t1] "=&s"(t1_), [t2] "=&s"(t2_), [t3] "=&s"(t3_), [t4] "=&s"(t4_)
                    : [zo] "v"(zoff), [sp] "v"(spa), [ix] "v"(sidx), [rw] "s"(rwin), [zb] "s"(zb), [z4] "s"(zb + 4096u), [pb] "s"(pb), [mo] "v"(moff), [fd] "n"(FDL),
                      [m0] "s"(mb), [m1] "s"(mb + PAIR), [m2] "s"(mb + mc2), [m3] "s"(mb + mc2 + PAIR), "{v[160:167]}"(zf[0]), "{v[168:175]}"(zf[1]), "{v[176:183]}"(zf[2]), "{v[184:191]}"(zf[3])
                    : "v96", "v97", "v98", "v99", "v100", "v101", "v102", "v103", "v104", "v105", "v106", "v107", "v128", "v129", "v130", "v131", "v132", "v133", "v134", "v135", "v136", "v137", "v138", "v139", "v140", "v141", "v142", "v143", "v144", "v145", "v146", "v147", "v148", "v149", "v150", "v151", "v152", "v153", "v154", "v155", "v156", "v157", "v158", "v159", "v208", "v209", "v210", "v211", "v212", "v213", "v214", "v215", "v216", "v217", "v218", "v219", "v220", "v221", "v222", "v223", "v224", "v225", "v226", "v227", "v228", "v229", "v230", "v231", "v232", "v233", "v234", "v235", "v236", "v237", "v238", "v239", "vcc", "scc", "memory");
                budget = tries;
                if (w == 3) *(volatile LAS int*)(smem + 4100) = s;
                __builtin_amdgcn_s_sleep(DPS - 4);
                if (w == 3) {
                    st128(SENT, rwin, ra, W_MEM + (unsigned)(s & 3) * 6u * PAIR, 16);
                    if (lane == 0 && s > 1) __builtin_amdgcn_raw_buffer_store_b32((unsigned)s - 2u, rwin, 0u, W_GO + fo + (unsigned)o * 128u, 16);
                }
                for (;;) {
                    const u32x2 x = *(volatile LAS u32x2*)(smem + 4096);
                    zr = __builtin_amdgcn_readfirstlane(x[0]);
                    if (__builtin_amdgcn_readfirstlane(x[1]) == s) break;
                    __builtin_amdgcn_s_sleep(2);
                }
                zb = zb == W_ZM + 2u * NOWN * PAIR ? W_ZM : zb + NOWN * PAIR;
            }
            const unsigned zq = zb == W_ZM ? W_ZM + 2u * NOWN * PAIR : zb - NOWN * PAIR;
            __builtin_amdgcn_s_sleep(DTK);
            for (int e = 0; e < 2; ++e) {
                f32x4 c[2];
                for (unsigned spins = 0;;) {
                    asm volatile("" ::: "memory");
                    u32x4 pt[16], ptm[2][2], zm[2];
                    const unsigned pl = W_PL + fo + (unsigned)e * (PTSEG * 2u);
#pragma unroll
                    for (int q = 0; q < 2; ++q) zm[q] = __builtin_amdgcn_raw_buffer_load_b128(rwin, zoff + hg * 2048u + q * 1024u, e ? zb : zq, 16);
#pragma unroll
                    for (int q = 0; q < 16; ++q) pt[q] = __builtin_amdgcn_raw_buffer_load_b128(rwin, ((unsigned)(16 * hg) * 1024u + lane * 16u) | (q & 1 ? 0u : (lane & 32u) << 25), pl + q * 1024u, 16);
#pragma unroll
                    for (int q = 0; q < 4; ++q) ptm[q >> 1][q & 1] = __builtin_amdgcn_raw_buffer_load_b128(rwin, (unsigned)(hg * 2048 + lane * 16), pl + 65536u + (q >> 1) * 8192u + (q & 1) * 1024u, 16);
                    unsigned m = max(smax(zm[0]), smax(zm[1]));
#pragma unroll
                    for (int t = 0; t < 2; ++t) {
                        c[t] = z4;
#pragma unroll
                        for (int q = 0; q < 8; ++q) { c[t] = mfma32(half8(zf[q >> 1], q & 1), pt[2 * q + t], c[t]); m = max(m, smax(pt[2 * q + t])); }
#pragma unroll
                        for (int q = 0; q < 2; ++q) { c[t] = mfma32((u32x4){zm[q][0], zm[q][2], zm[q][1], zm[q][3]}, ptm[t][q], c[t]); m = max(m, smax(ptm[t][q])); }
                    }
                    if (!__any(m == 0xFFFFFFFFu)) break;
                    if (spin_fail(spins, rwin)) break;
                }
#pragma unroll
                for (int t = 0; t < 2; ++t) *(f32x4*)(op + e * 8192 + hg * 2048 + (16 * t + fr) * 64 + fq * 16) = c[t];
                BAR();
#pragma unroll
                for (int q = 0; q < 4; ++q) zf[q] = cat8(zn[2 * q], zn[2 * q + 1]);
                if (e == 0) BAR0();
            }
#pragma unroll
            for (int q = 0; q < 4; ++q) st128(SENT, rwin, zoff + hg * 2048u + (q & 1) * 1024u, q < 2 ? zb : zq, 16);
            if (w == 3) st128(SENT, rwin, ra, W_MEM + 3u * 6u * PAIR, 16);
        } else if (w == 4) {
            const unsigned fl = (unsigned)(lane & 31) * 128u + (unsigned)(o / 10) * 4u + (lane & 32) * 128u;
            int zk = -1;
            auto note = [&](int f) INL {
                f = min(f, __builtin_amdgcn_update_dpp(f, f, 0xB1, 0xF, 0xF, false)); f = min(f, __builtin_amdgcn_update_dpp(f, f, 0x4E, 0xF, 0xF, false));
                f = min(f, __builtin_amdgcn_update_dpp(f, f, 0x141, 0xF, 0xF, false)); f = min(f, __builtin_amdgcn_update_dpp(f, f, 0x140, 0xF, 0xF, false));
                zk = seg_done(MXV, min(__builtin_amdgcn_readlane(f, 0), __builtin_amdgcn_readlane(f, 16)), min(__builtin_amdgcn_readlane(f, 32), __builtin_amdgcn_readlane(f, 48)));
                *(volatile LAS int*)(smem + 4096) = zk;
            };
            for (int s = 0; s < NSEG; ++s) {
                for (unsigned spins = 0; __builtin_expect(zk < s + (s + 2 == NSEG), 0);) {
                    asm volatile("" ::: "memory");
                    note((int)__builtin_amdgcn_raw_buffer_load_b32(rwin, s ? fl : fl | (lane & 32u) << 25, W_FZ + fo, 16));
                    FC(fc2);
                    if (spin_fail(spins, rwin)) note(NSEG);
                    if (zk < s) __builtin_amdgcn_s_sleep(ZPS);
                }
                asm volatile("s_waitcnt lgkmcnt(0)\n\ts_barrier\n\ts_barrier" ::: "memory");
                int f = 0;
                const bool look = zk < min(s + ZLA, NSEG - 1);
                if (look) { __builtin_amdgcn_s_sleep(6); f = (int)__builtin_amdgcn_raw_buffer_load_b32(rwin, fl, W_FZ + fo, 16); }
                SB();
                BAR0();
                if (look) note(f);
            }
        } else if (w < 7) {
            const unsigned ooff = (unsigned)((S_ - (7 - w) * SEG + (lane >> 1)) * D_ + ot * 16 + (lane & 1) * 8) * 2u;
            for (int s = 0; s < 3 * NSEG - 3 + (w - 5); ++s) BAR0();
            if (w == 5) __builtin_amdgcn_s_sleep(DPS);
            const u32x4 xr = __builtin_amdgcn_raw_buffer_load_b128(rws, ooff, O_X1, 16);
            BAR0();
            if (w == 6) BAR0();
            const char* src = op + (w - 5) * 8192 + lane * 32;
            f32x4 x = *(const f32x4*)src, y = *(const f32x4*)(src + 16);
#pragma unroll
            for (int ww = 1; ww < 4; ++ww) { x += *(const f32x4*)(src + ww * 2048); y += *(const f32x4*)(src + ww * 2048 + 16); }
            const u32x4 r = {pack2(lo2f(xr[0]) + bfr(x[0]), hi2f(xr[0]) + bfr(x[1])), pack2(lo2f(xr[1]) + bfr(x[2]), hi2f(xr[1]) + bfr(x[3])),
                             pack2(lo2f(xr[2]) + bfr(y[0]), hi2f(xr[2]) + bfr(y[1])), pack2(lo2f(xr[3]) + bfr(y[2]), hi2f(xr[3]) + bfr(y[3]))};
            st128(nosent4(r), rout(), ooff, 0u, 16);
            if (w == 5) { BAR0(); BAR0(); }
        } else {
            Tk tk = tk_init();
            __builtin_amdgcn_s_setprio(3);
            for (int s = 0; s < NSEG; ++s) {
                const unsigned ta0 = ck.ta, mm0 = ck.ma;
                unsigned tp = tpp + lt + xs + M1;
                if (s < SAD) {
                    if (s) wait_fine(tpp + CF, ck);
                    tp = M2 + xs + (s ? 0 : PEN1);
                    sched(tk, s);
                    tp = tpp - tp;
                }
                wait_fine_bar(tp - DZP, ck);
                wait_cyc_bar(cyc_at(ck, sx24(tp + (s + 1 < NSEG ? 0 : ML) - ck.ta)));
                clk_rate(ck, ta0, mm0);
                asm volatile("s_barrier ; 7" ::: "memory");
                if (s >= SAD && s + 1 < NSEG) sched(tk, s);
                RTS();
            }
        }
        __builtin_amdgcn_s_setprio(0);
        SST(23);
    };
    static_assert(NSEG % 3 == 1, "ring");
    auto out_role = [&]() INL {
        LANES();
        const int o = bid - NREC, cg = o % 10, send = NSEG - 2;
        const unsigned ztoff = (unsigned)((4 * cg * H_ + 4 * w) * 64 + lane) * 16u;
        const unsigned ptoff = (unsigned)(8 * w * 64 + lane) * 16u, ptmoff = 65536u + (unsigned)(4 * w * 16 + lane) * 16u;
        const unsigned ptoff0 = ptoff | (lane & 32u) << 25;
        const unsigned ooff = (unsigned)(((w & 1) * 16 + (lane >> 2)) * D_ + tile_of(4 * cg + (w & 2)) * 16 + (lane & 3) * 8) * 2u;
        const unsigned fg = fo + (unsigned)((4 * w + (lane & 3)) * 128), gw = (unsigned)((4 * cg + ((lane >> 2) & 1) * 3) / 10) * 4u;
        const unsigned tfoff = fg + (lane < 16 ? W_FPT + (lane >> 2) * 4u : W_FZ2 + gw), zfoff = fg + W_FZ + gw + (lane & 32u) * 128u;
        int rdy = -1, go = -1, zrdy = -1;
        auto wait_rdy = [&](int s) INL {
            for (unsigned spins = 0; rdy < s;) {
                asm volatile("" ::: "memory");
                if (s < TAILS || go < s - 2) {
                    go = (int)__builtin_amdgcn_readfirstlane(__builtin_amdgcn_raw_buffer_load_b32(rwin, 0u, W_GO + fo + (unsigned)o * 128u, 16));
                    rdy = max(rdy, go);
                    if (rdy < s) { if (s < TAILS || go < s - 3) __builtin_amdgcn_s_sleep(OSLP); else __builtin_amdgcn_s_sleep(OSLP3); }
                } else {
                    if (!__any((int)__builtin_amdgcn_raw_buffer_load_b32(rwin, tfoff, 0u, 16) < s)) rdy = s;
                    else __builtin_amdgcn_s_sleep(OSLP2);
                }
                if (rdy < s && spin_fail(spins, rwin)) break;
            }
        };
        for (int s = o / 10; s < send; s += 4) {
            u32x4 xr, zf[4][4], zx[4], pt[8], ptm[2];
            RT(0);
            for (unsigned spins = 0; zrdy < s;) {
                asm volatile("" ::: "memory");
                int f = (int)__builtin_amdgcn_raw_buffer_load_b32(rwin, zfoff, 0u, 16);
                f = min(f, __builtin_amdgcn_update_dpp(f, f, 0xB1, 0xF, 0xF, false)); f = min(f, __builtin_amdgcn_update_dpp(f, f, 0x4E, 0xF, 0xF, false));
                f = min(f, __builtin_amdgcn_update_dpp(f, f, 0x141, 0xF, 0xF, false));
                zrdy = seg_done(MXV, __builtin_amdgcn_readlane(f, 0), __builtin_amdgcn_readlane(f, 32));
                if (zrdy >= s || spin_fail(spins, rwin)) break;
                __builtin_amdgcn_s_sleep(OSLP);
            }
            if (w >= 4) xr = __builtin_amdgcn_raw_buffer_load_b128(rws, ooff, O_X1 + (unsigned)(s * SEG * D_ * 2), 16);
#pragma unroll
            for (int q = 0; q < 16; ++q) zf[q >> 2][q & 3] = __builtin_amdgcn_raw_buffer_load_b128(rws, ztoff + (q & 3) * 1024u, O_Z + (unsigned)(s * ZTSEG) * 2u + (q >> 2) * (H_ * 1024u), 16);
            if (s == 61) SST(27);
            wait_rdy(s);
            if (s == 61) SST(28);
            RT(1);
            const unsigned ptb = O_PT + (unsigned)(s * PTSEG) * 2u;
#pragma unroll
            for (int q = 0; q < 8; ++q) pt[q] = __builtin_amdgcn_raw_buffer_load_b128(rws, (q & 1 ? ptoff : ptoff0) + q * 1024u, ptb, 16);
#pragma unroll
            for (int q = 0; q < 2; ++q) ptm[q] = __builtin_amdgcn_raw_buffer_load_b128(rws, ptmoff + q * 8192u, ptb, 16);
#pragma unroll
            for (int q = 0; q < 4; ++q) zx[q] = __builtin_amdgcn_raw_buffer_load_b128(rws, tid * 16u, O_Z2 + (unsigned)(s * NOWN + 4 * cg + q) * 8192u, 16);
            char* op = smem + ((s >> 2) & 1) * 65536;
            f32x4 c[8];
#pragma unroll
            for (int u = 0; u < 8; ++u) {
                c[u] = z4;
#pragma unroll
                for (int q = 0; q < 4; ++q) c[u] = mfma32(zf[u >> 1][q], pt[2 * q + (u & 1)], c[u]);
            }
#pragma unroll
            for (int u = 0; u < 8; ++u) {
                const int tq = u & 1, t = u >> 1;
                c[u] = mfma32((u32x4){zx[t][0], zx[t][2], zx[t][1], zx[t][3]}, ptm[tq], c[u]);
                *(f32x4*)(op + w * 8192 + (t >> 1) * 4096 + (16 * tq + fr) * 128 + ((((t & 1) * 4 + fq) ^ (fr & 7)) << 4)) = c[u];
            }
            BAR();
            if (s == 61) SST(30);
            if (w >= 4) {
                const int tk = (w & 1) * 16 + (lane >> 2);
                const char* s0 = op + (w & 2) * 2048 + tk * 128 + (((2 * (lane & 3)) ^ (tk & 7)) << 4);
                const char* s1 = op + (w & 2) * 2048 + tk * 128 + (((2 * (lane & 3) + 1) ^ (tk & 7)) << 4);
                f32x4 x = *(const f32x4*)s0, y = *(const f32x4*)s1;
#pragma unroll
                for (int ww = 1; ww < 8; ++ww) { x += *(const f32x4*)(s0 + ww * 8192); y += *(const f32x4*)(s1 + ww * 8192); }
                const u32x4 r = {pack2(lo2f(xr[0]) + bfr(x[0]), hi2f(xr[0]) + bfr(x[1])), pack2(lo2f(xr[1]) + bfr(x[2]), hi2f(xr[1]) + bfr(x[3])),
                                 pack2(lo2f(xr[2]) + bfr(y[0]), hi2f(xr[2]) + bfr(y[1])), pack2(lo2f(xr[3]) + bfr(y[2]), hi2f(xr[3]) + bfr(y[3]))};
                if (layer) st128(r, rout(), ooff, (unsigned)(s * SEG * D_ * 2), OAUX);
                else {
                    st128(nosent4(r), rout(), ooff, (unsigned)(s * SEG * D_ * 2), 16);
                    if (s < 56) {
                        VMWAIT();
                        __builtin_amdgcn_raw_buffer_store_b32(0u, rwin, LANE0, W_OUT + (unsigned)(s * 64 + 4 * cg + (w & 3)) * 4u, 16);
                    }
                }
            } else __builtin_amdgcn_s_sleep(OSL4);
            if (s < 8) SST(8 + s);
            if (s >= 56) SST(s - 40);
            RT(2);
            RTS();
        }
    };
    auto a1_units = [&](auto late) INL {
        constexpr bool LATE = decltype(late)::v;
        const int tid = mytid();
        RowTw tw;
        row_tw(tw, tid);
        for (int u = bid - NWRK + (LATE ? 224 : 0); u < (LATE ? 256 : 224); u += 16) {
            if (w == 4) {
                for (unsigned spins = 0;;) {
                    asm volatile("" ::: "memory");
                    if (LATE) {
                        if ((int)__builtin_amdgcn_readfirstlane(__builtin_amdgcn_raw_buffer_load_b32(rwin, 0u, W_GO + (unsigned)(u & 31) * 128u, 16)) >= min((u >> 2) - 2, NSEG - 4)) break;
                    } else if (!__any(__builtin_amdgcn_raw_buffer_load_b32(rwin, min(fresh_lane(), (unsigned)NOWN - 1u) * 4u, W_OUT + (unsigned)(u >> 2) * 256u, 16) != 0u)) break;
                    if (spin_fail(spins, rwin)) break;
                    if (u < 208) __builtin_amdgcn_s_sleep(WSLP); else __builtin_amdgcn_s_sleep(16);
                }
            }
            BAR();
            SST(24);
            a1_unit<4, LATE>(mkrsrc(ws + OFF_X2, S_ * D_ * 2), u * 8, rws, rwin, tw, tid, fs);
            if (!LATE) VMWAIT();
            FST(8);
            BAR0();
            FST(9);
            if (!LATE && w == 0) __builtin_amdgcn_raw_buffer_store_b64(__builtin_bit_cast(u32x2, tag + 9ull), rwin, LANE0, W_A1 + (unsigned)u * 128u, 16);
            SST(25);
            FSW(7);
        }
    };
    u32x8 wt[2][10];
#define PINW() asm volatile("" : "+{v[0:7]}"(wt[0][0]), "+{v[8:15]}"(wt[1][0]), "+{v[16:23]}"(wt[0][1]), "+{v[24:31]}"(wt[1][1]), "+{v[32:39]}"(wt[0][2]), "+{v[40:47]}"(wt[1][2]), "+{v[48:55]}"(wt[0][3]), "+{v[56:63]}"(wt[1][3]), "+{v[64:71]}"(wt[0][4]), "+{v[72:79]}"(wt[1][4]), "+{v[80:87]}"(wt[0][5]), "+{v[88:95]}"(wt[1][5]), "+{v[96:103]}"(wt[0][6]), "+{v[104:111]}"(wt[1][6]), "+{v[112:119]}"(wt[0][7]), "+{v[120:127]}"(wt[1][7]), "+{v[128:135]}"(wt[0][8]), "+{v[136:143]}"(wt[1][8]), "+{v[144:151]}"(wt[0][9]), "+{v[152:159]}"(wt[1][9]))
#define KILLW() asm volatile("" : "={v[0:7]}"(wt[0][0]), "={v[8:15]}"(wt[1][0]), "={v[16:23]}"(wt[0][1]), "={v[24:31]}"(wt[1][1]), "={v[32:39]}"(wt[0][2]), "={v[40:47]}"(wt[1][2]), "={v[48:55]}"(wt[0][3]), "={v[56:63]}"(wt[1][3]), "={v[64:71]}"(wt[0][4]), "={v[72:79]}"(wt[1][4]), "={v[80:87]}"(wt[0][5]), "={v[88:95]}"(wt[1][5]), "={v[96:103]}"(wt[0][6]), "={v[104:111]}"(wt[1][6]), "={v[112:119]}"(wt[0][7]), "={v[120:127]}"(wt[1][7]), "={v[128:135]}"(wt[0][8]), "={v[136:143]}"(wt[1][8]), "={v[144:151]}"(wt[0][9]), "={v[152:159]}"(wt[1][9]))
    if (blockIdx.x < 64) {
        for (;;) {
            load_qv(wt, bid & 31, bid >= 32);
            const unsigned x = get_rot();
            if (__builtin_expect(x == vx, 1)) break;
            set_role(x);
        }
        SST(1);
        if (w == 0) wait_tags(rwin, 0, 1, 64u, tag + 8ull, true);
        PINW();
    } else if (bid < 64) { vo_start(wt); PINW(); }
    else KILLW();
    for (; layer < 2; ++layer) {
        new_layer();
        PINW();
        BAR();
        if (layer ? bid < 96 || qx : bid >= 96) {
            const int ci = layer ? (qx ? bid - 80 : bid) : bid - 96;
            SST(26);
            a2_unit(ci >= 100 ? ci - 100 : ci + 60, layer, rws, rwin, mytid(), [&](int k) INL {
                if (k == 2) SST(28);
                else if (w == 0) {
                    if (layer && bid >= 64) for (unsigned spins = 0;;) {
                        asm volatile("" ::: "memory");
                        if (__all(__builtin_bit_cast(u64, __builtin_amdgcn_raw_buffer_load_b64(rwin, 0u, W_A1 + (unsigned)(200 + (bid & 15)) * 128u, 16)) == tag + 9ull) || spin_fail(spins, rwin)) break;
                        __builtin_amdgcn_s_sleep(100);
                    }
                    wait_tags(rwin, 0, 4, layer ? 32u : 64u, tag + 8ull + layer, !layer);
                    SST(27);
                }
            }, fs);
            SST(29);
            VMWAIT();
            FST(8);
            BAR0();
            FST(9);
            if (w == 0) {
                __builtin_amdgcn_raw_buffer_store_b32(0u, rwin, LANE0, W_COL + fo + (unsigned)ci * 128u, 16);
                if (bid == NQT && layer == 1) __builtin_amdgcn_raw_buffer_store_b64(__builtin_bit_cast(u32x2, tag), rws, LANE0, 0u, 16);
            }
            SST(30);
            FSW(1 + layer);
        }
#define NEXTB() do { PINW(); asm volatile("" : "+s"(bid), "+s"(w)); } while (0)
        NEXTB();
        if (bid < 32) { fresh_rsrc(); qk_role(wt); PINW(); }
        for (int ph = 0; ph < 2; ++ph) {
            asm volatile("" : "+s"(ph));
            NEXTB();
            if (bid >= 32 && bid < 64) { fresh_rsrc(); vo_role(wt, ph); PINW(); }
            NEXTB();
            asm volatile("" : "+s"(ph));
            const int hh = bid < 96 ? bid & 31 : bid < NQT ? (bid - NREC) / 10 * 8 + (bid - NREC) % 10 : (bid - NQT) & 31;
            if (ph == 0 && ((qx && layer == 0) || xq)) { load_qv(wt, hh, xt); SST(1); PINW(); }
            NEXTB();
            asm volatile("" : "+s"(ph));
            if (((bid >= 32 && bid < 96) || xt) && ph == 0) {
                if (bid >= 64 && bid < 96 && layer == 0) near_init();
                fresh_rsrc();
                vt_role(wt, hh, bid < 64 ? 2ull : xt ? MXV & ~2ull : ~MXV);
                PINW();
            }
            NEXTB();
            asm volatile("" : "+s"(ph));
            if (((qx && !xt) || xq) && ph == 0) { fresh_rsrc(); qt_role(wt, hh, IC<1>()); PINW(); }
            NEXTB();
            asm volatile("" : "+s"(ph));
        }
        NEXTB();
        if (bid >= 96 && !qx) {
            const int fi = xq ? 256 : bid >= NWRK ? 255 - bid : bid >= NREC ? 8 + (bid - NREC) / 10 * 2 + (bid - NREC) % 10 : bid - 72, tid = mytid();
            RowTw tw;
            row_tw(tw, tid);
            if (layer && fi < 64) {
                for (unsigned spins = 0;;) {
                    asm volatile("" ::: "memory");
                    if (__all(__builtin_bit_cast(u64, __builtin_amdgcn_raw_buffer_load_b64(rwin, 0u, W_A1 + (unsigned)fi * 128u, 16)) == tag + 9ull) || spin_fail(spins, rwin)) break;
                    __builtin_amdgcn_s_sleep(8);
                }
            }
            bool cols = false;
            for (int u = fi; u < 256; u += 16) {
                a3_unit(mkrsrc(layer ? (const u16*)(ws + OFF_X2) : arg(A_X), S_ * D_ * 2), u * 8, layer, rws, rwin, tw, tid, [&]() INL {
                    if (cols) return;
                    cols = true;
                    if (w == 7) {
                        const unsigned l = fresh_lane();
                        unsigned o[3] = {l * 128u, l * 128u + 8192u, l < 32u ? l * 128u + 16384u : 0x40000000u};
                        for (unsigned spins = 0;;) {
                            asm volatile("" ::: "memory");
                            unsigned v[3];
#pragma unroll
                            for (int g = 0; g < 3; ++g) v[g] = __builtin_amdgcn_raw_buffer_load_b32(rwin, o[g], W_COL + fo, 16);
#pragma unroll
                            for (int g = 0; g < 3; ++g) if (v[g] == 0u) o[g] = 0x40000000u;
                            if (!__any((v[0] | v[1] | v[2]) != 0u) || spin_fail(spins, rwin)) break;
                            __builtin_amdgcn_s_sleep(CSLP);
                        }
                    }
                    BAR0();
                    SST(6);
                }, fs);
                VMWAIT();
                FST(8);
                BAR0();
                FST(9);
                if (w == 0) __builtin_amdgcn_raw_buffer_store_b32(0u, rwin, LANE0, W_H + fo + (unsigned)((u >> 2) * 128 + (u & 3) * 4), 16);
                if (u < 64) SST(7);
                FSW(u < 64 ? 3 + layer : 5 + layer);
                if (fi >= 16) break;
                if (u < 64) u += 48;
            }
            if (bid < NREC) { fresh_rsrc(); own_role(); }
            else if (bid < NQT) out_role();
            else if (layer == 0) { a1_units(IC<0>()); a1_units(IC<1>()); }
            KILLW();
        }
        NEXTB();
        SST(31);
    }
}

constexpr int L_DBG = 0;
extern "C" size_t dbg_offset(int i) {
    const size_t o[] = {OFF_WIN, W_TMO, W_NEAR, OFF_F, OFF_X1, OFF_X2, OFF_H, OFF_WFT};
    return o[i];
}

extern "C" void kernel_launch(void* const* d_in, const int* in_sizes, int n_in, void* d_out, int out_size, void* d_ws, size_t ws_size,
                              hipStream_t stream) {
    char* ws = (char*)d_ws;
    hipFuncSetAttribute((const void*)mega_kernel, hipFuncAttributeMaxDynamicSharedMemorySize, L_END + L_DBG);
    char* win = (char*)(((uintptr_t)(ws + OFF_WIN) + 0x1FFFFFu) & ~(uintptr_t)0x1FFFFFu);
    Args a{ws, win, (const u16*)d_in[0], (const u16*)d_in[1], (const u16*)d_in[2], (const u16*)d_in[4], (const u16*)d_in[5], (const u16*)d_in[6], (const u16*)d_in[7], (const u16*)d_in[8], (u16*)d_out};
    mega_kernel<<<256, 512, L_END + L_DBG, stream>>>(a);
}
```

```cpp
#include <hip/hip_runtime.h>
#include <hip/hip_bf16.h>
#include <stdint.h>

typedef unsigned short u16;
typedef unsigned long long u64;
typedef __attribute__((ext_vector_type(8))) __bf16 bf16x8;
typedef __attribute__((ext_vector_type(4))) short s16x4;
typedef __attribute__((ext_vector_type(4))) float f32x4;
typedef __attribute__((ext_vector_type(8))) float f32x8;
typedef __attribute__((ext_vector_type(16))) float f32x16;
typedef __attribute__((ext_vector_type(4))) unsigned u32x4;
typedef __attribute__((ext_vector_type(2))) unsigned u32x2;
typedef __attribute__((ext_vector_type(8))) unsigned u32x8;
typedef __attribute__((ext_vector_type(16))) __bf16 bf16x16;
typedef __attribute__((ext_vector_type(2))) unsigned short u16x2;
#define LAS __attribute__((address_space(3)))
#define INL __attribute__((always_inline))

constexpr int S_ = 2048, D_ = 640, DF = 321, H_ = 32, DH = 128, HD = 4096, NM = 8, SEG = 32, NSEG = 64;
constexpr int NOWN = 40;
constexpr int NCOL = 160;
constexpr size_t ZTSEG = (size_t)NOWN * H_ * 512;
constexpr unsigned PAIR = 16384, HALF = 8192;
constexpr unsigned W_MEM = 0, W_T = W_MEM + 24 * PAIR, W_P = 33 * PAIR, W_PROBE = 74 * PAIR, W_GO = W_PROBE + 2 * PAIR, W_FZ = W_GO + 5120, W_FPT = W_FZ + 8192, W_FZ2 = W_FPT + 4096,
                   W_FQ = W_FZ2 + 4096, W_H = W_FQ + 8192, W_COL = W_H + HALF, W_PL = W_COL + 20480, W_LAYER = W_PL + 2 * 81920 - W_GO, W_OUT = 32 * PAIR, W_MISC = W_OUT + 14336,
                   W_ARM = W_GO + 2 * W_LAYER, W_A1 = 106 * PAIR, W_ZM = 2u * 1024 * 1024, W_END = W_ZM + 120 * PAIR;
static_assert(W_ARM <= W_A1 && W_ARM % 1024 == 0, "window");
__device__ __forceinline__ unsigned p_slot(int s) { return W_P + (unsigned)((s & 3) * 10 + (((s & 3) + 1) >> 2)) * PAIR; }
constexpr unsigned W_TMO = W_MISC, W_NEAR = W_MISC + 128;
constexpr int PTSEG = 32768 + 8192;

#define FPAD 256
constexpr unsigned FST_ = S_ * 16 + FPAD, GST = 4096 + FPAD, WST = S_ * 8 + FPAD;
constexpr size_t al(size_t x) { return (x + 255) & ~(size_t)255; }
constexpr size_t OFF_CTL  = 0;
constexpr size_t OFF_WFT  = 4096 + 4 * 1024 * 1024;
constexpr size_t OFF_F    = OFF_WFT + al((size_t)(NCOL + 2) * WST);
constexpr size_t OFF_X1   = OFF_F + al((size_t)NCOL * FST_);
constexpr size_t OFF_X2   = OFF_X1 + al((size_t)S_ * D_ * 2);
constexpr size_t OFF_FL   = OFF_X2 + al((size_t)S_ * D_ * 2);
constexpr int NHF = 5;
constexpr size_t OFF_H    = OFF_FL + 3 * (size_t)NCOL * GST + NHF * 40960;
constexpr size_t OFF_Z    = OFF_H + al((size_t)S_ * D_ * 2) + ZTSEG * 2;
constexpr size_t OFF_Z2   = OFF_Z + al(ZTSEG * NSEG * 2);
constexpr size_t OFF_PT   = OFF_Z2 + al((size_t)NSEG * NOWN * 8192);
constexpr size_t OFF_QK   = OFF_PT + al((size_t)NSEG * PTSEG * 2);
constexpr size_t OFF_WIN  = OFF_QK + (size_t)NSEG * H_ * 16384;
constexpr size_t OFF_END  = OFF_WIN + (size_t)6 * 1024 * 1024;
static_assert(OFF_END <= (size_t)256 * 1024 * 1024, "workspace");
constexpr unsigned O_FL = (unsigned)OFF_FL, O_ARM = O_FL - 256 * D_ * 2, O_ARM1 = O_FL + 2 * NCOL * GST, O_WFT = (unsigned)OFF_WFT, O_F = (unsigned)OFF_F, O_H = (unsigned)OFF_H, O_X1 = (unsigned)OFF_X1, O_Z = (unsigned)OFF_Z, O_Z2 = (unsigned)OFF_Z2, O_PT = (unsigned)OFF_PT, O_QK = (unsigned)OFF_QK;

constexpr double PI_ = 3.14159265358979323846;
constexpr double tay(double x, int s) {
    double t = s ? x : 1.0, r = t;
    for (int i = s + 1; i < 18; i += 2) { t *= -x * x / (double)(i * (i + 1)); r += t; }
    return r;
}
struct Cs { float c, s; };
constexpr Cs cis(int m, int n) {
    m %= n;
    const int q = (m * 8 / n + 1) / 2;
    const double x = 2.0 * PI_ * (double)(4 * m - q * n) / (double)(4 * n), c = tay(x, 0), s = tay(x, 1);
    return (q & 3) == 0 ? Cs{(float)c, (float)-s} : (q & 3) == 1 ? Cs{(float)-s, (float)-c} : (q & 3) == 2 ? Cs{(float)-c, (float)s} : Cs{(float)s, (float)c};
}
template <int NI> struct alignas(16) Tw7 { Cs v[4][NI][2]; };
template <int NI, int N, int KIND> constexpr Tw7<NI> mk_tw() {
    Tw7<NI> r{};
    for (int i = 0; i < NI; ++i)
        for (int q = 1; q <= 8; ++q) r.v[(q - 1) >> 1][i][(q - 1) & 1] = cis((KIND == 0 ? i : KIND == 1 ? 8 * (i >> 3) : (i >> 3) + 10 * (i & 7)) * q, N);
    return r;
}
struct TwCol { Tw7<256> a; Tw7<32> b; Tw7<4> c; };
static __device__ const TwCol TWC = {mk_tw<256, 2048, 0>(), mk_tw<32, 256, 0>(), mk_tw<4, 32, 0>()};
static __device__ const Tw7<80> TWR2 = mk_tw<80, 640, 1>();
static __device__ const Tw7<80> TWR3 = mk_tw<80, 640, 2>();

#define st128(v, r, vo, so, aux) do { u32x4 d_ = (v); asm volatile("" : "+v"(d_)); __builtin_amdgcn_raw_buffer_store_b128(d_, r, vo, so, aux); asm volatile("s_nop 1" :: "v"(d_)); } while (0)
__device__ __forceinline__ float bf2f(u16 v) { return __uint_as_float((unsigned)v << 16); }
__device__ __forceinline__ u16 f2bf(float f) { __bf16 b = (__bf16)f; return __builtin_bit_cast(u16, b); }
__device__ __forceinline__ float bfr(float f) { return (float)(__bf16)f; }
typedef __attribute__((ext_vector_type(2))) float f32x2;
typedef __attribute__((ext_vector_type(2))) __bf16 bf16x2;
__device__ __forceinline__ unsigned pack2(float a, float b) { return __builtin_bit_cast(unsigned, __builtin_convertvector((f32x2){a, b}, bf16x2)); }
__device__ __forceinline__ float lo2f(unsigned v) { return __uint_as_float(v << 16); }
__device__ __forceinline__ float hi2f(unsigned v) { return __uint_as_float(v & 0xFFFF0000u); }
__device__ __forceinline__ float fmx(float a, float b) { return __builtin_elementwise_maximum(a, b); }
__device__ __forceinline__ f32x4 mfma32(u32x4 a, u32x4 b, f32x4 c) {
    return __builtin_amdgcn_mfma_f32_16x16x32_bf16(__builtin_bit_cast(bf16x8, a), __builtin_bit_cast(bf16x8, b), c, 0, 0, 0);
}
__device__ __forceinline__ f32x4 mfma16(u32x2 a, u32x2 b, f32x4 c) {
    return __builtin_amdgcn_mfma_f32_16x16x16bf16_1k(__builtin_bit_cast(s16x4, a), __builtin_bit_cast(s16x4, b), c, 0, 0, 0);
}
__device__ __forceinline__ f32x4 smfmac(u32x4 a, u32x8 b, f32x4 c, int idx) {
    return __builtin_amdgcn_smfmac_f32_16x16x64_bf16(__builtin_bit_cast(bf16x8, a), __builtin_bit_cast(bf16x16, b), c, idx, 0, 0);
}
__device__ __forceinline__ int pi16(int k) { return ((k & 3) << 2) | ((k >> 1) & 2) | (k >> 3); }
__device__ __forceinline__ float addswap32(float a, float b) {
    auto r = __builtin_amdgcn_permlane32_swap(__float_as_uint(a), __float_as_uint(b), false, false);
    return __uint_as_float(r[0]) + __uint_as_float(r[1]);
}
__device__ __forceinline__ float xmax3216(float v) {
    auto r = __builtin_amdgcn_permlane32_swap(__float_as_uint(v), __float_as_uint(v), false, false);
    v = fmx(__uint_as_float(r[0]), __uint_as_float(r[1]));
    auto q = __builtin_amdgcn_permlane16_swap(__float_as_uint(v), __float_as_uint(v), false, false);
    return fmx(__uint_as_float(q[0]), __uint_as_float(q[1]));
}
__device__ __forceinline__ float xsum3216(float v) {
    auto r = __builtin_amdgcn_permlane32_swap(__float_as_uint(v), __float_as_uint(v), false, false);
    v = __uint_as_float(r[0]) + __uint_as_float(r[1]);
    auto q = __builtin_amdgcn_permlane16_swap(__float_as_uint(v), __float_as_uint(v), false, false);
    return __uint_as_float(q[0]) + __uint_as_float(q[1]);
}
template <int CTRL> __device__ __forceinline__ float dppf(float v) { return __uint_as_float((unsigned)__builtin_amdgcn_update_dpp(0, (int)__float_as_uint(v), CTRL, 0xF, 0xF, false)); }
__device__ __forceinline__ float wsum(float v) {
    v += dppf<0xB1>(v); v += dppf<0x4E>(v); v += dppf<0x141>(v); v += dppf<0x140>(v);
    return xsum3216(v);
}
__device__ __forceinline__ unsigned ror8(unsigned v) { return (unsigned)__builtin_amdgcn_update_dpp(0, (int)v, 0x128, 0xF, 0xF, false); }
__device__ __forceinline__ float ror8f(float v) { return __uint_as_float(ror8(__float_as_uint(v))); }
__device__ __forceinline__ unsigned nosent(unsigned v) {
    u16x2 lim = {0xFFFE, 0xFFFE};
    return __builtin_bit_cast(unsigned, __builtin_elementwise_min(__builtin_bit_cast(u16x2, v), lim));
}
__device__ __forceinline__ unsigned pkmax(unsigned a, unsigned b) {
    return __builtin_bit_cast(unsigned, __builtin_elementwise_max(__builtin_bit_cast(u16x2, a), __builtin_bit_cast(u16x2, b)));
}
__device__ __forceinline__ bool has_sent(u32x4 v) {
    unsigned m = pkmax(pkmax(v.x, v.y), pkmax(v.z, v.w));
    return (m & 0xFFFFu) == 0xFFFFu || (m >> 16) == 0xFFFFu;
}

__device__ __forceinline__ unsigned nosent1(unsigned v) { return min(v, 0xFFFFFFFEu); }
__device__ __forceinline__ unsigned smax(u32x4 v) { return max(v[1], v[3]); }
__device__ __forceinline__ u32x4 sent4() { unsigned x; asm volatile("v_mov_b32 %0, -1" : "=v"(x)); return (u32x4){x, x, x, x}; }
#define SENT sent4()
__device__ __forceinline__ f32x4 zero4() { f32x2 x, y; asm volatile("v_mov_b64 %0, 0\n\tv_mov_b64 %1, 0" : "=v"(x), "=v"(y)); return (f32x4){x[0], x[1], y[0], y[1]}; }
__device__ __forceinline__ u32x4 nosent4(u32x4 v) { return (u32x4){nosent1(v[0]), nosent1(v[1]), nosent1(v[2]), nosent1(v[3])}; }
#define SPIN_CAP 400000u
__device__ __forceinline__ void set_tmo(__amdgpu_buffer_rsrc_t rwin) { __builtin_amdgcn_raw_buffer_store_b32(0u, rwin, 0u, W_TMO, 16); }
__device__ __forceinline__ bool spin_fail(unsigned& spins, __amdgpu_buffer_rsrc_t rwin) {
    if (++spins > SPIN_CAP) { set_tmo(rwin); return true; }
    return (spins & 1023u) == 0u && __builtin_amdgcn_readfirstlane(__builtin_amdgcn_raw_buffer_load_b32(rwin, 0u, W_TMO, 16)) == 0u;
}
#define GATE 1
#define PS1 3
#define PS2 3
#define PS3 2

typedef f32x2 c2;
__device__ __forceinline__ c2 swp(c2 a) { return __builtin_shufflevector(a, a, 1, 0); }
__device__ __forceinline__ c2 fm(c2 a, c2 b, c2 c) { return __builtin_elementwise_fma(a, b, c); }
__device__ __forceinline__ c2 bc(float x) { return (c2){x, x}; }
__device__ __forceinline__ c2 sgn() { c2 s = {-1.f, 1.f}; asm("" : "+s"(s)); return s; }
template <int SG> __device__ __forceinline__ c2 addi(c2 a, c2 b) { return fm(swp(b), SG > 0 ? sgn() : swp(sgn()), a); }
template <int SG> __device__ __forceinline__ c2 cfin(c2 a, c2 w, c2 t) {
    c2 d;
    if (SG < 0) asm("v_pk_fma_f32 %0, %1, %2, %3 op_sel:[1,1,0] op_sel_hi:[0,1,1] neg_lo:[1,0,0]" : "=v"(d) : "v"(a), "v"(w), "v"(t));
    else asm("v_pk_fma_f32 %0, %1, %2, %3 op_sel:[1,1,0] op_sel_hi:[0,1,1] neg_hi:[1,0,0]" : "=v"(d) : "v"(a), "v"(w), "v"(t));
    return d;
}
struct Tw { f32x4 v[4]; };
template <int NI> __device__ __forceinline__ void ldtw(Tw& t, const Tw7<NI>& tab, int i) {
#pragma unroll
    for (int j = 0; j < 4; ++j) t.v[j] = *(const f32x4*)tab.v[j][i];
}
template <int NI> __device__ __forceinline__ void ldtw(Tw& t, const LAS f32x4* p) {
#pragma unroll
    for (int j = 0; j < 4; ++j) t.v[j] = p[j * NI];
}
template <int SG> __device__ __forceinline__ void twid(c2 (&u)[8], const Tw& t) {
#pragma unroll
    for (int r = 1; r < 8; ++r) {
        const f32x4 x = t.v[(r - 1) >> 1];
        const c2 w = r & 1 ? (c2){x[0], x[1]} : (c2){x[2], x[3]};
        u[r] = cfin<SG>(u[r], w, u[r] * bc(w.x));
    }
}
template <int SG> __device__ __forceinline__ void dft4(c2& a, c2& b, c2& c, c2& d) {
    const c2 t0 = a + c, t1 = a - c, t2 = b + d, t3 = b - d;
    a = t0 + t2; c = t0 - t2; b = addi<SG>(t1, t3); d = addi<-SG>(t1, t3);
}
template <int SG> __device__ __forceinline__ void dft8(c2 (&u)[8]) {
    c2 e0 = u[0], e1 = u[2], e2 = u[4], e3 = u[6], o0 = u[1], o1 = u[3], o2 = u[5], o3 = u[7];
    dft4<SG>(e0, e1, e2, e3); dft4<SG>(o0, o1, o2, o3);
    const c2 r = bc(0.70710678118654752440f);
    o1 = addi<SG>(o1, o1); o3 = addi<-SG>(o3, o3);
    u[0] = e0 + o0; u[4] = e0 - o0;
    u[1] = fm(o1, r, e1); u[5] = fm(o1, -r, e1);
    u[2] = addi<SG>(e2, o2); u[6] = addi<-SG>(e2, o2);
    u[3] = fm(o3, -r, e3); u[7] = fm(o3, r, e3);
}
template <int SG> __device__ __forceinline__ void dft5(c2& x0, c2& x1, c2& x2, c2& x3, c2& x4) {
    const c2 c1 = bc(0.30901699437494742410f), c2_ = bc(-0.80901699437494742410f), s1 = bc(0.95105651629515357212f), s2 = bc(0.58778525229247312917f);
    const c2 t1 = x1 + x4, t2 = x2 + x3, t3 = x1 - x4, t4 = x2 - x3;
    const c2 a1 = fm(c2_, t2, fm(c1, t1, x0)), a2 = fm(c1, t2, fm(c2_, t1, x0));
    const c2 b1 = fm(s2, t4, s1 * t3), b2 = fm(-s1, t4, s2 * t3);
    x0 = x0 + t1 + t2;
    x1 = addi<SG>(a1, b1); x4 = addi<-SG>(a1, b1); x2 = addi<SG>(a2, b2); x3 = addi<-SG>(a2, b2);
}
template <int SG> __device__ __forceinline__ void dft10(c2 (&u)[10]) {
    c2 e0 = u[0], e1 = u[2], e2 = u[4], e3 = u[6], e4 = u[8], o0 = u[5], o1 = u[7], o2 = u[9], o3 = u[1], o4 = u[3];
    dft5<SG>(e0, e1, e2, e3, e4); dft5<SG>(o0, o1, o2, o3, o4);
    u[0] = e0 + o0; u[5] = e0 - o0; u[6] = e1 + o1; u[1] = e1 - o1; u[2] = e2 + o2; u[7] = e2 - o2; u[8] = e3 + o3; u[3] = e3 - o3; u[4] = e4 + o4; u[9] = e4 - o4;
}
__device__ __forceinline__ void swap32(c2& v) { auto r = __builtin_amdgcn_permlane32_swap(__float_as_uint(v.x), __float_as_uint(v.y), false, false); v = (c2){__uint_as_float(r[0]), __uint_as_float(r[1])}; }
template <int B> __device__ __forceinline__ void swapl(c2& a, c2& b) {
#pragma unroll
    for (int i = 0; i < 2; ++i) {
        auto r = B == 5 ? __builtin_amdgcn_permlane32_swap(__float_as_uint(a[i]), __float_as_uint(b[i]), false, false) : __builtin_amdgcn_permlane16_swap(__float_as_uint(a[i]), __float_as_uint(b[i]), false, false);
        a[i] = __uint_as_float(r[0]); b[i] = __uint_as_float(r[1]);
    }
}
__device__ __forceinline__ c2 unpk(unsigned v) { return (c2){lo2f(v), hi2f(v)}; }
__device__ __forceinline__ float hsum32(c2 s) {
    float v = s.x + s.y;
    v += dppf<0xB1>(v); v += dppf<0x4E>(v); v += dppf<0x141>(v); v += dppf<0x140>(v);
    auto q = __builtin_amdgcn_permlane16_swap(__float_as_uint(v), __float_as_uint(v), false, false);
    return __uint_as_float(q[0]) + __uint_as_float(q[1]);
}
__device__ __forceinline__ void ln_row(const c2 (&v)[10], unsigned (&o)[10]) {
    const c2 s = ((v[0] + v[1]) + (v[2] + v[3])) + ((v[4] + v[5]) + (v[6] + v[7])) + (v[8] + v[9]);
    const float mean = hsum32(s) * (1.f / 640.f);
    c2 q0 = bc(0.f), q1 = bc(0.f);
#pragma unroll
    for (int r = 0; r < 10; r += 2) { const c2 d = v[r] - bc(mean), e = v[r + 1] - bc(mean); q0 = fm(d, d, q0); q1 = fm(e, e, q1); }
    const float mb = bfr(mean);
    c2 d[10];
#pragma unroll
    for (int r = 0; r < 10; ++r) { const c2 x = v[r] - bc(mb); d[r] = unpk(pack2(x.x, x.y)); }
    const float var = hsum32(q0 + q1) * (1.f / 640.f);
    const float rs = __builtin_amdgcn_rcpf(bfr(sqrtf(bfr(bfr(var) + 1e-5f))));
#pragma unroll
    for (int r = 0; r < 10; ++r) { const c2 x = d[r] * bc(rs); o[r] = pack2(x.x, x.y); }
}

__device__ __forceinline__ __amdgpu_buffer_rsrc_t mkrsrc(const void* p, unsigned bytes) {
    return __builtin_amdgcn_make_buffer_rsrc((void*)p, 0, bytes, 0x00020000);
}
__device__ __forceinline__ unsigned xcc_id() { unsigned x; asm volatile("s_getreg_b32 %0, hwreg(HW_REG_XCC_ID)" : "=s"(x)); return x & 15u; }
#define VMWAIT() do { asm volatile("" ::: "memory"); __builtin_amdgcn_s_waitcnt(0x0F70); asm volatile("" ::: "memory"); } while (0)
#define BAR() asm volatile("s_waitcnt lgkmcnt(0)\n\ts_barrier" ::: "memory")
#define BAR0() asm volatile("s_barrier" ::: "memory")

#define FST(k) do {} while (0)
#define FSW(k) do {} while (0)
extern __shared__ __attribute__((aligned(16))) char smem[];
#define RST(k, lay, wv, sel) do {} while (0)
constexpr int R_C = 8 * 10 * 72 * 8;
__device__ __forceinline__ c2 ldv(const LAS void* p) { return *(const volatile LAS c2*)p; }
struct RowTw { Tw a, b; };
__device__ __forceinline__ void row_tw(RowTw& t, int tid) { const int i = tid % 80; ldtw(t.a, TWR2, i); ldtw(t.b, TWR3, i); }
template <int NF, bool LATE> __device__ __forceinline__ void a1_unit(__amdgpu_buffer_rsrc_t rx, int row0, __amdgpu_buffer_rsrc_t rws, __amdgpu_buffer_rsrc_t rwin, const RowTw& tw, int tid, unsigned (&fs)[12]) {
    const int wave = __builtin_amdgcn_readfirstlane(tid >> 6), lane = tid & 63;
    LAS c2* A = (LAS c2*)smem; LAS c2* C = (LAS c2*)(smem + R_C);
    FST(0);
    if (wave < NF) {
        unsigned d[10];
        for (unsigned spins = 0;;) {
            asm volatile("" ::: "memory");
#pragma unroll
            for (int r = 0; r < 10; ++r) d[r] = __builtin_amdgcn_raw_buffer_load_b32(rx, (lane & 31) * 4u + (lane >> 5) * (D_ * 2u) + r * 128u, (unsigned)(row0 + 2 * wave) * (D_ * 2), 16);
            if (!LATE) break;
            unsigned m = d[0];
#pragma unroll
            for (int r = 1; r < 10; ++r) m = max(m, d[r]);
            if (!__any(m == ~0u) || spin_fail(spins, rwin)) break;
            __builtin_amdgcn_s_sleep(PS1);
        }
        c2 u[10];
#pragma unroll
        for (int r = 0; r < 10; ++r) u[r] = unpk(d[r]);
        FST(1);
        if (LATE) RST(16, 0, wave, 0);
        ln_row(u, d);
        FST(2);
#pragma unroll
        for (int r = 0; r < 10; ++r) { u[r] = unpk(d[r]); swap32(u[r]); }
        dft10<-1>(u);
#pragma unroll
        for (int r = 0; r < 10; ++r) A[(wave * 10 + r) * 72 + (lane & 31) + 36 * (lane >> 5)] = u[r];
        FST(3);
    }
    BAR();
    FST(4);
#pragma unroll
    for (int c = 0; c < (NF > 4 ? 2 : 1); ++c) {
        if (tid < (NF > 4 ? (c ? 160 : 480) : 80 * NF)) {
            const int K = (tid >> 3) + 60 * c, m = tid & 7;
            LAS c2* a = A + K * 72;
            c2 u[8];
#pragma unroll
            for (int q = 0; q < 8; ++q) u[q] = ldv(a + (m >> 1) + 36 * (m & 1) + 4 * q);
            twid<-1>(u, tw.a);
            dft8<-1>(u);
#pragma unroll
            for (int p = 0; p < 8; ++p) a[9 * p + m] = u[p];
            asm volatile("" ::: "memory");
#pragma unroll
            for (int q = 0; q < 8; ++q) u[q] = ldv(a + 9 * m + q);
            twid<-1>(u, tw.b);
            dft8<-1>(u);
            LAS c2* o = C + (K / 10) * 638 + K + 10 * m;
#pragma unroll
            for (int p = 0; p < 8; ++p) o[80 * p] = u[p];
        }
    }
    FST(5);
    BAR();
    FST(6);
#pragma unroll
    for (int c = 0; c < (NF > 4 ? 5 : 3); ++c) {
        const int it = tid + 512 * c, kp = it / (2 * NF), row = it % (2 * NF);
        if (NF > 4 || c < 2 || tid < 256) {
            const LAS c2* z = C + (row >> 1) * 648;
            const f32x4 zk = *(const LAS f32x4*)(z + 2 * kp);
            const c2 n0 = ldv(z + (kp ? 640 - 2 * kp : 320)), n1 = ldv(z + 639 - 2 * kp);
            const bool odd = it & 1;
            f32x4 o = odd ? (f32x4){zk[1] + n0.y, n0.x - zk[0], zk[3] + n1.y, n1.x - zk[2]} : (f32x4){zk[0] + n0.x, zk[1] - n0.y, zk[2] + n1.x, zk[3] - n1.y};
            if (c == 0 && kp == 0) { o[0] = 2.f * (odd ? zk[1] : zk[0]); o[1] = 2.f * (odd ? n0.y : n0.x); }
            u32x4 ob = __builtin_bit_cast(u32x4, o);
            if (LATE) { ob[1] = nosent1(ob[1]); ob[3] = nosent1(ob[3]); }
            st128(ob, rws, kp * (LATE ? GST : FST_) + row * 16u, (LATE ? O_FL - (S_ - 256) * 16u : O_F) + (unsigned)row0 * 16u, 16);
        }
    }
    FST(7);
}
template <class FG> __device__ __forceinline__ void a3_unit(__amdgpu_buffer_rsrc_t rx, int row0, int layer, __amdgpu_buffer_rsrc_t rws, __amdgpu_buffer_rsrc_t rwin, const RowTw& tw, int tid, FG go, unsigned (&fs)[12]) {
    const int wave = __builtin_amdgcn_readfirstlane(tid >> 6), lane = tid & 63;
    LAS c2* A = (LAS c2*)smem; LAS c2* C = (LAS c2*)(smem + R_C);
    const unsigned n0 = (lane & 7) | ((lane & 24) << 1);
    c2 x[10];
    if (wave < 4) {
        const unsigned base = (unsigned)(row0 + 2 * wave) * (D_ * 2), vo = n0 * 2u + (lane >> 5) * (D_ * 2u);
#pragma unroll
        for (int r = 0; r < 10; ++r) x[r] = (c2){bf2f(__builtin_amdgcn_raw_buffer_load_b16(rx, vo + 128u * r, base, 16)), bf2f(__builtin_amdgcn_raw_buffer_load_b16(rx, vo + 16u + 128u * r, base, 16))};
    }
    const bool early = row0 < 256;
    if (!early) go();
    FST(0);
    u32x4 fv[3];
    {
        const unsigned vo = (tid >> 3) * (early ? GST : FST_) + (tid & 7) * 16u, st = (early ? GST : FST_) * 64u, so = (early ? O_FL + (unsigned)(2 - layer) * (NCOL * GST) : O_F) + (unsigned)row0 * 16u;
#pragma unroll
        for (int c = 0; c < 3; ++c) fv[c] = __builtin_amdgcn_raw_buffer_load_b128(rws, vo, c < 2 || wave < 4 ? so + c * st : 0x40000000u, 16);
        if (early) for (unsigned spins = 0;;) {
            if (!__any(max(max(smax(fv[0]), smax(fv[1])), smax(fv[2])) == ~0u) || spin_fail(spins, rwin)) break;
            __builtin_amdgcn_s_sleep(PS3);
            asm volatile("" ::: "memory");
            u32x4 x[3];
#pragma unroll
            for (int c = 0; c < 3; ++c) x[c] = __builtin_amdgcn_raw_buffer_load_b128(rws, smax(fv[c]) == ~0u ? vo : 0x40000000u, so + c * st, 16);
#pragma unroll
            for (int c = 0; c < 3; ++c) if (smax(fv[c]) == ~0u) fv[c] = x[c];
        }
    }
#pragma unroll
    for (int c = 0; c < 3; ++c) {
        const int it = tid + 512 * c, kp = it >> 3;
        if (c < 2 || tid < 256) {
            const bool odd = it & 1;
            const f32x4 v = __builtin_bit_cast(f32x4, fv[c]), p = {dppf<0xB1>(v[0]), dppf<0xB1>(v[1]), dppf<0xB1>(v[2]), dppf<0xB1>(v[3])};
            c2 z0 = odd ? (c2){p[0] + v[1], v[0] - p[1]} : (c2){v[0] - p[1], p[0] + v[1]};
            const c2 z1 = odd ? (c2){p[2] + v[3], v[2] - p[3]} : (c2){v[2] - p[3], p[2] + v[3]};
            int i0 = odd ? 640 - 2 * kp : 2 * kp;
            if (c == 0 && kp == 0) { z0 = odd ? (c2){p[1], v[1]} : (c2){v[0], p[0]}; i0 >>= 1; }
            LAS c2* z = C + ((it >> 1) & 3) * 648;
            z[i0] = z0; z[odd ? 639 - 2 * kp : 2 * kp + 1] = z1;
        }
    }
    FST(1);
    BAR();
    if (early) RST(3, layer, wave, 0);
    FST(2);
    if (tid < 320) {
        const int K = tid >> 3, m = tid & 7;
        LAS c2* a = A + K * 72;
        const LAS c2* o = C + (K / 10) * 638 + K + 10 * m;
        c2 u[8];
#pragma unroll
        for (int p = 0; p < 8; ++p) u[p] = ldv(o + 80 * p);
        dft8<1>(u);
        twid<1>(u, tw.b);
#pragma unroll
        for (int q = 0; q < 8; ++q) a[9 * q + m] = u[q];
        asm volatile("" ::: "memory");
#pragma unroll
        for (int p = 0; p < 8; ++p) u[p] = ldv(a + 9 * m + p);
        dft8<1>(u);
        twid<1>(u, tw.a);
#pragma unroll
        for (int q = 0; q < 8; ++q) a[m + 8 * (q >> 1) + 32 * (q & 1)] = u[q];
    }
    FST(3);
    BAR();
    FST(4);
    if (wave < 4) {
        c2 u[10];
#pragma unroll
        for (int r = 0; r < 10; ++r) u[r] = ldv(A + (wave * 10 + r) * 72 + lane);
        dft10<1>(u);
        unsigned x1[10], h[10];
#pragma unroll
        for (int r = 0; r < 10; ++r) {
            swap32(u[r]);
            const c2 y = x[r] + unpk(pack2(u[r].x, u[r].y));
            x1[r] = pack2(y.x, y.y);
            u[r] = unpk(x1[r]);
        }
        FST(5);
        ln_row(u, h);
        FST(6);
        LAS char* st = (LAS char*)C + wave * 5120;
        LAS char* hp = st + (lane >> 5) * 1280 + (n0 & 48) * 2 + pi16(n0 & 15) * 2;
#pragma unroll
        for (int r = 0; r < 10; ++r) *(LAS unsigned*)(hp + 128 * r) = nosent1(h[r]);
#pragma unroll
        for (int c = 0; c < 3; ++c) {
            if (c < 2 || lane < 32) {
                const int id = c * 64 + lane, rr = id >= 80, ch = id - 80 * rr, row = (row0 + 2 * wave + rr) & 31;
                st128(*(const LAS u32x4*)(st + id * 16), rws, (unsigned)((ch >> 3) * 4096 + row * 128 + (((ch & 7) ^ (row & 7)) << 4)), O_H + (unsigned)((row0 >> 5) - NHF * layer) * 40960u, 16);
            }
        }
        if (early) RST(4, layer, wave, 0);
        LAS char* xp = st + 2560 + (lane >> 5) * 1280 + n0 * 2;
#pragma unroll
        for (int r = 0; r < 10; ++r) { *(LAS u16*)(xp + 128 * r) = (u16)x1[r]; *(LAS u16*)(xp + 128 * r + 16) = (u16)(x1[r] >> 16); }
#pragma unroll
        for (int c = 0; c < 3; ++c)
            if (c < 2 || lane < 32) st128(*(const LAS u32x4*)(st + 2560 + (c * 64 + lane) * 16), rws, (c * 64 + lane) * 16u, O_X1 + (unsigned)(row0 + 2 * wave) * (D_ * 2), 16);
        FST(7);
    }
}

constexpr int C_T = 8 * 544 * 8, C_A = C_T + (int)sizeof(TwCol), C_B = C_A + 32768, C_W = C_B + 32768, C_END = C_W + 8 * 516 * 4;
constexpr float FSC = 0.5f / ((float)S_ * (float)D_);
template <class FG> __device__ __forceinline__ void a2_unit(int kp, int late, __amdgpu_buffer_rsrc_t rws, __amdgpu_buffer_rsrc_t rwin, int tid, FG go, unsigned (&fs)[12]) {
    const int w = __builtin_amdgcn_readfirstlane(tid >> 6), l = tid & 63;
    const unsigned fb = O_F + (unsigned)kp * FST_, wb = O_WFT + (unsigned)kp * WST;
    const int b = (l >> 1) & 7;
    const int k0 = w + 8 * b + 128 * (l >> 4);
    LAS c2* fa = (LAS c2*)(smem + C_A) + tid;
    c2 u[8];
    {
        f32x4 tv[3];
#pragma unroll
        for (int i = 0; i < 3; ++i) tv[i] = ((const f32x4*)&TWC)[min(tid + 512 * i, (int)sizeof(TwCol) / 16 - 1)];
        go(0);
#pragma unroll
        for (int i = 0; i < 3; ++i) ((LAS f32x4*)(smem + C_T))[min(tid + 512 * i, (int)sizeof(TwCol) / 16 - 1)] = tv[i];
        BAR0();
        FST(0);
        u32x4 wv[2];
#pragma unroll
        for (int r = 0; r < 2; ++r) wv[r] = __builtin_amdgcn_raw_buffer_load_b128(rws, tid * 16u, wb + r * 8192u, 16);
#pragma unroll
        for (int r = 0; r < 7; ++r) u[r] = __builtin_bit_cast(c2, __builtin_amdgcn_raw_buffer_load_b64(rws, tid * 8u, fb + r * 4096u, 16));
        if (!late) u[7] = __builtin_bit_cast(c2, __builtin_amdgcn_raw_buffer_load_b64(rws, tid * 8u, fb + 7 * 4096u, 16));
        LAS u32x2* wl = (LAS u32x2*)(smem + C_W) + (tid & 3) * 516 + (tid >> 2);
#pragma unroll
        for (int r = 0; r < 2; ++r) { wl[r * 128] = (u32x2){wv[r][0], wv[r][1]}; wl[r * 128 + 258] = (u32x2){wv[r][2], wv[r][3]}; }
        BAR();
        const LAS unsigned* wr = (const LAS unsigned*)(smem + C_W) + w * 516 + (l & 15) + 32 * (l >> 4);
#pragma unroll
        for (int r = 0; r < 8; ++r) fa[512 * r] = unpk(wr[16 * (r & 1) + 128 * (r >> 1)]) * bc(FSC);
    }
    const LAS f32x4* t1 = (const LAS f32x4*)(smem + C_T) + (tid >> 1);
    const LAS f32x4* t2 = (const LAS f32x4*)(smem + C_T + sizeof(Tw7<256>)) + (l >> 1);
    const LAS f32x4* t3 = (const LAS f32x4*)(smem + C_T + sizeof(Tw7<256>) + sizeof(Tw7<32>)) + (l >> 4);
    LAS c2* e0 = (LAS c2*)smem + tid;
    LAS c2* e1 = (LAS c2*)smem + w * 544 + l;
    LAS c2* e2 = (LAS c2*)smem + w * 544 + 2 * (l >> 4) + (l & 1);
    Tw tw;
    ldtw<256>(tw, t1);
    if (late) {
        go(2);
        FST(1);
        for (unsigned spins = 0;;) {
            asm volatile("" ::: "memory");
            const u32x2 x = __builtin_amdgcn_raw_buffer_load_b64(rws, tid * 8u, O_FL + (unsigned)kp * GST, 16);
            u[7] = __builtin_bit_cast(c2, x);
            if (!__any(x[1] == ~0u) || spin_fail(spins, rwin)) break;
            if (w < 6) __builtin_amdgcn_s_sleep(24); else __builtin_amdgcn_s_sleep(PS2);
        }
    }
    FST(2);
    dft8<-1>(u);
    twid<-1>(u, tw);
    if (kp == 0) {
#pragma unroll
        for (int i = 0; i < 4; ++i)
            asm volatile("s_mov_b32 m0, %3\n\ts_nop 3\n\tbuffer_load_dwordx4 %0, %1, %2 offen sc1 lds" :: "v"(l * 16u), "s"(rws), "s"(O_WFT + NCOL * WST + (w * 4 + i) * 1024u), "s"(C_B + (w * 4 + i) * 1024u), "v"(u[7]) : "m0", "memory");
    }
#pragma unroll
    for (int r = 0; r < 8; ++r) e0[544 * r] = u[r];
    ldtw<32>(tw, t2);
    BAR();
#pragma unroll
    for (int r = 0; r < 8; ++r) u[r] = ldv(e1 + 64 * r);
    dft8<-1>(u);
    twid<-1>(u, tw);
#pragma unroll
    for (int r = 0; r < 8; ++r) e1[68 * r] = u[r];
    asm volatile("" ::: "memory");
#pragma unroll
    for (int r = 0; r < 8; ++r) u[r] = ldv(e2 + 68 * b + 8 * r);
    ldtw<4>(tw, t3);
    dft8<-1>(u);
    twid<-1>(u, tw);
    c2 f[8];
#pragma unroll
    for (int r = 0; r < 8; ++r) f[r] = ldv(fa + 512 * r);
    asm volatile("s_nop 1" : "+v"(u[1]), "+v"(u[2]), "+v"(u[3]), "+v"(u[4]), "+v"(u[5]), "+v"(u[6]), "+v"(u[7]));
#pragma unroll
    for (int r = 0; r < 4; ++r) swapl<5>(u[r], u[r + 4]);
#pragma unroll
    for (int r = 0; r < 4; ++r) swapl<4>(u[(r & 1) + 2 * (r & 2)], u[(r & 1) + 2 * (r & 2) + 2]);
    dft4<-1>(u[0], u[2], u[4], u[6]); dft4<-1>(u[1], u[3], u[5], u[7]);
    FST(3);
    if (kp == 0) {
        LAS c2* z = (LAS c2*)(smem + C_W);
#pragma unroll
        for (int r = 0; r < 8; ++r) if (!(l & 1)) z[w * 258 + b + 16 * (l >> 4) + 8 * (r & 1) + 64 * (r >> 1)] = u[r];
        VMWAIT();
        BAR();
#pragma unroll
        for (int r = 0; r < 8; ++r) {
            const int kn = (S_ - k0 - 64 * (r & 1) - 512 * (r >> 1)) & (S_ - 1);
            const f32x4 ab = ((const LAS f32x4*)(smem + C_B))[256 * r + (tid >> 1)];
            const c2 zn = z[(kn & 7) * 258 + (kn >> 3)], g = {ab[2], ab[3]}, h = l & 1 ? f[r] : (c2){ab[0], ab[1]};
            const c2 y = cfin<1>(g, zn, g * bc(zn.x));
            u[r] = cfin<-1>(u[r], h, fm(u[r], bc(h.x), l & 1 ? bc(0.f) : y));
        }
    } else {
#pragma unroll
        for (int r = 0; r < 8; ++r) u[r] = cfin<-1>(u[r], f[r], u[r] * bc(f[r].x));
    }
    FST(4);
    dft4<1>(u[0], u[2], u[4], u[6]); dft4<1>(u[1], u[3], u[5], u[7]);
#pragma unroll
    for (int r = 0; r < 4; ++r) swapl<4>(u[(r & 1) + 2 * (r & 2)], u[(r & 1) + 2 * (r & 2) + 2]);
#pragma unroll
    for (int r = 0; r < 4; ++r) swapl<5>(u[r], u[r + 4]);
    twid<1>(u, tw);
    dft8<1>(u);
#pragma unroll
    for (int r = 0; r < 8; ++r) e2[66 * b + 8 * r] = u[r];
    asm volatile("" ::: "memory");
    ldtw<32>(tw, t2);
#pragma unroll
    for (int r = 0; r < 8; ++r) u[r] = ldv(e1 + 66 * r);
    twid<1>(u, tw);
    dft8<1>(u);
#pragma unroll
    for (int r = 0; r < 8; ++r) e1[64 * r] = u[r];
    ldtw<256>(tw, t1);
    BAR();
#pragma unroll
    for (int r = 0; r < 8; ++r) u[r] = ldv(e0 + 544 * r);
    twid<1>(u, tw);
    dft8<1>(u);
    FST(5);
    __builtin_amdgcn_raw_buffer_store_b64((u32x2){__float_as_uint(u[0].x), nosent1(__float_as_uint(u[0].y))}, rws, tid * 8u, O_FL + (unsigned)((2 - late) * NCOL + kp) * GST, 16);
#pragma unroll
    for (int r = 1; r < 8; ++r) __builtin_amdgcn_raw_buffer_store_b64(__builtin_bit_cast(u32x2, u[r]), rws, tid * 8u, fb + r * 4096u, 16);
    FST(6);
}

struct Args { char* ws; char* win; const u16 *x, *wr, *wi, *mem0, *wq, *wk, *wv, *wo; u16* out; };
__device__ __forceinline__ const u16* arg(int i) {
    const __attribute__((address_space(4))) u64* p = (const __attribute__((address_space(4))) u64*)__builtin_amdgcn_kernarg_segment_ptr();
    asm volatile("" : "+s"(p));
    return (const u16*)p[i];
}
enum { A_X = 2, A_WR, A_WI, A_MEM0, A_WQ, A_WK, A_WV, A_WO, A_OUT };
#define MXV 0x5556ull
#define MXQ 0x1554ull
#define NTAIL 7
static_assert(NTAIL >= 3, "the owners release segments up to NSEG - 4");
constexpr int TAILS = NSEG - NTAIL;
static_assert((MXV & 31) == 22 && (MXQ & 31) == 20, "segment sets");
constexpr int NREC = 96 + NOWN, NQT = NREC + NOWN, NXT = NQT + 32, NWRK = NXT + 32;
#define SST(k) do {} while (0)
#define LSEL 0
#define RT(k) do {} while (0)
#define RTS() do {} while (0)
#define B3ATTR
#define MTA(k)
#define MTRT() do {} while (0)
#define MT(k) do {} while (0)
#define MTS() do {} while (0)
#define FC(x) do {} while (0)
#define DQC 1200
#define DVC 1200
#define M1 44
#define M2 230
#define PEN 280
#define DZP 55
#define LT0 342
#define SAD 1
#define PEN1 230
#define PEN0 330
#define TSLP 1
#define OPR 2
#define BSPARSE 0
#define ZLAG 3
#define TKP 0
#define DPS 12
#define DXT 3000
#define LTM 12
#define XS0 10
#define XSU 8
#define XSM 32
#define LTU 4
#define LTS 3
#define XSS 3
#define TKS 3
#define LT1 450
#define FDL 2
#define ZLA 3
#define NORND 0
#define LTX 40
#define TKE 200
#define DVO 4000
#define ZPS 2
#define HLA 8
#define QB2 2
#define NB2 6
#define DZR 24
#define ML 150
#define NB1 3
#define DPT 3
#define DDMA 8
#define DZ2 800
#define QEARLY 4
#define OSLP 32
#define OSLP2 1
#define WSLP 64
#define DO0 5500
#define DW0 1200
#define DQT 2000
#define ZEARLY 4
#define HSLP 4
#define ASLP 4
#define CSLP 4
#define A1S 0
#define OSLP3 12
#define DVT 860
#define VSL 0
#define WARM 1
constexpr unsigned CODE_BYTES = 96000;
#define DTK 4
#define OSL4 10
#define OAUX 0
#define TSL0 6
#define SE 6
#define XE 20
#define PENS 20
#define CF 190
#define HSL2 16
#define DH1 10
#define DZ0 400
#define FZP 1
__device__ __forceinline__ unsigned fz_at(unsigned h, unsigned g) { return FZP ? g * 1024u + (h >> 2) * 128u + (h & 3u) * 4u : h * 128u + g * 4u; }
#define LKE 0
#define DW4 6
#define DIE 0
#define SB() __builtin_amdgcn_sched_barrier(0)
#define PIN1(a) asm volatile("" : "+v"(a) :: "memory")
#define PIN2(a, b) asm volatile("" : "+v"(a), "+v"(b) :: "memory")
__device__ __forceinline__ unsigned fresh_lane() { unsigned l; asm volatile("v_mbcnt_lo_u32_b32 %0, -1, 0\n\tv_mbcnt_hi_u32_b32 %0, -1, %0" : "=v"(l)); return l; }
#define LANE0 (min(fresh_lane(), 1u) << 30)
__device__ __forceinline__ void wait_until(unsigned long long t) { while ((long long)(__builtin_amdgcn_s_memtime() - t) < 0) __builtin_amdgcn_s_sleep(1); }
struct Clk { unsigned ta, ma, cpq, qpc; };
__device__ __forceinline__ int sx24(unsigned v) { return (int)(v << 8) >> 8; }
#define WAIT_FINE \
        "s_memrealtime vcc\n\t" \
        "s_memtime s[100:101]\n\t" \
        "s_brev_b32 %[fi], -2\n\t" \
        "s_waitcnt lgkmcnt(0)\n\t" \
        "s_mov_b32 %[rp], vcc_lo\n\t" \
        "s_lshl_b32 vcc_hi, vcc_lo, 2\n\t" \
        "s_sub_u32 %[x], %[t], vcc_hi\n\t" \
        "s_bfe_i32 %[x], %[x], 0x180000\n\t" \
        "s_min_i32 %[x], %[x], 4000\n\t" \
        "s_max_i32 %[x], %[x], -4000\n\t" \
        "s_add_u32 %[t], vcc_hi, %[x]\n\t" \
        "s_mul_i32 %[x], %[x], %[cq]\n\t" \
        "s_ashr_i32 %[x], %[x], 8\n\t" \
        "s_sub_u32 %[tm], %[t], %[ta]\n\t" \
        "s_bfe_i32 %[tm], %[tm], 0x180000\n\t" \
        "s_min_i32 %[tm], %[tm], 1000000\n\t" \
        "s_max_i32 %[tm], %[tm], -1000000\n\t" \
        "s_mul_i32 %[tm], %[tm], %[cq]\n\t" \
        "s_ashr_i32 %[tm], %[tm], 8\n\t" \
        "s_add_u32 %[tm], %[tm], %[ma]\n\t" \
        "s_sub_u32 %[tm], %[tm], s100\n\t" \
        "s_lshr_b32 vcc_hi, %[cq], 4\n\t" \
        "s_sub_u32 vcc_hi, %[x], vcc_hi\n\t" \
        "s_max_i32 %[tm], %[tm], vcc_hi\n\t" \
        "s_min_i32 %[tm], %[tm], %[x]\n\t" \
        "s_add_u32 %[tm], %[tm], s100\n" \
        "1:\n\t" \
        "s_memrealtime vcc\n\t" \
        "s_memtime s[100:101]\n\t" \
        "s_waitcnt lgkmcnt(0)\n\t" \
        "s_cmp_eq_u32 vcc_lo, %[rp]\n\t" \
        "s_cbranch_scc1 2f\n\t" \
        "s_mov_b32 %[rp], vcc_lo\n\t" \
        "s_lshl_b32 %[x], vcc_lo, 2\n\t" \
        "s_sub_u32 %[x], %[t], %[x]\n\t" \
        "s_bfe_i32 %[x], %[x], 0x180000\n\t" \
        "s_mul_i32 %[x], %[x], %[cq]\n\t" \
        "s_ashr_i32 %[x], %[x], 8\n\t" \
        "s_add_u32 %[x], %[x], s100\n\t" \
        "s_sub_u32 s101, %[x], %[tm]\n\t" \
        "s_cmp_lt_i32 s101, %[fi]\n\t" \
        "s_cselect_b32 %[tm], %[x], %[tm]\n\t" \
        "s_mov_b32 %[fi], 0\n" \
        "2:\n\t" \
        "s_sub_u32 %[x], %[tm], s100\n\t" \
        "s_cmp_gt_i32 %[x], 260\n\t" \
        "s_cbranch_scc1 1b\n\t" \
        "s_cmp_eq_u32 %[fi], 0\n\t" \
        "s_cselect_b32 %[ta], %[t], %[ta]\n\t" \
        "s_cselect_b32 %[ma], %[tm], %[ma]\n" \
        "3:\n\t" \
        "s_memtime s[100:101]\n\t" \
        "s_waitcnt lgkmcnt(0)\n\t" \
        "s_sub_u32 %[x], %[tm], s100\n\t" \
        "s_cmp_gt_i32 %[x], 92\n\t" \
        "s_cbranch_scc1 3b\n\t" \
        "s_max_i32 %[x], %[x], 0\n\t" \
        "s_andn2_b32 %[x], %[x], 3\n\t" \
        "s_sub_u32 %[x], 104, %[x]\n\t" \
        "s_getpc_b64 vcc\n\t" \
        "s_add_u32 vcc_lo, vcc_lo, %[x]\n\t" \
        "s_addc_u32 vcc_hi, vcc_hi, 0\n\t" \
        "s_setpc_b64 vcc\n\t" \
        ".rept 23\n\t" \
        "s_nop 0\n\t" \
        ".endr\n\t"
#define WAIT_FINE_OUT [tm] "=&s"(tm), [rp] "=&s"(rp), [x] "=&s"(x), [fi] "=&s"(fi), [t] "+s"(t), [ta] "+s"(c.ta), [ma] "+s"(c.ma)
__device__ __forceinline__ void wait_fine(unsigned t, Clk& c) {
    unsigned tm, rp, x, fi;
    asm volatile(WAIT_FINE : WAIT_FINE_OUT : [cq] "s"(c.cpq) : "vcc", "scc", "s100", "s101", "memory");
}
__device__ __forceinline__ void wait_fine_bar(unsigned t, Clk& c) {
    unsigned tm, rp, x, fi;
    asm volatile(WAIT_FINE "s_barrier" : WAIT_FINE_OUT : [cq] "s"(c.cpq) : "vcc", "scc", "s100", "s101", "memory");
}
__device__ __forceinline__ void wait_cyc_bar(unsigned tm) {
    unsigned x;
    asm volatile(
        "3:\n\t"
        "s_memtime s[100:101]\n\t"
        "s_waitcnt lgkmcnt(0)\n\t"
        "s_sub_u32 %0, %1, s100\n\t"
        "s_cmp_gt_i32 %0, 92\n\t"
        "s_cbranch_scc1 3b\n\t"
        "s_max_i32 %0, %0, 0\n\t"
        "s_andn2_b32 %0, %0, 3\n\t"
        "s_sub_u32 %0, 104, %0\n\t"
        "s_getpc_b64 vcc\n\t"
        "s_add_u32 vcc_lo, vcc_lo, %0\n\t"
        "s_addc_u32 vcc_hi, vcc_hi, 0\n\t"
        "s_setpc_b64 vcc\n\t"
        ".rept 23\n\t"
        "s_nop 0\n\t"
        ".endr\n\t"
        "s_barrier"
        : "=&s"(x) : "s"(tm) : "vcc", "scc", "s100", "s101", "memory");
}
__device__ __forceinline__ unsigned cyc_at(const Clk& c, int d) {
    d = min(d, 20000);
    asm volatile("" : "+s"(d));
    return c.ma + (unsigned)((max(d, -4000) * (int)c.cpq) >> 8);
}
__device__ __forceinline__ void clk_rate(Clk& c, unsigned ta0, unsigned ma0) {
    const int dq = sx24(c.ta - ta0);
    if (dq > 200) {
        const float r = fminf(fmaxf((float)(c.ma - ma0) * __builtin_amdgcn_rcpf((float)dq), 1.f), 8.f);
        asm volatile("s_nop 1\n\tv_readfirstlane_b32 %0, %2\n\tv_readfirstlane_b32 %1, %3\n\ts_nop 3" : "=s"(c.cpq), "=s"(c.qpc)
                     : "v"((unsigned)(256.f * r + 0.5f)), "v"((unsigned)(65536.f * __builtin_amdgcn_rcpf(r))));
    }
}
__device__ __forceinline__ void clk_init(Clk& c) {
    c.ta = 4u * (unsigned)__builtin_amdgcn_s_memrealtime(); c.ma = (unsigned)__builtin_amdgcn_s_memtime();
    for (int i = 0; i < 2; ++i) {
        const unsigned ta0 = c.ta, ma0 = c.ma;
        wait_fine(c.ta + (i ? 360u : 60u), c);
        clk_rate(c, ta0, ma0);
    }
}
__device__ __forceinline__ unsigned sched_lane(int s) { const unsigned l = fresh_lane(); return (l & 31u) * 128u + (l >> 5) * 4u; }
__device__ __forceinline__ int read_sched(__amdgpu_buffer_rsrc_t rwin, int s, unsigned vo, unsigned to, unsigned f, unsigned g0, int& lt, int& xs, unsigned& fc) {
    int d = (int)((f - g0) & 0xFFFFFFu);
    if (__builtin_expect(__any((f >> 24) != (unsigned)s) || s == 0, 0)) {
        for (unsigned spins = 0; __any((f >> 24) != (unsigned)s);) {
            FC(fc);
            if (spin_fail(spins, rwin)) break;
            if (s == 0) __builtin_amdgcn_s_sleep(TSL0); else __builtin_amdgcn_s_sleep(TSLP);
            asm volatile("" ::: "memory");
            if ((f >> 24) != (unsigned)s) f = __builtin_amdgcn_raw_buffer_load_b32(rwin, vo, to, 16);
        }
        const unsigned f0 = __builtin_amdgcn_readfirstlane(f) - 0x100000u;
        d = (int)((f - f0) & 0xFFFFFFu) + (int)((f0 - g0) & 0xFFFFFFu);
    }
    d = max(d, __builtin_amdgcn_update_dpp(0, d, 0xB1, 0xF, 0xF, true));
    d = max(d, __builtin_amdgcn_update_dpp(0, d, 0x4E, 0xF, 0xF, true));
    d = max(d, __builtin_amdgcn_update_dpp(0, d, 0x141, 0xF, 0xF, true));
    d = max(d, __builtin_amdgcn_update_dpp(0, d, 0x140, 0xF, 0xF, true));
    d = max(d, __builtin_amdgcn_update_dpp(0, d, 0x142, 0xA, 0xF, true));
    d = max(__builtin_amdgcn_readlane(d, 16), __builtin_amdgcn_readlane(d, 48));
    const int l0 = lt, x0 = xs;
    d -= 0x400000;
    const bool miss = d > l0 + min(x0 + LTM, LTX);
    if (s >= SAD) { lt = miss ? l0 : min(max(d, l0 - 1 - ((l0 - d) >> LTS)), l0 + LTU); xs = miss ? min(x0 + XSU, XSM) : max(x0 - 1 - (x0 >> XSS), 0); }
    if (s == 1) lt = LT0;
    if (s < SE) xs = max(xs, XE);
    return 0x400000 + (s < SAD ? d + (s ? PENS : PEN0) : miss ? d + PEN : l0 + x0) + M1 + M2 + x0;
}
constexpr int QS = 272;
constexpr int TQS = 1152;
constexpr int L_Q = 10240, L_K = L_Q + 2 * TQS, L_S = L_K + 2 * TQS, L_TOK = L_S + 256, L_H = L_TOK + 49152, L_END = L_H + 2 * 40960;
static_assert(C_END <= L_END && L_END == 146176, "LDS");
typedef __attribute__((ext_vector_type(4))) short v4i16_t;
__device__ __forceinline__ u32x2 ldtr(const char* p) { return __builtin_bit_cast(u32x2, __builtin_amdgcn_ds_read_tr16_b64_v4i16((LAS v4i16_t*)p)); }
__device__ __forceinline__ u32x2 ldtr1(const LAS char* p) { return __builtin_bit_cast(u32x2, __builtin_amdgcn_ds_read_tr16_b64_v4i16((LAS v4i16_t*)p)); }
__device__ __forceinline__ u32x4 ldtr2(const LAS char* p0, const LAS char* p1) {
    const u32x2 x = __builtin_bit_cast(u32x2, __builtin_amdgcn_ds_read_tr16_b64_v4i16((LAS v4i16_t*)p0)), y = __builtin_bit_cast(u32x2, __builtin_amdgcn_ds_read_tr16_b64_v4i16((LAS v4i16_t*)p1));
    return (u32x4){x[0], x[1], y[0], y[1]};
}
__device__ __forceinline__ u32x4 ldtr2(const char* p0, const char* p1) { const u32x2 x = ldtr(p0), y = ldtr(p1); return (u32x4){x[0], x[1], y[0], y[1]}; }

__device__ __forceinline__ unsigned poll_lim(int& budget) {
    --budget;
    unsigned lim = budget > 0 ? 0xFFFFFFFEu : 0xFFFFFFFFu;
    asm volatile("" : "+s"(lim));
    return lim;
}
#define GATHER \
        "buffer_load_dwordx4 %[vo], %[rw], %[s0] offen sc1 lds\n\t" \
        "buffer_load_dwordx4 %[vo], %[rw], %[s0] offen offset:1024 sc1 lds\n\t" \
        "buffer_load_dwordx4 %[vo], %[rw], %[s0] offen offset:2048 sc1 lds\n\t" \
        "buffer_load_dwordx4 %[vo], %[rw], %[s0] offen offset:3072 sc1 lds\n\t" \
        "s_movk_i32 m0, 0x1000\n\t" \
        "s_nop 0\n\t" \
        "buffer_load_dwordx4 %[vo], %[rw], %[s4] offen sc1 lds\n\t" \
        "buffer_load_dwordx4 %[vo], %[rw], %[s4] offen offset:1024 sc1 lds\n\t" \
        "buffer_load_dwordx4 %[vo], %[rw], %[s4] offen offset:2048 sc1 lds\n\t" \
        "buffer_load_dwordx4 %[vo], %[rw], %[s4] offen offset:3072 sc1 lds\n\t" \
        "s_movk_i32 m0, 0x2000\n\t" \
        "s_nop 0\n\t" \
        "buffer_load_dwordx4 %[vo], %[rw], %[s1] offen sc1 lds\n\t" \
        "buffer_load_dwordx4 %[vo], %[rw], %[s1] offen offset:1024 sc1 lds"
#define GATHER_IN [vo] "v"(vo), [rw] "s"(rwin), [s0] "s"(so0), [s4] "s"(so0 + 4096u), [s1] "s"(so1)
__device__ __forceinline__ void gather_dma(__amdgpu_buffer_rsrc_t rwin, unsigned vo, unsigned so0, unsigned so1) {
    asm volatile("s_mov_b32 m0, 0\n\ts_nop 0\n\t" GATHER :: GATHER_IN : "m0", "memory");
}
__device__ __forceinline__ void gather_at(unsigned t, Clk& c, __amdgpu_buffer_rsrc_t rwin, unsigned vo, unsigned so0, unsigned so1) {
    unsigned tm, rp, x, fi;
    asm volatile("s_mov_b32 m0, 0\n\t" WAIT_FINE GATHER : WAIT_FINE_OUT : [cq] "s"(c.cpq), GATHER_IN : "vcc", "scc", "s100", "s101", "m0", "memory");
}
__device__ __forceinline__ void dma4k(__amdgpu_buffer_rsrc_t r, unsigned vo, unsigned so, unsigned lds) {
    asm volatile(
        "s_mov_b32 m0, %3\n\t"
        "s_nop 0\n\t"
        "buffer_load_dwordx4 %0, %1, %2 offen sc1 lds\n\t"
        "buffer_load_dwordx4 %0, %1, %2 offen offset:1024 sc1 lds\n\t"
        "buffer_load_dwordx4 %0, %1, %2 offen offset:2048 sc1 lds\n\t"
        "buffer_load_dwordx4 %0, %1, %2 offen offset:3072 sc1 lds"
        :: "v"(vo), "s"(r), "s"(so), "s"(lds) : "m0", "memory");
}
__device__ __forceinline__ int bs2(int c) { return (c & ~3) | ((c & 1) << 1) | ((c >> 1) & 1); }
__device__ __forceinline__ void gather_mem0(char* memL, int tid) {
    const u16* mem0 = arg(A_MEM0);
    for (int c = tid; c < 640; c += 512) {
        const u32x2* src = (const u32x2*)(mem0 + ((c & 6) + ((c >> 3) & 1)) * D_ + (c >> 4) * 16 + (c & 1) * 4);
        const u32x2 x = src[0], y = src[2];
        *(u32x4*)(memL + c * 16) = (u32x4){(x[0] & 0xFFFFu) | (y[0] << 16), (x[0] >> 16) | (y[0] & 0xFFFF0000u), (x[1] & 0xFFFFu) | (y[1] << 16), (x[1] >> 16) | (y[1] & 0xFFFF0000u)};
    }
}
__device__ __forceinline__ f32x2 hadd(f32x4 c) { return (f32x2){c[0], c[1]} + (f32x2){c[2], c[3]}; }
__device__ __forceinline__ unsigned hpack(f32x4 c) { const f32x2 y = hadd(c); return pack2(y[0], y[1]); }
__device__ __forceinline__ u32x8 cat8(u32x4 lo, u32x4 hi) { return (u32x8){lo[0], lo[1], lo[2], lo[3], hi[0], hi[1], hi[2], hi[3]}; }
__device__ __forceinline__ u32x4 half8(const u32x8& v, int u) { return u ? (u32x4){v[4], v[5], v[6], v[7]} : (u32x4){v[0], v[1], v[2], v[3]}; }
__device__ __forceinline__ void dma1k(__amdgpu_buffer_rsrc_t r, unsigned vo, unsigned so, unsigned lds) {
    asm volatile("s_mov_b32 m0, %3\n\ts_nop 0\n\tbuffer_load_dwordx4 %0, %1, %2 offen sc1 lds" :: "v"(vo), "s"(r), "s"(so), "s"(lds) : "m0", "memory");
}
__device__ __forceinline__ void load_w(u32x8 (&wt)[2][10], const u16* src, unsigned rs, bool perm, unsigned b, unsigned c, int w) {
    const __amdgpu_buffer_rsrc_t r = mkrsrc(src, 0x7FFFFFFFu);
    const unsigned l = fresh_lane(), q = l >> 4, lds = (unsigned)(w >> 2) * 65536u, v = w & 3;
    const unsigned vo0 = (perm ? ((q & 1u) << 3) | ((q & 2u) << 1) : q) * rs + ((l & 15u) ^ (q << 1)) * 16u, vo1 = vo0 ^ 128u, a = perm ? rs : 4u * rs;
    const LAS char* p0 = (const LAS char*)(size_t)(lds + q * 2048u + ((l >> 2) & 3u) * 256u + ((2u * v) ^ ((l >> 2) & 7u)) * 32u + (l & 3u) * 8u);
    const LAS char* p1 = (const LAS char*)((size_t)p0 ^ 32u);
    auto issue = [&](int g) {
#pragma unroll
        for (int i = 0; i < 4; ++i)
            asm volatile("s_mov_b32 m0, %3\n\ts_nop 3\n\tbuffer_load_dwordx4 %0, %1, %2 offen lds" :: "v"(i < 2 ? vo0 : vo1), "s"(r), "s"(i * a + (g & 1) * b + (g >> 1) * c), "s"(lds + (g & 3) * 16384u + v * 4096u + i * 1024u) : "m0", "memory");
    };
#pragma unroll
    for (int g = 0; g < 3; ++g) issue(g);
#pragma unroll
    for (int g = 0; g < 10; ++g) {
        asm volatile("s_waitcnt vmcnt(%0)\n\ts_barrier" :: "n"(g < 8 ? 8 : 4 * (9 - g)) : "memory");
        if (g + 3 < 10) issue(g + 3);
        const int o = (g & 3) * 16384;
        wt[0][g] = cat8(ldtr2(p0 + o, p0 + o + 1024), ldtr2(p0 + o + 8192, p0 + o + 9216));
        wt[1][g] = cat8(ldtr2(p1 + o, p1 + o + 1024), ldtr2(p1 + o + 8192, p1 + o + 9216));
        asm volatile("s_waitcnt lgkmcnt(0)" : "+v"(wt[0][g]), "+v"(wt[1][g]) :: "memory");
    }
    BAR0();
}
__device__ __forceinline__ int tile_of(int o) { return 8 * ((o % 10) >> 1) + 2 * (o / 10) + (o & 1); }
__device__ __forceinline__ void wait_tags(__amdgpu_buffer_rsrc_t rwin, int g0, int g1, unsigned n1, u64 val, bool boot) {
    const unsigned l = fresh_lane();
    unsigned o[4];
#pragma unroll
    for (int g = 0; g < 4; ++g) o[g] = g >= g0 && g < g1 && (g + 1 < g1 || l < n1) ? l * 128u + g * 8192u : 0x40000000u;
    for (unsigned spins = 0;;) {
        asm volatile("" ::: "memory");
        u32x2 v[4];
#pragma unroll
        for (int g = 0; g < 4; ++g) v[g] = __builtin_amdgcn_raw_buffer_load_b64(rwin, o[g], W_A1, 16);
#pragma unroll
        for (int g = 0; g < 4; ++g) if (__builtin_bit_cast(u64, v[g]) == val) o[g] = 0x40000000u;
        if (!__any((o[0] & o[1] & o[2] & o[3]) != 0x40000000u)) break;
        if (boot ? ++spins > SPIN_CAP : spin_fail(spins, rwin)) break;
        __builtin_amdgcn_s_sleep(ASLP);
    }
}
template <int N> __device__ __forceinline__ void stage_h(__amdgpu_buffer_rsrc_t rws, unsigned src, unsigned lds, unsigned st) {
    const unsigned l = fresh_lane() * 16u;
#pragma unroll
    for (int i = 0; i < N; ++i) dma1k(rws, l, src + i * st, lds + i * st);
}
template <int N> __device__ __forceinline__ void check_h(__amdgpu_buffer_rsrc_t rws, __amdgpu_buffer_rsrc_t rwin, unsigned src, unsigned lds, unsigned st) {
    const LAS char* p = (const LAS char*)smem + lds + fresh_lane() * 16u;
    for (unsigned spins = 0;;) {
        unsigned m = 0;
#pragma unroll
        for (int i = 0; i < N; ++i) { const u32x4 x = *(const volatile LAS u32x4*)(p + i * st); m = max(max(m, max(x[0], x[1])), max(x[2], x[3])); }
        if (__builtin_expect(!__any(m == ~0u), 1) || spin_fail(spins, rwin)) break;
        stage_h<N>(rws, src, lds, st);
        VMWAIT();
    }
}
__device__ __forceinline__ void wait_h(__amdgpu_buffer_rsrc_t rwin, unsigned fo, int seg, int& hrdy) {
    unsigned lim = 63u;
    if (__builtin_expect(hrdy <= seg, 0)) for (unsigned spins = 0;;) {
        asm volatile("" ::: "memory");
        const u32x4 f = __builtin_amdgcn_raw_buffer_load_b128(rwin, min(fresh_lane(), lim) * 128u, W_H + fo, 16);
        const unsigned long long miss = __ballot((f[0] | f[1] | f[2] | f[3]) != 0u);
        hrdy = miss ? __builtin_ctzll(miss) : (int)lim + 1;
        if (hrdy > seg) break;
        if (spin_fail(spins, rwin)) break;
        lim = (unsigned)min(seg + HLA, 63);
        if (seg < 8) __builtin_amdgcn_s_sleep(HSLP); else __builtin_amdgcn_s_sleep(HSL2);
    }
}
template <int CTRL> __device__ __forceinline__ unsigned dppu(unsigned v) { return (unsigned)__builtin_amdgcn_mov_dpp((int)v, CTRL, 0xF, 0xF, true); }
template <int CTRL> __device__ __forceinline__ u32x4 ror4(u32x4 v) { return (u32x4){dppu<CTRL>(v[0]), dppu<CTRL>(v[1]), dppu<CTRL>(v[2]), dppu<CTRL>(v[3])}; }
template <int N> struct IC { static constexpr int v = N; };
__device__ __forceinline__ float xmax16(float v) {
    auto q = __builtin_amdgcn_permlane16_swap(__float_as_uint(v), __float_as_uint(v), false, false);
    return fmx(__uint_as_float(q[0]), __uint_as_float(q[1]));
}
__device__ __forceinline__ float xsum16(float v) {
    auto q = __builtin_amdgcn_permlane16_swap(__float_as_uint(v), __float_as_uint(v), false, false);
    return __uint_as_float(q[0]) + __uint_as_float(q[1]);
}
__device__ __forceinline__ float addswap16(float a, float b) {
    auto r = __builtin_amdgcn_permlane16_swap(__float_as_uint(a), __float_as_uint(b), false, false);
    return __uint_as_float(r[0]) + __uint_as_float(r[1]);
}
#define LANES() \
    const int lane = (int)fresh_lane(), tid = w * 64 + lane, fr = lane & 15, fq = lane >> 4; \
    const int sidx = (lane & 2) ? 0xEEEE : 0x4444; \
    const int ma0 = fq * 256 + (fr & 1) * 128 + (fr >> 1) * 16; \
    const int prow = 16 * ((fr >> 2) & 1) + 4 * (fr >> 3) + (fr & 3), hoff = fr * 128; \
    (void)tid; (void)sidx; (void)ma0; (void)prow; (void)hoff

__device__ __forceinline__ int seg_after(unsigned long long m, int j) { m = j >= 63 ? 0ull : m >> (j + 1); return m ? j + 1 + __builtin_ctzll(m) : NSEG; }
__device__ __forceinline__ int seg_done(unsigned long long m, int f0, int f1, unsigned long long x = 0) { return min(seg_after(~m, f0), seg_after(m | x, f1)) - 1; }
constexpr unsigned long long MXA = 1ull << 40;
template <int OFF> __device__ __forceinline__ void ldfrag(u32x4& d, const LAS char* p) { asm volatile("ds_read_b128 %0, %1 offset:%2" : "=v"(d) : "v"(p), "n"(OFF) : "memory"); }
template <int U, class FM, class FV> __device__ __forceinline__ void project(const u32x8 (&wt)[2][10], f32x4 (&tc)[2][2], u32x4 (&bb)[8], const LAS char* c0, const LAS char* c1, const LAS char* n0, const LAS char* n1, FM mid, FV vm) {
    constexpr int t = U >> 2, k = U & 3, v = (U + 8) % 40;
    if (U == 32) mid();
    vm(IC<U>());
    const LAS char* p = U < 32 ? (k >> 1 ? c1 : c0) : (k >> 1 ? n1 : n0);
    if (U < 2) asm volatile("s_nop 0\n\ts_waitcnt lgkmcnt(7)\n\tv_mfma_f32_16x16x32_bf16 %0, %3, %2, 0\n\tv_mfma_f32_16x16x32_bf16 %1, %4, %2, 0\n\tds_read_b128 %2, %5 offset:%6"
                            : "=&v"(tc[0][k & 1]), "=&v"(tc[1][k & 1]), "+v"(bb[U & 7]) : "v"(half8(wt[0][t], k >> 1)), "v"(half8(wt[1][t], k >> 1)), "v"(p), "n"((v >> 2) * 4096 + (k & 1) * 2048) : "memory");
    else asm volatile(".rept %7\n\ts_nop 0\n\t.endr\n\ts_waitcnt lgkmcnt(7)\n\tv_mfma_f32_16x16x32_bf16 %0, %3, %2, %0\n\tv_mfma_f32_16x16x32_bf16 %1, %4, %2, %1\n\tds_read_b128 %2, %5 offset:%6"
                      : "+v"(tc[0][k & 1]), "+v"(tc[1][k & 1]), "+v"(bb[U & 7]) : "v"(half8(wt[0][t], k >> 1)), "v"(half8(wt[1][t], k >> 1)), "v"(p), "n"((v >> 2) * 4096 + (k & 1) * 2048), "n"(U < 8 ? 1 : 0) : "memory");
    if constexpr (U < 39) project<U + 1>(wt, tc, bb, c0, c1, n0, n1, mid, vm);
    else asm volatile("s_nop 7" : "+v"(tc[0][0]), "+v"(tc[0][1]), "+v"(tc[1][0]), "+v"(tc[1][1]));
}
__device__ __forceinline__ void project_end(u32x4 (&bb)[8]) {
    asm volatile("s_waitcnt lgkmcnt(0)" : "+v"(bb[0]), "+v"(bb[1]), "+v"(bb[2]), "+v"(bb[3]), "+v"(bb[4]), "+v"(bb[5]), "+v"(bb[6]), "+v"(bb[7]) :: "memory");
}
__device__ __forceinline__ void project0(u32x4 (&bb)[8], const LAS char* c0, const LAS char* c1) {
    ldfrag<0>(bb[0], c0); ldfrag<2048>(bb[1], c0); ldfrag<0>(bb[2], c1); ldfrag<2048>(bb[3], c1);
    ldfrag<4096>(bb[4], c0); ldfrag<6144>(bb[5], c0); ldfrag<4096>(bb[6], c1); ldfrag<6144>(bb[7], c1);
    project_end(bb);
}

__global__ __launch_bounds__(512, 2) B3ATTR void mega_kernel(Args a) {
    char* memL = smem;
    char* tokL = smem + L_TOK;
    char* hL = smem + L_H;
    int w = __builtin_amdgcn_readfirstlane(threadIdx.x >> 6);
    __amdgpu_buffer_rsrc_t rwin = mkrsrc(a.win, W_END);
    char* ws = a.ws;
    __amdgpu_buffer_rsrc_t rws = mkrsrc(ws, (unsigned)OFF_WIN);
    auto fresh_rsrc = [&]() INL { ws = (char*)arg(0); rws = mkrsrc(ws, (unsigned)OFF_WIN); rwin = mkrsrc(arg(1), W_END); };
    const char* pc;
    asm volatile("s_getpc_b64 %0" : "=s"(pc));
    unsigned fs[12] = {};
    const u32x2 tg0 = __builtin_amdgcn_raw_buffer_load_b64(rws, 0u, 0u, 16);
    u64 tag;
    u32x2 rot0;
    {
        const int tid = threadIdx.x;
        if (blockIdx.x < 64) {
            const unsigned j = blockIdx.x;
            if (j == 0 && w == 0) __builtin_amdgcn_raw_buffer_store_b64(__builtin_bit_cast(u32x2, (__builtin_bit_cast(u64, tg0) & ~15ull) + 16ull + (xcc_id() & 7u)), rws, LANE0, 128u, 16);
            const unsigned g0 = (j * 512u + tid) * 16u;
#pragma unroll
            for (int i = 0; i < 3; ++i) st128(SENT, mkrsrc(ws + O_ARM1, O_H + NHF * 40960u - O_ARM1), g0 + i * 524288u, 0, 16);
#pragma unroll
            for (int i = 0; i < 7; ++i) {
                const unsigned g = g0 + i * 524288u;
                st128(SENT, mkrsrc(a.win, W_END + 2 * PAIR), g + (g < W_ARM - W_LAYER ? 0u : W_ZM - (W_ARM - W_LAYER)), 0, 16);
            }
#pragma unroll
            for (int i = 0; i < 3; ++i) st128(SENT, mkrsrc(ws + O_Z, ZTSEG * 2), g0 + i * 524288u, 0, 16);
            static_assert(W_ARM - W_LAYER + W_END + 2 * PAIR - W_ZM <= 7u * 524288u && O_ARM1 + 3u * 524288u >= O_H + NHF * 40960u && ZTSEG * 2 <= 3u * 524288u, "arming");
        } else if (blockIdx.x >= 128) {
            RowTw tw;
            row_tw(tw, tid);
            a1_unit<8, false>(mkrsrc(a.x, S_ * D_ * 2), (blockIdx.x - 128) * 16, rws, rwin, tw, tid, fs);
        } else {
            const unsigned j = blockIdx.x - 64;
            u32x4 v[6];
#pragma unroll
            for (int i = 0; i < 6; ++i) {
                const int id = min(tid + 512 * i, 8 * DF - 1);
                v[i] = ((const u32x4*)((id < 4 * DF ? a.wr : a.wi) + (size_t)j * 32 * DF))[id < 4 * DF ? id : id - 4 * DF];
            }
            u16 n[4];
            if (w == 7) {
                const size_t kn = (size_t)((S_ - j * 32 - (tid & 31)) & (S_ - 1)) * DF;
                n[0] = a.wr[kn]; n[1] = a.wi[kn]; n[2] = a.wr[kn + 320]; n[3] = a.wi[kn + 320];
            }
            const unsigned wo = WARM ? (w * 8u + (j >> 3)) * 8192u : 0x40000000u;
            const unsigned x0 = __builtin_amdgcn_raw_buffer_load_b32(mkrsrc(pc, CODE_BYTES), (tid & 63) * 128u, wo, 0), x1 = __builtin_amdgcn_raw_buffer_load_b32(mkrsrc(&TWC, sizeof(TwCol)), (tid & 63) * 128u, wo, 0);
#pragma unroll
            for (int i = 0; i < 6; ++i) ((u32x4*)smem)[min(tid + 512 * i, 8 * DF - 1)] = v[i];
            BAR();
#pragma unroll
            for (int i = 0; i < 5; ++i) {
                const int id = tid + 512 * i, kp = id >> 4;
                const u16* q = (const u16*)smem + (id & 15) * 2 * DF + 2 * kp;
                u32x4 o;
#pragma unroll
                for (int e = 0; e < 4; ++e) o[e] = (unsigned)q[(e >> 1) * DF + (e & 1)] | ((unsigned)q[(32 + (e >> 1)) * DF + (e & 1)] << 16);
                st128(o, rws, kp * WST + (id & 15) * 16u, O_WFT + j * 256u, 16);
            }
            if (w == 7) {
                const u16* q = (const u16*)smem + (tid & 31) * DF;
                const unsigned k = j * 32u + (tid & 31);
                const c2 h0 = (c2){bf2f(q[0]) + bf2f(n[0]), bf2f(q[32 * DF]) - bf2f(n[1])} * bc(0.25f * FSC), h1 = (c2){bf2f(q[320]) + bf2f(n[2]), bf2f(q[32 * DF + 320]) - bf2f(n[3])} * bc(0.25f * FSC);
                st128(__builtin_bit_cast(u32x4, (f32x4){h0.x + h1.x, h0.y + h1.y, h0.x - h1.x, h0.y - h1.y}), rws, (((k >> 8) & 6u) * 256u + (k & 64u) * 4u + (k & 7u) * 32u + ((k >> 4) & 24u) + ((k >> 3) & 7u)) * 16u, O_WFT + NCOL * WST, 16);
            }
            asm volatile("" :: "v"(x0), "v"(x1));
        }
        rot0 = __builtin_amdgcn_raw_buffer_load_b64(rws, 0u, 128u, 16);
        VMWAIT();
        FST(8);
        BAR0();
        FST(9);
    }
    tag = (((u64)__builtin_amdgcn_readfirstlane(tg0[1]) << 32) | (__builtin_amdgcn_readfirstlane(tg0[0]) & ~15u)) + 16ull;
    if (w == 0) __builtin_amdgcn_raw_buffer_store_b64(__builtin_bit_cast(u32x2, tag + 8ull), rwin, LANE0, W_A1 + blockIdx.x * 128u, 16);
    if (blockIdx.x >= 128) FSW(0);
    unsigned vx;
    int bid;
    auto set_role = [&](unsigned x0) INL {
        vx = x0;
        const int j = blockIdx.x >> 3, x = vx & 3;
        if (!(vx & 4)) bid = j < 8 ? 8 * x + j : j < 16 ? 24 + 8 * x + j : j < 26 ? 80 + 10 * x + j : j < 30 ? NWRK + 4 * (j - 26) + x : NXT - 24 + 8 * x + j;
        else bid = j < 8 ? 64 + 8 * x + j : j < 16 ? NQT - 8 + 8 * x + j : j < 26 ? NREC - 16 + 10 * x + j : NXT - 26 + 8 * x + j;
    };
    auto get_rot = [&]() INL {
        unsigned r = 0;
        for (unsigned spins = 0; spins < SPIN_CAP; ++spins) {
            const u64 x = __builtin_bit_cast(u64, rot0) - tag;
            r = __builtin_amdgcn_readfirstlane((unsigned)x);
            if (__all(x < 8ull)) break;
            __builtin_amdgcn_s_sleep(2);
            asm volatile("" ::: "memory");
            rot0 = __builtin_amdgcn_raw_buffer_load_b64(rws, 0u, 128u, 16);
        }
        return (r + blockIdx.x + 4u * DIE) & 7u;
    };
    set_role(blockIdx.x < 64 ? (xcc_id() ^ (4u * DIE)) & 7u : get_rot());
    unsigned long long tref = 0;
    int layer = 0;
    unsigned fo = 0;
    unsigned nh = 0, nz = 0, mc = 0, qz = 0, mo = 0, mo1 = 0, tro = 0;
    auto near_init = [&]() INL {
        unsigned f;
        for (unsigned spins = 0;;) {
            asm volatile("" ::: "memory");
            f = __builtin_amdgcn_raw_buffer_load_b32(rwin, (fresh_lane() & 15u) * 4u, W_NEAR, 16);
            if (!__any(f == ~0u) || spin_fail(spins, rwin)) break;
            __builtin_amdgcn_s_sleep(8);
        }
        const unsigned h = (unsigned)__ballot(f & 1u), q = (unsigned)__ballot(f & 2u) ^ 0xAAAAu;
        auto most = [&](unsigned m) { return (__builtin_popcount(m & 0x0F0Fu) + 3u) >> 3; };
        nh = most(h) * HALF; nz = most(h >> 4) * HALF;
        mc = (vx & 3u) ^ most(q);
        qz = most(q >> 4);
        mo = W_MEM + mc * PAIR + nh; mo1 = W_MEM + (4u + (mc & 1u)) * PAIR + nh + (mc >> 1) * 2048u;
        tro = W_T + (mc & 1u) * PAIR + nh + (mc >> 1) * 4096u;
    };
    auto zpair = [&](unsigned x, unsigned k) { return (((x >> 1) * 10u + k) * 2u + ((x & 1u) ^ qz)) * PAIR + nz; };
    auto two = [&]() { const unsigned l = fresh_lane(); return l < 4 ? (l >> 1) * PAIR + (l & 1) * 4096u + nh : 0x40000000u; };
    const f32x4 z4 = {0.f, 0.f, 0.f, 0.f};
    int hrdy = 0, budget = (int)SPIN_CAP;
    unsigned fc0 = 0, fc1 = 0, fc2 = 0;
    Clk ck = {0u, 0u, 1600u, 10486u};
    SST(5);
#define xt (bid >= NXT && bid < NWRK)
#define qx (bid >= NQT && bid < NWRK)
#define xq (bid >= NREC && bid < NQT && (bid - NREC) % 10 < 8)
    unsigned tpp = 0;
    int lt = LT1, xs = XS0;
    struct Tk { unsigned vo, tw, sl; const LAS char* sp; };
    auto tk_init = [&]() {
        const unsigned l = fresh_lane();
        Tk k = {l * 16u, two(), (l & 31u) * 128u + (l >> 5) * 4u, (const LAS char*)(memL + l * 64 + 4 + ((l >> 1) & 7) * 8)};
        PIN2(k.vo, k.tw); PIN2(k.sl, k.sp);
        return k;
    };
    auto sched2 = [&](Tk& k, int s, unsigned f) INL {
        const unsigned g0 = tpp - 0x400000u;
        tpp = g0 + (unsigned)read_sched(rwin, s, k.sl, tro + (unsigned)(s & 3) * 2u * PAIR, f, g0, lt, xs, fc1);
        k.sl &= ~4u;
        RT(6);
    };
    auto sched = [&](Tk& k, int s) INL { sched2(k, s, __builtin_amdgcn_raw_buffer_load_b32(rwin, k.sl, tro + (unsigned)(s & 3) * 2u * PAIR, 16)); };
    unsigned it = 0;
    auto fetch = [&](const Tk& k, int s, bool first, unsigned lim, unsigned dc, unsigned fo, bool tell) INL {
        const unsigned sb = (unsigned)(s & 3) * 6u * PAIR, ta0 = ck.ta, mm0 = ck.ma;
        if (__builtin_expect(s > 0, 1)) {
            if (__builtin_expect(first, 1)) { const unsigned te = cyc_at(ck, sx24(tpp - TKE - ck.ta)); while ((int)((unsigned)__builtin_amdgcn_s_memtime() - te) < 0) __builtin_amdgcn_s_sleep(1); }
            if (__builtin_expect(first, 1)) gather_at(tpp, ck, rwin, k.vo, mo + sb, mo1 + sb); else { const unsigned c = (mc + 1u + (it & 1u)) & 3u; gather_dma(rwin, k.vo, W_MEM + c * PAIR + nh + sb, W_MEM + (4u + (c & 1u)) * PAIR + nh + (c >> 1) * 2048u + sb); }
        }
        __builtin_amdgcn_s_setprio(0);
        RT(1);
        clk_rate(ck, ta0, mm0);
        unsigned tb = ck.ma - dc, tg = (unsigned)s << 24, so = W_T + (unsigned)(s & 3) * 2u * PAIR + fo;
        asm volatile("" : "+s"(tb), "+s"(tg), "+s"(so));
        asm volatile("s_waitcnt vmcnt(%1) lgkmcnt(0)\n\ts_barrier\n\ts_waitcnt vmcnt(%2)\n\ts_barrier\n\ts_waitcnt vmcnt(0)\n\ts_memtime %0\n\ts_barrier"
                     : "=s"(tref) : "n"(10 - NB1), "n"(10 - NB2) : "memory");
        MT(1);
        if (!tell) { RT(3); return; }
        unsigned x0 = *(const LAS unsigned*)k.sp, x1 = *(const LAS unsigned*)(k.sp + 4096), x2 = *(const LAS unsigned*)(k.sp + 6144);
        __builtin_amdgcn_s_waitcnt(0xC07F);
        asm volatile("" : "+s"(tref) :: "memory");
        const unsigned fw = ((ck.ta + ((((unsigned)tref - tb) * ck.qpc) >> 16)) & 0xFFFFFFu) | tg;
        __builtin_amdgcn_raw_buffer_store_b32(fw, rwin, k.tw, __any(max(max(x0, x1), x2) > lim) ? 0x40000000u : so, 16);
        RT(3);
    };
    auto rout = [&]() { return mkrsrc(layer ? arg(A_OUT) : (const u16*)(ws + OFF_X2), S_ * D_ * 2); };
    auto rec_end = [&]() INL {
        if (GATE) for (unsigned spins = 0;;) {
            asm volatile("" ::: "memory");
            if (__builtin_amdgcn_readfirstlane(__builtin_amdgcn_raw_buffer_load_b32(rws, 0u, O_FL - 2u * SEG * D_ * 2u + (unsigned)bid * 128u, 16)) != -1 || spin_fail(spins, rwin)) break;
            __builtin_amdgcn_s_sleep(16);
        }
    };
    auto mytid = [&]() { return w * 64 + (int)fresh_lane(); };
    auto new_layer = [&]() { fo = (unsigned)layer * W_LAYER; asm volatile("" : "+s"(fo), "+s"(w), "+s"(bid), "+s"(nh), "+s"(nz), "+s"(mc), "+s"(qz), "+s"(tag)); hrdy = 0; tpp = 0; lt = LT1; xs = XS0; };
    auto load_qv = [&](u32x8 (&wt)[2][10], int hh, bool v) INL {
        const bool o = v && w >= 4;
        load_w(wt, arg(A_WQ + 2 * v + (w >> 2)) + (o ? (hh * 128 + (w & 3) * 32) * D_ : hh * 128 + (w & 3) * 16 * HD), o ? D_ * 2 : HD * 2, !o, o ? 16 * D_ * 2 : 64 * HD * 2, o ? 256 : 128 * HD * 2, w);
    };
    auto hseg = [&](int j) { return O_H + (unsigned)(j - NHF * layer) * 40960u; };
    auto stage2 = [&](int j0, int j1, bool vt) INL {
        if (w == 0) {
            if (j1 >= NHF) wait_h(rwin, fo, j1, hrdy);
            else for (unsigned spins = 0;;) {
                asm volatile("" ::: "memory");
                const unsigned l = fresh_lane();
                if (!__any(__builtin_amdgcn_raw_buffer_load_b32(rws, (l & 31u) * 128u + (l >> 5) * (unsigned)(j1 - j0) * 40960u, hseg(j0) + 36864u, 16) == ~0u) || spin_fail(spins, rwin)) break;
                __builtin_amdgcn_s_sleep(HSLP);
            }
            *(volatile LAS int*)(smem + L_S + 32) = hrdy;
        }
        BAR();
        hrdy = __builtin_amdgcn_readfirstlane(*(volatile LAS int*)(smem + L_S + 32));
        SST(2);
        if (j0 == 1 && vt) __builtin_amdgcn_s_sleep(DH1);
        stage_h<5>(rws, hseg(j0) + w * 1024u, L_H + w * 1024u, 8192u);
        if (!vt) {
            stage_h<5>(rws, hseg(j1) + w * 1024u, L_H + 40960u + w * 1024u, 8192u);
            asm volatile("s_waitcnt vmcnt(5)" ::: "memory");
        } else if (w >= 4 && j1 != j0) {
            stage_h<10>(rws, hseg(j1) + (w - 4) * 1024u, L_H + 40960u + (w - 4) * 1024u, 4096u);
            asm volatile("s_waitcnt vmcnt(10)" ::: "memory");
        } else VMWAIT();
        check_h<5>(rws, rwin, hseg(j0) + w * 1024u, L_H + w * 1024u, 8192u);
        BAR0();
        SST(3);
        if (vt && w >= 4 && j1 != j0) {
            VMWAIT();
            check_h<10>(rws, rwin, hseg(j1) + (w - 4) * 1024u, L_H + 40960u + (w - 4) * 1024u, 4096u);
        }
    };
#define WO(eb, nt) wt[(nt) & 1][((nt) & ~1) + (eb)]
    unsigned zb = W_ZM;
    auto probe = [&]() INL {
        {
            unsigned* best = (unsigned*)smem;
            const int tid = mytid();
            const __amdgpu_buffer_rsrc_t rw = mkrsrc(a.win + (bid & 4 ? W_END : W_PROBE), 2 * PAIR);
            const unsigned base = (unsigned)(bid & 8) * 64u + w * 1024u;
            if (tid < 4) best[tid] = 0u;
            if (w == 0) wait_tags(rwin, 0, 1, 64u, tag + 8ull, true);
            BAR();
            unsigned mn[4] = {~0u, ~0u, ~0u, ~0u};
            for (int r = 0; r < 4; ++r) {
#pragma unroll
                for (int i = 0; i < 4; ++i) {
                    const unsigned long long t1 = __builtin_amdgcn_s_memtime();
                    asm volatile("s_waitcnt lgkmcnt(0)" ::: "memory");
                    unsigned v = __builtin_amdgcn_raw_buffer_load_b32(rw, base + r * 128u + i * HALF, 0, 16);
                    asm volatile("s_waitcnt vmcnt(0)" : "+v"(v) :: "memory");
                    const unsigned long long t2 = __builtin_amdgcn_s_memtime();
                    mn[i] = min(mn[i], (unsigned)(t2 - t1));
                }
            }
            if ((tid & 63) == 0) {
#pragma unroll
                for (int i = 0; i < 4; ++i) atomicAdd(&best[i], min(mn[i], 800u));
            }
            BAR();
            const unsigned h = best[1] + best[3] < best[0] + best[2];
            if (w == 0) __builtin_amdgcn_raw_buffer_store_b32(h + 2u * (best[h + 2] < best[h]), rwin, LANE0, W_NEAR + (unsigned)(bid & 15) * 4u, 16);
            BAR();
        }
    };
    auto vo_role = [&](const u32x8 (&wt)[2][10]) INL {
        const int hh = bid & 31;
        auto steps = [&](auto cc) INL {
            constexpr int C = decltype(cc)::v & 1;
            constexpr bool TK = decltype(cc)::v == 3;
            LANES();
            Tk tk = {0u, 0u, 0x40000000u, nullptr};
            if (TK) { tk = tk_init(); __builtin_amdgcn_s_setprio(TKP); }
            const unsigned zi = (unsigned)(hh * 128 + fr * 8) * 2u;
            unsigned zo0 = zpair(w & 3, fq) + zi, zo1 = zo0 + 8 * PAIR, zo2 = zpair(w & 3, 8 + (fq >> 1)) + zi + (fq & 1) * 8;
            const LAS char* va = (const LAS char*)(smem + L_Q + (fr & 1) * 544 + ((fr >> 1) & 1) * 1152 + (16 * fq + 2 * (fr >> 2)) * 4);
            LAS char* vw = (LAS char*)(smem + L_Q + (fq & 1) * 544 + (fq >> 1) * 1152 + (16 * w + bs2(fr)) * 4);
            const LAS char* mp0 = (const LAS char*)(memL + ma0);
            PIN2(zo0, zo1); PIN1(zo2);
            if (C == 0) { PIN2(mp0, vw); } else PIN1(va);
            u32x4 za = SENT, zc = SENT;
            u32x2 ze = {0u, 0u};
            auto put_z2 = [&](int s) {
                const unsigned z2b = s < NSEG ? O_Z2 + (unsigned)s * (NOWN * 8192u) + (unsigned)(10 * (w & 3) * 8192 + hh * 256) : 0x40000000u, l2 = fresh_lane();
                st128(za, rws, (l2 >> 4) * 8192u + (l2 & 15u) * 16u, z2b, 16);
                st128(zc, rws, (l2 >> 4) * 8192u + (l2 & 15u) * 16u, z2b + 4u * 8192u, 16);
                __builtin_amdgcn_raw_buffer_store_b64(ze, rws, (l2 >> 5) * 8192u + (l2 & 15u) * 16u + ((l2 >> 4) & 1u) * 8u, z2b + 8u * 8192u, 16);
            };
            for (int s = 0; s < NSEG; ++s) {
                RT(0);
                bool redo = false;
                unsigned sf = 0;
                do {
                    ++it;
                    if (C == 0) {
                        const unsigned lim = s > 0 ? poll_lim(budget) : ~0u;
                        asm volatile(
                            "v_mov_b64 v[200:201], 0\n\t"
                            "v_mov_b64 v[202:203], 0\n\t"
                            "v_mov_b64 v[204:205], 0\n\t"
                            "v_mov_b64 v[206:207], 0\n\t"
                            "v_mov_b32 v208, 0\n\t"
                            "s_setprio 2\n\t"
                            "s_waitcnt lgkmcnt(0)\n\t"
                            "s_barrier\n\t"
                            MTA(0)
                            "ds_read_b128 v[212:215], %[mp] offset:0\n\t"
                            "ds_read_b128 v[216:219], %[mp] offset:1024\n\t"
                            "ds_read_b128 v[220:223], %[mp] offset:2048\n\t"
                            "s_barrier\n\t"
                            "ds_read_b128 v[224:227], %[mp] offset:3072\n\t"
                            "ds_read_b128 v[228:231], %[mp] offset:4096\n\t"
                            "ds_read_b128 v[232:235], %[mp] offset:5120\n\t"
                            "s_waitcnt lgkmcnt(5)\n\t"
                            "v_smfmac_f32_16x16x64_bf16 v[200:203], v[212:215], %[w00], %[ix]\n\t"
                            "s_waitcnt lgkmcnt(4)\n\t"
                            "v_smfmac_f32_16x16x64_bf16 v[200:203], v[216:219], %[w01], %[ix]\n\t"
                            "s_barrier\n\t"
                            MTA(1)
                            "ds_read_b128 v[236:239], %[mp] offset:6144\n\t"
                            "s_waitcnt lgkmcnt(4)\n\t"
                            "v_smfmac_f32_16x16x64_bf16 v[200:203], v[220:223], %[w02], %[ix]\n\t"
                            "ds_read_b128 v[240:243], %[mp] offset:7168\n\t"
                            "s_waitcnt lgkmcnt(4)\n\t"
                            "v_smfmac_f32_16x16x64_bf16 v[200:203], v[224:227], %[w03], %[ix]\n\t"
                            "ds_read_b128 v[244:247], %[mp] offset:8192\n\t"
                            "s_waitcnt lgkmcnt(4)\n\t"
                            "v_smfmac_f32_16x16x64_bf16 v[200:203], v[228:231], %[w04], %[ix]\n\t"
                            "ds_read_b128 v[248:251], %[mp] offset:9216\n\t"
                            "s_waitcnt lgkmcnt(4)\n\t"
                            "v_smfmac_f32_16x16x64_bf16 v[200:203], v[232:235], %[w05], %[ix]\n\t"
                            "s_waitcnt lgkmcnt(3)\n\t"
                            "v_smfmac_f32_16x16x64_bf16 v[200:203], v[236:239], %[w06], %[ix]\n\t"
                            "s_waitcnt lgkmcnt(2)\n\t"
                            "v_smfmac_f32_16x16x64_bf16 v[200:203], v[240:243], %[w07], %[ix]\n\t"
                            "s_waitcnt lgkmcnt(1)\n\t"
                            "v_smfmac_f32_16x16x64_bf16 v[200:203], v[244:247], %[w08], %[ix]\n\t"
                            "s_waitcnt lgkmcnt(0)\n\t"
                            "v_smfmac_f32_16x16x64_bf16 v[200:203], v[248:251], %[w09], %[ix]\n\t"
                            "s_nop 2\n\t"
                            "v_smfmac_f32_16x16x64_bf16 v[204:207], v[212:215], %[w10], %[ix]\n\t"
                            "s_nop 2\n\t"
                            "v_smfmac_f32_16x16x64_bf16 v[204:207], v[216:219], %[w11], %[ix]\n\t"
                            "v_add_f32 v200, v200, v202\n\t"
                            "v_add_f32 v201, v201, v203\n\t"
                            "v_cvt_pk_bf16_f32 v200, v200, v201\n\t"
                            "ds_write_b32 %[vw], v200\n\t"
                            MTA(2)
                            "v_smfmac_f32_16x16x64_bf16 v[204:207], v[220:223], %[w12], %[ix]\n\t"
                            "v_max3_u32 v208, v208, v212, v213\n\t"
                            "v_max3_u32 v208, v208, v214, v215\n\t"
                            "v_smfmac_f32_16x16x64_bf16 v[204:207], v[224:227], %[w13], %[ix]\n\t"
                            "v_max3_u32 v208, v208, v216, v217\n\t"
                            "v_max3_u32 v208, v208, v218, v219\n\t"
                            "v_smfmac_f32_16x16x64_bf16 v[204:207], v[228:231], %[w14], %[ix]\n\t"
                            "v_max3_u32 v208, v208, v220, v221\n\t"
                            "v_max3_u32 v208, v208, v222, v223\n\t"
                            "v_smfmac_f32_16x16x64_bf16 v[204:207], v[232:235], %[w15], %[ix]\n\t"
                            "v_max3_u32 v208, v208, v224, v225\n\t"
                            "v_max3_u32 v208, v208, v226, v227\n\t"
                            "v_smfmac_f32_16x16x64_bf16 v[204:207], v[236:239], %[w16], %[ix]\n\t"
                            "v_max3_u32 v208, v208, v228, v229\n\t"
                            "v_max3_u32 v208, v208, v230, v231\n\t"
                            "v_smfmac_f32_16x16x64_bf16 v[204:207], v[240:243], %[w17], %[ix]\n\t"
                            "s_waitcnt lgkmcnt(0)\n\t"
                            "s_barrier\n\t"
                            MTA(5)
                            "v_smfmac_f32_16x16x64_bf16 v[204:207], v[244:247], %[w18], %[ix]\n\t"
                            "v_max3_u32 v208, v208, v232, v233\n\t"
                            "v_max3_u32 v208, v208, v234, v235\n\t"
                            "v_smfmac_f32_16x16x64_bf16 v[204:207], v[248:251], %[w19], %[ix]\n\t"
                            "v_max3_u32 v208, v208, v236, v237\n\t"
                            "v_max3_u32 v208, v208, v238, v239\n\t"
                            "v_max3_u32 v208, v208, v240, v241\n\t"
                            "v_max3_u32 v208, v208, v242, v243\n\t"
                            "v_max3_u32 v208, v208, v244, v245\n\t"
                            "v_max3_u32 v208, v208, v246, v247\n\t"
                            "v_max3_u32 v208, v208, v248, v249\n\t"
                            "v_max3_u32 v208, v208, v250, v251\n\t"
                            "v_cmp_lt_u32 vcc, %[lim], v208\n\t"
                            "v_add_f32 v204, v204, v206\n\t"
                            "v_add_f32 v205, v205, v207\n\t"
                            "v_cvt_pk_bf16_f32 v204, v204, v205\n\t"
                            "ds_write_b32 %[vw], v204 offset:256\n\t"
                            MTA(3)
                            "s_cbranch_vccz 1f\n\t"
                            "v_mov_b32 v208, %[it]\n\t"
                            "v_mov_b32 v209, 0\n\t"
                            "ds_write_b32 v209, v208 offset:%[ls]\n\t"
                            "1:\n\t"
                            "s_waitcnt vmcnt(0) lgkmcnt(0)\n\t"
                            "s_barrier\n\t"
                            "s_setprio 0\n\t"
                            :: [mp] "v"(mp0), [vw] "v"(vw), [ix] "v"(sidx), [lim] "s"(lim), [it] "s"(it), [ls] "n"(L_S),
                              [w00] "v"(wt[0][0]), [w01] "v"(wt[0][1]), [w02] "v"(wt[0][2]), [w03] "v"(wt[0][3]), [w04] "v"(wt[0][4]), [w05] "v"(wt[0][5]), [w06] "v"(wt[0][6]), [w07] "v"(wt[0][7]), [w08] "v"(wt[0][8]), [w09] "v"(wt[0][9]), [w10] "v"(wt[1][0]), [w11] "v"(wt[1][1]), [w12] "v"(wt[1][2]), [w13] "v"(wt[1][3]), [w14] "v"(wt[1][4]), [w15] "v"(wt[1][5]), [w16] "v"(wt[1][6]), [w17] "v"(wt[1][7]), [w18] "v"(wt[1][8]), [w19] "v"(wt[1][9])
                            : "v200", "v201", "v202", "v203", "v204", "v205", "v206", "v207", "v208", "v209", "v212", "v213", "v214", "v215", "v216", "v217", "v218", "v219", "v220", "v221", "v222", "v223", "v224", "v225", "v226", "v227", "v228", "v229", "v230", "v231", "v232", "v233", "v234", "v235", "v236", "v237", "v238", "v239", "v240", "v241", "v242", "v243", "v244", "v245", "v246", "v247", "v248", "v249", "v250", "v251", "vcc", "memory");
                        MT(4);
                        redo = __builtin_amdgcn_readfirstlane(*(volatile LAS unsigned*)(smem + L_S)) == it;
                        if (redo) FC(fc0);
                    } else {
                        f32x16 ca, cb;
                        f32x8 cc;
                        asm volatile("v_mov_b64 v[212:213], 0\n\tv_mov_b64 v[214:215], 0\n\tv_mov_b64 v[216:217], 0\n\tv_mov_b64 v[218:219], 0\n\tv_mov_b64 v[220:221], 0\n\tv_mov_b64 v[222:223], 0\n\tv_mov_b64 v[224:225], 0\n\tv_mov_b64 v[226:227], 0\n\tv_mov_b64 v[228:229], 0\n\tv_mov_b64 v[230:231], 0\n\tv_mov_b64 v[232:233], 0\n\tv_mov_b64 v[234:235], 0\n\tv_mov_b64 v[236:237], 0\n\tv_mov_b64 v[238:239], 0\n\tv_mov_b64 v[240:241], 0\n\tv_mov_b64 v[242:243], 0\n\tv_mov_b64 v[244:245], 0\n\tv_mov_b64 v[246:247], 0\n\tv_mov_b64 v[248:249], 0\n\tv_mov_b64 v[250:251], 0\n\t" : "={v[212:227]}"(ca), "={v[228:243]}"(cb), "={v[244:251]}"(cc));
                        unsigned sr = tro + (unsigned)(s & 3) * 2u * PAIR;
                        asm volatile("" : "+s"(sr));
                        if (TK) fetch(tk, s, !redo, s > 0 ? poll_lim(budget) : ~0u, 0u, 0u, false);
                        else asm volatile("s_waitcnt lgkmcnt(0)\n\ts_barrier\n\ts_barrier\n\ts_barrier" ::: "memory");
                        unsigned vd, vv, zs = zb;
                        u32x4 af1;
                        asm volatile(
                            "s_setprio 1\n\t"
                            "s_waitcnt lgkmcnt(0)\n\t"
                            "s_barrier\n\t"
                            "ds_read_b64_tr_b16 v[192:193], %[va]\n\t"
                            "ds_read_b64_tr_b16 v[194:195], %[va] offset:32\n\t"
                            "s_waitcnt lgkmcnt(0)\n\t"
                            "v_smfmac_f32_16x16x64_bf16 v[212:215], v[192:195], %[w0], %[ix]\n\t"
                            "v_smfmac_f32_16x16x64_bf16 v[220:223], v[192:195], %[w2], %[ix]\n\t"
                            "v_smfmac_f32_16x16x64_bf16 v[216:219], v[192:195], %[w1], %[ix]\n\t"
                            "v_smfmac_f32_16x16x64_bf16 v[224:227], v[192:195], %[w3], %[ix]\n\t"
                            "s_barrier\n\t"
                            MTA(4)
                            "ds_read_b64_tr_b16 v[196:197], %[va] offset:256\n\t"
                            "v_smfmac_f32_16x16x64_bf16 v[228:231], v[192:195], %[w4], %[ix]\n\t"
                            "ds_read_b64_tr_b16 v[198:199], %[va] offset:288\n\t"
                            "v_smfmac_f32_16x16x64_bf16 v[236:239], v[192:195], %[w6], %[ix]\n\t"
                            "ds_read_b32 v210, %[vz] offset:%[ls]\n\t"
                            "v_smfmac_f32_16x16x64_bf16 v[232:235], v[192:195], %[w5], %[ix]\n\t"
                            "v_smfmac_f32_16x16x64_bf16 v[240:243], v[192:195], %[w7], %[ix]\n\t"
                            "v_smfmac_f32_16x16x64_bf16 v[244:247], v[192:195], %[w8], %[ix]\n\t"
                            "v_smfmac_f32_16x16x64_bf16 v[248:251], v[192:195], %[w9], %[ix]\n\t"
                            "buffer_load_dword %[sf], %[sl], %[rw], %[sr] offen sc1\n\t"
                            : [sf] "=&v"(sf), "+{v[212:227]}"(ca), "+{v[228:243]}"(cb), "+{v[244:251]}"(cc), "=&{v[196:199]}"(af1), "=&{v210}"(vv)
                            : [va] "v"(va), [vz] "v"(0u), [ls] "n"(L_S), [ix] "v"(sidx), [sl] "v"(tk.sl), [sr] "s"(sr), [rw] "s"(rwin),
                              [w0] "v"(WO(0, 0)), [w1] "v"(WO(0, 1)), [w2] "v"(WO(0, 2)), [w3] "v"(WO(0, 3)), [w4] "v"(WO(0, 4)), [w5] "v"(WO(0, 5)), [w6] "v"(WO(0, 6)), [w7] "v"(WO(0, 7)), [w8] "v"(WO(0, 8)), [w9] "v"(WO(0, 9))
                            : "v192", "v193", "v194", "v195", "memory");
                        asm volatile(
                            "s_waitcnt lgkmcnt(0)\n\t"
                            "v_smfmac_f32_16x16x64_bf16 v[212:215], v[196:199], %[w0], %[ix]\n\t"
                            "v_readfirstlane_b32 %[vd], v210\n\t"
                            "v_smfmac_f32_16x16x64_bf16 v[220:223], v[196:199], %[w2], %[ix]\n\t"
                            "s_cmp_eq_u32 %[vd], %[it]\n\t"
                            "s_cselect_b32 %[zs], 2.0, %[zs]\n\t"
                            "v_smfmac_f32_16x16x64_bf16 v[216:219], v[196:199], %[w1], %[ix]\n\t"
                            "s_nop 1\n\t"
                            "v_smfmac_f32_16x16x64_bf16 v[224:227], v[196:199], %[w3], %[ix]\n\t"
                            "v_add_f32 v212, v212, v214\n\t"
                            "v_smfmac_f32_16x16x64_bf16 v[228:231], v[196:199], %[w4], %[ix]\n\t"
                            "v_add_f32 v213, v213, v215\n\t"
                            "v_smfmac_f32_16x16x64_bf16 v[236:239], v[196:199], %[w6], %[ix]\n\t"
                            "v_add_f32 v220, v220, v222\n\t"
                            "v_smfmac_f32_16x16x64_bf16 v[232:235], v[196:199], %[w5], %[ix]\n\t"
                            "v_add_f32 v221, v221, v223\n\t"
                            "v_smfmac_f32_16x16x64_bf16 v[240:243], v[196:199], %[w7], %[ix]\n\t"
                            "v_cvt_pk_bf16_f32 v200, v212, v213\n\t"
                            "v_smfmac_f32_16x16x64_bf16 v[244:247], v[196:199], %[w8], %[ix]\n\t"
                            "v_cvt_pk_bf16_f32 v201, v220, v221\n\t"
                            "v_smfmac_f32_16x16x64_bf16 v[248:251], v[196:199], %[w9], %[ix]\n\t"
                            "v_add_f32 v216, v216, v218\n\t"
                            "v_add_f32 v217, v217, v219\n\t"
                            "v_add_f32 v224, v224, v226\n\t"
                            "v_add_f32 v225, v225, v227\n\t"
                            "v_cvt_pk_bf16_f32 v202, v216, v217\n\t"
                            "v_cvt_pk_bf16_f32 v203, v224, v225\n\t"
                            "v_permlane32_swap_b32 v200, v201\n\t"
                            "v_pk_add_f32 v[228:229], v[228:229], v[230:231]\n\t"
                            "v_permlane32_swap_b32 v202, v203\n\t"
                            "v_min_u32 v201, -2, v201\n\t"
                            "v_pk_add_f32 v[236:237], v[236:237], v[238:239]\n\t"
                            "v_min_u32 v203, -2, v203\n\t"
                            "v_permlane16_swap_b32 v200, v202\n\t"
                            "v_pk_add_f32 v[232:233], v[232:233], v[234:235]\n\t"
                            "v_permlane16_swap_b32 v201, v203\n\t"
                            "v_pk_add_f32 v[240:241], v[240:241], v[242:243]\n\t"
                            MTA(5)
                            "buffer_store_dwordx4 v[200:203], %[z0], %[rw], %[zs] offen sc1\n\t"
                            "v_cvt_pk_bf16_f32 v204, v228, v229\n\t"
                            "v_cvt_pk_bf16_f32 v205, v236, v237\n\t"
                            "v_cvt_pk_bf16_f32 v206, v232, v233\n\t"
                            "v_cvt_pk_bf16_f32 v207, v240, v241\n\t"
                            "v_permlane32_swap_b32 v204, v205\n\t"
                            "v_pk_add_f32 v[244:245], v[244:245], v[246:247]\n\t"
                            "v_permlane32_swap_b32 v206, v207\n\t"
                            "v_min_u32 v205, -2, v205\n\t"
                            "v_pk_add_f32 v[248:249], v[248:249], v[250:251]\n\t"
                            "v_min_u32 v207, -2, v207\n\t"
                            "v_permlane16_swap_b32 v204, v206\n\t"
                            "v_cvt_pk_bf16_f32 v208, v244, v245\n\t"
                            "v_permlane16_swap_b32 v205, v207\n\t"
                            "v_cvt_pk_bf16_f32 v209, v248, v249\n\t"
                            "buffer_store_dwordx4 v[204:207], %[z1], %[rw], %[zs] offen sc1\n\t"
                            "s_nop 0\n\t"
                            "v_permlane32_swap_b32 v208, v209\n\t"
                            "v_min_u32 v209, -2, v209\n\t"
                            "buffer_store_dwordx2 v[208:209], %[z2], %[rw], %[zs] offen sc1\n\t"
                            : [vd] "=&s"(vd), [zs] "+s"(zs), "=&{v[200:203]}"(za), "=&{v[204:207]}"(zc), "=&{v[208:209]}"(ze), "+{v[212:227]}"(ca), "+{v[228:243]}"(cb), "+{v[244:251]}"(cc), "+v"(sf)
                            : [ix] "v"(sidx), [it] "s"(it), [rw] "s"(rwin), [z0] "v"(zo0), [z1] "v"(zo1), [z2] "v"(zo2), "{v[196:199]}"(af1), "{v210}"(vv),
                              [w0] "v"(WO(1, 0)), [w1] "v"(WO(1, 1)), [w2] "v"(WO(1, 2)), [w3] "v"(WO(1, 3)), [w4] "v"(WO(1, 4)), [w5] "v"(WO(1, 5)), [w6] "v"(WO(1, 6)), [w7] "v"(WO(1, 7)), [w8] "v"(WO(1, 8)), [w9] "v"(WO(1, 9))
                            : "scc", "memory");
                        redo = vd == it;
                        RT(4);
                        MT(6);
                        tref = __builtin_amdgcn_s_memtime();
                        __builtin_amdgcn_s_setprio(TKP);
                        asm volatile("s_waitcnt vmcnt(3)" : "+v"(sf) :: "memory");
                    }
                } while (__builtin_expect(redo, 0));
                if (s < 8) SST(8 + s);
                if (C == 0) {
                    if (s > 0 && s + 1 < NSEG) {
                        __builtin_amdgcn_s_sleep(DZR);
                        const unsigned zq = zb == W_ZM ? W_ZM + 2u * NOWN * PAIR : zb - NOWN * PAIR;
                        unsigned s1;
                        asm volatile("v_mov_b32 %0, -1" : "=v"(s1));
                        st128(((u32x4){s1, s1, s1, s1}), rwin, zo0, zq, 16);
                        st128(((u32x4){s1, s1, s1, s1}), rwin, zo1, zq, 16);
                        __builtin_amdgcn_raw_buffer_store_b64((u32x2){s1, s1}, rwin, zo2, zq, 16);
                    }
                } else {
                    if (s > TAILS) __builtin_amdgcn_raw_buffer_store_b32((unsigned)s - 1u, rwin, 0u, W_FZ2 + fo + (unsigned)(hh * 128 + (w & 3) * 4), 16);
                    if (TK && __builtin_expect(s == 0, 0)) { wait_h(rwin, fo, 5, hrdy); clk_init(ck); }
                    if (TK && s + 1 < NSEG) sched2(tk, s, sf);
                    if (!TK) wait_until(tref + DZ2);
                    if (s + 2 < NSEG) put_z2(s);
                }
                if (budget <= 0) set_tmo(rwin);
                zb = zb == W_ZM + 2u * NOWN * PAIR ? W_ZM : zb + NOWN * PAIR;
                RT(5);
                RTS();
                MT(7);
                MTS();
            }
        };
        const int tid = mytid();
        gather_mem0(memL, tid);
        if (tid < 8) *(unsigned*)(smem + L_S + tid * 4) = 0u;
        if (w < 4) steps(IC<0>()); else if (w < 7) steps(IC<1>()); else steps(IC<3>());
    };
    auto vt_role = [&](const u32x8 (&wt)[2][10], int hh, unsigned long long mv) INL {
        constexpr bool vt = true;
        const int jl = 63 - __builtin_clzll(mv);
        auto nextv = [&](int j) { return min(seg_after(mv, j), jl); };
        const int jv1 = nextv(-1), jv2 = nextv(jv1);
        if (w == 0) *(unsigned*)(smem + L_S + (fresh_lane() & 7u) * 4) = 0u;
        stage2(jv1, jv2, true);
        {
            LANES();
            const int ni = __builtin_popcountll(mv) + 1;
            if (w < 4) {
                const LAS char* c0 = (const LAS char*)(hL + hoff + ((fq ^ (fr & 7)) << 4));
                const LAS char* c1 = (const LAS char*)(hL + hoff + (((4 + fq) ^ (fr & 7)) << 4));
                const LAS char* n0 = c0 + 40960; const LAS char* n1 = c1 + 40960;
                u32x4 bb[8];
                project0(bb, c0, c1);
                for (int i = 1; i < ni; ++i) {
                    if (vt) MT(0);
                    f32x4 tc[2][2];
                    project<0>(wt, tc, bb, c0, c1, n0, n1,
                        [&]() {
                            MT(1);
                            if (i == 1) SST(16);
                            asm volatile("s_waitcnt vmcnt(0) lgkmcnt(0)\n\ts_barrier" ::: "memory");
                            if (i == 1) SST(17);
                            MT(2);
                        }, [](auto) {});
                    { const LAS char* x = c0; c0 = n0; n0 = x; x = c1; c1 = n1; n1 = x; }
                    char* tb = tokL + (i & 1) * 32 * QS + (16 * w + 4 * fq) * 2;
#pragma unroll
                    for (int g = 0; g < 2; ++g)
#pragma unroll
                        for (int tt = 0; tt < 2; ++tt) *(u32x2*)(tb + (prow + 8 * tt) * QS + g * 128) = (u32x2){pack2(tc[g][tt][0], tc[g][tt][1]), pack2(tc[g][tt][2], tc[g][tt][3])};
                    asm volatile("" ::: "memory");
                    *(volatile LAS int*)(smem + L_S + 16 + w * 4) = i;
                    if (i == 1) SST(18);
                    if (__builtin_expect(i == 1, 0)) __builtin_amdgcn_s_sleep(VSL);
                    if (vt) { MT(3); const int s = i & 63; MTRT(); MTS(); }
                }
                project_end(bb);
            } else {
                __builtin_amdgcn_s_setprio(OPR);
                const unsigned hv = lane * 16u, fz_ = W_FZ + fo + fz_at(hh, w - 4) + (jv1 ? 4096u : 0u);
                unsigned fp = ~0u;
                unsigned fzp = 0x40000000u;
                int jo = jv1, jh = jv2;
                for (int i = 1; i < ni; ++i, jo = nextv(jo)) {
                    const int b = i + (i >= ZLAG);
                    if (__builtin_expect(i == 1 || i == ZLAG, 0)) {
                        VMWAIT();
                        if (i == ZLAG) {
                            jh = nextv(jh);
                            wait_h(rwin, fo, jh, hrdy);
                            BAR0();
                            for (int t = 0; t < 10; ++t) dma1k(rws, hv, hseg(jh) + (unsigned)((w - 4) * 1024 + t * 4096), (unsigned)(L_H + ((i + 1) & 1) * 40960 + (w - 4) * 1024 + t * 4096));
                            VMWAIT();
                        }
                    }
                    jh = nextv(jh);
                    if (b < ni) BAR0();
                    wait_h(rwin, fo, jh, hrdy);
                    if (i == 1) SST(19);
                    if (vt) MT(1);
                    for (;;) {
                        const u32x4 f = *(volatile LAS u32x4*)(smem + L_S + 16);
                        if (__builtin_amdgcn_readfirstlane(min(min(f[0], f[1]), min(f[2], f[3]))) >= (unsigned)i) break;
                        __builtin_amdgcn_s_sleep(1);
                    }
                    asm volatile("" ::: "memory");
                    if (i == 1) SST(20);
                    if (__builtin_expect(jo == 0 && w == 7, 0)) __builtin_amdgcn_raw_buffer_store_b32((4u * (unsigned)__builtin_amdgcn_s_memrealtime() + DVT) & 0xFFFFFFu, rwin, two(), W_T + (unsigned)hh * 128u + 4u, 16);
                    if (vt) MT(0);
                    u32x4 vf[2][4];
#pragma unroll
                    for (int t = 0; t < 2; ++t)
#pragma unroll
                        for (int ks = 0; ks < 4; ++ks) vf[t][ks] = *(const u32x4*)(tokL + (i & 1) * 32 * QS + (16 * t + fr) * QS + ks * 64 + fq * 16);
                    const unsigned ztb = O_Z + (unsigned)((jo - layer) * (int)ZTSEG) * 2u + (unsigned)(10 * (w - 4) * H_ + hh) * 1024u;
                    f32x4 c[2][2];
                    u32x4 r;
                    unsigned hs = hseg(jh) + (unsigned)((w - 4) * 1024), hd = (unsigned)(L_H + ((b + 1) & 1) * 40960 + (w - 4) * 1024);
                    asm volatile("" : "+s"(hs), "+s"(hd));
#pragma unroll
                    for (int nt = 0; nt < 11; ++nt) {
                        auto& cc = c[nt & 1]; auto& cp = c[(nt + 1) & 1];
#pragma unroll
                        for (int ks = 0; ks < 4; ++ks) {
                            if (nt < 10) { cc[0] = mfma32(vf[0][ks], half8(WO(ks >> 1, nt < 10 ? nt : 0), ks & 1), ks ? cc[0] : z4); PIN1(cc[0]); }
                            if (nt > 0 && ks >= 1 && ks < 3) { r[2 * ks - 2] = pack2(cp[ks - 1][0], cp[ks - 1][1]); PIN1(r); }
                            if (nt < 10) { cc[1] = mfma32(vf[1][ks], half8(WO(ks >> 1, nt < 10 ? nt : 0), ks & 1), ks ? cc[1] : z4); PIN1(cc[1]); }
                            if (nt > 0 && ks >= 1 && ks < 3) { r[2 * ks - 1] = nosent1(pack2(cp[ks - 1][2], cp[ks - 1][3])); PIN1(r); }
                            if (nt > 0 && ks == 2) st128(r, rws, lane * 16u, ztb + (unsigned)((nt - 1) * H_) * 1024u, 16);
                            if (nt < 5 && !(ks & 1)) dma1k(rws, hv, hs + (2 * nt + (ks >> 1)) * 4096u, hd + (2 * nt + (ks >> 1)) * 4096u);
                            if (nt == 5 && ks == 0) {
                                asm volatile("s_waitcnt vmcnt(14)" ::: "memory");
                                __builtin_amdgcn_raw_buffer_store_b32(fp, rwin, 0u, fzp, 16);
                            }
                            SB();
                        }
                    }
                    if (vt) MT(2);
                    if (i == 1) SST(21);
                    fp = (unsigned)jo;
                    fzp = fz_;
                    if (__builtin_expect(jo <= ZEARLY || i + 1 == ni, 0)) {
                        VMWAIT();
                        __builtin_amdgcn_raw_buffer_store_b32(fp, rwin, 0u, fz_, 16);
                    } else asm volatile("s_waitcnt vmcnt(7)" ::: "memory");
                    if (jo < 8) SST(8 + jo);
                    if (vt && (!BSPARSE || jo % 6 == 2)) { const int s = jo; RT(0); RTS(); }
                    if (vt) { MT(3); const int s = i & 63; MTRT(); MTS(); }
                }
            }
            __builtin_amdgcn_s_setprio(0);
            VMWAIT();
            BAR();
        }
    };
    auto qt_role = [&](const u32x8 (&wt)[2][10], int hh, auto qq) INL {
        constexpr bool qt = decltype(qq)::v;
        const unsigned long long mq = bid < NQT ? MXQ : ~MXQ;
        const int jl = 63 - __builtin_clzll(mq);
        auto nextq = [&](int j) { return min(seg_after(mq, j), jl); };
        const int jq0 = qt ? nextq(0) : 0, jq1 = qt ? nextq(jq0) : 0;
        stage2(jq0, jq1, !qt);
        auto check2 = [&]() INL { VMWAIT(); check_h<5>(rws, rwin, hseg(jq1) + w * 1024u, L_H + 40960u + w * 1024u, 8192u); };
        {
            LANES();
            const unsigned to0 = (unsigned)((w ^ 4) >> 2) * 8192u + (unsigned)((w & 3) * 64 + (lane ^ ((fq & 1) * (w < 4 ? 8 : 5)))) * 16u, to1 = to0 + 4096u, hv = lane * 16u;
            const LAS char* c0 = (const LAS char*)(hL + hoff + ((fq ^ (fr & 7)) << 4));
            const LAS char* c1 = (const LAS char*)(hL + hoff + (((4 + fq) ^ (fr & 7)) << 4));
            u32x4 bb[8], g0 = SENT, g1 = SENT;
            f32x4 tc[2][2];
            auto pack = [&]() {
                g0 = (u32x4){pack2(tc[0][0][0], tc[0][0][1]), pack2(tc[0][0][2], tc[0][0][3]), pack2(tc[1][0][0], tc[1][0][1]), pack2(tc[1][0][2], tc[1][0][3])};
                g1 = (u32x4){pack2(tc[0][1][0], tc[0][1][1]), pack2(tc[0][1][2], tc[0][1][3]), pack2(tc[1][1][0], tc[1][1][1]), pack2(tc[1][1][2], tc[1][1][3])};
            };
            const LAS char* n0 = c0 + 40960; const LAS char* n1 = c1 + 40960;
            if (!qt) {
                project0(bb, c0, c1);
                project<0>(wt, tc, bb, c0, c1, c0, c1, []() {}, [](auto) {});
                pack();
                *(u32x4*)(tokL + to0) = g0; *(u32x4*)(tokL + to1) = g1;
                project_end(bb);
            } else {
                project0(bb, c0, c1);
                unsigned hs = hseg(jq1) + (unsigned)(w * 1024), hd = (unsigned)(L_H + 40960 + w * 1024), fv = tid == 0 ? 0u : 0x40000000u;
                const unsigned fq_ = W_FQ + fo + (unsigned)(hh * 128 + (jq0 > 1 ? 4096 : 0));
                for (int jp = 0, j = jq0, jn = jq1;; jp = j, j = jn, jn = nextq(jn)) {
                    MT(0);
                    project<0>(wt, tc, bb, c0, c1, n0, n1,
                        [&]() {
                            if (__builtin_expect(j == jq0, 0)) check2();
                            wait_h(rwin, fo, nextq(jn), hrdy);
                            hs = hseg(nextq(jn)) + (unsigned)(w * 1024); hd = (unsigned)(2 * L_H + 40960 + 2 * w * 1024) - hd;
                            unsigned fj = (unsigned)jp;
                            asm volatile("" : "+s"(hs), "+s"(hd));
                            PIN1(fj);
                            MT(1);
                            VMWAIT();
                            BAR();
                            MT(2);
                            if (w == 0) __builtin_amdgcn_raw_buffer_store_b32(fj, rwin, fv, fq_, 16);
                        },
                        [&](auto uu) {
                            constexpr int u = decltype(uu)::v;
                            if (u == 34 || u == 38 || u == 2 || u == 6 || u == 10) { constexpr int t = ((u + 6) % 40) / 4; dma1k(rws, hv, hs + t * 8192u, hd + t * 8192u); }
                        });
                    pack();
                    const unsigned qs = O_QK + (unsigned)(j * H_ + hh) * 16384u;
                    st128(g0, rws, to0, qs, 16);
                    st128(g1, rws, to1, qs, 16);
                    { const LAS char* x = c0; c0 = n0; n0 = x; x = c1; c1 = n1; n1 = x; }
                    if (__builtin_expect(j <= QEARLY || j == jl, 0)) {
                        VMWAIT();
                        BAR0();
                        if (w == 0) __builtin_amdgcn_raw_buffer_store_b32((unsigned)j, rwin, fv, fq_, 16);
                        if (j == jl) break;
                    }
                    MT(3);
                    if (j < 8) SST(8 + j);
                    if (!BSPARSE || j % 6 == 2) { const int s = j; RT(0); RTS(); MTRT(); MTS(); }
                }
                project_end(bb);
                VMWAIT();
                BAR0();
            }
        }
        SST(4);
    };
    auto qk_role = [&](const u32x8 (&wt)[2][10]) INL {
        const int hh = bid;
        int qrdy = 0;
        auto steps = [&](auto cc) INL {
        constexpr int C = decltype(cc)::v >> 1, R = decltype(cc)::v & 1;
        LANES();
        const LAS char* ka = (const LAS char*)(smem + L_K + (fr & 1) * TQS + (4 * fq + (fr >> 2)) * 8);
        const LAS char* qa = (const LAS char*)(smem + L_Q + (fr & 1) * 544 + ((fr >> 1) & 1) * 1152 + (16 * fq + 2 * (fr >> 2)) * 4);
        LAS char* qw = (LAS char*)(C < 2 ? smem + L_Q + (fq & 1) * 544 + (fq >> 1) * 1152 + (32 * w + 8 * (fr >> 2) + bs2(fr & 3)) * 4
                                         : smem + L_K + (fq >> 1) * TQS + (32 * (w & 3) + fr) * 8 + (fq & 1) * 4);
        const LAS char* mp0 = (const LAS char*)(memL + ma0);
        const int z = 16 * (fr >> 3) + 4 * (fr & 3) + 2 * ((fr >> 2) & 1);
        const int ko = ((z >> 4) * 256 + fq * 16 + ((z & 15) ^ ((fq & 1) * 5))) * 16;
        unsigned pa0 = (4u * C + 2u * (hh >> 4)) * PAIR + (unsigned)((hh & 15) * 512 + R * 256 + lane * 4);
        unsigned pa2 = ((8u + (fr >> 3)) * PAIR + C * 4096u + (unsigned)((hh >> 1) * 256 + R * 128 + (hh & 1) * 64 + fq * 16 + (fr & 6) * 2)) | (fr & 1) << 30;
        PIN2(ka, qw); PIN1(mp0);
        constexpr bool TK = C == 2 && R == 1;
        Tk tk = {0u, 0u, 0x40000000u, nullptr};
        if (TK) { tk = tk_init(); __builtin_amdgcn_s_setprio(TKP); }
        if (C < 2) { PIN2(qa, pa0); PIN1(pa2); }
        int qf_ = 0;
        int tbo = 0;
        for (int s = 0; s < NSEG; ++s) {
            RT(0);
            const char* tb = tokL + tbo;
            unsigned pb = __builtin_amdgcn_readfirstlane(s + 1 < NSEG ? p_slot(s) + nh : 0x40000000u);
            asm volatile("" : "+s"(pb));
            bool redo = false;
            unsigned sf = 0;
            if (s <= 1) BAR();
            u32x8 kx[2][2];
            if (C < 2) {
#pragma unroll
                for (int u = 0; u < 4; ++u) kx[u >> 1][u & 1] = cat8(*(const u32x4*)(tb + (ko ^ ((u >> 1) * 16)) + (u & 1) * 2048), *(const u32x4*)(tb + (ko ^ ((u >> 1) * 16)) + (u & 1) * 2048 + 1024));
                PIN2(kx[0][0], kx[0][1]); PIN2(kx[1][0], kx[1][1]);
            }
            do {
                ++it;
                f32x4 a0 = zero4(), a1 = zero4();
                PIN2(a0, a1);
                u32x4 m[10];
                if (C < 2) {
                    __builtin_amdgcn_s_setprio(2);
                    BAR();
                    MT(0);
#pragma unroll
                    for (int t = 0; t < NB1; ++t) { m[t] = *(const LAS u32x4*)(mp0 + t * 1024); SB(); }
                    BAR0();
#pragma unroll
                    for (int t = 0; t < 10; ++t) {
                        if (t == QB2) { BAR0(); MT(1); }
                        if (t + NB1 < 10) m[t + NB1] = *(const LAS u32x4*)(mp0 + (t + NB1) * 1024);
                        SB();
                        a0 = smfmac(m[t], wt[0][t], a0, sidx); a1 = smfmac(m[t], wt[1][t], a1, sidx);
                        PIN2(a0, a1);
                    }
                } else {
                    unsigned mx = 0;
                    asm volatile("" : "+{v[200:203]}"(a0), "+{v[204:207]}"(a1), "+{v208}"(mx));
                    unsigned lim = s > 0 ? poll_lim(budget) : ~0u, sr = tro + (unsigned)(s & 3) * 2u * PAIR;
                    asm volatile("" : "+s"(lim), "+s"(sr));
                    if (TK) fetch(tk, s, !redo, lim, DQC, (unsigned)hh * 128u, true);
                    else asm volatile("s_waitcnt lgkmcnt(0)\n\ts_barrier\n\ts_barrier\n\ts_barrier" ::: "memory");
                    asm volatile(
                        "ds_read_b128 v[212:215], %[mp] offset:0\n\t"
                        "ds_read_b128 v[216:219], %[mp] offset:1024\n\t"
                        "ds_read_b128 v[220:223], %[mp] offset:2048\n\t"
                        "ds_read_b128 v[224:227], %[mp] offset:3072\n\t"
                        "ds_read_b128 v[228:231], %[mp] offset:4096\n\t"
                        "ds_read_b128 v[232:235], %[mp] offset:5120\n\t"
                        "ds_read_b128 v[236:239], %[mp] offset:6144\n\t"
                        "ds_read_b128 v[240:243], %[mp] offset:7168\n\t"
                        "ds_read_b128 v[244:247], %[mp] offset:8192\n\t"
                        "ds_read_b128 v[248:251], %[mp] offset:9216\n\t"
                        MTA(4)
                        "s_waitcnt lgkmcnt(9)\n\t"
                        "v_smfmac_f32_16x16x64_bf16 v[200:203], v[212:215], %[w00], %[ix]\n\t"
                        "v_max3_u32 v208, v208, v212, v213\n\t"
                        "v_smfmac_f32_16x16x64_bf16 v[204:207], v[212:215], %[w10], %[ix]\n\t"
                        "s_waitcnt lgkmcnt(8)\n\t"
                        "v_max3_u32 v208, v208, v214, v215\n\t"
                        "v_smfmac_f32_16x16x64_bf16 v[200:203], v[216:219], %[w01], %[ix]\n\t"
                        "v_max3_u32 v208, v208, v216, v217\n\t"
                        "v_smfmac_f32_16x16x64_bf16 v[204:207], v[216:219], %[w11], %[ix]\n\t"
                        "s_waitcnt lgkmcnt(7)\n\t"
                        "v_max3_u32 v208, v208, v218, v219\n\t"
                        "v_smfmac_f32_16x16x64_bf16 v[200:203], v[220:223], %[w02], %[ix]\n\t"
                        "v_max3_u32 v208, v208, v220, v221\n\t"
                        "v_smfmac_f32_16x16x64_bf16 v[204:207], v[220:223], %[w12], %[ix]\n\t"
                        "s_waitcnt lgkmcnt(6)\n\t"
                        "v_max3_u32 v208, v208, v222, v223\n\t"
                        "v_smfmac_f32_16x16x64_bf16 v[200:203], v[224:227], %[w03], %[ix]\n\t"
                        "v_max3_u32 v208, v208, v224, v225\n\t"
                        "v_smfmac_f32_16x16x64_bf16 v[204:207], v[224:227], %[w13], %[ix]\n\t"
                        "s_waitcnt lgkmcnt(0)\n\t"
                        "v_max3_u32 v208, v208, v226, v227\n\t"
                        "v_smfmac_f32_16x16x64_bf16 v[200:203], v[228:231], %[w04], %[ix]\n\t"
                        "v_max3_u32 v208, v208, v228, v229\n\t"
                        "v_smfmac_f32_16x16x64_bf16 v[204:207], v[228:231], %[w14], %[ix]\n\t"
                        "v_max3_u32 v208, v208, v230, v231\n\t"
                        "v_smfmac_f32_16x16x64_bf16 v[200:203], v[232:235], %[w05], %[ix]\n\t"
                        "v_max3_u32 v208, v208, v232, v233\n\t"
                        "v_smfmac_f32_16x16x64_bf16 v[204:207], v[232:235], %[w15], %[ix]\n\t"
                        "v_max3_u32 v208, v208, v234, v235\n\t"
                        "v_smfmac_f32_16x16x64_bf16 v[200:203], v[236:239], %[w06], %[ix]\n\t"
                        "v_max3_u32 v208, v208, v236, v237\n\t"
                        "v_smfmac_f32_16x16x64_bf16 v[204:207], v[236:239], %[w16], %[ix]\n\t"
                        "v_max3_u32 v208, v208, v238, v239\n\t"
                        "v_smfmac_f32_16x16x64_bf16 v[200:203], v[240:243], %[w07], %[ix]\n\t"
                        "v_max3_u32 v208, v208, v240, v241\n\t"
                        "v_smfmac_f32_16x16x64_bf16 v[204:207], v[240:243], %[w17], %[ix]\n\t"
                        "v_max3_u32 v208, v208, v242, v243\n\t"
                        "v_smfmac_f32_16x16x64_bf16 v[200:203], v[244:247], %[w08], %[ix]\n\t"
                        "v_max3_u32 v208, v208, v244, v245\n\t"
                        "v_smfmac_f32_16x16x64_bf16 v[204:207], v[244:247], %[w18], %[ix]\n\t"
                        "v_max3_u32 v208, v208, v246, v247\n\t"
                        "v_smfmac_f32_16x16x64_bf16 v[200:203], v[248:251], %[w09], %[ix]\n\t"
                        "v_max3_u32 v208, v208, v248, v249\n\t"
                        "v_smfmac_f32_16x16x64_bf16 v[204:207], v[248:251], %[w19], %[ix]\n\t"
                        "v_max3_u32 v208, v208, v250, v251\n\t"
                        "buffer_load_dword %[sf], %[sl], %[rw], %[sr] offen sc1\n\t"
                        "v_cmp_lt_u32 vcc, %[lim], v208\n\t"
                        "s_nop 3\n\t"
                        "v_pk_add_f32 v[200:201], v[200:201], v[202:203]\n\t"
                        "v_pk_add_f32 v[204:205], v[204:205], v[206:207]\n\t"
                        "v_cvt_pk_bf16_f32 v200, v200, v201\n\t"
                        "v_cvt_pk_bf16_f32 v204, v204, v205\n\t"
                        "ds_write2_b32 %[qw], v200, v204 offset1:32\n\t"
                        MTA(2)
                        "s_cbranch_vccz 1f\n\t"
                        "v_mov_b32 v208, %[it]\n\t"
                        "v_mov_b32 v209, 0\n\t"
                        "ds_write_b32 v209, v208 offset:%[ls]\n\t"
                        "1:\n\t"
                        "s_waitcnt vmcnt(%[nv]) lgkmcnt(0)\n\t"
                        "s_barrier\n\t"
                        "s_waitcnt vmcnt(0)\n\t"
                        : [sf] "=&v"(sf), "+v"(qf_), "+{v[200:203]}"(a0), "+{v[204:207]}"(a1), "+{v208}"(mx)
                        : [mp] "v"(mp0), [qw] "v"(qw), [ix] "v"(sidx), [lim] "s"(lim), [it] "s"(it), [ls] "n"(L_S), [sl] "v"(tk.sl), [sr] "s"(sr), [rw] "s"(rwin), [nv] "n"(TK ? 2 : 1),
                          [w00] "v"(wt[0][0]), [w01] "v"(wt[0][1]), [w02] "v"(wt[0][2]), [w03] "v"(wt[0][3]), [w04] "v"(wt[0][4]), [w05] "v"(wt[0][5]), [w06] "v"(wt[0][6]), [w07] "v"(wt[0][7]), [w08] "v"(wt[0][8]), [w09] "v"(wt[0][9]), [w10] "v"(wt[1][0]), [w11] "v"(wt[1][1]), [w12] "v"(wt[1][2]), [w13] "v"(wt[1][3]), [w14] "v"(wt[1][4]), [w15] "v"(wt[1][5]), [w16] "v"(wt[1][6]), [w17] "v"(wt[1][7]), [w18] "v"(wt[1][8]), [w19] "v"(wt[1][9])
                        : "v209", "v212", "v213", "v214", "v215", "v216", "v217", "v218", "v219", "v220", "v221", "v222", "v223", "v224", "v225", "v226", "v227", "v228", "v229", "v230", "v231", "v232", "v233", "v234", "v235", "v236", "v237", "v238", "v239", "v240", "v241", "v242", "v243", "v244", "v245", "v246", "v247", "v248", "v249", "v250", "v251", "vcc", "memory");
                }
                if (C < 2) {
                    *(LAS unsigned*)qw = hpack(a0); *(LAS unsigned*)(qw + 16) = hpack(a1);
                    SB();
                    MT(2);
                    unsigned vd;
#define SC0 "v[212:215]"
#define SC1 "v[216:219]"
#define SC2 "v[220:223]"
#define DPP(op, d, ctl) op " " d ", " d ", " d " " ctl " row_mask:0xf bank_mask:0xf\n\t"
#define TAIL(A0, A2, B0, B2, C0, C2) \
                        "v_mov_b64 v[212:213], 0\n\tv_mov_b64 v[214:215], 0\n\tv_mov_b64 v[216:217], 0\n\tv_mov_b64 v[218:219], 0\n\tv_mov_b64 v[220:221], 0\n\tv_mov_b64 v[222:223], 0\n\t" \
                        "s_setprio 3\n\t" \
                        "s_waitcnt lgkmcnt(0)\n\t" \
                        "s_barrier\n\t" MTA(3) \
                        "ds_read_b64_tr_b16 v[224:225], %[qa]\n\t" \
                        "ds_read_b64_tr_b16 v[226:227], %[qa] offset:32\n\t" \
                        "ds_read_b64_tr_b16 v[228:229], %[qa] offset:256\n\t" \
                        "ds_read_b64_tr_b16 v[230:231], %[qa] offset:288\n\t" \
                        "ds_read_b64_tr_b16 v[232:233], %[ka]\n\t" \
                        "ds_read_b64_tr_b16 v[234:235], %[ka] offset:128\n\t" \
                        "ds_read_b64_tr_b16 v[236:237], %[ka] offset:256\n\t" \
                        "ds_read_b64_tr_b16 v[238:239], %[ka] offset:384\n\t" \
                        "ds_read_b64_tr_b16 v[240:241], %[ka] offset:512\n\t" \
                        "ds_read_b64_tr_b16 v[242:243], %[ka] offset:640\n\t" \
                        "s_waitcnt lgkmcnt(8)\n\t" \
                        "v_smfmac_f32_16x16x64_bf16 " SC0 ", v[224:227], %[k00], %[ix]\n\t" \
                        "ds_read_b64_tr_b16 v[244:245], %[ka] offset:768\n\t" \
                        "v_smfmac_f32_16x16x64_bf16 " SC1 ", v[224:227], %[k10], %[ix]\n\t" \
                        "ds_read_b64_tr_b16 v[246:247], %[ka] offset:896\n\t" \
                        "s_waitcnt lgkmcnt(8)\n\t" \
                        "v_smfmac_f32_16x16x64_bf16 " SC0 ", v[228:231], %[k01], %[ix]\n\t" \
                        "ds_read_b32 v248, %[vz] offset:%[ls]\n\t" \
                        "v_smfmac_f32_16x16x64_bf16 " SC1 ", v[228:231], %[k11], %[ix]\n\t" \
                        "s_waitcnt lgkmcnt(5)\n\t" \
                        "v_smfmac_f32_16x16x64_bf16 " SC2 ", v[224:227], v[232:239], %[ix]\n\t" \
                        "s_waitcnt lgkmcnt(1)\n\t" \
                        "v_smfmac_f32_16x16x64_bf16 " SC2 ", v[228:231], v[240:247], %[ix]\n\t" \
                        "s_waitcnt lgkmcnt(0)\n\t" \
                        MTA(4) "v_readfirstlane_b32 %[vd], v248\n\t" \
                        "v_add_f32 v232, " A0 ", " A2 "\n\t" \
                        "s_nop 0\n\t" \
                        "v_add_f32 v233, " B0 ", " B2 "\n\t" \
                        "v_max_f32 v235, v232, v233\n\t" \
                        "s_cmp_eq_u32 %[vd], %[it]\n\t" \
                        "s_cselect_b32 %[p0], 2.0, %[p0]\n\t" \
                        DPP("v_max_f32_dpp", "v235", "row_ror:8") \
                        "v_add_f32 v234, " C0 ", " C2 "\n\t" \
                        "v_max_f32 v235, v235, v234\n\t" \
                        "s_add_u32 %[p1], %[p0], 0x4000\n\t" \
                        "s_nop 0\n\t" DPP("v_max_f32_dpp", "v235", "row_ror:4") \
                        "s_nop 1\n\t" DPP("v_max_f32_dpp", "v235", "row_ror:2") \
                        "s_nop 1\n\t" DPP("v_max_f32_dpp", "v235", "row_ror:1") \
                        "v_mul_f32 v235, 0xbe0293ee, v235\n\t" \
                        "v_fmamk_f32 v232, v232, 0x3e0293ee, v235\n\t" \
                        "v_fmamk_f32 v233, v233, 0x3e0293ee, v235\n\t" \
                        "v_exp_f32 v232, v232\n\t" \
                        "v_exp_f32 v233, v233\n\t" \
                        "v_fmamk_f32 v234, v234, 0x3e0293ee, v235\n\t" \
                        "v_add_f32 v235, v232, v233\n\t" \
                        "v_exp_f32 v234, v234\n\t" \
                        "s_nop 0\n\t" DPP("v_add_f32_dpp", "v235", "row_ror:8") \
                        "v_add_f32 v235, v235, v234\n\t" \
                        "v_mov_b32_dpp v236, v234 quad_perm:[1,1,3,3] row_mask:0xf bank_mask:0xf\n\t" \
                        "s_nop 0\n\t" DPP("v_add_f32_dpp", "v235", "row_ror:4") \
                        "s_nop 1\n\t" DPP("v_add_f32_dpp", "v235", "row_ror:2") \
                        "s_nop 1\n\t" DPP("v_add_f32_dpp", "v235", "row_ror:1") \
                        "v_rcp_f32 v235, v235\n\t" \
                        "s_nop 0\n\t" \
                        "v_mul_f32_e64 v232, v232, v235 clamp\n\t" \
                        "v_mul_f32_e64 v233, v233, v235 clamp\n\t" \
                        "v_cvt_pk_bf16_f32 v232, v232, v233\n\t" \
                        MTA(5) "buffer_store_dword v232, %[pa], %[rw], %[p0] offen sc1\n\t" \
                        "v_mul_f32_e64 v234, v234, v235 clamp\n\t" \
                        "v_mul_f32_e64 v236, v236, v235 clamp\n\t" \
                        "buffer_store_dword v232, %[pa], %[rw], %[p1] offen sc1\n\t" \
                        "v_cvt_pk_bf16_f32 v234, v234, v236\n\t" \
                        "buffer_store_dword v234, %[pm], %[rw], %[p0] offen sc1\n\t" \
                        "s_setprio 0"
#define TAILIO : [vd] "=&s"(vd), [p0] "+s"(ps), [p1] "=&s"(ps1) \
                        : [qa] "v"(qa), [ka] "v"(ka), [vz] "v"(0u), [ls] "n"(L_S), [ix] "v"(sidx), [it] "s"(it), [pa] "v"(pa0), [pm] "v"(pa2), [rw] "s"(rwin), \
                          [k00] "v"(kx[0][0]), [k01] "v"(kx[0][1]), [k10] "v"(kx[1][0]), [k11] "v"(kx[1][1]) \
                        : "v212", "v213", "v214", "v215", "v216", "v217", "v218", "v219", "v220", "v221", "v222", "v223", "v224", "v225", "v226", "v227", "v228", "v229", "v230", "v231", "v232", \
                          "v233", "v234", "v235", "v236", "v237", "v238", "v239", "v240", "v241", "v242", "v243", "v244", "v245", "v246", "v247", "v248", "scc", "memory"
                    unsigned ps = pb, ps1;
                    if (R == 0) asm volatile(TAIL("v212", "v214", "v216", "v218", "v220", "v222") TAILIO);
                    else asm volatile(TAIL("v213", "v215", "v217", "v219", "v221", "v223") TAILIO);
                    redo = vd == it;
                    RT(4);
                    MT(6);
                } else {
                    MT(3);
                    if (TK) { __builtin_amdgcn_s_setprio(TKP); if (TKS) __builtin_amdgcn_s_sleep(TKS); }
                    redo = __builtin_amdgcn_readfirstlane(*(volatile LAS unsigned*)(smem + L_S)) == it;
                    if (redo) FC(fc0);
                }
            } while (__builtin_expect(redo, 0));
            if (s < 8) SST(8 + s);
            if (s >= 56) SST(s - 40);
            if (C + R == 1) {
                if (s + layer > 0) {
                    __builtin_amdgcn_s_sleep(DDMA);
                    unsigned l2 = fresh_lane(), s1;
                    asm volatile("v_mov_b32 %0, -1" : "=v"(s1));
                    const unsigned base = p_slot(s - 1) + nh;
                    asm volatile("s_waitcnt vmcnt(3)" ::: "memory");
                    st128(((u32x4){s1, s1, s1, s1}), rwin, (l2 >> 5) * PAIR + (l2 & 31u) * 16u, base + (unsigned)(R * 4 + 2 * (hh >> 4)) * PAIR + (unsigned)(hh & 15) * 512u, 16);
                    if (R) __builtin_amdgcn_raw_buffer_store_b64((u32x2){s1, s1}, rwin, (l2 >> 5) * PAIR + (l2 & 16u) * 256u + (l2 & 8u) * 16u + (l2 & 7u) * 8u, base + 8u * PAIR + (unsigned)((hh >> 1) * 256 + (hh & 1) * 64), 16);
                    else __builtin_amdgcn_raw_buffer_store_b64((u32x2){s1, s1}, rwin, two(), W_T + (unsigned)((s + 2) & 3) * 2u * PAIR + (unsigned)hh * 128u, 16);
                }
            } else if (TK) {
                if (s + 1 < NSEG) sched2(tk, s, sf);
            } else if (C == 2 && w == 4) {
                if (s + 2 < NSEG) {
                    qrdy = max(qrdy, seg_done(MXQ, __builtin_amdgcn_readlane(qf_, 0), __builtin_amdgcn_readlane(qf_, 1)));
                    const int sq = s + 2;
                    if (__builtin_expect(qrdy < sq, 0)) for (unsigned spins = 0;;) {
                        asm volatile("" ::: "memory");
                        const int f = (int)__builtin_amdgcn_raw_buffer_load_b32(rwin, (unsigned)(lane & 1) * 4096u, W_FQ + fo + (unsigned)hh * 128u, 16);
                        qrdy = seg_done(MXQ, __builtin_amdgcn_readlane(f, 0), __builtin_amdgcn_readlane(f, 1));
                        if (qrdy >= sq) break;
                        FC(fc2);
                        if (spin_fail(spins, rwin)) break;
                        __builtin_amdgcn_s_sleep(2);
                    }
                    const unsigned so = O_QK + (unsigned)(((s + 2) * H_ + hh) * 16384), lo = (unsigned)(L_TOK + (tbo ? tbo - 16384 : 32768)), l2 = fresh_lane();
                    if (__builtin_expect(s == 0, 0)) {
                        for (int i = 0; i < 4; ++i) dma4k(rws, l2 * 16u, so - H_ * 16384u + i * 4096u, lo - 16384u + i * 4096u);
                    }
#pragma unroll
                    for (int i = 0; i < 4; ++i) dma4k(rws, l2 * 16u, so + i * 4096u, lo + i * 4096u);
                    if (__builtin_expect(s == 0, 0)) asm volatile("s_waitcnt vmcnt(16)" ::: "memory");
                    if (qrdy < min(s + 5, NSEG - 1))
                        asm volatile("buffer_load_dword %0, %1, %2, %3 offen sc1" : "+v"(qf_) : "v"((l2 & 1u) * 4096u), "s"(rwin), "s"(W_FQ + fo + (unsigned)hh * 128u) : "memory");
                }
            } else {
                asm volatile("s_waitcnt vmcnt(3)" ::: "memory");
                if (s > TAILS) __builtin_amdgcn_raw_buffer_store_b32((unsigned)s - 1u, rwin, 0u, W_FPT + fo + (unsigned)(hh * 128 + (w & 3) * 4), 16);
                if (C == 2 && s + 1 < NSEG) __builtin_amdgcn_s_sleep(DPT);
                const float scale = 0.08838834764831845f, l2e = 1.44269504088896340736f * (NORND ? scale : 1.f);
                const int tq = (w >> 1) & 1, qh = w & 1;
                f32x4 sc[3] = {z4, z4, z4};
                u32x4 kt[2][4], qf[4], kf[4];
                const unsigned l2 = fresh_lane(), kto = ((l2 & 8u) * 512u + (l2 & 51u) * 16u + (l2 & 4u) * 32u) ^ ((l2 & 16u) * 5u), qto = ((l2 & 55u) * 16u + qh * 128u) ^ ((l2 & 16u) * 8u);
#pragma unroll
                for (int ks = 0; ks < 4; ++ks) {
                    qf[ks] = *(const u32x4*)(tb + 8192 + (tq * 4 + ks) * 1024 + qto);
                    kt[0][ks] = *(const u32x4*)(tb + ks * 1024 + kto);
                    kt[1][ks] = *(const u32x4*)(tb + ks * 1024 + (kto ^ 64u));
                    SB();
                }
#pragma unroll
                for (int ks = 0; ks < 4; ++ks) {
                    sc[0] = mfma32(kt[0][ks], qf[ks], sc[0]); PIN1(sc[0]);
                    kf[ks] = ldtr2(ka + ks * 256, ka + ks * 256 + 128);
                    SB();
                    sc[1] = mfma32(kt[1][ks], qf[ks], sc[1]); PIN1(sc[1]);
                }
#pragma unroll
                for (int ks = 0; ks < 4; ++ks) sc[2] = mfma32(kf[ks], qf[ks], sc[2]);
                const bool hi = (l2 & 8u) != 0u;
                float v[6];
#pragma unroll
                for (int r = 0; r < 4; ++r) v[r] = hi ? sc[1][r] : sc[0][r];
                v[4] = hi ? sc[2][2] : sc[2][0]; v[5] = hi ? sc[2][3] : sc[2][1];
#pragma unroll
                for (int r = 0; r < 6; r += 2) {
                    if (NORND) break;
                    const unsigned b0 = pack2(v[r], v[r + 1]);
                    const unsigned b1 = pack2(lo2f(b0) * scale, hi2f(b0) * scale);
                    v[r] = lo2f(b1); v[r + 1] = hi2f(b1);
                }
                const int lim = 8 * (w & 3) + (int)(l2 & 7u) - 8 * (int)(l2 >> 4) - (int)((l2 & 8u) >> 1);
#pragma unroll
                for (int r = 0; r < 4; ++r) if (r > lim) v[r] = NORND ? -1000000000.0f / scale : bfr(-1000000000.0f);
                float mx = fmx(fmx(fmx(fmx(fmx(v[0], v[1]), v[2]), v[3]), v[4]), v[5]);
                mx = xmax3216(fmx(mx, ror8f(mx)));
                const float mb = -mx * l2e;
#pragma unroll
                for (int r = 0; r < 6; ++r) v[r] = __builtin_amdgcn_exp2f(fmaf(v[r], l2e, mb));
                float sum = fmaf(v[4] + v[5], 0.5f, (v[0] + v[1]) + (v[2] + v[3]));
                sum = xsum3216(sum + ror8f(sum));
                const float inv = __builtin_amdgcn_rcpf(sum);
                const unsigned p0 = pack2(v[0] * inv, v[1] * inv), p1 = nosent1(pack2(v[2] * inv, v[3] * inv)), p2 = nosent1(pack2(v[4] * inv, v[5] * inv));
                const unsigned base = (s + 2 < NSEG ? O_PT : W_PL + fo - (NSEG - 2) * PTSEG * 2u) + (unsigned)(s * PTSEG) * 2u;
                const __amdgpu_buffer_rsrc_t rp = s + 2 < NSEG ? rws : rwin;
                const unsigned oob = (l2 & 8u) << 27;
                st128(((u32x4){p0, p1, dppu<0x128>(p0), dppu<0x128>(p1)}), rp, (l2 * 16u) | oob, base + (unsigned)((hh * 2 + tq) * 1024 + qh * 128), 16);
                __builtin_amdgcn_raw_buffer_store_b64((u32x2){p2, dppu<0x128>(p2)}, rp, ((l2 & 7u) * 16u + (l2 >> 4) * 8u) | oob | (l2 & 32u) << 25, base + 65536u + (unsigned)((tq * 32 + hh) * 256 + qh * 128), 16);
            }
            tbo = tbo == 32768 ? 0 : tbo + 16384;
            if (C == 2 && __builtin_expect(budget <= 0, 0)) set_tmo(rwin);
            RT(5);
            RTS();
            MT(7);
            MTS();
        }
        };
        const int tid = mytid();
        gather_mem0(memL, tid);
        if (tid < 4) *(unsigned*)(smem + L_S + tid * 4) = 0u;
        if (layer == 0) near_init();
        if (w == 7) clk_init(ck);
        qt_role(wt, hh, IC<0>());
        if (w == 0) steps(IC<0>()); else if (w == 1) steps(IC<1>()); else if (w == 2) steps(IC<2>()); else if (w == 3) steps(IC<3>()); else if (w < 7) steps(IC<4>()); else steps(IC<5>());
    };
    auto own_role = [&]() INL {
        const unsigned long long tstart = __builtin_amdgcn_s_memtime();
        if (layer == 0) near_init();
        LANES();
        const int o = bid - 96, hg = w & 3, ot = tile_of(o);
        const unsigned ztoff = (unsigned)((o * H_ + 8 * hg) * 64 + lane) * 16u;
        const unsigned zoff = zpair(o / 10, o % 10) + (unsigned)(lane * 16);
        const unsigned poff = (4u * (mc >> 1) + 2u * (hg >> 1) + (mc & 1u)) * PAIR + nh + (unsigned)((hg & 1) * 4096 + (fq >> 1) * 512 + (fr >> 2) * 128 + (fr & 1) * 64 + (fq & 1) * 32 + (fr & 2) * 8);
        const unsigned pmoff = (8u + (mc & 1u)) * PAIR + nh + (mc >> 1) * 4096u + (unsigned)(fq * 256 + (fr >> 3) * 128 + (fr & 2) * 32 + (fr & 4) * 8 + (fr & 1) * 16);
        const unsigned moff = (ot < 32 ? (unsigned)ot * 256u : 4u * PAIR + (unsigned)(ot - 32) * 256u) + nh + (unsigned)(lane * 4);
        const unsigned mc2 = ot < 32 ? 2u * PAIR : 2048u;
        char* op = smem + 8192;
        if (w == 4 && lane < 2) *(volatile LAS int*)(smem + 4096 + lane * 4) = -1;
        BAR();
        SST(2);
        if (w == 7) clk_init(ck);
        wait_until(tstart + DO0);
        if (w < 4) {
            u32x8 zf[4];
            int zr = -1;
            unsigned zb = W_ZM + (unsigned)layer * (NOWN * PAIR), zso = O_Z - (unsigned)layer * (unsigned)(ZTSEG * 2);
            const unsigned ra = (ot < 32 ? (unsigned)fq * PAIR + (unsigned)ot * 256u : (4u + (fq & 1)) * PAIR + (unsigned)(fq >> 1) * 2048u + (unsigned)(ot - 32) * 256u) + nh + (unsigned)(fr * 16);
            int np = 0;
            auto piece = [&]() INL {
                asm volatile("s_mov_b32 m0, %3\n\ts_nop 3\n\tbuffer_load_dwordx4 %0, %1, %2 offen sc1 lds\n\tbuffer_load_dwordx4 %0, %1, %2 offen offset:1024 sc1 lds"
                             :: "v"(ztoff), "s"(rws), "s"(O_Z + (unsigned)((NSEG - 1 - layer) * (int)ZTSEG) * 2u + np * 2048u), "s"(32768 + hg * 8192 + np * 2048) : "m0", "memory");
                ++np;
            };
            for (int s = 0; s + 1 < NSEG; ++s) {
                const unsigned pb = p_slot(s);
                const int sz = s;
                while (__builtin_expect(zr < sz, 0)) {
                    __builtin_amdgcn_s_sleep(1);
                    zr = __builtin_amdgcn_readfirstlane(*(volatile LAS int*)(smem + 4096));
                }
                if (s == 0) SST(1);
                asm volatile(
                    "1:\n\t"
                    "buffer_load_dwordx4 v[160:163], %[zt], %[rs], %[so] offen sc1\n\t"
                    "buffer_load_dwordx4 v[164:167], %[zt], %[rs], %[so] offen offset:1024 sc1\n\t"
                    "buffer_load_dwordx4 v[168:171], %[zt], %[rs], %[so] offen offset:2048 sc1\n\t"
                    "buffer_load_dwordx4 v[172:175], %[zt], %[rs], %[so] offen offset:3072 sc1\n\t"
                    "buffer_load_dwordx4 v[176:179], %[zt], %[rs], %[s4] offen sc1\n\t"
                    "buffer_load_dwordx4 v[180:183], %[zt], %[rs], %[s4] offen offset:1024 sc1\n\t"
                    "buffer_load_dwordx4 v[184:187], %[zt], %[rs], %[s4] offen offset:2048 sc1\n\t"
                    "buffer_load_dwordx4 v[188:191], %[zt], %[rs], %[s4] offen offset:3072 sc1\n\t"
                    "s_cmp_lg_u32 %[s], 0\n\t"
                    "s_cbranch_scc1 2f\n\t"
                    "s_waitcnt vmcnt(0)\n\t"
                    "v_max3_u32 v100, v161, v163, v165\n\t"
                    "v_max3_u32 v100, v100, v167, v169\n\t"
                    "v_max3_u32 v100, v100, v171, v173\n\t"
                    "v_max3_u32 v100, v100, v175, v177\n\t"
                    "v_max3_u32 v100, v100, v179, v181\n\t"
                    "v_max3_u32 v100, v100, v183, v185\n\t"
                    "v_max3_u32 v100, v100, v187, v189\n\t"
                    "v_max_u32 v100, v100, v191\n\t"
                    "v_cmp_eq_u32 vcc, -1, v100\n\t"
                    "s_sub_i32 %[tr], %[tr], 1\n\t"
                    "s_cbranch_vccz 2f\n\t"
                    "s_cmp_gt_i32 %[tr], 0\n\t"
                    "s_cbranch_scc1 1b\n\t"
                    "2:"
                    : "=&{v[160:167]}"(zf[0]), "=&{v[168:175]}"(zf[1]), "=&{v[176:183]}"(zf[2]), "=&{v[184:191]}"(zf[3]), [tr] "+s"(budget)
                    : [zt] "v"(ztoff), [rs] "s"(rws), [so] "s"(zso), [s4] "s"(zso + 4096u), [s] "s"(s) : "v100", "vcc", "scc", "memory");
                if (s == 0) SST(3);
                zso += (unsigned)(ZTSEG * 2);
                if (zr == NSEG - 1 && np < 4) piece();
                if (__builtin_expect(budget <= 0, 0)) set_tmo(rwin);
                RT(6);
                const unsigned spa = (unsigned)((s & 1) * 2048 + (w < 3 ? w * 512 : 0) + lane * 8);
#define OST(r)
                unsigned long long t0_, t1_, t2_, t3_, t4_ = 0, bad;
                int tries = budget;
                unsigned po = poff, pm = pmoff, x0 = 4u * PAIR, x1 = 4096u;
                const unsigned mb = W_MEM + (unsigned)((s + 1) & 3) * 6u * PAIR;
                if (w < 3) asm volatile(
                    "s_barrier\n\t"
                    OST("%[t0]")
                    "v_mov_b64 v[96:97], 0\n\t"
                    "v_mov_b64 v[98:99], 0\n\t"
                    "v_mov_b32 v100, 0\n\t"
                    "s_barrier\n\t"
                    "1:\n\t"
                    "buffer_load_dwordx4 v[208:211], %[po], %[rw], %[pb] offen sc1\n\t"
                    "buffer_load_dwordx4 v[212:215], %[po], %[rw], %[pb] offen offset:1024 sc1\n\t"
                    "buffer_load_dwordx4 v[216:219], %[po], %[rw], %[pb] offen offset:2048 sc1\n\t"
                    "buffer_load_dwordx4 v[220:223], %[po], %[rw], %[pb] offen offset:3072 sc1\n\t"
                    OST("%[t1]")
                    "s_waitcnt vmcnt(3)\n\t"
                    OST("%[t3]")
                    "v_smfmac_f32_16x16x64_bf16 v[96:99], v[208:211], v[160:167], %[ix]\n\t"
                    "v_max3_u32 v100, v100, v208, v209\n\t"
                    "v_max3_u32 v100, v100, v210, v211\n\t"
                    "s_waitcnt vmcnt(2)\n\t"
                    "v_smfmac_f32_16x16x64_bf16 v[96:99], v[212:215], v[168:175], %[ix]\n\t"
                    "v_max3_u32 v100, v100, v212, v213\n\t"
                    "v_max3_u32 v100, v100, v214, v215\n\t"
                    "s_waitcnt vmcnt(1)\n\t"
                    "v_smfmac_f32_16x16x64_bf16 v[96:99], v[216:219], v[176:183], %[ix]\n\t"
                    "v_max3_u32 v100, v100, v216, v217\n\t"
                    "v_max3_u32 v100, v100, v218, v219\n\t"
                    "s_waitcnt vmcnt(0)\n\t"
                    "v_smfmac_f32_16x16x64_bf16 v[96:99], v[220:223], v[184:191], %[ix]\n\t"
                    "v_max3_u32 v100, v100, v220, v221\n\t"
                    "v_max3_u32 v100, v100, v222, v223\n\t"
                    "v_cmp_eq_u32 vcc, -1, v100\n\t"
                    OST("%[t2]")
                    "s_nop 4\n\t"
                    "v_pk_add_f32 v[96:97], v[96:97], v[98:99]\n\t"
                    "ds_write_b64 %[sp], v[96:97]\n\t"
                    "s_cbranch_vccz 2f\n\t"
                    "s_sub_i32 %[tr], %[tr], 1\n\t"
                    "s_cmp_lt_i32 %[tr], 1\n\t"
                    "s_cbranch_scc1 2f\n\t"
                    "v_xor_b32 %[po], %[x0], %[po]\n\t"
                    "s_xor_b32 %[x0], %[x0], 0x14000\n\t"
                    "s_waitcnt lgkmcnt(0)\n\t"
                    "v_mov_b64 v[96:97], 0\n\t"
                    "v_mov_b64 v[98:99], 0\n\t"
                    "v_mov_b32 v100, 0\n\t"
                    "s_branch 1b\n\t"
                    "2:\n\t"
                    "s_waitcnt lgkmcnt(0)\n\t"
                    "s_barrier\n\t"
                    "s_sleep 3\n\t"
                    : [tr] "+s"(tries), [po] "+v"(po), [x0] "+s"(x0), [t0] "=&s"(t0_), [t1] "=&s"(t1_), [t2] "=&s"(t2_), [t3] "=&s"(t3_)
                    : [sp] "v"(spa), [ix] "v"(sidx), [rw] "s"(rwin), [pb] "s"(pb), "{v[160:167]}"(zf[0]), "{v[168:175]}"(zf[1]), "{v[176:183]}"(zf[2]), "{v[184:191]}"(zf[3])
                    : "v96", "v97", "v98", "v99", "v100", "v208", "v209", "v210", "v211", "v212", "v213", "v214", "v215", "v216", "v217", "v218", "v219", "v220", "v221", "v222", "v223", "vcc", "scc", "memory");
                else asm volatile(
                    "s_barrier\n\t"
                    "buffer_load_dwordx4 v[128:131], %[zo], %[rw], %[zb] offen sc1\n\t"
                    "buffer_load_dwordx4 v[132:135], %[zo], %[rw], %[zb] offen offset:1024 sc1\n\t"
                    "buffer_load_dwordx4 v[136:139], %[zo], %[rw], %[zb] offen offset:2048 sc1\n\t"
                    "buffer_load_dwordx4 v[140:143], %[zo], %[rw], %[zb] offen offset:3072 sc1\n\t"
                    "buffer_load_dwordx4 v[144:147], %[zo], %[rw], %[z4] offen sc1\n\t"
                    "buffer_load_dwordx4 v[148:151], %[zo], %[rw], %[z4] offen offset:1024 sc1\n\t"
                    "buffer_load_dwordx4 v[152:155], %[zo], %[rw], %[z4] offen offset:2048 sc1\n\t"
                    "buffer_load_dwordx4 v[156:159], %[zo], %[rw], %[z4] offen offset:3072 sc1\n\t"
                    OST("%[t0]")
                    "v_mov_b64 v[96:97], 0\n\t"
                    "v_mov_b64 v[98:99], 0\n\t"
                    "v_mov_b32 v100, 0\n\t"
                    "s_barrier\n\t"
                    "s_sleep %[fd]\n\t"
                    "buffer_load_dwordx4 v[208:211], %[po], %[rw], %[pb] offen sc1\n\t"
                    "buffer_load_dwordx4 v[212:215], %[po], %[rw], %[pb] offen offset:1024 sc1\n\t"
                    "buffer_load_dwordx4 v[216:219], %[po], %[rw], %[pb] offen offset:2048 sc1\n\t"
                    "buffer_load_dwordx4 v[220:223], %[po], %[rw], %[pb] offen offset:3072 sc1\n\t"
                    "buffer_load_dwordx4 v[224:227], %[pm], %[rw], %[pb] offen sc1\n\t"
                    "buffer_load_dwordx4 v[228:231], %[pm], %[rw], %[pb] offen offset:1024 sc1\n\t"
                    "buffer_load_dwordx4 v[232:235], %[pm], %[rw], %[pb] offen offset:2048 sc1\n\t"
                    "buffer_load_dwordx4 v[236:239], %[pm], %[rw], %[pb] offen offset:3072 sc1\n\t"
                    OST("%[t1]")
                    "s_waitcnt vmcnt(8)\n\t"
                    "v_max3_u32 v101, v129, v131, v133\n\t"
                    "v_max3_u32 v101, v101, v135, v137\n\t"
                    "v_max3_u32 v101, v101, v139, v141\n\t"
                    "v_max3_u32 v101, v101, v143, v145\n\t"
                    "v_max3_u32 v101, v101, v147, v149\n\t"
                    "v_max3_u32 v101, v101, v151, v153\n\t"
                    "v_max3_u32 v101, v101, v155, v157\n\t"
                    "v_max_u32 v101, v101, v159\n\t"
                    "v_cmp_eq_u32 vcc, -1, v101\n\t"
                    "s_cbranch_vccz 3f\n\t"
                    "buffer_load_dwordx4 v[128:131], %[zo], %[rw], %[zb] offen sc1\n\t"
                    "buffer_load_dwordx4 v[132:135], %[zo], %[rw], %[zb] offen offset:1024 sc1\n\t"
                    "buffer_load_dwordx4 v[136:139], %[zo], %[rw], %[zb] offen offset:2048 sc1\n\t"
                    "buffer_load_dwordx4 v[140:143], %[zo], %[rw], %[zb] offen offset:3072 sc1\n\t"
                    "buffer_load_dwordx4 v[144:147], %[zo], %[rw], %[z4] offen sc1\n\t"
                    "buffer_load_dwordx4 v[148:151], %[zo], %[rw], %[z4] offen offset:1024 sc1\n\t"
                    "buffer_load_dwordx4 v[152:155], %[zo], %[rw], %[z4] offen offset:2048 sc1\n\t"
                    "buffer_load_dwordx4 v[156:159], %[zo], %[rw], %[z4] offen offset:3072 sc1\n\t"
                    "s_waitcnt vmcnt(0)\n\t"
                    "v_max3_u32 v101, v129, v131, v133\n\t"
                    "v_max3_u32 v101, v101, v135, v137\n\t"
                    "v_max3_u32 v101, v101, v139, v141\n\t"
                    "v_max3_u32 v101, v101, v143, v145\n\t"
                    "v_max3_u32 v101, v101, v147, v149\n\t"
                    "v_max3_u32 v101, v101, v151, v153\n\t"
                    "v_max3_u32 v101, v101, v155, v157\n\t"
                    "v_max_u32 v101, v101, v159\n\t"
                    "v_mov_b32 v100, v101\n\t"
                    "3:\n\t"
                    "v_permlane16_swap_b32 v128, v129\n\t"
                    "v_permlane16_swap_b32 v130, v131\n\t"
                    "v_permlane16_swap_b32 v132, v133\n\t"
                    "v_permlane16_swap_b32 v134, v135\n\t"
                    "v_permlane16_swap_b32 v136, v137\n\t"
                    "v_permlane16_swap_b32 v138, v139\n\t"
                    "v_permlane16_swap_b32 v140, v141\n\t"
                    "v_permlane16_swap_b32 v142, v143\n\t"
                    "v_permlane16_swap_b32 v144, v145\n\t"
                    "v_permlane16_swap_b32 v146, v147\n\t"
                    "v_permlane16_swap_b32 v148, v149\n\t"
                    "v_permlane16_swap_b32 v150, v151\n\t"
                    "v_permlane16_swap_b32 v152, v153\n\t"
                    "v_permlane16_swap_b32 v154, v155\n\t"
                    "v_permlane16_swap_b32 v156, v157\n\t"
                    "v_permlane16_swap_b32 v158, v159\n\t"
                    "s_mov_b32 vcc_lo, 0x00ff00ff\n\t"
                    "s_mov_b32 vcc_hi, 0x00ff00ff\n\t"
                    "s_waitcnt vmcnt(7)\n\t"
                    OST("%[t3]")
                    "v_smfmac_f32_16x16x64_bf16 v[96:99], v[208:211], v[160:167], %[ix]\n\t"
                    "v_max3_u32 v100, v100, v208, v209\n\t"
                    "v_max3_u32 v100, v100, v210, v211\n\t"
                    "s_waitcnt vmcnt(6)\n\t"
                    "v_smfmac_f32_16x16x64_bf16 v[96:99], v[212:215], v[168:175], %[ix]\n\t"
                    "v_max3_u32 v100, v100, v212, v213\n\t"
                    "v_max3_u32 v100, v100, v214, v215\n\t"
                    "s_waitcnt vmcnt(5)\n\t"
                    "v_smfmac_f32_16x16x64_bf16 v[96:99], v[216:219], v[176:183], %[ix]\n\t"
                    "v_max3_u32 v100, v100, v216, v217\n\t"
                    "v_max3_u32 v100, v100, v218, v219\n\t"
                    "s_waitcnt vmcnt(4)\n\t"
                    "v_smfmac_f32_16x16x64_bf16 v[96:99], v[220:223], v[184:191], %[ix]\n\t"
                    "v_max3_u32 v100, v100, v220, v221\n\t"
                    "v_max3_u32 v100, v100, v222, v223\n\t"
                    "s_waitcnt vmcnt(3)\n\t"
                    "v_smfmac_f32_16x16x64_bf16 v[96:99], v[224:227], v[128:135], %[ix]\n\t"
                    "v_max3_u32 v100, v100, v224, v225\n\t"
                    "v_max3_u32 v100, v100, v226, v227\n\t"
                    "s_waitcnt vmcnt(2)\n\t"
                    "v_smfmac_f32_16x16x64_bf16 v[96:99], v[228:231], v[136:143], %[ix]\n\t"
                    "s_barrier\n\t"
                    "ds_read_b64 v[102:103], %[sp]\n\t"
                    "ds_read_b64 v[104:105], %[sp] offset:512\n\t"
                    "ds_read_b64 v[106:107], %[sp] offset:1024\n\t"
                    "v_max3_u32 v100, v100, v228, v229\n\t"
                    "v_max3_u32 v100, v100, v230, v231\n\t"
                    "s_waitcnt vmcnt(1)\n\t"
                    "v_smfmac_f32_16x16x64_bf16 v[96:99], v[232:235], v[144:151], %[ix]\n\t"
                    "v_max3_u32 v100, v100, v232, v233\n\t"
                    "v_max3_u32 v100, v100, v234, v235\n\t"
                    "s_waitcnt vmcnt(0)\n\t"
                    "v_smfmac_f32_16x16x64_bf16 v[96:99], v[236:239], v[152:159], %[ix]\n\t"
                    "v_max3_u32 v100, v100, v236, v237\n\t"
                    "v_max3_u32 v100, v100, v238, v239\n\t"
                    OST("%[t2]")
                    "v_cmp_eq_u32 %[bad], -1, v100\n\t"
                    "s_waitcnt lgkmcnt(0)\n\t"
                    "v_add_f32 v102, v102, v104\n\t"
                    "v_add_f32 v103, v103, v105\n\t"
                    "v_add_f32 v102, v102, v106\n\t"
                    "v_add_f32 v103, v103, v107\n\t"
                    "v_add_f32 v96, v96, v102\n\t"
                    "v_add_f32 v97, v97, v103\n\t"
                    "v_add_f32 v96, v96, v98\n\t"
                    "v_add_f32 v97, v97, v99\n\t"
                    "s_cmp_lg_u64 %[bad], 0\n\t"
                    "s_cbranch_scc1 4f\n\t"
                    "2:\n\t"
                    "v_cndmask_b32_dpp v98, v97, v96, vcc row_ror:8 row_mask:0xf bank_mask:0xf\n\t"
                    "s_not_b64 vcc, vcc\n\t"
                    "v_cndmask_b32_dpp v99, v96, v97, vcc row_ror:8 row_mask:0xf bank_mask:0xf\n\t"
                    "v_cvt_pk_bf16_f32 v98, v98, v99\n\t"
                    "v_min_u32 v98, -2, v98\n\t"
                    "buffer_store_dword v98, %[mo], %[rw], %[m0] offen sc1\n\t"
                    "buffer_store_dword v98, %[mo], %[rw], %[m1] offen sc1\n\t"
                    "buffer_store_dword v98, %[mo], %[rw], %[m2] offen sc1\n\t"
                    "buffer_store_dword v98, %[mo], %[rw], %[m3] offen sc1\n\t"
                    OST("%[t4]")
                    "s_branch 5f\n\t"
                    "4:\n\t"
                    "s_sub_i32 %[tr], %[tr], 1\n\t"
                    "s_cmp_lt_i32 %[tr], 1\n\t"
                    "s_cbranch_scc1 2b\n\t"
                    "v_xor_b32 %[po], %[x0], %[po]\n\t"
                    "s_xor_b32 %[x0], %[x0], 0x14000\n\t"
                    "v_xor_b32 %[pm], %[x1], %[pm]\n\t"
                    "s_xor_b32 %[x1], %[x1], 0x5000\n\t"
                    "buffer_load_dwordx4 v[128:131], %[zo], %[rw], %[zb] offen sc1\n\t"
                    "buffer_load_dwordx4 v[132:135], %[zo], %[rw], %[zb] offen offset:1024 sc1\n\t"
                    "buffer_load_dwordx4 v[136:139], %[zo], %[rw], %[zb] offen offset:2048 sc1\n\t"
                    "buffer_load_dwordx4 v[140:143], %[zo], %[rw], %[zb] offen offset:3072 sc1\n\t"
                    "buffer_load_dwordx4 v[144:147], %[zo], %[rw], %[z4] offen sc1\n\t"
                    "buffer_load_dwordx4 v[148:151], %[zo], %[rw], %[z4] offen offset:1024 sc1\n\t"
                    "buffer_load_dwordx4 v[152:155], %[zo], %[rw], %[z4] offen offset:2048 sc1\n\t"
                    "buffer_load_dwordx4 v[156:159], %[zo], %[rw], %[z4] offen offset:3072 sc1\n\t"
                    "buffer_load_dwordx4 v[208:211], %[po], %[rw], %[pb] offen sc1\n\t"
                    "buffer_load_dwordx4 v[212:215], %[po], %[rw], %[pb] offen offset:1024 sc1\n\t"
                    "buffer_load_dwordx4 v[216:219], %[po], %[rw], %[pb] offen offset:2048 sc1\n\t"
                    "buffer_load_dwordx4 v[220:223], %[po], %[rw], %[pb] offen offset:3072 sc1\n\t"
                    "buffer_load_dwordx4 v[224:227], %[pm], %[rw], %[pb] offen sc1\n\t"
                    "buffer_load_dwordx4 v[228:231], %[pm], %[rw], %[pb] offen offset:1024 sc1\n\t"
                    "buffer_load_dwordx4 v[232:235], %[pm], %[rw], %[pb] offen offset:2048 sc1\n\t"
                    "buffer_load_dwordx4 v[236:239], %[pm], %[rw], %[pb] offen offset:3072 sc1\n\t"
                    "v_mov_b64 v[96:97], 0\n\t"
                    "v_mov_b64 v[98:99], 0\n\t"
                    "v_mov_b32 v100, 0\n\t"
                    "s_waitcnt vmcnt(0)\n\t"
                    "v_max3_u32 v101, v129, v131, v133\n\t"
                    "v_max3_u32 v101, v101, v135, v137\n\t"
                    "v_max3_u32 v101, v101, v139, v141\n\t"
                    "v_max3_u32 v101, v101, v143, v145\n\t"
                    "v_max3_u32 v101, v101, v147, v149\n\t"
                    "v_max3_u32 v101, v101, v151, v153\n\t"
                    "v_max3_u32 v101, v101, v155, v157\n\t"
                    "v_max_u32 v101, v101, v159\n\t"
                    "v_mov_b32 v100, v101\n\t"
                    "v_permlane16_swap_b32 v128, v129\n\t"
                    "v_permlane16_swap_b32 v130, v131\n\t"
                    "v_permlane16_swap_b32 v132, v133\n\t"
                    "v_permlane16_swap_b32 v134, v135\n\t"
                    "v_permlane16_swap_b32 v136, v137\n\t"
                    "v_permlane16_swap_b32 v138, v139\n\t"
                    "v_permlane16_swap_b32 v140, v141\n\t"
                    "v_permlane16_swap_b32 v142, v143\n\t"
                    "v_permlane16_swap_b32 v144, v145\n\t"
                    "v_permlane16_swap_b32 v146, v147\n\t"
                    "v_permlane16_swap_b32 v148, v149\n\t"
                    "v_permlane16_swap_b32 v150, v151\n\t"
                    "v_permlane16_swap_b32 v152, v153\n\t"
                    "v_permlane16_swap_b32 v154, v155\n\t"
                    "v_permlane16_swap_b32 v156, v157\n\t"
                    "v_permlane16_swap_b32 v158, v159\n\t"
                    "s_nop 1\n\t"
                    "v_smfmac_f32_16x16x64_bf16 v[96:99], v[208:211], v[160:167], %[ix]\n\t"
                    "v_max3_u32 v100, v100, v208, v209\n\t"
                    "v_max3_u32 v100, v100, v210, v211\n\t"
                    "v_smfmac_f32_16x16x64_bf16 v[96:99], v[212:215], v[168:175], %[ix]\n\t"
                    "v_max3_u32 v100, v100, v212, v213\n\t"
                    "v_max3_u32 v100, v100, v214, v215\n\t"
                    "v_smfmac_f32_16x16x64_bf16 v[96:99], v[216:219], v[176:183], %[ix]\n\t"
                    "v_max3_u32 v100, v100, v216, v217\n\t"
                    "v_max3_u32 v100, v100, v218, v219\n\t"
                    "v_smfmac_f32_16x16x64_bf16 v[96:99], v[220:223], v[184:191], %[ix]\n\t"
                    "v_max3_u32 v100, v100, v220, v221\n\t"
                    "v_max3_u32 v100, v100, v222, v223\n\t"
                    "v_smfmac_f32_16x16x64_bf16 v[96:99], v[224:227], v[128:135], %[ix]\n\t"
                    "v_max3_u32 v100, v100, v224, v225\n\t"
                    "v_max3_u32 v100, v100, v226, v227\n\t"
                    "v_smfmac_f32_16x16x64_bf16 v[96:99], v[228:231], v[136:143], %[ix]\n\t"
                    "v_max3_u32 v100, v100, v228, v229\n\t"
                    "v_max3_u32 v100, v100, v230, v231\n\t"
                    "v_smfmac_f32_16x16x64_bf16 v[96:99], v[232:235], v[144:151], %[ix]\n\t"
                    "v_max3_u32 v100, v100, v232, v233\n\t"
                    "v_max3_u32 v100, v100, v234, v235\n\t"
                    "v_smfmac_f32_16x16x64_bf16 v[96:99], v[236:239], v[152:159], %[ix]\n\t"
                    "v_max3_u32 v100, v100, v236, v237\n\t"
                    "v_max3_u32 v100, v100, v238, v239\n\t"
                    "v_cmp_eq_u32 %[bad], -1, v100\n\t"
                    "s_nop 5\n\t"
                    "v_add_f32 v96, v96, v102\n\t"
                    "v_add_f32 v97, v97, v103\n\t"
                    "v_add_f32 v96, v96, v98\n\t"
                    "v_add_f32 v97, v97, v99\n\t"
                    "s_cmp_lg_u64 %[bad], 0\n\t"
                    "s_cbranch_scc1 4b\n\t"
                    "s_branch 2b\n\t"
                    "5:\n\t"
                    : [tr] "+s"(tries), [po] "+v"(po), [pm] "+v"(pm), [x0] "+s"(x0), [x1] "+s"(x1), [bad] "=&s"(bad), [t0] "=&s"(t0_), [t1] "=&s"(t1_), [t2] "=&s"(t2_), [t3] "=&s"(t3_), [t4] "=&s"(t4_)
                    : [zo] "v"(zoff), [sp] "v"(spa), [ix] "v"(sidx), [rw] "s"(rwin), [zb] "s"(zb), [z4] "s"(zb + 4096u), [pb] "s"(pb), [mo] "v"(moff), [fd] "n"(FDL),
                      [m0] "s"(mb), [m1] "s"(mb + PAIR), [m2] "s"(mb + mc2), [m3] "s"(mb + mc2 + PAIR), "{v[160:167]}"(zf[0]), "{v[168:175]}"(zf[1]), "{v[176:183]}"(zf[2]), "{v[184:191]}"(zf[3])
                    : "v96", "v97", "v98", "v99", "v100", "v101", "v102", "v103", "v104", "v105", "v106", "v107", "v128", "v129", "v130", "v131", "v132", "v133", "v134", "v135", "v136", "v137", "v138", "v139", "v140", "v141", "v142", "v143", "v144", "v145", "v146", "v147", "v148", "v149", "v150", "v151", "v152", "v153", "v154", "v155", "v156", "v157", "v158", "v159", "v208", "v209", "v210", "v211", "v212", "v213", "v214", "v215", "v216", "v217", "v218", "v219", "v220", "v221", "v222", "v223", "v224", "v225", "v226", "v227", "v228", "v229", "v230", "v231", "v232", "v233", "v234", "v235", "v236", "v237", "v238", "v239", "vcc", "scc", "memory");
                budget = tries;
                if (w == 3) *(volatile LAS int*)(smem + 4100) = s;
                __builtin_amdgcn_s_sleep(DPS - 4);
                if (w == 3) {
                    st128(SENT, rwin, ra, W_MEM + (unsigned)(s & 3) * 6u * PAIR, 16);
                    if (lane == 0 && s > 1) __builtin_amdgcn_raw_buffer_store_b32((unsigned)s - 2u, rwin, 0u, W_GO + fo + (unsigned)o * 128u, 16);
                }
                for (;;) {
                    const u32x2 x = *(volatile LAS u32x2*)(smem + 4096);
                    zr = __builtin_amdgcn_readfirstlane(x[0]);
                    if (__builtin_amdgcn_readfirstlane(x[1]) == s) break;
                    __builtin_amdgcn_s_sleep(2);
                }
                zb = zb == W_ZM + 2u * NOWN * PAIR ? W_ZM : zb + NOWN * PAIR;
            }
            const unsigned zq = zb == W_ZM ? W_ZM + 2u * NOWN * PAIR : zb - NOWN * PAIR;
            while (np < 4) {
                while (zr < NSEG - 1) { __builtin_amdgcn_s_sleep(1); zr = __builtin_amdgcn_readfirstlane(*(volatile LAS int*)(smem + 4096)); }
                piece();
            }
            __builtin_amdgcn_s_sleep(DTK);
            for (int e = 0; e < 2; ++e) {
                f32x4 c[2];
                for (unsigned spins = 0;;) {
                    asm volatile("" ::: "memory");
                    u32x4 pt[16], ptm[2][2], zm[2];
                    const unsigned pl = W_PL + fo + (unsigned)e * (PTSEG * 2u);
#pragma unroll
                    for (int q = 0; q < 2; ++q) zm[q] = __builtin_amdgcn_raw_buffer_load_b128(rwin, zoff + hg * 2048u + q * 1024u, e ? zb : zq, 16);
#pragma unroll
                    for (int q = 0; q < 16; ++q) pt[q] = __builtin_amdgcn_raw_buffer_load_b128(rwin, ((unsigned)(16 * hg) * 1024u + lane * 16u) | (q & 1 ? 0u : (lane & 32u) << 25), pl + q * 1024u, 16);
#pragma unroll
                    for (int q = 0; q < 4; ++q) ptm[q >> 1][q & 1] = __builtin_amdgcn_raw_buffer_load_b128(rwin, (unsigned)(hg * 2048 + lane * 16), pl + 65536u + (q >> 1) * 8192u + (q & 1) * 1024u, 16);
                    unsigned m = max(smax(zm[0]), smax(zm[1]));
#pragma unroll
                    for (int t = 0; t < 2; ++t) {
                        c[t] = z4;
#pragma unroll
                        for (int q = 0; q < 8; ++q) { c[t] = mfma32(half8(zf[q >> 1], q & 1), pt[2 * q + t], c[t]); m = max(m, smax(pt[2 * q + t])); }
#pragma unroll
                        for (int q = 0; q < 2; ++q) { c[t] = mfma32((u32x4){zm[q][0], zm[q][2], zm[q][1], zm[q][3]}, ptm[t][q], c[t]); m = max(m, smax(ptm[t][q])); }
                    }
                    if (!__any(m == 0xFFFFFFFFu)) break;
                    if (spin_fail(spins, rwin)) break;
                }
#pragma unroll
                for (int t = 0; t < 2; ++t) *(f32x4*)(op + e * 8192 + hg * 2048 + (16 * t + fr) * 64 + fq * 16) = c[t];
                BAR();
                VMWAIT();
#pragma unroll
                for (int q = 0; q < 4; ++q) zf[q] = cat8(*(const u32x4*)(smem + 32768 + hg * 8192 + q * 2048 + lane * 16), *(const u32x4*)(smem + 33792 + hg * 8192 + q * 2048 + lane * 16));
                if (e == 0) BAR0();
            }
#pragma unroll
            for (int q = 0; q < 4; ++q) st128(SENT, rwin, zoff + hg * 2048u + (q & 1) * 1024u, q < 2 ? zb : zq, 16);
            if (w == 3) st128(SENT, rwin, ra, W_MEM + 3u * 6u * PAIR, 16);
        } else if (w == 4) {
            const unsigned fl = fz_at(lane & 31, o / 10) + (lane & 32) * 128u;
            int zk = -1;
            auto note = [&](int f) INL {
                f = min(f, __builtin_amdgcn_update_dpp(f, f, 0xB1, 0xF, 0xF, false)); f = min(f, __builtin_amdgcn_update_dpp(f, f, 0x4E, 0xF, 0xF, false));
                f = min(f, __builtin_amdgcn_update_dpp(f, f, 0x141, 0xF, 0xF, false)); f = min(f, __builtin_amdgcn_update_dpp(f, f, 0x140, 0xF, 0xF, false));
                zk = seg_done(MXV, min(__builtin_amdgcn_readlane(f, 0), __builtin_amdgcn_readlane(f, 16)), min(__builtin_amdgcn_readlane(f, 32), __builtin_amdgcn_readlane(f, 48)), MXA);
                *(volatile LAS int*)(smem + 4096) = zk;
            };
            for (int s = 0; s < NSEG; ++s) {
                for (unsigned spins = 0; __builtin_expect(zk < s && s > 0, 0);) {
                    if (spins == 0) __builtin_amdgcn_s_sleep(DW4);
                    asm volatile("" ::: "memory");
                    note((int)__builtin_amdgcn_raw_buffer_load_b32(rwin, fl, W_FZ + fo, 16));
                    FC(fc2);
                    if (spin_fail(spins, rwin)) note(NSEG);
                    if (zk < s) __builtin_amdgcn_s_sleep(ZPS);
                }
                if (LKE && s > 0 && zk < (s + 8 >= NSEG ? NSEG - 1 : s + ZLA)) { __builtin_amdgcn_s_sleep(LKE); note((int)__builtin_amdgcn_raw_buffer_load_b32(rwin, fl, W_FZ + fo, 16)); }
                asm volatile("s_waitcnt lgkmcnt(0)\n\ts_barrier\n\ts_barrier" ::: "memory");
                int f = 0;
                const bool look = !LKE && zk < (s + 8 >= NSEG ? NSEG - 1 : s + ZLA);
                if (look) { __builtin_amdgcn_s_sleep(6); f = (int)__builtin_amdgcn_raw_buffer_load_b32(rwin, fl, W_FZ + fo, 16); }
                SB();
                BAR0();
                if (look) note(f);
            }
        } else if (w < 7) {
            const unsigned ooff = (unsigned)((S_ - (7 - w) * SEG + (lane >> 1)) * D_ + ot * 16 + (lane & 1) * 8) * 2u;
            for (int s = 0; s < 3 * NSEG - 3 + (w - 5); ++s) BAR0();
            if (w == 5) __builtin_amdgcn_s_sleep(DPS);
            const u32x4 xr = __builtin_amdgcn_raw_buffer_load_b128(rws, ooff, O_X1, 16);
            BAR0();
            if (w == 6) BAR0();
            const char* src = op + (w - 5) * 8192 + lane * 32;
            f32x4 x = *(const f32x4*)src, y = *(const f32x4*)(src + 16);
#pragma unroll
            for (int ww = 1; ww < 4; ++ww) { x += *(const f32x4*)(src + ww * 2048); y += *(const f32x4*)(src + ww * 2048 + 16); }
            const u32x4 r = {pack2(lo2f(xr[0]) + bfr(x[0]), hi2f(xr[0]) + bfr(x[1])), pack2(lo2f(xr[1]) + bfr(x[2]), hi2f(xr[1]) + bfr(x[3])),
                             pack2(lo2f(xr[2]) + bfr(y[0]), hi2f(xr[2]) + bfr(y[1])), pack2(lo2f(xr[3]) + bfr(y[2]), hi2f(xr[3]) + bfr(y[3]))};
            st128(nosent4(r), rout(), ooff, 0u, 16);
            if (w == 5) { BAR0(); BAR0(); }
        } else {
            Tk tk = tk_init();
            __builtin_amdgcn_s_setprio(3);
            for (int s = 0; s < NSEG; ++s) {
                const unsigned ta0 = ck.ta, mm0 = ck.ma;
                unsigned tp = tpp + lt + xs + M1;
                if (s < SAD) {
                    if (s) wait_fine(tpp + CF, ck);
                    tp = M2 + xs + (s ? 0 : PEN1);
                    sched(tk, s);
                    tp = tpp - tp;
                    if (s == 0) { wait_fine(tp - DZ0, ck); *(volatile LAS int*)(smem + 4096) = 0; }
                }
                wait_fine_bar(tp - DZP, ck);
                wait_cyc_bar(cyc_at(ck, sx24(tp + (s + 1 < NSEG ? 0 : ML) - ck.ta)));
                clk_rate(ck, ta0, mm0);
                asm volatile("s_barrier ; 7" ::: "memory");
                if (s >= SAD && s + 1 < NSEG) sched(tk, s);
                RTS();
            }
        }
        __builtin_amdgcn_s_setprio(0);
        SST(23);
    };
    static_assert(NSEG % 3 == 1, "ring");
    auto out_role = [&]() INL {
        LANES();
        const int o = bid - NREC, cg = o % 10, send = NSEG - 2;
        const unsigned ztoff = (unsigned)((4 * cg * H_ + 4 * w) * 64 + lane) * 16u;
        const unsigned ptoff = (unsigned)(8 * w * 64 + lane) * 16u, ptmoff = 65536u + (unsigned)(4 * w * 16 + lane) * 16u;
        const unsigned ptoff0 = ptoff | (lane & 32u) << 25;
        const unsigned ooff = (unsigned)(((w & 1) * 16 + (lane >> 2)) * D_ + tile_of(4 * cg + (w & 2)) * 16 + (lane & 3) * 8) * 2u;
        const unsigned fg = fo + (unsigned)((4 * w + (lane & 3)) * 128), gw = (unsigned)((4 * cg + ((lane >> 2) & 1) * 3) / 10) * 4u;
        const unsigned tfoff = fg + (lane < 16 ? W_FPT + (lane >> 2) * 4u : W_FZ2 + gw), zfoff = fo + W_FZ + fz_at(4 * w + (lane & 3), gw / 4u) + (lane & 32u) * 128u;
        int rdy = -1, go = -1, zrdy = -1;
        auto wait_rdy = [&](int s) INL {
            for (unsigned spins = 0; rdy < s;) {
                asm volatile("" ::: "memory");
                if (s < TAILS || go < s - 2) {
                    go = (int)__builtin_amdgcn_readfirstlane(__builtin_amdgcn_raw_buffer_load_b32(rwin, 0u, W_GO + fo + (unsigned)o * 128u, 16));
                    rdy = max(rdy, go);
                    if (rdy < s) { if (s < TAILS || go < s - 3) __builtin_amdgcn_s_sleep(OSLP); else __builtin_amdgcn_s_sleep(OSLP3); }
                } else {
                    if (!__any((int)__builtin_amdgcn_raw_buffer_load_b32(rwin, tfoff, 0u, 16) < s)) rdy = s;
                    else __builtin_amdgcn_s_sleep(OSLP2);
                }
                if (rdy < s && spin_fail(spins, rwin)) break;
            }
        };
        for (int s = o / 10; s < send; s += 4) {
            u32x4 xr, zf[4][4], zx[4], pt[8], ptm[2];
            RT(0);
            for (unsigned spins = 0; zrdy < s;) {
                asm volatile("" ::: "memory");
                int f = (int)__builtin_amdgcn_raw_buffer_load_b32(rwin, zfoff, 0u, 16);
                f = min(f, __builtin_amdgcn_update_dpp(f, f, 0xB1, 0xF, 0xF, false)); f = min(f, __builtin_amdgcn_update_dpp(f, f, 0x4E, 0xF, 0xF, false));
                f = min(f, __builtin_amdgcn_update_dpp(f, f, 0x141, 0xF, 0xF, false));
                zrdy = seg_done(MXV, __builtin_amdgcn_readlane(f, 0), __builtin_amdgcn_readlane(f, 32));
                if (zrdy >= s || spin_fail(spins, rwin)) break;
                __builtin_amdgcn_s_sleep(OSLP);
            }
            if (w >= 4) xr = __builtin_amdgcn_raw_buffer_load_b128(rws, ooff, O_X1 + (unsigned)(s * SEG * D_ * 2), 16);
#pragma unroll
            for (int q = 0; q < 16; ++q) zf[q >> 2][q & 3] = __builtin_amdgcn_raw_buffer_load_b128(rws, ztoff + (q & 3) * 1024u, O_Z + (unsigned)((s - layer) * (int)ZTSEG) * 2u + (q >> 2) * (H_ * 1024u), 16);
            if (s == 61) SST(27);
            wait_rdy(s);
            if (s == 61) SST(28);
            RT(1);
            const unsigned ptb = O_PT + (unsigned)(s * PTSEG) * 2u;
#pragma unroll
            for (int q = 0; q < 8; ++q) pt[q] = __builtin_amdgcn_raw_buffer_load_b128(rws, (q & 1 ? ptoff : ptoff0) + q * 1024u, ptb, 16);
#pragma unroll
            for (int q = 0; q < 2; ++q) ptm[q] = __builtin_amdgcn_raw_buffer_load_b128(rws, ptmoff + q * 8192u, ptb, 16);
#pragma unroll
            for (int q = 0; q < 4; ++q) zx[q] = __builtin_amdgcn_raw_buffer_load_b128(rws, tid * 16u, O_Z2 + (unsigned)(s * NOWN + 4 * cg + q) * 8192u, 16);
            char* op = smem + ((s >> 2) & 1) * 65536;
            f32x4 c[8];
#pragma unroll
            for (int u = 0; u < 8; ++u) {
                c[u] = z4;
#pragma unroll
                for (int q = 0; q < 4; ++q) c[u] = mfma32(zf[u >> 1][q], pt[2 * q + (u & 1)], c[u]);
            }
#pragma unroll
            for (int u = 0; u < 8; ++u) {
                const int tq = u & 1, t = u >> 1;
                c[u] = mfma32((u32x4){zx[t][0], zx[t][2], zx[t][1], zx[t][3]}, ptm[tq], c[u]);
                *(f32x4*)(op + w * 8192 + (t >> 1) * 4096 + (16 * tq + fr) * 128 + ((((t & 1) * 4 + fq) ^ (fr & 7)) << 4)) = c[u];
            }
            BAR();
            if (s == 61) SST(30);
            if (w >= 4) {
                const int tk = (w & 1) * 16 + (lane >> 2);
                const char* s0 = op + (w & 2) * 2048 + tk * 128 + (((2 * (lane & 3)) ^ (tk & 7)) << 4);
                const char* s1 = op + (w & 2) * 2048 + tk * 128 + (((2 * (lane & 3) + 1) ^ (tk & 7)) << 4);
                f32x4 x = *(const f32x4*)s0, y = *(const f32x4*)s1;
#pragma unroll
                for (int ww = 1; ww < 8; ++ww) { x += *(const f32x4*)(s0 + ww * 8192); y += *(const f32x4*)(s1 + ww * 8192); }
                const u32x4 r = {pack2(lo2f(xr[0]) + bfr(x[0]), hi2f(xr[0]) + bfr(x[1])), pack2(lo2f(xr[1]) + bfr(x[2]), hi2f(xr[1]) + bfr(x[3])),
                                 pack2(lo2f(xr[2]) + bfr(y[0]), hi2f(xr[2]) + bfr(y[1])), pack2(lo2f(xr[3]) + bfr(y[2]), hi2f(xr[3]) + bfr(y[3]))};
                if (layer) st128(r, rout(), ooff, (unsigned)(s * SEG * D_ * 2), OAUX);
                else {
                    st128(nosent4(r), rout(), ooff, (unsigned)(s * SEG * D_ * 2), 16);
                    if (s < 56) {
                        VMWAIT();
                        __builtin_amdgcn_raw_buffer_store_b32(0u, rwin, LANE0, W_OUT + (unsigned)(s * 64 + 4 * cg + (w & 3)) * 4u, 16);
                    }
                }
            } else __builtin_amdgcn_s_sleep(OSL4);
            if (s < 8) SST(8 + s);
            if (s >= 56) SST(s - 40);
            RT(2);
            RTS();
        }
    };
    auto a1_units = [&](auto late) INL {
        constexpr bool LATE = decltype(late)::v;
        const int tid = mytid();
        RowTw tw;
        row_tw(tw, tid);
        for (int u = bid - NWRK + (LATE ? 224 : 0); u < (LATE ? 256 : 224); u += 16) {
            if (w == 4) {
                for (unsigned spins = 0;;) {
                    asm volatile("" ::: "memory");
                    if (LATE) {
                        if ((int)__builtin_amdgcn_readfirstlane(__builtin_amdgcn_raw_buffer_load_b32(rwin, 0u, W_GO + (unsigned)(u & 31) * 128u, 16)) >= min((u >> 2) - 2, NSEG - 4)) break;
                    } else if (!__any(__builtin_amdgcn_raw_buffer_load_b32(rwin, min(fresh_lane(), (unsigned)NOWN - 1u) * 4u, W_OUT + (unsigned)(u >> 2) * 256u, 16) != 0u)) break;
                    if (spin_fail(spins, rwin)) break;
                    if (u < 208) __builtin_amdgcn_s_sleep(WSLP); else __builtin_amdgcn_s_sleep(16);
                }
            }
            BAR();
            SST(24);
            a1_unit<4, LATE>(mkrsrc(ws + OFF_X2, S_ * D_ * 2), u * 8, rws, rwin, tw, tid, fs);
            if (!LATE) VMWAIT();
            FST(8);
            BAR0();
            FST(9);
            if (!LATE && w == 0) __builtin_amdgcn_raw_buffer_store_b64(__builtin_bit_cast(u32x2, tag + 9ull), rwin, LANE0, W_A1 + (unsigned)u * 128u, 16);
            SST(25);
            FSW(7);
            if (!LATE && u < 176) __builtin_amdgcn_s_sleep(A1S);
        }
    };
    u32x8 wt[2][10];
#define PINW() asm volatile("" : "+{v[0:7]}"(wt[0][0]), "+{v[8:15]}"(wt[1][0]), "+{v[16:23]}"(wt[0][1]), "+{v[24:31]}"(wt[1][1]), "+{v[32:39]}"(wt[0][2]), "+{v[40:47]}"(wt[1][2]), "+{v[48:55]}"(wt[0][3]), "+{v[56:63]}"(wt[1][3]), "+{v[64:71]}"(wt[0][4]), "+{v[72:79]}"(wt[1][4]), "+{v[80:87]}"(wt[0][5]), "+{v[88:95]}"(wt[1][5]), "+{v[96:103]}"(wt[0][6]), "+{v[104:111]}"(wt[1][6]), "+{v[112:119]}"(wt[0][7]), "+{v[120:127]}"(wt[1][7]), "+{v[128:135]}"(wt[0][8]), "+{v[136:143]}"(wt[1][8]), "+{v[144:151]}"(wt[0][9]), "+{v[152:159]}"(wt[1][9]))
#define KILLW() asm volatile("" : "={v[0:7]}"(wt[0][0]), "={v[8:15]}"(wt[1][0]), "={v[16:23]}"(wt[0][1]), "={v[24:31]}"(wt[1][1]), "={v[32:39]}"(wt[0][2]), "={v[40:47]}"(wt[1][2]), "={v[48:55]}"(wt[0][3]), "={v[56:63]}"(wt[1][3]), "={v[64:71]}"(wt[0][4]), "={v[72:79]}"(wt[1][4]), "={v[80:87]}"(wt[0][5]), "={v[88:95]}"(wt[1][5]), "={v[96:103]}"(wt[0][6]), "={v[104:111]}"(wt[1][6]), "={v[112:119]}"(wt[0][7]), "={v[120:127]}"(wt[1][7]), "={v[128:135]}"(wt[0][8]), "={v[136:143]}"(wt[1][8]), "={v[144:151]}"(wt[0][9]), "={v[152:159]}"(wt[1][9]))
    if (blockIdx.x < 64) {
        for (;;) {
            load_qv(wt, bid & 31, bid >= 32);
            const unsigned x = get_rot();
            if (__builtin_expect(x == vx, 1)) break;
            set_role(x);
        }
        SST(1);
        if (w == 0) wait_tags(rwin, 0, 1, 64u, tag + 8ull, true);
        PINW();
    } else {
        if (bid >= NWRK) probe();
        KILLW();
    }
    for (; layer < 2; ++layer) {
        new_layer();
        PINW();
        BAR();
        if (qx || (layer ? bid < 96 : (bid >= 32 && bid < 64) || (bid >= 104 && bid < NREC) || xq)) {
            const int ci = qx ? bid - 80 : bid < 96 ? bid : bid < NREC ? bid - 104 : 64 + (bid - NREC) / 10 * 8 + (bid - NREC) % 10;
            SST(26);
            a2_unit(ci >= 100 ? ci - 100 : ci + 60, layer, rws, rwin, mytid(), [&](int k) INL {
                if (k == 2) SST(28);
                else if (w == 0) {
                    if (layer && bid >= 64) for (unsigned spins = 0;;) {
                        asm volatile("" ::: "memory");
                        if (__all(__builtin_bit_cast(u64, __builtin_amdgcn_raw_buffer_load_b64(rwin, 0u, W_A1 + (unsigned)(200 + (bid & 15)) * 128u, 16)) == tag + 9ull) || spin_fail(spins, rwin)) break;
                        __builtin_amdgcn_s_sleep(100);
                    }
                    if (layer && bid >= 64) rec_end();
                    wait_tags(rwin, 0, 4, layer ? 32u : 64u, tag + 8ull + layer, !layer);
                    SST(27);
                }
            }, fs);
            SST(29);
            VMWAIT();
            FST(8);
            BAR0();
            FST(9);
            if (w == 0) {
                __builtin_amdgcn_raw_buffer_store_b32(0u, rwin, LANE0, W_COL + fo + (unsigned)ci * 128u, 16);
                if (bid == NQT && layer == 1) __builtin_amdgcn_raw_buffer_store_b64(__builtin_bit_cast(u32x2, tag), rws, LANE0, 0u, 16);
            }
            SST(30);
            FSW(1 + layer);
        }
#define NEXTB() do { PINW(); asm volatile("" : "+s"(bid), "+s"(w)); } while (0)
        NEXTB();
        if (bid < 32) { fresh_rsrc(); qk_role(wt); PINW(); }
        NEXTB();
        const int hh = bid < 96 ? bid & 31 : bid < NQT ? (bid - NREC) / 10 * 8 + (bid - NREC) % 10 : (bid - NQT) & 31;
        if (((qx || (bid >= 32 && bid < 64)) && layer == 0) || xq) { if (layer) { if (w == 0) rec_end(); BAR0(); } load_qv(wt, hh, xt || bid < 64); SST(1); PINW(); }
        NEXTB();
        if (bid >= 32 && bid < 64) { if (layer == 0) near_init(); fresh_rsrc(); vo_role(wt); PINW(); }
        NEXTB();
        if ((bid >= 64 && bid < 96) || xt) {
            if (bid < 96 && layer == 0) near_init();
            fresh_rsrc();
            vt_role(wt, hh, xt ? MXV : ~MXV);
            if (xt) {
                const unsigned g0 = (unsigned)(hh * 512 + mytid()) * 16u;
                if (layer == 0) for (unsigned i = 0; i < 13; ++i) {
                    st128(SENT, i < 7 ? mkrsrc(ws + O_ARM, O_ARM1 - O_ARM) : i < 12 ? mkrsrc(ws + O_Z - ZTSEG * 2, ZTSEG * 2) : mkrsrc(arg(1) + (W_ARM - W_LAYER) / 2, W_LAYER), g0, (i < 7 ? i : i < 12 ? i - 7 : 0) * 262144u, 16);
                    __builtin_amdgcn_s_sleep(40);
                }
                static_assert(O_ARM + 7u * 262144u >= O_ARM1 && ZTSEG * 2 <= 5u * 262144u && W_LAYER <= 262144u && !(MXV >> 40), "arming");
                VMWAIT();
                BAR0();
                if (w >= 4) __builtin_amdgcn_raw_buffer_store_b32(NSEG - 1, rwin, LANE0, W_FZ + fo + fz_at(hh, w - 4) + 4096u, 16);
            }
            PINW();
        }
        NEXTB();
        if ((qx && !xt) || xq) { fresh_rsrc(); qt_role(wt, hh, IC<1>()); PINW(); }
        NEXTB();
        if (bid >= 96 && !qx) {
            const int fi = xq ? 256 : bid >= NWRK ? 255 - bid : bid >= NREC ? 8 + (bid - NREC) / 10 * 2 + (bid - NREC) % 10 : bid - 72, tid = mytid();
            RowTw tw;
            row_tw(tw, tid);
            if (layer == 0 && fi >= 16 && fi < 32) { if (w == 0) wait_tags(rwin, 0, 1, 64u, tag + 8ull, true); BAR0(); }
            if (layer && fi < 64) {
                for (unsigned spins = 0;;) {
                    asm volatile("" ::: "memory");
                    if (__all(__builtin_bit_cast(u64, __builtin_amdgcn_raw_buffer_load_b64(rwin, 0u, W_A1 + (unsigned)fi * 128u, 16)) == tag + 9ull) || spin_fail(spins, rwin)) break;
                    __builtin_amdgcn_s_sleep(8);
                }
            }
            bool cols = false;
            for (int u = fi; u < 256; u += 16) {
                a3_unit(mkrsrc(layer ? (const u16*)(ws + OFF_X2) : arg(A_X), S_ * D_ * 2), u * 8, layer, rws, rwin, tw, tid, [&]() INL {
                    if (cols) return;
                    cols = true;
                    if (w == 7) {
                        const unsigned l = fresh_lane();
                        unsigned o[3] = {l * 128u, l * 128u + 8192u, l < 32u ? l * 128u + 16384u : 0x40000000u};
                        for (unsigned spins = 0;;) {
                            asm volatile("" ::: "memory");
                            unsigned v[3];
#pragma unroll
                            for (int g = 0; g < 3; ++g) v[g] = __builtin_amdgcn_raw_buffer_load_b32(rwin, o[g], W_COL + fo, 16);
#pragma unroll
                            for (int g = 0; g < 3; ++g) if (v[g] == 0u) o[g] = 0x40000000u;
                            if (!__any((v[0] | v[1] | v[2]) != 0u) || spin_fail(spins, rwin)) break;
                            __builtin_amdgcn_s_sleep(CSLP);
                        }
                    }
                    BAR0();
                    SST(6);
                }, fs);
                VMWAIT();
                FST(8);
                BAR0();
                FST(9);
                if (w == 0) __builtin_amdgcn_raw_buffer_store_b32(0u, rwin, LANE0, W_H + fo + (unsigned)((u >> 2) * 128 + (u & 3) * 4), 16);
                if (u < 64) SST(7);
                FSW(u < 64 ? 3 + layer : 5 + layer);
                if (fi >= 16) break;
                if (u < 64) u += 48;
            }
            if (bid < NREC) { fresh_rsrc(); own_role(); }
            else if (bid < NQT) out_role();
            else if (layer == 0) { a1_units(IC<0>()); a1_units(IC<1>()); }
            KILLW();
        }
        NEXTB();
        SST(31);
    }
}

constexpr int L_DBG = 0;
extern "C" size_t dbg_offset(int i) {
    const size_t o[] = {OFF_WIN, W_TMO, W_NEAR, OFF_F, OFF_X1, OFF_X2, OFF_H, OFF_WFT};
    return o[i];
}

extern "C" void kernel_launch(void* const* d_in, const int* in_sizes, int n_in, void* d_out, int out_size, void* d_ws, size_t ws_size,
                              hipStream_t stream) {
    char* ws = (char*)d_ws;
    hipFuncSetAttribute((const void*)mega_kernel, hipFuncAttributeMaxDynamicSharedMemorySize, L_END + L_DBG);
    char* win = (char*)(((uintptr_t)(ws + OFF_WIN) + 0x1FFFFFu) & ~(uintptr_t)0x1FFFFFu);
    Args a{ws, win, (const u16*)d_in[0], (const u16*)d_in[1], (const u16*)d_in[2], (const u16*)d_in[4], (const u16*)d_in[5], (const u16*)d_in[6], (const u16*)d_in[7], (const u16*)d_in[8], (u16*)d_out};
    mega_kernel<<<256, 512, L_END + L_DBG, stream>>>(a);
}
```

```cpp
#include <hip/hip_runtime.h>
#include <hip/hip_bf16.h>
#include <stdint.h>

typedef unsigned short u16;
typedef unsigned long long u64;
typedef __attribute__((ext_vector_type(8))) __bf16 bf16x8;
typedef __attribute__((ext_vector_type(4))) short s16x4;
typedef __attribute__((ext_vector_type(4))) float f32x4;
typedef __attribute__((ext_vector_type(8))) float f32x8;
typedef __attribute__((ext_vector_type(16))) float f32x16;
typedef __attribute__((ext_vector_type(4))) unsigned u32x4;
typedef __attribute__((ext_vector_type(2))) unsigned u32x2;
typedef __attribute__((ext_vector_type(8))) unsigned u32x8;
typedef __attribute__((ext_vector_type(16))) __bf16 bf16x16;
typedef __attribute__((ext_vector_type(2))) unsigned short u16x2;
#define LAS __attribute__((address_space(3)))
#define INL __attribute__((always_inline))

constexpr int S_ = 2048, D_ = 640, DF = 321, H_ = 32, DH = 128, HD = 4096, NM = 8, SEG = 32, NSEG = 64;
constexpr int NOWN = 40;
constexpr int NCOL = 160;
constexpr size_t ZTSEG = (size_t)NOWN * H_ * 512;
constexpr unsigned PAIR = 16384, HALF = 8192;
constexpr unsigned W_MEM = 0, W_T = W_MEM + 24 * PAIR, W_P = 33 * PAIR, W_PROBE = 74 * PAIR, W_GO = W_PROBE + 2 * PAIR, W_FZ = W_GO + 5120, W_FPT = W_FZ + 8192, W_FZ2 = W_FPT + 4096,
                   W_FQ = W_FZ2 + 4096, W_H = W_FQ + 8192, W_COL = W_H + HALF, W_PL = W_COL + 20480, W_LAYER = W_PL + 2 * 81920 - W_GO, W_OUT = 32 * PAIR, W_MISC = W_OUT + 14336,
                   W_ARM = W_GO + 2 * W_LAYER, W_A1 = 106 * PAIR, W_ZM = 2u * 1024 * 1024, W_END = W_ZM + 120 * PAIR;
static_assert(W_ARM <= W_A1 && W_ARM % 1024 == 0, "window");
__device__ __forceinline__ unsigned p_slot(int s) { return W_P + (unsigned)((s & 3) * 10 + (((s & 3) + 1) >> 2)) * PAIR; }
constexpr unsigned W_TMO = W_MISC, W_NEAR = W_MISC + 128;
constexpr int PTSEG = 32768 + 8192;

#define FPAD 256
constexpr unsigned FST_ = S_ * 16 + FPAD, GST = 4096 + FPAD, WST = S_ * 8 + FPAD;
constexpr size_t al(size_t x) { return (x + 255) & ~(size_t)255; }
constexpr size_t OFF_CTL  = 0;
constexpr size_t OFF_WFT  = 4096 + 4 * 1024 * 1024;
constexpr size_t OFF_F    = OFF_WFT + al((size_t)(NCOL + 2) * WST);
constexpr size_t OFF_X1   = OFF_F + al((size_t)NCOL * FST_);
constexpr size_t OFF_X2   = OFF_X1 + al((size_t)S_ * D_ * 2);
constexpr size_t OFF_FL   = OFF_X2 + al((size_t)S_ * D_ * 2);
constexpr int NHF = 5;
constexpr size_t OFF_H    = OFF_FL + 3 * (size_t)NCOL * GST + NHF * 40960;
constexpr size_t OFF_Z    = OFF_H + al((size_t)S_ * D_ * 2) + ZTSEG * 2;
constexpr size_t OFF_Z2   = OFF_Z + al(ZTSEG * NSEG * 2);
constexpr size_t OFF_PT   = OFF_Z2 + al((size_t)NSEG * NOWN * 8192);
constexpr size_t OFF_QK   = OFF_PT + al((size_t)NSEG * PTSEG * 2);
constexpr size_t OFF_WIN  = OFF_QK + (size_t)NSEG * H_ * 16384;
constexpr size_t OFF_END  = OFF_WIN + (size_t)6 * 1024 * 1024;
static_assert(OFF_END <= (size_t)256 * 1024 * 1024, "workspace");
constexpr unsigned O_FL = (unsigned)OFF_FL, O_ARM = O_FL - 256 * D_ * 2, O_ARM1 = O_FL + 2 * NCOL * GST, O_WFT = (unsigned)OFF_WFT, O_F = (unsigned)OFF_F, O_H = (unsigned)OFF_H, O_X1 = (unsigned)OFF_X1, O_Z = (unsigned)OFF_Z, O_Z2 = (unsigned)OFF_Z2, O_PT = (unsigned)OFF_PT, O_QK = (unsigned)OFF_QK;

constexpr double PI_ = 3.14159265358979323846;
constexpr double tay(double x, int s) {
    double t = s ? x : 1.0, r = t;
    for (int i = s + 1; i < 18; i += 2) { t *= -x * x / (double)(i * (i + 1)); r += t; }
    return r;
}
struct Cs { float c, s; };
constexpr Cs cis(int m, int n) {
    m %= n;
    const int q = (m * 8 / n + 1) / 2;
    const double x = 2.0 * PI_ * (double)(4 * m - q * n) / (double)(4 * n), c = tay(x, 0), s = tay(x, 1);
    return (q & 3) == 0 ? Cs{(float)c, (float)-s} : (q & 3) == 1 ? Cs{(float)-s, (float)-c} : (q & 3) == 2 ? Cs{(float)-c, (float)s} : Cs{(float)s, (float)c};
}
template <int NI> struct alignas(16) Tw7 { Cs v[4][NI][2]; };
template <int NI, int N, int KIND> constexpr Tw7<NI> mk_tw() {
    Tw7<NI> r{};
    for (int i = 0; i < NI; ++i)
        for (int q = 1; q <= 8; ++q) r.v[(q - 1) >> 1][i][(q - 1) & 1] = cis((KIND == 0 ? i : KIND == 1 ? 8 * (i >> 3) : (i >> 3) + 10 * (i & 7)) * q, N);
    return r;
}
struct TwCol { Tw7<256> a; Tw7<32> b; Tw7<4> c; };
static __device__ const TwCol TWC = {mk_tw<256, 2048, 0>(), mk_tw<32, 256, 0>(), mk_tw<4, 32, 0>()};
static __device__ const Tw7<80> TWR2 = mk_tw<80, 640, 1>();
static __device__ const Tw7<80> TWR3 = mk_tw<80, 640, 2>();

#define st128(v, r, vo, so, aux) do { u32x4 d_ = (v); asm volatile("" : "+v"(d_)); __builtin_amdgcn_raw_buffer_store_b128(d_, r, vo, so, aux); asm volatile("s_nop 1" :: "v"(d_)); } while (0)
__device__ __forceinline__ float bf2f(u16 v) { return __uint_as_float((unsigned)v << 16); }
__device__ __forceinline__ u16 f2bf(float f) { __bf16 b = (__bf16)f; return __builtin_bit_cast(u16, b); }
__device__ __forceinline__ float bfr(float f) { return (float)(__bf16)f; }
typedef __attribute__((ext_vector_type(2))) float f32x2;
typedef __attribute__((ext_vector_type(2))) __bf16 bf16x2;
__device__ __forceinline__ unsigned pack2(float a, float b) { return __builtin_bit_cast(unsigned, __builtin_convertvector((f32x2){a, b}, bf16x2)); }
__device__ __forceinline__ float lo2f(unsigned v) { return __uint_as_float(v << 16); }
__device__ __forceinline__ float hi2f(unsigned v) { return __uint_as_float(v & 0xFFFF0000u); }
__device__ __forceinline__ float fmx(float a, float b) { return __builtin_elementwise_maximum(a, b); }
__device__ __forceinline__ f32x4 mfma32(u32x4 a, u32x4 b, f32x4 c) {
    return __builtin_amdgcn_mfma_f32_16x16x32_bf16(__builtin_bit_cast(bf16x8, a), __builtin_bit_cast(bf16x8, b), c, 0, 0, 0);
}
__device__ __forceinline__ f32x4 mfma16(u32x2 a, u32x2 b, f32x4 c) {
    return __builtin_amdgcn_mfma_f32_16x16x16bf16_1k(__builtin_bit_cast(s16x4, a), __builtin_bit_cast(s16x4, b), c, 0, 0, 0);
}
__device__ __forceinline__ f32x4 smfmac(u32x4 a, u32x8 b, f32x4 c, int idx) {
    return __builtin_amdgcn_smfmac_f32_16x16x64_bf16(__builtin_bit_cast(bf16x8, a), __builtin_bit_cast(bf16x16, b), c, idx, 0, 0);
}
__device__ __forceinline__ int pi16(int k) { return ((k & 3) << 2) | ((k >> 1) & 2) | (k >> 3); }
__device__ __forceinline__ float addswap32(float a, float b) {
    auto r = __builtin_amdgcn_permlane32_swap(__float_as_uint(a), __float_as_uint(b), false, false);
    return __uint_as_float(r[0]) + __uint_as_float(r[1]);
}
__device__ __forceinline__ float xmax3216(float v) {
    auto r = __builtin_amdgcn_permlane32_swap(__float_as_uint(v), __float_as_uint(v), false, false);
    v = fmx(__uint_as_float(r[0]), __uint_as_float(r[1]));
    auto q = __builtin_amdgcn_permlane16_swap(__float_as_uint(v), __float_as_uint(v), false, false);
    return fmx(__uint_as_float(q[0]), __uint_as_float(q[1]));
}
__device__ __forceinline__ float xsum3216(float v) {
    auto r = __builtin_amdgcn_permlane32_swap(__float_as_uint(v), __float_as_uint(v), false, false);
    v = __uint_as_float(r[0]) + __uint_as_float(r[1]);
    auto q = __builtin_amdgcn_permlane16_swap(__float_as_uint(v), __float_as_uint(v), false, false);
    return __uint_as_float(q[0]) + __uint_as_float(q[1]);
}
template <int CTRL> __device__ __forceinline__ float dppf(float v) { return __uint_as_float((unsigned)__builtin_amdgcn_update_dpp(0, (int)__float_as_uint(v), CTRL, 0xF, 0xF, false)); }
__device__ __forceinline__ float wsum(float v) {
    v += dppf<0xB1>(v); v += dppf<0x4E>(v); v += dppf<0x141>(v); v += dppf<0x140>(v);
    return xsum3216(v);
}
__device__ __forceinline__ unsigned ror8(unsigned v) { return (unsigned)__builtin_amdgcn_update_dpp(0, (int)v, 0x128, 0xF, 0xF, false); }
__device__ __forceinline__ float ror8f(float v) { return __uint_as_float(ror8(__float_as_uint(v))); }
__device__ __forceinline__ unsigned nosent(unsigned v) {
    u16x2 lim = {0xFFFE, 0xFFFE};
    return __builtin_bit_cast(unsigned, __builtin_elementwise_min(__builtin_bit_cast(u16x2, v), lim));
}
__device__ __forceinline__ unsigned pkmax(unsigned a, unsigned b) {
    return __builtin_bit_cast(unsigned, __builtin_elementwise_max(__builtin_bit_cast(u16x2, a), __builtin_bit_cast(u16x2, b)));
}
__device__ __forceinline__ bool has_sent(u32x4 v) {
    unsigned m = pkmax(pkmax(v.x, v.y), pkmax(v.z, v.w));
    return (m & 0xFFFFu) == 0xFFFFu || (m >> 16) == 0xFFFFu;
}

__device__ __forceinline__ unsigned nosent1(unsigned v) { return min(v, 0xFFFFFFFEu); }
__device__ __forceinline__ unsigned smax(u32x4 v) { return max(v[1], v[3]); }
__device__ __forceinline__ u32x4 sent4() { unsigned x; asm volatile("v_mov_b32 %0, -1" : "=v"(x)); return (u32x4){x, x, x, x}; }
#define SENT sent4()
__device__ __forceinline__ f32x4 zero4() { f32x2 x, y; asm volatile("v_mov_b64 %0, 0\n\tv_mov_b64 %1, 0" : "=v"(x), "=v"(y)); return (f32x4){x[0], x[1], y[0], y[1]}; }
__device__ __forceinline__ u32x4 nosent4(u32x4 v) { return (u32x4){nosent1(v[0]), nosent1(v[1]), nosent1(v[2]), nosent1(v[3])}; }
#define SPIN_CAP 400000u
__device__ __forceinline__ void set_tmo(__amdgpu_buffer_rsrc_t rwin) { __builtin_amdgcn_raw_buffer_store_b32(0u, rwin, 0u, W_TMO, 16); }
__device__ __forceinline__ bool spin_fail(unsigned& spins, __amdgpu_buffer_rsrc_t rwin) {
    if (++spins > SPIN_CAP) { set_tmo(rwin); return true; }
    return (spins & 1023u) == 0u && __builtin_amdgcn_readfirstlane(__builtin_amdgcn_raw_buffer_load_b32(rwin, 0u, W_TMO, 16)) == 0u;
}
#define GATE 2
#define GATX 2
__device__ __forceinline__ void tell_at(__amdgpu_buffer_rsrc_t rwin, unsigned so, unsigned d) {
    __builtin_amdgcn_raw_buffer_store_b32(((unsigned)__builtin_amdgcn_s_memrealtime() + d) & 0xFFFFFFu, rwin, 0u, so, 16);
}
template <int N> __device__ __forceinline__ void wait_told(__amdgpu_buffer_rsrc_t rwin, const unsigned (&vo)[N], unsigned so, int slp) {
    unsigned v[N];
#pragma unroll
    for (int g = 0; g < N; ++g) v[g] = ~0u;
    for (unsigned spins = 0;;) {
        asm volatile("" ::: "memory");
        unsigned x[N], m = 0;
#pragma unroll
        for (int g = 0; g < N; ++g) x[g] = __builtin_amdgcn_raw_buffer_load_b32(rwin, v[g] == ~0u ? vo[g] : 0x40000000u, so, 16);
#pragma unroll
        for (int g = 0; g < N; ++g) { if (v[g] == ~0u) v[g] = x[g]; m = max(m, v[g]); }
        if (!__any(m == ~0u) || spin_fail(spins, rwin)) break;
        if (slp) __builtin_amdgcn_s_sleep(8); else __builtin_amdgcn_s_sleep(3);
    }
    for (int i = 0; i < 400; ++i) {
        const unsigned t = (unsigned)__builtin_amdgcn_s_memrealtime();
        bool e = false;
#pragma unroll
        for (int g = 0; g < N; ++g) e |= ((v[g] - t) << 8) - 1u < 0x7FFFFFFFu;
        if (!__any(e)) break;
    }
}
#define TG 250
#define TH 155
#define TH0 185
#define DVT0 1000
#define PS1 3
#define PS2 3
#define PS3 2

typedef f32x2 c2;
__device__ __forceinline__ c2 swp(c2 a) { return __builtin_shufflevector(a, a, 1, 0); }
__device__ __forceinline__ c2 fm(c2 a, c2 b, c2 c) { return __builtin_elementwise_fma(a, b, c); }
__device__ __forceinline__ c2 bc(float x) { return (c2){x, x}; }
__device__ __forceinline__ c2 sgn() { c2 s = {-1.f, 1.f}; asm("" : "+s"(s)); return s; }
template <int SG> __device__ __forceinline__ c2 addi(c2 a, c2 b) { return fm(swp(b), SG > 0 ? sgn() : swp(sgn()), a); }
template <int SG> __device__ __forceinline__ c2 cfin(c2 a, c2 w, c2 t) {
    c2 d;
    if (SG < 0) asm("v_pk_fma_f32 %0, %1, %2, %3 op_sel:[1,1,0] op_sel_hi:[0,1,1] neg_lo:[1,0,0]" : "=v"(d) : "v"(a), "v"(w), "v"(t));
    else asm("v_pk_fma_f32 %0, %1, %2, %3 op_sel:[1,1,0] op_sel_hi:[0,1,1] neg_hi:[1,0,0]" : "=v"(d) : "v"(a), "v"(w), "v"(t));
    return d;
}
struct Tw { f32x4 v[4]; };
template <int NI> __device__ __forceinline__ void ldtw(Tw& t, const Tw7<NI>& tab, int i) {
#pragma unroll
    for (int j = 0; j < 4; ++j) t.v[j] = *(const f32x4*)tab.v[j][i];
}
template <int NI> __device__ __forceinline__ void ldtw(Tw& t, const LAS f32x4* p) {
#pragma unroll
    for (int j = 0; j < 4; ++j) t.v[j] = p[j * NI];
}
template <int SG> __device__ __forceinline__ void twid(c2 (&u)[8], const Tw& t) {
#pragma unroll
    for (int r = 1; r < 8; ++r) {
        const f32x4 x = t.v[(r - 1) >> 1];
        const c2 w = r & 1 ? (c2){x[0], x[1]} : (c2){x[2], x[3]};
        u[r] = cfin<SG>(u[r], w, u[r] * bc(w.x));
    }
}
template <int SG> __device__ __forceinline__ void dft4(c2& a, c2& b, c2& c, c2& d) {
    const c2 t0 = a + c, t1 = a - c, t2 = b + d, t3 = b - d;
    a = t0 + t2; c = t0 - t2; b = addi<SG>(t1, t3); d = addi<-SG>(t1, t3);
}
template <int SG> __device__ __forceinline__ void dft8(c2 (&u)[8]) {
    c2 e0 = u[0], e1 = u[2], e2 = u[4], e3 = u[6], o0 = u[1], o1 = u[3], o2 = u[5], o3 = u[7];
    dft4<SG>(e0, e1, e2, e3); dft4<SG>(o0, o1, o2, o3);
    const c2 r = bc(0.70710678118654752440f);
    o1 = addi<SG>(o1, o1); o3 = addi<-SG>(o3, o3);
    u[0] = e0 + o0; u[4] = e0 - o0;
    u[1] = fm(o1, r, e1); u[5] = fm(o1, -r, e1);
    u[2] = addi<SG>(e2, o2); u[6] = addi<-SG>(e2, o2);
    u[3] = fm(o3, -r, e3); u[7] = fm(o3, r, e3);
}
template <int SG> __device__ __forceinline__ void dft5(c2& x0, c2& x1, c2& x2, c2& x3, c2& x4) {
    const c2 c1 = bc(0.30901699437494742410f), c2_ = bc(-0.80901699437494742410f), s1 = bc(0.95105651629515357212f), s2 = bc(0.58778525229247312917f);
    const c2 t1 = x1 + x4, t2 = x2 + x3, t3 = x1 - x4, t4 = x2 - x3;
    const c2 a1 = fm(c2_, t2, fm(c1, t1, x0)), a2 = fm(c1, t2, fm(c2_, t1, x0));
    const c2 b1 = fm(s2, t4, s1 * t3), b2 = fm(-s1, t4, s2 * t3);
    x0 = x0 + t1 + t2;
    x1 = addi<SG>(a1, b1); x4 = addi<-SG>(a1, b1); x2 = addi<SG>(a2, b2); x3 = addi<-SG>(a2, b2);
}
template <int SG> __device__ __forceinline__ void dft10(c2 (&u)[10]) {
    c2 e0 = u[0], e1 = u[2], e2 = u[4], e3 = u[6], e4 = u[8], o0 = u[5], o1 = u[7], o2 = u[9], o3 = u[1], o4 = u[3];
    dft5<SG>(e0, e1, e2, e3, e4); dft5<SG>(o0, o1, o2, o3, o4);
    u[0] = e0 + o0; u[5] = e0 - o0; u[6] = e1 + o1; u[1] = e1 - o1; u[2] = e2 + o2; u[7] = e2 - o2; u[8] = e3 + o3; u[3] = e3 - o3; u[4] = e4 + o4; u[9] = e4 - o4;
}
__device__ __forceinline__ void swap32(c2& v) { auto r = __builtin_amdgcn_permlane32_swap(__float_as_uint(v.x), __float_as_uint(v.y), false, false); v = (c2){__uint_as_float(r[0]), __uint_as_float(r[1])}; }
template <int B> __device__ __forceinline__ void swapl(c2& a, c2& b) {
#pragma unroll
    for (int i = 0; i < 2; ++i) {
        auto r = B == 5 ? __builtin_amdgcn_permlane32_swap(__float_as_uint(a[i]), __float_as_uint(b[i]), false, false) : __builtin_amdgcn_permlane16_swap(__float_as_uint(a[i]), __float_as_uint(b[i]), false, false);
        a[i] = __uint_as_float(r[0]); b[i] = __uint_as_float(r[1]);
    }
}
__device__ __forceinline__ c2 unpk(unsigned v) { return (c2){lo2f(v), hi2f(v)}; }
__device__ __forceinline__ float hsum32(c2 s) {
    float v = s.x + s.y;
    v += dppf<0xB1>(v); v += dppf<0x4E>(v); v += dppf<0x141>(v); v += dppf<0x140>(v);
    auto q = __builtin_amdgcn_permlane16_swap(__float_as_uint(v), __float_as_uint(v), false, false);
    return __uint_as_float(q[0]) + __uint_as_float(q[1]);
}
__device__ __forceinline__ void ln_row(const c2 (&v)[10], unsigned (&o)[10]) {
    const c2 s = ((v[0] + v[1]) + (v[2] + v[3])) + ((v[4] + v[5]) + (v[6] + v[7])) + (v[8] + v[9]);
    const float mean = hsum32(s) * (1.f / 640.f);
    c2 q0 = bc(0.f), q1 = bc(0.f);
#pragma unroll
    for (int r = 0; r < 10; r += 2) { const c2 d = v[r] - bc(mean), e = v[r + 1] - bc(mean); q0 = fm(d, d, q0); q1 = fm(e, e, q1); }
    const float mb = bfr(mean);
    c2 d[10];
#pragma unroll
    for (int r = 0; r < 10; ++r) { const c2 x = v[r] - bc(mb); d[r] = unpk(pack2(x.x, x.y)); }
    const float var = hsum32(q0 + q1) * (1.f / 640.f);
    const float rs = __builtin_amdgcn_rcpf(bfr(sqrtf(bfr(bfr(var) + 1e-5f))));
#pragma unroll
    for (int r = 0; r < 10; ++r) { const c2 x = d[r] * bc(rs); o[r] = pack2(x.x, x.y); }
}

__device__ __forceinline__ __amdgpu_buffer_rsrc_t mkrsrc(const void* p, unsigned bytes) {
    return __builtin_amdgcn_make_buffer_rsrc((void*)p, 0, bytes, 0x00020000);
}
__device__ __forceinline__ unsigned xcc_id() { unsigned x; asm volatile("s_getreg_b32 %0, hwreg(HW_REG_XCC_ID)" : "=s"(x)); return x & 15u; }
#define VMWAIT() do { asm volatile("" ::: "memory"); __builtin_amdgcn_s_waitcnt(0x0F70); asm volatile("" ::: "memory"); } while (0)
#define BAR() asm volatile("s_waitcnt lgkmcnt(0)\n\ts_barrier" ::: "memory")
#define BAR0() asm volatile("s_barrier" ::: "memory")

#define FST(k) do {} while (0)
#define FSW(k) do {} while (0)
extern __shared__ __attribute__((aligned(16))) char smem[];
#define RST(k, lay, wv, sel) do {} while (0)
constexpr int R_C = 8 * 10 * 72 * 8;
constexpr float FSC = 0.5f / ((float)S_ * (float)D_);
__device__ __forceinline__ c2 ldv(const LAS void* p) { return *(const volatile LAS c2*)p; }
struct RowTw { Tw a, b; };
__device__ __forceinline__ void row_tw(RowTw& t, int tid) { const int i = tid % 80; ldtw(t.a, TWR2, i); ldtw(t.b, TWR3, i); }
template <int NF, bool LATE> __device__ __forceinline__ void a1_unit(__amdgpu_buffer_rsrc_t rx, int row0, __amdgpu_buffer_rsrc_t rws, __amdgpu_buffer_rsrc_t rwin, RowTw& tw, int tid, unsigned (&fs)[12]) {
    const int wave = __builtin_amdgcn_readfirstlane(tid >> 6), lane = tid & 63;
    LAS c2* A = (LAS c2*)smem; LAS c2* C = (LAS c2*)(smem + R_C);
    FST(0);
    if (wave < NF) {
        unsigned d[10];
        for (unsigned spins = 0;;) {
            asm volatile("" ::: "memory");
#pragma unroll
            for (int r = 0; r < 10; ++r) d[r] = __builtin_amdgcn_raw_buffer_load_b32(rx, (lane & 31) * 4u + (lane >> 5) * (D_ * 2u) + r * 128u, (unsigned)(row0 + 2 * wave) * (D_ * 2), 16);
            if (!LATE) break;
            unsigned m = d[0];
#pragma unroll
            for (int r = 1; r < 10; ++r) m = max(m, d[r]);
            if (!__any(m == ~0u) || spin_fail(spins, rwin)) break;
            __builtin_amdgcn_s_sleep(PS1);
        }
        c2 u[10];
#pragma unroll
        for (int r = 0; r < 10; ++r) u[r] = unpk(d[r]);
        FST(1);
        if (LATE) RST(16, 0, wave, 0);
        ln_row(u, d);
        FST(2);
#pragma unroll
        for (int r = 0; r < 10; ++r) { u[r] = unpk(d[r]); swap32(u[r]); }
        dft10<-1>(u);
#pragma unroll
        for (int r = 0; r < 10; ++r) A[(wave * 10 + r) * 72 + (lane & 31) + 36 * (lane >> 5)] = u[r];
        FST(3);
    }
    BAR();
    if (NF == 4 && !LATE) row_tw(tw, tid);
    FST(4);
#pragma unroll
    for (int c = 0; c < (NF > 4 ? 2 : 1); ++c) {
        if (tid < (NF > 4 ? (c ? 160 : 480) : 80 * NF)) {
            const int K = (tid >> 3) + 60 * c, m = tid & 7;
            LAS c2* a = A + K * 72;
            c2 u[8];
#pragma unroll
            for (int q = 0; q < 8; ++q) u[q] = ldv(a + (m >> 1) + 36 * (m & 1) + 4 * q);
            twid<-1>(u, tw.a);
            dft8<-1>(u);
#pragma unroll
            for (int p = 0; p < 8; ++p) a[9 * p + m] = u[p];
            asm volatile("" ::: "memory");
#pragma unroll
            for (int q = 0; q < 8; ++q) u[q] = ldv(a + 9 * m + q);
            twid<-1>(u, tw.b);
            dft8<-1>(u);
            LAS c2* o = C + (K / 10) * 638 + K + 10 * m;
#pragma unroll
            for (int p = 0; p < 8; ++p) o[80 * p] = u[p];
        }
    }
    FST(5);
    BAR();
    FST(6);
#pragma unroll
    for (int c = 0; c < (NF > 4 ? 5 : 3); ++c) {
        const int it = tid + 512 * c, kp = it / (2 * NF), row = it % (2 * NF);
        if (NF > 4 || c < 2 || tid < 256) {
            const LAS c2* z = C + (row >> 1) * 648;
            const f32x4 zk = *(const LAS f32x4*)(z + 2 * kp);
            const c2 n0 = ldv(z + (kp ? 640 - 2 * kp : 320)), n1 = ldv(z + 639 - 2 * kp);
            const bool odd = it & 1;
            f32x4 o = odd ? (f32x4){zk[1] + n0.y, n0.x - zk[0], zk[3] + n1.y, n1.x - zk[2]} : (f32x4){zk[0] + n0.x, zk[1] - n0.y, zk[2] + n1.x, zk[3] - n1.y};
            if (c == 0 && kp == 0) { o[0] = 2.f * (odd ? zk[1] : zk[0]); o[1] = 2.f * (odd ? n0.y : n0.x); }
            u32x4 ob = __builtin_bit_cast(u32x4, o * FSC);
            if (LATE) { ob[1] = nosent1(ob[1]); ob[3] = nosent1(ob[3]); }
            st128(ob, rws, kp * (LATE ? GST : FST_) + row * 16u, (LATE ? O_FL - (S_ - 256) * 16u : O_F) + (unsigned)row0 * 16u, 16);
        }
    }
    FST(7);
}
template <class FG> __device__ __forceinline__ void a3_unit(__amdgpu_buffer_rsrc_t rx, int row0, int layer, __amdgpu_buffer_rsrc_t rws, __amdgpu_buffer_rsrc_t rwin, const RowTw& tw, int tid, FG go, unsigned (&fs)[12]) {
    const int wave = __builtin_amdgcn_readfirstlane(tid >> 6), lane = tid & 63;
    LAS c2* A = (LAS c2*)smem; LAS c2* C = (LAS c2*)(smem + R_C);
    const unsigned n0 = (lane & 7) | ((lane & 24) << 1);
    c2 x[10];
    if (wave < 4) {
        const unsigned base = (unsigned)(row0 + 2 * wave) * (D_ * 2), vo = n0 * 2u + (lane >> 5) * (D_ * 2u);
#pragma unroll
        for (int r = 0; r < 10; ++r) x[r] = (c2){bf2f(__builtin_amdgcn_raw_buffer_load_b16(rx, vo + 128u * r, base, 16)), bf2f(__builtin_amdgcn_raw_buffer_load_b16(rx, vo + 16u + 128u * r, base, 16))};
    }
    const bool early = row0 < 256;
    if (!early) go();
    else if (TG) {
        if (wave == 7) { const unsigned vo[3] = {lane * 128u, lane * 128u + 8192u, min(lane, 31) * 128u + 16384u}; wait_told(rwin, vo, W_COL + (unsigned)layer * W_LAYER + 4u, 1); }
        BAR0();
    }
    FST(0);
    u32x4 fv[3];
    {
        const unsigned vo = (tid >> 3) * (early ? GST : FST_) + (tid & 7) * 16u, st = (early ? GST : FST_) * 64u, so = (early ? O_FL + (unsigned)(2 - layer) * (NCOL * GST) : O_F) + (unsigned)row0 * 16u;
#pragma unroll
        for (int c = 0; c < 3; ++c) fv[c] = __builtin_amdgcn_raw_buffer_load_b128(rws, vo, c < 2 || wave < 4 ? so + c * st : 0x40000000u, 16);
        if (early) for (unsigned spins = 0;;) {
            if (!__any(max(max(smax(fv[0]), smax(fv[1])), smax(fv[2])) == ~0u) || spin_fail(spins, rwin)) break;
            __builtin_amdgcn_s_sleep(PS3);
            asm volatile("" ::: "memory");
            u32x4 x[3];
#pragma unroll
            for (int c = 0; c < 3; ++c) x[c] = __builtin_amdgcn_raw_buffer_load_b128(rws, smax(fv[c]) == ~0u ? vo : 0x40000000u, so + c * st, 16);
#pragma unroll
            for (int c = 0; c < 3; ++c) if (smax(fv[c]) == ~0u) fv[c] = x[c];
        }
    }
#pragma unroll
    for (int c = 0; c < 3; ++c) {
        const int it = tid + 512 * c, kp = it >> 3;
        if (c < 2 || tid < 256) {
            const bool odd = it & 1;
            const f32x4 v = __builtin_bit_cast(f32x4, fv[c]), p = {dppf<0xB1>(v[0]), dppf<0xB1>(v[1]), dppf<0xB1>(v[2]), dppf<0xB1>(v[3])};
            c2 z0 = odd ? (c2){p[0] + v[1], v[0] - p[1]} : (c2){v[0] - p[1], p[0] + v[1]};
            const c2 z1 = odd ? (c2){p[2] + v[3], v[2] - p[3]} : (c2){v[2] - p[3], p[2] + v[3]};
            int i0 = odd ? 640 - 2 * kp : 2 * kp;
            if (c == 0 && kp == 0) { z0 = odd ? (c2){p[1], v[1]} : (c2){v[0], p[0]}; i0 >>= 1; }
            LAS c2* z = C + ((it >> 1) & 3) * 648;
            z[i0] = z0; z[odd ? 639 - 2 * kp : 2 * kp + 1] = z1;
        }
    }
    FST(1);
    BAR();
    if (early && TH && wave == 7) tell_at(rwin, W_H + (unsigned)layer * W_LAYER + (unsigned)((row0 >> 5) * 128 + 16 + ((row0 >> 3) & 3) * 4), layer ? TH : TH0);
    if (early) RST(3, layer, wave, 0);
    FST(2);
    if (tid < 320) {
        const int K = tid >> 3, m = tid & 7;
        LAS c2* a = A + K * 72;
        const LAS c2* o = C + (K / 10) * 638 + K + 10 * m;
        c2 u[8];
#pragma unroll
        for (int p = 0; p < 8; ++p) u[p] = ldv(o + 80 * p);
        dft8<1>(u);
        twid<1>(u, tw.b);
#pragma unroll
        for (int q = 0; q < 8; ++q) a[9 * q + m] = u[q];
        asm volatile("" ::: "memory");
#pragma unroll
        for (int p = 0; p < 8; ++p) u[p] = ldv(a + 9 * m + p);
        dft8<1>(u);
        twid<1>(u, tw.a);
#pragma unroll
        for (int q = 0; q < 8; ++q) a[m + 8 * (q >> 1) + 32 * (q & 1)] = u[q];
    }
    FST(3);
    BAR();
    FST(4);
    if (wave < 4) {
        c2 u[10];
#pragma unroll
        for (int r = 0; r < 10; ++r) u[r] = ldv(A + (wave * 10 + r) * 72 + lane);
        dft10<1>(u);
        unsigned x1[10], h[10];
#pragma unroll
        for (int r = 0; r < 10; ++r) {
            swap32(u[r]);
            const c2 y = x[r] + unpk(pack2(u[r].x, u[r].y));
            x1[r] = pack2(y.x, y.y);
            u[r] = unpk(x1[r]);
        }
        FST(5);
        ln_row(u, h);
        FST(6);
        LAS char* st = (LAS char*)C + wave * 5120;
        LAS char* hp = st + (lane >> 5) * 1280 + (n0 & 48) * 2 + pi16(n0 & 15) * 2;
#pragma unroll
        for (int r = 0; r < 10; ++r) *(LAS unsigned*)(hp + 128 * r) = nosent1(h[r]);
#pragma unroll
        for (int c = 0; c < 3; ++c) {
            if (c < 2 || lane < 32) {
                const int id = c * 64 + lane, rr = id >= 80, ch = id - 80 * rr, row = (row0 + 2 * wave + rr) & 31;
                st128(*(const LAS u32x4*)(st + id * 16), rws, (unsigned)((ch >> 3) * 4096 + row * 128 + (((ch & 7) ^ (row & 7)) << 4)), O_H + (unsigned)((row0 >> 5) - NHF * layer) * 40960u, 16);
            }
        }
        if (early) RST(4, layer, wave, 0);
        LAS char* xp = st + 2560 + (lane >> 5) * 1280 + n0 * 2;
#pragma unroll
        for (int r = 0; r < 10; ++r) { *(LAS u16*)(xp + 128 * r) = (u16)x1[r]; *(LAS u16*)(xp + 128 * r + 16) = (u16)(x1[r] >> 16); }
#pragma unroll
        for (int c = 0; c < 3; ++c)
            if (c < 2 || lane < 32) st128(*(const LAS u32x4*)(st + 2560 + (c * 64 + lane) * 16), rws, (c * 64 + lane) * 16u, O_X1 + (unsigned)(row0 + 2 * wave) * (D_ * 2), 16);
        FST(7);
    }
}

constexpr int C_T = 8 * 544 * 8, C_B = C_T + (int)sizeof(TwCol), C_W = C_B + 32768, C_END = C_W + 8 * 516 * 4;
template <class FG> __device__ __forceinline__ void a2_unit(int kp, int late, __amdgpu_buffer_rsrc_t rws, __amdgpu_buffer_rsrc_t rwin, int tid, FG go, unsigned (&fs)[12]) {
    const int w = __builtin_amdgcn_readfirstlane(tid >> 6), l = tid & 63;
    const unsigned fb = O_F + (unsigned)kp * FST_, wb = O_WFT + (unsigned)kp * WST;
    const int b = (l >> 1) & 7;
    const int k0 = w + 8 * b + 128 * (l >> 4);
    c2 u[8];
    u32x4 wv[2];
    {
        f32x4 tv[3];
#pragma unroll
        for (int i = 0; i < 3; ++i) tv[i] = ((const f32x4*)&TWC)[min(tid + 512 * i, (int)sizeof(TwCol) / 16 - 1)];
        go(0);
#pragma unroll
        for (int i = 0; i < 3; ++i) ((LAS f32x4*)(smem + C_T))[min(tid + 512 * i, (int)sizeof(TwCol) / 16 - 1)] = tv[i];
        BAR();
        FST(0);
        u32x4 x[4];
#pragma unroll
        for (int r = 0; r < 4; ++r) x[r] = __builtin_amdgcn_raw_buffer_load_b128(rws, (tid >> 1) * 16u + (tid & 1) * 4096u, fb + r * 8192u, 16);
#pragma unroll
        for (int r = 0; r < 2; ++r) wv[r] = __builtin_amdgcn_raw_buffer_load_b128(rws, tid * 16u, wb + r * 8192u, 16);
#pragma unroll
        for (int r = 0; r < 4; ++r) {
            const f32x4 y = __builtin_bit_cast(f32x4, x[r]);
            const c2 z = {dppf<0xB1>(l & 1 ? y[0] : y[2]), dppf<0xB1>(l & 1 ? y[1] : y[3])};
            u[2 * r] = l & 1 ? z : (c2){y[0], y[1]};
            u[2 * r + 1] = l & 1 ? (c2){y[2], y[3]} : z;
        }
    }
    const LAS f32x4* t1 = (const LAS f32x4*)(smem + C_T) + (tid >> 1);
    const LAS f32x4* t2 = (const LAS f32x4*)(smem + C_T + sizeof(Tw7<256>)) + (l >> 1);
    const LAS f32x4* t3 = (const LAS f32x4*)(smem + C_T + sizeof(Tw7<256>) + sizeof(Tw7<32>)) + (l >> 4);
    LAS c2* e0 = (LAS c2*)smem + tid;
    LAS c2* e1 = (LAS c2*)smem + w * 544 + l;
    LAS c2* e2 = (LAS c2*)smem + w * 544 + 2 * (l >> 4) + (l & 1);
    Tw tw;
    ldtw<256>(tw, t1);
    if (late) {
        go(2);
        FST(1);
        for (unsigned spins = 0;;) {
            asm volatile("" ::: "memory");
            const u32x2 x = __builtin_amdgcn_raw_buffer_load_b64(rws, tid * 8u, O_FL + (unsigned)kp * GST, 16);
            u[7] = __builtin_bit_cast(c2, x);
            if (!__any(x[1] == ~0u) || spin_fail(spins, rwin)) break;
            if (w < 6) __builtin_amdgcn_s_sleep(24); else __builtin_amdgcn_s_sleep(PS2);
        }
    }
    FST(2);
    dft8<-1>(u);
    twid<-1>(u, tw);
    if (kp == 0) {
#pragma unroll
        for (int i = 0; i < 4; ++i)
            asm volatile("s_mov_b32 m0, %3\n\ts_nop 3\n\tbuffer_load_dwordx4 %0, %1, %2 offen sc1 lds" :: "v"(l * 16u), "s"(rws), "s"(O_WFT + NCOL * WST + (w * 4 + i) * 1024u), "s"(C_B + (w * 4 + i) * 1024u), "v"(u[7]) : "m0", "memory");
    }
#pragma unroll
    for (int r = 0; r < 8; ++r) e0[544 * r] = u[r];
    ldtw<32>(tw, t2);
    BAR();
    if (TG && w == 0) tell_at(rwin, W_COL + (unsigned)late * W_LAYER + (unsigned)kp * 128u + 4u, kp ? TG : TG + 30);
#pragma unroll
    for (int r = 0; r < 8; ++r) u[r] = ldv(e1 + 64 * r);
    dft8<-1>(u);
    twid<-1>(u, tw);
#pragma unroll
    for (int r = 0; r < 8; ++r) e1[68 * r] = u[r];
    asm volatile("" ::: "memory");
#pragma unroll
    for (int r = 0; r < 8; ++r) u[r] = ldv(e2 + 68 * b + 8 * r);
    ldtw<4>(tw, t3);
    dft8<-1>(u);
    twid<-1>(u, tw);
    c2 f[8];
#pragma unroll
    for (int r = 0; r < 8; ++r) f[r] = unpk(wv[r >> 2][r & 3]);
    asm volatile("s_nop 1" : "+v"(u[1]), "+v"(u[2]), "+v"(u[3]), "+v"(u[4]), "+v"(u[5]), "+v"(u[6]), "+v"(u[7]));
#pragma unroll
    for (int r = 0; r < 4; ++r) swapl<5>(u[r], u[r + 4]);
#pragma unroll
    for (int r = 0; r < 4; ++r) swapl<4>(u[(r & 1) + 2 * (r & 2)], u[(r & 1) + 2 * (r & 2) + 2]);
    dft4<-1>(u[0], u[2], u[4], u[6]); dft4<-1>(u[1], u[3], u[5], u[7]);
    FST(3);
    if (kp == 0) {
        LAS c2* z = (LAS c2*)(smem + C_W);
#pragma unroll
        for (int r = 0; r < 8; ++r) if (!(l & 1)) z[w * 258 + b + 16 * (l >> 4) + 8 * (r & 1) + 64 * (r >> 1)] = u[r];
        VMWAIT();
        BAR();
#pragma unroll
        for (int r = 0; r < 8; ++r) {
            const int kn = (S_ - k0 - 64 * (r & 1) - 512 * (r >> 1)) & (S_ - 1);
            const f32x4 ab = ((const LAS f32x4*)(smem + C_B))[256 * r + (tid >> 1)];
            const c2 zn = z[(kn & 7) * 258 + (kn >> 3)], g = {ab[2], ab[3]}, h = l & 1 ? f[r] : (c2){ab[0], ab[1]};
            const c2 y = cfin<1>(g, zn, g * bc(zn.x));
            u[r] = cfin<-1>(u[r], h, fm(u[r], bc(h.x), l & 1 ? bc(0.f) : y));
        }
    } else {
#pragma unroll
        for (int r = 0; r < 8; ++r) u[r] = cfin<-1>(u[r], f[r], u[r] * bc(f[r].x));
    }
    FST(4);
    dft4<1>(u[0], u[2], u[4], u[6]); dft4<1>(u[1], u[3], u[5], u[7]);
#pragma unroll
    for (int r = 0; r < 4; ++r) swapl<4>(u[(r & 1) + 2 * (r & 2)], u[(r & 1) + 2 * (r & 2) + 2]);
#pragma unroll
    for (int r = 0; r < 4; ++r) swapl<5>(u[r], u[r + 4]);
    twid<1>(u, tw);
    dft8<1>(u);
#pragma unroll
    for (int r = 0; r < 8; ++r) e2[66 * b + 8 * r] = u[r];
    asm volatile("" ::: "memory");
    ldtw<32>(tw, t2);
#pragma unroll
    for (int r = 0; r < 8; ++r) u[r] = ldv(e1 + 66 * r);
    twid<1>(u, tw);
    dft8<1>(u);
#pragma unroll
    for (int r = 0; r < 8; ++r) e1[64 * r] = u[r];
    ldtw<256>(tw, t1);
    BAR();
#pragma unroll
    for (int r = 0; r < 8; ++r) u[r] = ldv(e0 + 544 * r);
    twid<1>(u, tw);
    dft8<1>(u);
    FST(5);
    __builtin_amdgcn_raw_buffer_store_b64((u32x2){__float_as_uint(u[0].x), nosent1(__float_as_uint(u[0].y))}, rws, tid * 8u, O_FL + (unsigned)((2 - late) * NCOL + kp) * GST, 16);
#pragma unroll
    for (int r = 1; r < 8; ++r) __builtin_amdgcn_raw_buffer_store_b64(__builtin_bit_cast(u32x2, u[r]), rws, tid * 8u, fb + r * 4096u, 16);
    FST(6);
}

struct Args { char* ws; char* win; const u16 *x, *wr, *wi, *mem0, *wq, *wk, *wv, *wo; u16* out; };
__device__ __forceinline__ const u16* arg(int i) {
    const __attribute__((address_space(4))) u64* p = (const __attribute__((address_space(4))) u64*)__builtin_amdgcn_kernarg_segment_ptr();
    asm volatile("" : "+s"(p));
    return (const u16*)p[i];
}
enum { A_X = 2, A_WR, A_WI, A_MEM0, A_WQ, A_WK, A_WV, A_WO, A_OUT };
#define MXV 0x5556ull
#define MXQ 0x1554ull
#define NTAIL 7
static_assert(NTAIL >= 3, "the owners release segments up to NSEG - 4");
constexpr int TAILS = NSEG - NTAIL;
static_assert((MXV & 31) == 22 && (MXQ & 31) == 20, "segment sets");
constexpr int NREC = 96 + NOWN, NQT = NREC + NOWN, NXT = NQT + 32, NWRK = NXT + 32;
#define SST(k) do {} while (0)
#define LSEL 0
#define RT(k) do {} while (0)
#define RTS() do {} while (0)
#define B3ATTR
#define MTA(k)
#define MTRT() do {} while (0)
#define MT(k) do {} while (0)
#define MTS() do {} while (0)
#define FC(x) do {} while (0)
#define DQC 1200
#define DVC 1200
#define M1 44
#define M2 226
#define PEN 280
#define DZP 55
#define LT0 342
#define SAD 1
#define PEN1 230
#define PEN0 330
#define TSLP 1
#define OPR 2
#define BSPARSE 0
#define ZLAG 3
#define TKP 0
#define DPS 12
#define DXT 3000
#define LTM 12
#define XS0 10
#define XSU 8
#define XSM 32
#define LTU 4
#define LTS 3
#define XSS 3
#define TKS 3
#define LT1 450
#define FDL 2
#define ZLA 3
#define NORND 0
#define LTX 40
#define TKE 200
#define DVO 4000
#define ZPS 2
#define HLA 8
#define QB2 2
#define NB2 6
#define DZR 24
#define ML 150
#define NB1 3
#define DPT 3
#define DDMA 8
#define DZ2 800
#define QEARLY 2
#define OSLP 32
#define OSLP2 1
#define WSLP 64
#define DO0 4000
#define DW0 1200
#define DQT 2000
#define ZEARLY 4
#define HSLP 4
#define ASLP 4
#define CSLP 4
#define A1S 0
#define OSLP3 12
#define DVT 860
#define VSL 0
#define WARM 1
constexpr unsigned CODE_BYTES = 96000;
#define DTK 4
#define OSL4 10
#define OAUX 0
#define TSL0 6
#define SE 6
#define XE 20
#define PENS 20
#define CF 190
#define HSL2 16
#define DH1 10
#define DZ0 400
#define FZP 1
__device__ __forceinline__ unsigned fz_at(unsigned h, unsigned g) { return FZP ? g * 1024u + (h >> 2) * 128u + (h & 3u) * 4u : h * 128u + g * 4u; }
#define LKE 0
#define DW4 6
#define DBIG 580
#define PENH 280
#define ROT 0
#define VREV 0
#define DGV 0
#define VOFF0 "0"
#define VOFF1 "1024"
#define VOFF2 "2048"
#define VOFF3 "3072"
#define VOFF4 "4096"
#define VOFF5 "5120"
#define VOFF6 "6144"
#define VOFF7 "7168"
#define VOFF8 "8192"
#define VOFF9 "9216"
#define STK 1
#define STX 0
#define UFQ 14
#define DQ0 36
#define QSL 6
#define OST0 40
#define NPR 3
#define DIE 0
#define ALN ".p2align 6\n\t"
#define SB() __builtin_amdgcn_sched_barrier(0)
#define PIN1(a) asm volatile("" : "+v"(a) :: "memory")
#define PIN2(a, b) asm volatile("" : "+v"(a), "+v"(b) :: "memory")
__device__ __forceinline__ unsigned fresh_lane() { unsigned l; asm volatile("v_mbcnt_lo_u32_b32 %0, -1, 0\n\tv_mbcnt_hi_u32_b32 %0, -1, %0" : "=v"(l)); return l; }
#define LANE0 (min(fresh_lane(), 1u) << 30)
__device__ __forceinline__ void wait_until(unsigned long long t) { while ((long long)(__builtin_amdgcn_s_memtime() - t) < 0) __builtin_amdgcn_s_sleep(1); }
struct Clk { unsigned ta, ma, cpq, qpc; };
__device__ __forceinline__ int sx24(unsigned v) { return (int)(v << 8) >> 8; }
#define WAIT_FINE ALN \
        "s_memrealtime vcc\n\t" \
        "s_memtime s[100:101]\n\t" \
        "s_brev_b32 %[fi], -2\n\t" \
        "s_waitcnt lgkmcnt(0)\n\t" \
        "s_mov_b32 %[rp], vcc_lo\n\t" \
        "s_lshl_b32 vcc_hi, vcc_lo, 2\n\t" \
        "s_sub_u32 %[x], %[t], vcc_hi\n\t" \
        "s_bfe_i32 %[x], %[x], 0x180000\n\t" \
        "s_min_i32 %[x], %[x], 4000\n\t" \
        "s_max_i32 %[x], %[x], -4000\n\t" \
        "s_add_u32 %[t], vcc_hi, %[x]\n\t" \
        "s_mul_i32 %[x], %[x], %[cq]\n\t" \
        "s_ashr_i32 %[x], %[x], 8\n\t" \
        "s_sub_u32 %[tm], %[t], %[ta]\n\t" \
        "s_bfe_i32 %[tm], %[tm], 0x180000\n\t" \
        "s_min_i32 %[tm], %[tm], 1000000\n\t" \
        "s_max_i32 %[tm], %[tm], -1000000\n\t" \
        "s_mul_i32 %[tm], %[tm], %[cq]\n\t" \
        "s_ashr_i32 %[tm], %[tm], 8\n\t" \
        "s_add_u32 %[tm], %[tm], %[ma]\n\t" \
        "s_sub_u32 %[tm], %[tm], s100\n\t" \
        "s_lshr_b32 vcc_hi, %[cq], 4\n\t" \
        "s_sub_u32 vcc_hi, %[x], vcc_hi\n\t" \
        "s_max_i32 %[tm], %[tm], vcc_hi\n\t" \
        "s_min_i32 %[tm], %[tm], %[x]\n\t" \
        "s_add_u32 %[tm], %[tm], s100\n" \
        "1:\n\t" \
        "s_memrealtime vcc\n\t" \
        "s_memtime s[100:101]\n\t" \
        "s_waitcnt lgkmcnt(0)\n\t" \
        "s_cmp_eq_u32 vcc_lo, %[rp]\n\t" \
        "s_cbranch_scc1 2f\n\t" \
        "s_mov_b32 %[rp], vcc_lo\n\t" \
        "s_lshl_b32 %[x], vcc_lo, 2\n\t" \
        "s_sub_u32 %[x], %[t], %[x]\n\t" \
        "s_bfe_i32 %[x], %[x], 0x180000\n\t" \
        "s_mul_i32 %[x], %[x], %[cq]\n\t" \
        "s_ashr_i32 %[x], %[x], 8\n\t" \
        "s_add_u32 %[x], %[x], s100\n\t" \
        "s_sub_u32 s101, %[x], %[tm]\n\t" \
        "s_cmp_lt_i32 s101, %[fi]\n\t" \
        "s_cselect_b32 %[tm], %[x], %[tm]\n\t" \
        "s_mov_b32 %[fi], 0\n" \
        "2:\n\t" \
        "s_sub_u32 %[x], %[tm], s100\n\t" \
        "s_cmp_gt_i32 %[x], 260\n\t" \
        "s_cbranch_scc1 1b\n\t" \
        "s_cmp_eq_u32 %[fi], 0\n\t" \
        "s_cselect_b32 %[ta], %[t], %[ta]\n\t" \
        "s_cselect_b32 %[ma], %[tm], %[ma]\n" \
        "3:\n\t" \
        "s_memtime s[100:101]\n\t" \
        "s_waitcnt lgkmcnt(0)\n\t" \
        "s_sub_u32 %[x], %[tm], s100\n\t" \
        "s_cmp_gt_i32 %[x], 92\n\t" \
        "s_cbranch_scc1 3b\n\t" \
        "s_max_i32 %[x], %[x], 0\n\t" \
        "s_andn2_b32 %[x], %[x], 3\n\t" \
        "s_sub_u32 %[x], 104, %[x]\n\t" \
        "s_getpc_b64 vcc\n\t" \
        "s_add_u32 vcc_lo, vcc_lo, %[x]\n\t" \
        "s_addc_u32 vcc_hi, vcc_hi, 0\n\t" \
        "s_setpc_b64 vcc\n\t" \
        ".rept 23\n\t" \
        "s_nop 0\n\t" \
        ".endr\n\t"
#define WAIT_FINE_OUT [tm] "=&s"(tm), [rp] "=&s"(rp), [x] "=&s"(x), [fi] "=&s"(fi), [t] "+s"(t), [ta] "+s"(c.ta), [ma] "+s"(c.ma)
__device__ __forceinline__ void wait_fine(unsigned t, Clk& c) {
    unsigned tm, rp, x, fi;
    asm volatile(WAIT_FINE : WAIT_FINE_OUT : [cq] "s"(c.cpq) : "vcc", "scc", "s100", "s101", "memory");
}
__device__ __forceinline__ void wait_fine_bar(unsigned t, Clk& c) {
    unsigned tm, rp, x, fi;
    asm volatile(WAIT_FINE "s_barrier" : WAIT_FINE_OUT : [cq] "s"(c.cpq) : "vcc", "scc", "s100", "s101", "memory");
}
__device__ __forceinline__ void wait_cyc_bar(unsigned tm) {
    unsigned x;
    asm volatile(
        ALN
        "3:\n\t"
        "s_memtime s[100:101]\n\t"
        "s_waitcnt lgkmcnt(0)\n\t"
        "s_sub_u32 %0, %1, s100\n\t"
        "s_cmp_gt_i32 %0, 92\n\t"
        "s_cbranch_scc1 3b\n\t"
        "s_max_i32 %0, %0, 0\n\t"
        "s_andn2_b32 %0, %0, 3\n\t"
        "s_sub_u32 %0, 104, %0\n\t"
        "s_getpc_b64 vcc\n\t"
        "s_add_u32 vcc_lo, vcc_lo, %0\n\t"
        "s_addc_u32 vcc_hi, vcc_hi, 0\n\t"
        "s_setpc_b64 vcc\n\t"
        ".rept 23\n\t"
        "s_nop 0\n\t"
        ".endr\n\t"
        "s_barrier"
        : "=&s"(x) : "s"(tm) : "vcc", "scc", "s100", "s101", "memory");
}
__device__ __forceinline__ unsigned cyc_at(const Clk& c, int d) {
    d = min(d, 20000);
    asm volatile("" : "+s"(d));
    return c.ma + (unsigned)((max(d, -4000) * (int)c.cpq) >> 8);
}
__device__ __forceinline__ void clk_rate(Clk& c, unsigned ta0, unsigned ma0) {
    const int dq = sx24(c.ta - ta0);
    if (dq > 200) {
        const float r = fminf(fmaxf((float)(c.ma - ma0) * __builtin_amdgcn_rcpf((float)dq), 1.f), 8.f);
        asm volatile("s_nop 1\n\tv_readfirstlane_b32 %0, %2\n\tv_readfirstlane_b32 %1, %3\n\ts_nop 3" : "=s"(c.cpq), "=s"(c.qpc)
                     : "v"((unsigned)(256.f * r + 0.5f)), "v"((unsigned)(65536.f * __builtin_amdgcn_rcpf(r))));
    }
}
__device__ __forceinline__ void clk_init(Clk& c) {
    c.ta = 4u * (unsigned)__builtin_amdgcn_s_memrealtime(); c.ma = (unsigned)__builtin_amdgcn_s_memtime();
    for (int i = 0; i < 2; ++i) {
        const unsigned ta0 = c.ta, ma0 = c.ma;
        wait_fine(c.ta + (i ? 360u : 60u), c);
        clk_rate(c, ta0, ma0);
    }
}
__device__ __forceinline__ unsigned sched_lane(int s) { const unsigned l = fresh_lane(); return (l & 31u) * 128u + (l >> 5) * 4u; }
__device__ __forceinline__ int read_sched(__amdgpu_buffer_rsrc_t rwin, int s, unsigned vo, unsigned to, unsigned f, unsigned g0, int& lt, int& xs, unsigned& fc) {
    int d = (int)((f - g0) & 0xFFFFFFu);
    if (__builtin_expect(__any((f >> 24) != (unsigned)s) || s == 0, 0)) {
        for (unsigned spins = 0; __any((f >> 24) != (unsigned)s);) {
            FC(fc);
            if (spin_fail(spins, rwin)) break;
            if (s == 0) __builtin_amdgcn_s_sleep(TSL0); else __builtin_amdgcn_s_sleep(TSLP);
            asm volatile("" ::: "memory");
            if ((f >> 24) != (unsigned)s) f = __builtin_amdgcn_raw_buffer_load_b32(rwin, vo, to, 16);
        }
        const unsigned f0 = __builtin_amdgcn_readfirstlane(f) - 0x100000u;
        d = (int)((f - f0) & 0xFFFFFFu) + (int)((f0 - g0) & 0xFFFFFFu);
    }
    d = max(d, __builtin_amdgcn_update_dpp(0, d, 0xB1, 0xF, 0xF, true));
    d = max(d, __builtin_amdgcn_update_dpp(0, d, 0x4E, 0xF, 0xF, true));
    d = max(d, __builtin_amdgcn_update_dpp(0, d, 0x141, 0xF, 0xF, true));
    d = max(d, __builtin_amdgcn_update_dpp(0, d, 0x140, 0xF, 0xF, true));
    d = max(d, __builtin_amdgcn_update_dpp(0, d, 0x142, 0xA, 0xF, true));
    d = max(__builtin_amdgcn_readlane(d, 16), __builtin_amdgcn_readlane(d, 48));
    const int l0 = lt, x0 = xs;
    d -= 0x400000;
    const bool miss = d > l0 + min(x0 + LTM, LTX);
    if (s >= SAD) { lt = miss ? l0 : min(max(d, l0 - 1 - ((l0 - d) >> LTS)), l0 + LTU); xs = miss ? min(x0 + XSU, XSM) : max(x0 - 1 - (x0 >> XSS), 0); }
    if (s == 1) lt = LT0;
    if (s < SE) xs = max(xs, XE);
    return 0x400000 + (s < SAD ? d + (s ? PENS : PEN0) : miss ? d + (d < DBIG ? PENH : PEN) : l0 + x0) + M1 + M2 + x0;
}
constexpr int QS = 272;
constexpr int TQS = 1152;
constexpr int L_Q = 10240, L_K = L_Q + 2 * TQS, L_S = L_K + 2 * TQS, L_TOK = L_S + 256, L_H = L_TOK + 49152, L_END = L_H + 2 * 40960;
static_assert(C_END <= L_END && L_END == 146176, "LDS");
typedef __attribute__((ext_vector_type(4))) short v4i16_t;
__device__ __forceinline__ u32x2 ldtr(const char* p) { return __builtin_bit_cast(u32x2, __builtin_amdgcn_ds_read_tr16_b64_v4i16((LAS v4i16_t*)p)); }
__device__ __forceinline__ u32x2 ldtr1(const LAS char* p) { return __builtin_bit_cast(u32x2, __builtin_amdgcn_ds_read_tr16_b64_v4i16((LAS v4i16_t*)p)); }
__device__ __forceinline__ u32x4 ldtr2(const LAS char* p0, const LAS char* p1) {
    const u32x2 x = __builtin_bit_cast(u32x2, __builtin_amdgcn_ds_read_tr16_b64_v4i16((LAS v4i16_t*)p0)), y = __builtin_bit_cast(u32x2, __builtin_amdgcn_ds_read_tr16_b64_v4i16((LAS v4i16_t*)p1));
    return (u32x4){x[0], x[1], y[0], y[1]};
}
__device__ __forceinline__ u32x4 ldtr2(const char* p0, const char* p1) { const u32x2 x = ldtr(p0), y = ldtr(p1); return (u32x4){x[0], x[1], y[0], y[1]}; }

__device__ __forceinline__ unsigned poll_lim(int& budget) {
    --budget;
    unsigned lim = budget > 0 ? 0xFFFFFFFEu : 0xFFFFFFFFu;
    asm volatile("" : "+s"(lim));
    return lim;
}
#define GATHER \
        "buffer_load_dwordx4 %[vo], %[rw], %[s0] offen sc1 lds\n\t" \
        "buffer_load_dwordx4 %[vo], %[rw], %[s0] offen offset:1024 sc1 lds\n\t" \
        "buffer_load_dwordx4 %[vo], %[rw], %[s0] offen offset:2048 sc1 lds\n\t" \
        "buffer_load_dwordx4 %[vo], %[rw], %[s0] offen offset:3072 sc1 lds\n\t" \
        "s_movk_i32 m0, 0x1000\n\t" \
        "s_nop 0\n\t" \
        "buffer_load_dwordx4 %[vo], %[rw], %[s4] offen sc1 lds\n\t" \
        "buffer_load_dwordx4 %[vo], %[rw], %[s4] offen offset:1024 sc1 lds\n\t" \
        "buffer_load_dwordx4 %[vo], %[rw], %[s4] offen offset:2048 sc1 lds\n\t" \
        "buffer_load_dwordx4 %[vo], %[rw], %[s4] offen offset:3072 sc1 lds\n\t" \
        "s_movk_i32 m0, 0x2000\n\t" \
        "s_nop 0\n\t" \
        "buffer_load_dwordx4 %[vo], %[rw], %[s1] offen sc1 lds\n\t" \
        "buffer_load_dwordx4 %[vo], %[rw], %[s1] offen offset:1024 sc1 lds"
#define GATHER_R \
        "buffer_load_dwordx4 %[vo], %[rw], %[s1] offen offset:1024 sc1 lds\n\t" \
        "buffer_load_dwordx4 %[vo], %[rw], %[s1] offen sc1 lds\n\t" \
        "s_movk_i32 m0, 0x1000\n\t" \
        "s_nop 0\n\t" \
        "buffer_load_dwordx4 %[vo], %[rw], %[s4] offen offset:3072 sc1 lds\n\t" \
        "buffer_load_dwordx4 %[vo], %[rw], %[s4] offen offset:2048 sc1 lds\n\t" \
        "buffer_load_dwordx4 %[vo], %[rw], %[s4] offen offset:1024 sc1 lds\n\t" \
        "buffer_load_dwordx4 %[vo], %[rw], %[s4] offen sc1 lds\n\t" \
        "s_movk_i32 m0, 0\n\t" \
        "s_nop 0\n\t" \
        "buffer_load_dwordx4 %[vo], %[rw], %[s0] offen offset:3072 sc1 lds\n\t" \
        "buffer_load_dwordx4 %[vo], %[rw], %[s0] offen offset:2048 sc1 lds\n\t" \
        "buffer_load_dwordx4 %[vo], %[rw], %[s0] offen offset:1024 sc1 lds\n\t" \
        "buffer_load_dwordx4 %[vo], %[rw], %[s0] offen sc1 lds"
#define GATHER_IN [vo] "v"(vo), [rw] "s"(rwin), [s0] "s"(so0), [s4] "s"(so0 + 2u * PAIR), [s1] "s"(so0 + 4u * PAIR)
__device__ __forceinline__ void gather_dma(__amdgpu_buffer_rsrc_t rwin, unsigned vo, unsigned so0, bool rev) {
    if (rev) asm volatile("s_movk_i32 m0, 0x2000\n\ts_nop 0\n\t" GATHER_R :: GATHER_IN : "m0", "memory");
    else asm volatile("s_mov_b32 m0, 0\n\ts_nop 0\n\t" GATHER :: GATHER_IN : "m0", "memory");
}
__device__ __forceinline__ void gather_at(unsigned t, Clk& c, __amdgpu_buffer_rsrc_t rwin, unsigned vo, unsigned so0, bool rev) {
    unsigned tm, rp, x, fi;
    if (rev) asm volatile("s_movk_i32 m0, 0x2000\n\t" WAIT_FINE GATHER_R : WAIT_FINE_OUT : [cq] "s"(c.cpq), GATHER_IN : "vcc", "scc", "s100", "s101", "m0", "memory");
    else asm volatile("s_mov_b32 m0, 0\n\t" WAIT_FINE GATHER : WAIT_FINE_OUT : [cq] "s"(c.cpq), GATHER_IN : "vcc", "scc", "s100", "s101", "m0", "memory");
}
__device__ __forceinline__ void dma4k(__amdgpu_buffer_rsrc_t r, unsigned vo, unsigned so, unsigned lds) {
    asm volatile(
        "s_mov_b32 m0, %3\n\t"
        "s_nop 0\n\t"
        "buffer_load_dwordx4 %0, %1, %2 offen sc1 lds\n\t"
        "buffer_load_dwordx4 %0, %1, %2 offen offset:1024 sc1 lds\n\t"
        "buffer_load_dwordx4 %0, %1, %2 offen offset:2048 sc1 lds\n\t"
        "buffer_load_dwordx4 %0, %1, %2 offen offset:3072 sc1 lds"
        :: "v"(vo), "s"(r), "s"(so), "s"(lds) : "m0", "memory");
}
__device__ __forceinline__ int bs2(int c) { return (c & ~3) | ((c & 1) << 1) | ((c >> 1) & 1); }
__device__ __forceinline__ void gather_mem0(char* memL, int tid, int nt = 512) {
    const u16* mem0 = arg(A_MEM0);
    for (int c = tid; c < 640; c += nt) {
        const u32x2* src = (const u32x2*)(mem0 + ((c & 6) + ((c >> 3) & 1)) * D_ + (c >> 4) * 16 + (c & 1) * 4);
        const u32x2 x = src[0], y = src[2];
        *(u32x4*)(memL + c * 16) = (u32x4){(x[0] & 0xFFFFu) | (y[0] << 16), (x[0] >> 16) | (y[0] & 0xFFFF0000u), (x[1] & 0xFFFFu) | (y[1] << 16), (x[1] >> 16) | (y[1] & 0xFFFF0000u)};
    }
}
__device__ __forceinline__ f32x2 hadd(f32x4 c) { return (f32x2){c[0], c[1]} + (f32x2){c[2], c[3]}; }
__device__ __forceinline__ unsigned hpack(f32x4 c) { const f32x2 y = hadd(c); return pack2(y[0], y[1]); }
__device__ __forceinline__ u32x8 cat8(u32x4 lo, u32x4 hi) { return (u32x8){lo[0], lo[1], lo[2], lo[3], hi[0], hi[1], hi[2], hi[3]}; }
__device__ __forceinline__ u32x4 half8(const u32x8& v, int u) { return u ? (u32x4){v[4], v[5], v[6], v[7]} : (u32x4){v[0], v[1], v[2], v[3]}; }
__device__ __forceinline__ void dma1k(__amdgpu_buffer_rsrc_t r, unsigned vo, unsigned so, unsigned lds) {
    asm volatile("s_mov_b32 m0, %3\n\ts_nop 0\n\tbuffer_load_dwordx4 %0, %1, %2 offen sc1 lds" :: "v"(vo), "s"(r), "s"(so), "s"(lds) : "m0", "memory");
}
template <class FM> __device__ __forceinline__ void load_w(u32x8 (&wt)[2][10], const u16* src, unsigned rs, bool perm, unsigned b, unsigned c, int w, FM mid) {
    const __amdgpu_buffer_rsrc_t r = mkrsrc(src, 0x7FFFFFFFu);
    const unsigned l = fresh_lane(), q = l >> 4, lds = (unsigned)(w >> 2) * 65536u, v = w & 3;
    const unsigned vo0 = (perm ? ((q & 1u) << 3) | ((q & 2u) << 1) : q) * rs + ((l & 15u) ^ (q << 1)) * 16u, vo1 = vo0 ^ 128u, a = perm ? rs : 4u * rs;
    const LAS char* p0 = (const LAS char*)(size_t)(lds + q * 2048u + ((l >> 2) & 3u) * 256u + ((2u * v) ^ ((l >> 2) & 7u)) * 32u + (l & 3u) * 8u);
    const LAS char* p1 = (const LAS char*)((size_t)p0 ^ 32u);
    auto issue = [&](int g) {
#pragma unroll
        for (int i = 0; i < 4; ++i)
            asm volatile("s_mov_b32 m0, %3\n\ts_nop 3\n\tbuffer_load_dwordx4 %0, %1, %2 offen lds" :: "v"(i < 2 ? vo0 : vo1), "s"(r), "s"(i * a + (g & 1) * b + (g >> 1) * c), "s"(lds + (g & 3) * 16384u + v * 4096u + i * 1024u) : "m0", "memory");
    };
#pragma unroll
    for (int g = 0; g < 3; ++g) issue(g);
#pragma unroll
    for (int g = 0; g < 10; ++g) {
        asm volatile("s_waitcnt vmcnt(%0)\n\ts_barrier" :: "n"(g < 8 ? 8 : 4 * (9 - g)) : "memory");
        if (g + 3 < 10) issue(g + 3);
        const int o = (g & 3) * 16384;
        wt[0][g] = cat8(ldtr2(p0 + o, p0 + o + 1024), ldtr2(p0 + o + 8192, p0 + o + 9216));
        wt[1][g] = cat8(ldtr2(p1 + o, p1 + o + 1024), ldtr2(p1 + o + 8192, p1 + o + 9216));
        asm volatile("s_waitcnt lgkmcnt(0)" : "+v"(wt[0][g]), "+v"(wt[1][g]) :: "memory");
        if (g == 6) mid();
    }
    BAR0();
}
__device__ __forceinline__ int tile_of(int o) { return 8 * ((o % 10) >> 1) + 2 * (o / 10) + (o & 1); }
__device__ __forceinline__ void wait_tags(__amdgpu_buffer_rsrc_t rwin, int g0, int g1, unsigned n1, u64 val, bool boot) {
    const unsigned l = fresh_lane();
    unsigned o[4];
#pragma unroll
    for (int g = 0; g < 4; ++g) o[g] = g >= g0 && g < g1 && (g + 1 < g1 || l < n1) ? l * 128u + g * 8192u : 0x40000000u;
    for (unsigned spins = 0;;) {
        asm volatile("" ::: "memory");
        u32x2 v[4];
#pragma unroll
        for (int g = 0; g < 4; ++g) v[g] = __builtin_amdgcn_raw_buffer_load_b64(rwin, o[g], W_A1, 16);
#pragma unroll
        for (int g = 0; g < 4; ++g) if (__builtin_bit_cast(u64, v[g]) == val) o[g] = 0x40000000u;
        if (!__any((o[0] & o[1] & o[2] & o[3]) != 0x40000000u)) break;
        if (boot ? ++spins > SPIN_CAP : spin_fail(spins, rwin)) break;
        __builtin_amdgcn_s_sleep(ASLP);
    }
}
template <int N> __device__ __forceinline__ void stage_h(__amdgpu_buffer_rsrc_t rws, unsigned src, unsigned lds, unsigned st) {
    const unsigned l = fresh_lane() * 16u;
#pragma unroll
    for (int i = 0; i < N; ++i) dma1k(rws, l, src + i * st, lds + i * st);
}
template <int N> __device__ __forceinline__ void check_h(__amdgpu_buffer_rsrc_t rws, __amdgpu_buffer_rsrc_t rwin, unsigned src, unsigned lds, unsigned st) {
    const LAS char* p = (const LAS char*)smem + lds + fresh_lane() * 16u;
    for (unsigned spins = 0;;) {
        unsigned m = 0;
#pragma unroll
        for (int i = 0; i < N; ++i) { const u32x4 x = *(const volatile LAS u32x4*)(p + i * st); m = max(max(m, max(x[0], x[1])), max(x[2], x[3])); }
        if (__builtin_expect(!__any(m == ~0u), 1) || spin_fail(spins, rwin)) break;
        stage_h<N>(rws, src, lds, st);
        VMWAIT();
    }
}
__device__ __forceinline__ void wait_h(__amdgpu_buffer_rsrc_t rwin, unsigned fo, int seg, int& hrdy) {
    unsigned lim = 63u;
    if (__builtin_expect(hrdy <= seg, 0)) for (unsigned spins = 0;;) {
        asm volatile("" ::: "memory");
        const u32x4 f = __builtin_amdgcn_raw_buffer_load_b128(rwin, min(fresh_lane(), lim) * 128u, W_H + fo, 16);
        const unsigned long long miss = __ballot((f[0] | f[1] | f[2] | f[3]) != 0u);
        hrdy = miss ? __builtin_ctzll(miss) : (int)lim + 1;
        if (hrdy > seg) break;
        if (spin_fail(spins, rwin)) break;
        lim = (unsigned)min(seg + HLA, 63);
        if (seg < 8) __builtin_amdgcn_s_sleep(HSLP); else __builtin_amdgcn_s_sleep(HSL2);
    }
}
template <int CTRL> __device__ __forceinline__ unsigned dppu(unsigned v) { return (unsigned)__builtin_amdgcn_mov_dpp((int)v, CTRL, 0xF, 0xF, true); }
template <int CTRL> __device__ __forceinline__ u32x4 ror4(u32x4 v) { return (u32x4){dppu<CTRL>(v[0]), dppu<CTRL>(v[1]), dppu<CTRL>(v[2]), dppu<CTRL>(v[3])}; }
template <int N> struct IC { static constexpr int v = N; };
__device__ __forceinline__ float xmax16(float v) {
    auto q = __builtin_amdgcn_permlane16_swap(__float_as_uint(v), __float_as_uint(v), false, false);
    return fmx(__uint_as_float(q[0]), __uint_as_float(q[1]));
}
__device__ __forceinline__ float xsum16(float v) {
    auto q = __builtin_amdgcn_permlane16_swap(__float_as_uint(v), __float_as_uint(v), false, false);
    return __uint_as_float(q[0]) + __uint_as_float(q[1]);
}
__device__ __forceinline__ float addswap16(float a, float b) {
    auto r = __builtin_amdgcn_permlane16_swap(__float_as_uint(a), __float_as_uint(b), false, false);
    return __uint_as_float(r[0]) + __uint_as_float(r[1]);
}
#define LANES() \
    const int lane = (int)fresh_lane(), tid = w * 64 + lane, fr = lane & 15, fq = lane >> 4; \
    const int sidx = (lane & 2) ? 0xEEEE : 0x4444; \
    const int ma0 = fq * 256 + (fr & 1) * 128 + (fr >> 1) * 16; \
    const int prow = 16 * ((fr >> 2) & 1) + 4 * (fr >> 3) + (fr & 3), hoff = fr * 128; \
    (void)tid; (void)sidx; (void)ma0; (void)prow; (void)hoff

__device__ __forceinline__ int seg_after(unsigned long long m, int j) { m = j >= 63 ? 0ull : m >> (j + 1); return m ? j + 1 + __builtin_ctzll(m) : NSEG; }
__device__ __forceinline__ int seg_done(unsigned long long m, int f0, int f1, unsigned long long x = 0) { return min(seg_after(~m, f0), seg_after(m | x, f1)) - 1; }
constexpr unsigned long long MXA = 1ull << 40;
template <int OFF> __device__ __forceinline__ void ldfrag(u32x4& d, const LAS char* p) { asm volatile("ds_read_b128 %0, %1 offset:%2" : "=v"(d) : "v"(p), "n"(OFF) : "memory"); }
template <int U, class FM, class FV> __device__ __forceinline__ void project(const u32x8 (&wt)[2][10], f32x4 (&tc)[2][2], u32x4 (&bb)[8], const LAS char* c0, const LAS char* c1, const LAS char* n0, const LAS char* n1, FM mid, FV vm) {
    constexpr int t = U >> 2, k = U & 3, v = (U + 8) % 40;
    if (U == 32) mid();
    vm(IC<U>());
    const LAS char* p = U < 32 ? (k >> 1 ? c1 : c0) : (k >> 1 ? n1 : n0);
    if (U < 2) asm volatile("s_nop 0\n\ts_waitcnt lgkmcnt(7)\n\tv_mfma_f32_16x16x32_bf16 %0, %3, %2, 0\n\tv_mfma_f32_16x16x32_bf16 %1, %4, %2, 0\n\tds_read_b128 %2, %5 offset:%6"
                            : "=&v"(tc[0][k & 1]), "=&v"(tc[1][k & 1]), "+v"(bb[U & 7]) : "v"(half8(wt[0][t], k >> 1)), "v"(half8(wt[1][t], k >> 1)), "v"(p), "n"((v >> 2) * 4096 + (k & 1) * 2048) : "memory");
    else asm volatile(".rept %7\n\ts_nop 0\n\t.endr\n\ts_waitcnt lgkmcnt(7)\n\tv_mfma_f32_16x16x32_bf16 %0, %3, %2, %0\n\tv_mfma_f32_16x16x32_bf16 %1, %4, %2, %1\n\tds_read_b128 %2, %5 offset:%6"
                      : "+v"(tc[0][k & 1]), "+v"(tc[1][k & 1]), "+v"(bb[U & 7]) : "v"(half8(wt[0][t], k >> 1)), "v"(half8(wt[1][t], k >> 1)), "v"(p), "n"((v >> 2) * 4096 + (k & 1) * 2048), "n"(U < 8 ? 1 : 0) : "memory");
    if constexpr (U < 39) project<U + 1>(wt, tc, bb, c0, c1, n0, n1, mid, vm);
    else asm volatile("s_nop 7" : "+v"(tc[0][0]), "+v"(tc[0][1]), "+v"(tc[1][0]), "+v"(tc[1][1]));
}
__device__ __forceinline__ void project_end(u32x4 (&bb)[8]) {
    asm volatile("s_waitcnt lgkmcnt(0)" : "+v"(bb[0]), "+v"(bb[1]), "+v"(bb[2]), "+v"(bb[3]), "+v"(bb[4]), "+v"(bb[5]), "+v"(bb[6]), "+v"(bb[7]) :: "memory");
}
__device__ __forceinline__ void project0(u32x4 (&bb)[8], const LAS char* c0, const LAS char* c1) {
    ldfrag<0>(bb[0], c0); ldfrag<2048>(bb[1], c0); ldfrag<0>(bb[2], c1); ldfrag<2048>(bb[3], c1);
    ldfrag<4096>(bb[4], c0); ldfrag<6144>(bb[5], c0); ldfrag<4096>(bb[6], c1); ldfrag<6144>(bb[7], c1);
    project_end(bb);
}

__global__ __launch_bounds__(512, 2) B3ATTR void mega_kernel(Args a) {
    char* memL = smem;
    char* tokL = smem + L_TOK;
    char* hL = smem + L_H;
    int w = __builtin_amdgcn_readfirstlane(threadIdx.x >> 6);
    __amdgpu_buffer_rsrc_t rwin = mkrsrc(a.win, W_END);
    char* ws = a.ws;
    __amdgpu_buffer_rsrc_t rws = mkrsrc(ws, (unsigned)OFF_WIN);
    auto fresh_rsrc = [&]() INL { ws = (char*)arg(0); rws = mkrsrc(ws, (unsigned)OFF_WIN); rwin = mkrsrc(arg(1), W_END); };
    const char* pc;
    asm volatile("s_getpc_b64 %0" : "=s"(pc));
    unsigned fs[12] = {};
    const u32x2 tg0 = __builtin_amdgcn_raw_buffer_load_b64(rws, 0u, 0u, 16);
    u64 tag;
    u32x2 rot0;
    {
        const int tid = threadIdx.x;
        if (blockIdx.x < 64) {
            const unsigned j = blockIdx.x;
            if (j == 0 && w == 0) __builtin_amdgcn_raw_buffer_store_b64(__builtin_bit_cast(u32x2, (__builtin_bit_cast(u64, tg0) & ~15ull) + 16ull + (xcc_id() & 7u)), rws, LANE0, 128u, 16);
            const unsigned g0 = (j * 512u + tid) * 16u;
#pragma unroll
            for (int i = 0; i < 3; ++i) st128(SENT, mkrsrc(ws + O_ARM1, O_H + NHF * 40960u - O_ARM1), g0 + i * 524288u, 0, 16);
#pragma unroll
            for (int i = 0; i < 7; ++i) {
                const unsigned g = g0 + i * 524288u;
                st128(SENT, mkrsrc(a.win, W_END + 2 * PAIR), g + (g < W_ARM - W_LAYER ? 0u : W_ZM - (W_ARM - W_LAYER)), 0, 16);
            }
#pragma unroll
            for (int i = 0; i < 3; ++i) st128(SENT, mkrsrc(ws + O_Z, ZTSEG * 2), g0 + i * 524288u, 0, 16);
            static_assert(W_ARM - W_LAYER + W_END + 2 * PAIR - W_ZM <= 7u * 524288u && O_ARM1 + 3u * 524288u >= O_H + NHF * 40960u && ZTSEG * 2 <= 3u * 524288u, "arming");
        } else if (blockIdx.x >= 128) {
            RowTw tw;
            row_tw(tw, tid);
            a1_unit<8, false>(mkrsrc(a.x, S_ * D_ * 2), (blockIdx.x - 128) * 16, rws, rwin, tw, tid, fs);
        } else {
            const unsigned j = blockIdx.x - 64;
            FST(0);
            auto row = [&](unsigned i) { return (j & 7u) + 8u * (i & 15u) + 128u * ((j >> 3) & 3u) + 512u * (((j >> 4) & 2u) + ((i >> 4) & 1u)); };
            u32x4 v[6];
#pragma unroll
            for (int i = 0; i < 6; ++i) {
                const unsigned id = min(tid + 512 * i, 64 * 41 - 1), r = id / 41u;
                v[i] = ((const u32x4*)(r < 32u ? a.wr : a.wi))[row(r) * (DF * 2) / 16u + id - r * 41u];
            }
            u16 n[4];
            if (w == 7) {
                const size_t kn = (size_t)((S_ - row(tid)) & (S_ - 1)) * DF;
                n[0] = a.wr[kn]; n[1] = a.wi[kn]; n[2] = a.wr[kn + 320]; n[3] = a.wi[kn + 320];
            }
            const unsigned wo = WARM ? (w * 8u + (j >> 3)) * 8192u : 0x40000000u;
            const unsigned x0 = __builtin_amdgcn_raw_buffer_load_b32(mkrsrc(pc, CODE_BYTES), (tid & 63) * 128u, wo, 0), x1 = __builtin_amdgcn_raw_buffer_load_b32(mkrsrc(&TWC, sizeof(TwCol)), (tid & 63) * 128u, wo, 0);
#pragma unroll
            for (int i = 0; i < 6; ++i) ((u32x4*)smem)[min(tid + 512 * i, 64 * 41 - 1)] = v[i];
            FST(1);
            BAR();
            FST(2);
            const u16* q0 = (const u16*)smem + (j & 7u);
            u32x4 o[5];
#pragma unroll
            for (int i = 0; i < 5; ++i) {
                const u16* q = q0 + ((tid >> 1) & 7) * 328 + (tid >> 4) * 2 + (tid & 1) + 64 * i;
#pragma unroll
                for (int e = 0; e < 4; ++e) o[i][e] = (unsigned)q[e * 8 * 328] | ((unsigned)q[(32 + e * 8) * 328] << 16);
            }
#pragma unroll
            for (int i = 0; i < 5; ++i) st128(o[i], rws, (tid >> 4) * WST + (tid & 15) * 16u, O_WFT + i * 32u * WST + (j >> 5) * 8192u + (j & 7u) * 1024u + ((j >> 3) & 3u) * 256u, 16);
            if (w == 7) {
                const u16* q = q0 + (tid & 31) * 328;
                const unsigned k = row(tid);
                const c2 h0 = (c2){bf2f(q[0]) + bf2f(n[0]), bf2f(q[32 * 328]) - bf2f(n[1])} * bc(0.25f), h1 = (c2){bf2f(q[320]) + bf2f(n[2]), bf2f(q[32 * 328 + 320]) - bf2f(n[3])} * bc(0.25f);
                st128(__builtin_bit_cast(u32x4, (f32x4){h0.x + h1.x, h0.y + h1.y, h0.x - h1.x, h0.y - h1.y}), rws, (((k >> 8) & 6u) * 256u + (k & 64u) * 4u + (k & 7u) * 32u + ((k >> 4) & 24u) + ((k >> 3) & 7u)) * 16u, O_WFT + NCOL * WST, 16);
            }
            FST(3);
            asm volatile("" :: "v"(x0), "v"(x1));
            FST(4);
        }
        rot0 = __builtin_amdgcn_raw_buffer_load_b64(rws, 0u, 128u, 16);
        VMWAIT();
        FST(8);
        BAR0();
        FST(9);
    }
    tag = (((u64)__builtin_amdgcn_readfirstlane(tg0[1]) << 32) | (__builtin_amdgcn_readfirstlane(tg0[0]) & ~15u)) + 16ull;
    if (w == 0) __builtin_amdgcn_raw_buffer_store_b64(__builtin_bit_cast(u32x2, tag + 8ull), rwin, LANE0, W_A1 + blockIdx.x * 128u, 16);
    if (blockIdx.x >= 128) FSW(0); else if (blockIdx.x >= 64) FSW(6);
    unsigned vx;
    int bid;
    auto set_role = [&](unsigned x0) INL {
        vx = x0;
        const int j = blockIdx.x >> 3, x = vx & 3;
        if (!(vx & 4)) bid = j < 8 ? 8 * x + j : j < 16 ? 24 + 8 * x + j : j < 26 ? 80 + 10 * x + j : j < 30 ? NWRK + 4 * (j - 26) + x : NXT - 24 + 8 * x + j;
        else bid = j < 8 ? 64 + 8 * x + j : j < 16 ? NQT - 8 + 8 * x + j : j < 26 ? NREC - 16 + 10 * x + j : NXT - 26 + 8 * x + j;
    };
    auto get_rot = [&]() INL {
        unsigned r = 0;
        for (unsigned spins = 0; spins < SPIN_CAP; ++spins) {
            const u64 x = __builtin_bit_cast(u64, rot0) - tag;
            r = __builtin_amdgcn_readfirstlane((unsigned)x);
            if (__all(x < 8ull)) break;
            __builtin_amdgcn_s_sleep(2);
            asm volatile("" ::: "memory");
            rot0 = __builtin_amdgcn_raw_buffer_load_b64(rws, 0u, 128u, 16);
        }
        return (r + blockIdx.x + 4u * DIE) & 7u;
    };
    set_role(blockIdx.x < 64 ? (xcc_id() ^ (4u * DIE)) & 7u : get_rot());
    unsigned long long tref = 0;
    int layer = 0;
    unsigned fo = 0;
    unsigned nh = 0, nz = 0, mc = 0, qz = 0, mo = 0, tro = 0;
    auto near_init = [&]() INL {
        unsigned f;
        for (unsigned spins = 0;;) {
            asm volatile("" ::: "memory");
            f = __builtin_amdgcn_raw_buffer_load_b32(rwin, (fresh_lane() & 15u) * 4u, W_NEAR, 16);
            if (!__any(f == ~0u) || spin_fail(spins, rwin)) break;
            __builtin_amdgcn_s_sleep(8);
        }
        const unsigned h = (unsigned)__ballot(f & 1u), q = (unsigned)__ballot(f & 2u) ^ 0xAAAAu;
        auto most = [&](unsigned m) { return (__builtin_popcount(m & 0x0F0Fu) + 3u) >> 3; };
        nh = most(h) * HALF; nz = most(h >> 4) * HALF;
        mc = (vx & 3u) ^ most(q) ^ (STK ? 2u * most((unsigned)__ballot(f & 4u) ^ 0x3333u) : STX);
        qz = most(q >> 4);
        mo = W_MEM + (mc & 1u) * PAIR + nh + (mc >> 1) * 4096u;
        tro = mo - W_MEM + W_T;
    };
    auto zpair = [&](unsigned x, unsigned k) { return (((x >> 1) * 10u + k) * 2u + ((x & 1u) ^ qz)) * PAIR + nz; };
    auto two = [&]() { const unsigned l = fresh_lane(); return l < 4 ? (l >> 1) * PAIR + (l & 1) * 4096u + nh : 0x40000000u; };
    const f32x4 z4 = {0.f, 0.f, 0.f, 0.f};
    int hrdy = 0, budget = (int)SPIN_CAP;
    unsigned fc0 = 0, fc1 = 0, fc2 = 0;
    Clk ck = {0u, 0u, 1600u, 10486u};
    SST(5);
#define xt (bid >= NXT && bid < NWRK)
#define qx (bid >= NQT && bid < NWRK)
#define xq (bid >= NREC && bid < NQT && (bid - NREC) % 10 < 8)
    unsigned tpp = 0;
    int lt = LT1, xs = XS0;
    struct Tk { unsigned vo, tw, sl; const LAS char* sp; };
    auto tk_init = [&]() {
        const unsigned l = fresh_lane();
        Tk k = {l * 16u, two(), (l & 31u) * 128u + (l >> 5) * 4u, (const LAS char*)(memL + l * 64 + 4 + ((l >> 1) & 7) * 8)};
        PIN2(k.vo, k.tw); PIN2(k.sl, k.sp);
        return k;
    };
    auto sched2 = [&](Tk& k, int s, unsigned f) INL {
        const unsigned g0 = tpp - 0x400000u;
        tpp = g0 + (unsigned)read_sched(rwin, s, k.sl, tro + (unsigned)(s & 3) * 2u * PAIR, f, g0, lt, xs, fc1);
        k.sl &= ~4u;
        RT(6);
    };
    auto sched = [&](Tk& k, int s) INL { sched2(k, s, __builtin_amdgcn_raw_buffer_load_b32(rwin, k.sl, tro + (unsigned)(s & 3) * 2u * PAIR, 16)); };
    unsigned it = 0;
    auto fetch = [&](const Tk& k, int s, bool first, unsigned lim, unsigned dc, unsigned fo, bool tell) INL {
        const unsigned sb = (unsigned)(s & 3) * 6u * PAIR, ta0 = ck.ta, mm0 = ck.ma;
        if (__builtin_expect(s > 0, 1)) {
            if (__builtin_expect(first, 1)) { const unsigned te = cyc_at(ck, sx24(tpp - TKE - ck.ta)); while ((int)((unsigned)__builtin_amdgcn_s_memtime() - te) < 0) __builtin_amdgcn_s_sleep(1); }
            if (__builtin_expect(first, 1)) gather_at(tpp + (tell ? 0 : DGV), ck, rwin, k.vo, mo + sb, VREV && !tell); else { const unsigned c = (mc + 1u + (it & 1u)) & 3u; gather_dma(rwin, k.vo, W_MEM + (c & 1u) * PAIR + (c >> 1) * 4096u + nh + sb, VREV && !tell); }
        }
        __builtin_amdgcn_s_setprio(0);
        RT(1);
        clk_rate(ck, ta0, mm0);
        unsigned tb = ck.ma - dc, tg = (unsigned)s << 24, so = W_T + (unsigned)(s & 3) * 2u * PAIR + fo;
        asm volatile("" : "+s"(tb), "+s"(tg), "+s"(so));
        asm volatile(ALN "s_waitcnt vmcnt(%1) lgkmcnt(0)\n\ts_barrier\n\ts_waitcnt vmcnt(%2)\n\ts_barrier\n\ts_waitcnt vmcnt(0)\n\ts_memtime %0\n\ts_barrier"
                     : "=s"(tref) : "n"(10 - NB1), "n"(10 - NB2) : "memory");
        MT(1);
        if (!tell) { RT(3); return; }
        unsigned x0 = *(const LAS unsigned*)k.sp, x1 = *(const LAS unsigned*)(k.sp + 4096), x2 = *(const LAS unsigned*)(k.sp + 6144);
        __builtin_amdgcn_s_waitcnt(0xC07F);
        asm volatile("" : "+s"(tref) :: "memory");
        const unsigned fw = ((ck.ta + ((((unsigned)tref - tb) * ck.qpc) >> 16)) & 0xFFFFFFu) | tg;
        __builtin_amdgcn_raw_buffer_store_b32(fw, rwin, k.tw, __any(max(max(x0, x1), x2) > lim) ? 0x40000000u : so, 16);
        RT(3);
    };
    auto rout = [&]() { return mkrsrc(layer ? arg(A_OUT) : (const u16*)(ws + OFF_X2), S_ * D_ * 2); };
    auto rec_end = [&](unsigned g) INL {
        if (g) for (unsigned spins = 0;;) {
            asm volatile("" ::: "memory");
            if (__builtin_amdgcn_readfirstlane(__builtin_amdgcn_raw_buffer_load_b32(rws, 0u, g == 3 ? O_FL + (unsigned)(bid & 127) * GST + 4092u : O_FL - g * SEG * D_ * 2u + (unsigned)bid * 128u, 16)) != -1 || spin_fail(spins, rwin)) break;
            __builtin_amdgcn_s_sleep(16);
        }
    };
    auto mytid = [&]() { return w * 64 + (int)fresh_lane(); };
    auto new_layer = [&]() { fo = (unsigned)layer * W_LAYER; asm volatile("" : "+s"(fo), "+s"(w), "+s"(bid), "+s"(nh), "+s"(nz), "+s"(mc), "+s"(qz), "+s"(tag)); hrdy = 0; tpp = 0; lt = LT1; xs = XS0; };
    auto load_qv = [&](u32x8 (&wt)[2][10], int hh, bool v, auto mid) INL {
        const bool o = v && w >= 4;
        load_w(wt, arg(A_WQ + 2 * v + (w >> 2)) + (o ? (hh * 128 + (w & 3) * 32) * D_ : hh * 128 + (w & 3) * 16 * HD), o ? D_ * 2 : HD * 2, !o, o ? 16 * D_ * 2 : 64 * HD * 2, o ? 256 : 128 * HD * 2, w, mid);
    };
    auto hseg = [&](int j) { return O_H + (unsigned)(j - NHF * layer) * 40960u; };
    auto stage2 = [&](int j0, int j1, bool vt) INL {
        if (w == 0) {
            if (j1 >= NHF) wait_h(rwin, fo, j1, hrdy);
            else if (TH) { const unsigned vo[1] = {(fresh_lane() & 3u) * 4u}; wait_told(rwin, vo, W_H + fo + (unsigned)j0 * 128u + 16u, 1); }
            else for (unsigned spins = 0;;) {
                asm volatile("" ::: "memory");
                const unsigned l = fresh_lane();
                if (!__any(__builtin_amdgcn_raw_buffer_load_b32(rws, (l & 31u) * 128u + (l >> 5) * (unsigned)(j1 - j0) * 40960u, hseg(j0) + 36864u, 16) == ~0u) || spin_fail(spins, rwin)) break;
                __builtin_amdgcn_s_sleep(HSLP);
            }
            *(volatile LAS int*)(smem + L_S + 32) = hrdy;
        }
        BAR();
        hrdy = __builtin_amdgcn_readfirstlane(*(volatile LAS int*)(smem + L_S + 32));
        SST(2);
        if (j0 == 1 && vt) __builtin_amdgcn_s_sleep(DH1);
        stage_h<5>(rws, hseg(j0) + w * 1024u, L_H + w * 1024u, 8192u);
        if (!vt) {
            stage_h<5>(rws, hseg(j1) + w * 1024u, L_H + 40960u + w * 1024u, 8192u);
            asm volatile("s_waitcnt vmcnt(5)" ::: "memory");
        } else if (w >= 4 && j1 != j0) {
            stage_h<10>(rws, hseg(j1) + (w - 4) * 1024u, L_H + 40960u + (w - 4) * 1024u, 4096u);
            asm volatile("s_waitcnt vmcnt(10)" ::: "memory");
        } else VMWAIT();
        check_h<5>(rws, rwin, hseg(j0) + w * 1024u, L_H + w * 1024u, 8192u);
        BAR0();
        SST(3);
        if (vt && w >= 4 && j1 != j0) {
            VMWAIT();
            check_h<10>(rws, rwin, hseg(j1) + (w - 4) * 1024u, L_H + 40960u + (w - 4) * 1024u, 4096u);
        }
    };
#define WO(eb, nt) wt[(nt) & 1][((nt) & ~1) + (eb)]
    unsigned zb = W_ZM;
    auto probe = [&]() INL {
        {
            unsigned* best = (unsigned*)smem;
            const int tid = mytid();
            const __amdgpu_buffer_rsrc_t rw = mkrsrc(a.win + (bid & 4 ? W_END : W_PROBE), 2 * PAIR);
            const unsigned base = (unsigned)(bid & 8) * 64u + w * 1024u;
            if (tid < 8) best[tid] = 100000u;
            if (w == 0) wait_tags(rwin, 0, 1, 64u, tag + 8ull, true);
            BAR();
            unsigned mn[4] = {~0u, ~0u, ~0u, ~0u};
            for (int r = 0; r < NPR; ++r) {
#pragma unroll
                for (int i = 0; i < 4; ++i) {
                    const unsigned long long t1 = __builtin_amdgcn_s_memtime();
                    asm volatile("s_waitcnt lgkmcnt(0)" ::: "memory");
                    unsigned v = __builtin_amdgcn_raw_buffer_load_b32(rw, base + r * 128u + i * HALF, 0, 16);
                    asm volatile("s_waitcnt vmcnt(0)" : "+v"(v) :: "memory");
                    const unsigned long long t2 = __builtin_amdgcn_s_memtime();
                    mn[i] = min(mn[i], (unsigned)(t2 - t1));
                }
            }
            if ((tid & 63) == 0) {
#pragma unroll
                for (int i = 0; i < 4; ++i) atomicMin(&best[i + (w & 4)], mn[i]);
            }
            BAR();
            const unsigned h = min(min(best[1], best[3]), min(best[5], best[7])) < min(min(best[0], best[2]), min(best[4], best[6])), q = best[h + 2] + best[h + 6] < best[h] + best[h + 4], k = best[h + 4] + best[h + 6] < best[h] + best[h + 2];
            if (w == 0) __builtin_amdgcn_raw_buffer_store_b32(h + 2u * q + 4u * k, rwin, LANE0, W_NEAR + (unsigned)(bid & 15) * 4u, 16);
            BAR();
        }
    };
    auto vo_role = [&](const u32x8 (&wt)[2][10]) INL {
        const int hh = bid & 31;
        auto steps = [&](auto cc) INL {
            constexpr int C = decltype(cc)::v & 1;
            constexpr bool TK = decltype(cc)::v == 3;
            LANES();
            Tk tk = {0u, 0u, 0x40000000u, nullptr};
            if (TK) { tk = tk_init(); __builtin_amdgcn_s_setprio(TKP); }
            const unsigned zi = (unsigned)(hh * 128 + fr * 8) * 2u;
            unsigned zo0 = zpair(w & 3, fq) + zi, zo1 = zo0 + 8 * PAIR, zo2 = zpair(w & 3, 8 + (fq >> 1)) + zi + (fq & 1) * 8;
            const LAS char* va = (const LAS char*)(smem + L_Q + (fr & 1) * 544 + ((fr >> 1) & 1) * 1152 + (16 * fq + 2 * (fr >> 2)) * 4);
            LAS char* vw = (LAS char*)(smem + L_Q + (fq & 1) * 544 + (fq >> 1) * 1152 + (16 * w + bs2(fr)) * 4);
            const LAS char* mp0 = (const LAS char*)(memL + ma0);
            PIN2(zo0, zo1); PIN1(zo2);
            if (C == 0) { PIN2(mp0, vw); } else PIN1(va);
            u32x4 za = SENT, zc = SENT;
            u32x2 ze = {0u, 0u};
            auto put_z2 = [&](int s) {
                const unsigned z2b = s < NSEG ? O_Z2 + (unsigned)s * (NOWN * 8192u) + (unsigned)(10 * (w & 3) * 8192 + hh * 256) : 0x40000000u, l2 = fresh_lane();
                st128(za, rws, (l2 >> 4) * 8192u + (l2 & 15u) * 16u, z2b, 16);
                st128(zc, rws, (l2 >> 4) * 8192u + (l2 & 15u) * 16u, z2b + 4u * 8192u, 16);
                __builtin_amdgcn_raw_buffer_store_b64(ze, rws, (l2 >> 5) * 8192u + (l2 & 15u) * 16u + ((l2 >> 4) & 1u) * 8u, z2b + 8u * 8192u, 16);
            };
            for (int s = 0; s < NSEG; ++s) {
                RT(0);
                bool redo = false;
                unsigned sf = 0;
                do {
                    ++it;
                    if (C == 0) {
                        const unsigned lim = s > 0 ? poll_lim(budget) : ~0u;
                        asm volatile(
                            "v_mov_b64 v[200:201], 0\n\t"
                            "v_mov_b64 v[202:203], 0\n\t"
                            "v_mov_b64 v[204:205], 0\n\t"
                            "v_mov_b64 v[206:207], 0\n\t"
                            "v_mov_b32 v208, 0\n\t"
                            "s_setprio 2\n\t"
                            ALN
                            "s_waitcnt lgkmcnt(0)\n\t"
                            "s_barrier\n\t"
                            MTA(0)
                            "ds_read_b128 v[212:215], %[mp] offset:" VOFF0 "\n\t"
                            "ds_read_b128 v[216:219], %[mp] offset:" VOFF1 "\n\t"
                            "ds_read_b128 v[220:223], %[mp] offset:" VOFF2 "\n\t"
                            "s_barrier\n\t"
                            "ds_read_b128 v[224:227], %[mp] offset:" VOFF3 "\n\t"
                            "ds_read_b128 v[228:231], %[mp] offset:" VOFF4 "\n\t"
                            "ds_read_b128 v[232:235], %[mp] offset:" VOFF5 "\n\t"
                            "s_waitcnt lgkmcnt(5)\n\t"
                            "v_smfmac_f32_16x16x64_bf16 v[200:203], v[212:215], %[w00], %[ix]\n\t"
                            "s_waitcnt lgkmcnt(4)\n\t"
                            "v_smfmac_f32_16x16x64_bf16 v[200:203], v[216:219], %[w01], %[ix]\n\t"
                            "s_barrier\n\t"
                            MTA(1)
                            "ds_read_b128 v[236:239], %[mp] offset:" VOFF6 "\n\t"
                            "s_waitcnt lgkmcnt(4)\n\t"
                            "v_smfmac_f32_16x16x64_bf16 v[200:203], v[220:223], %[w02], %[ix]\n\t"
                            "ds_read_b128 v[240:243], %[mp] offset:" VOFF7 "\n\t"
                            "s_waitcnt lgkmcnt(4)\n\t"
                            "v_smfmac_f32_16x16x64_bf16 v[200:203], v[224:227], %[w03], %[ix]\n\t"
                            "ds_read_b128 v[244:247], %[mp] offset:" VOFF8 "\n\t"
                            "s_waitcnt lgkmcnt(4)\n\t"
                            "v_smfmac_f32_16x16x64_bf16 v[200:203], v[228:231], %[w04], %[ix]\n\t"
                            "ds_read_b128 v[248:251], %[mp] offset:" VOFF9 "\n\t"
                            "s_waitcnt lgkmcnt(4)\n\t"
                            "v_smfmac_f32_16x16x64_bf16 v[200:203], v[232:235], %[w05], %[ix]\n\t"
                            "s_waitcnt lgkmcnt(3)\n\t"
                            "v_smfmac_f32_16x16x64_bf16 v[200:203], v[236:239], %[w06], %[ix]\n\t"
                            "s_waitcnt lgkmcnt(2)\n\t"
                            "v_smfmac_f32_16x16x64_bf16 v[200:203], v[240:243], %[w07], %[ix]\n\t"
                            "s_waitcnt lgkmcnt(1)\n\t"
                            "v_smfmac_f32_16x16x64_bf16 v[200:203], v[244:247], %[w08], %[ix]\n\t"
                            "s_waitcnt lgkmcnt(0)\n\t"
                            "v_smfmac_f32_16x16x64_bf16 v[200:203], v[248:251], %[w09], %[ix]\n\t"
                            "s_nop 2\n\t"
                            "v_smfmac_f32_16x16x64_bf16 v[204:207], v[212:215], %[w10], %[ix]\n\t"
                            "s_nop 2\n\t"
                            "v_smfmac_f32_16x16x64_bf16 v[204:207], v[216:219], %[w11], %[ix]\n\t"
                            "v_add_f32 v200, v200, v202\n\t"
                            "v_add_f32 v201, v201, v203\n\t"
                            "v_cvt_pk_bf16_f32 v200, v200, v201\n\t"
                            "ds_write_b32 %[vw], v200\n\t"
                            MTA(2)
                            "v_smfmac_f32_16x16x64_bf16 v[204:207], v[220:223], %[w12], %[ix]\n\t"
                            "v_max3_u32 v208, v208, v212, v213\n\t"
                            "v_max3_u32 v208, v208, v214, v215\n\t"
                            "v_smfmac_f32_16x16x64_bf16 v[204:207], v[224:227], %[w13], %[ix]\n\t"
                            "v_max3_u32 v208, v208, v216, v217\n\t"
                            "v_max3_u32 v208, v208, v218, v219\n\t"
                            "v_smfmac_f32_16x16x64_bf16 v[204:207], v[228:231], %[w14], %[ix]\n\t"
                            "v_max3_u32 v208, v208, v220, v221\n\t"
                            "v_max3_u32 v208, v208, v222, v223\n\t"
                            "v_smfmac_f32_16x16x64_bf16 v[204:207], v[232:235], %[w15], %[ix]\n\t"
                            "v_max3_u32 v208, v208, v224, v225\n\t"
                            "v_max3_u32 v208, v208, v226, v227\n\t"
                            "v_smfmac_f32_16x16x64_bf16 v[204:207], v[236:239], %[w16], %[ix]\n\t"
                            "v_max3_u32 v208, v208, v228, v229\n\t"
                            "v_max3_u32 v208, v208, v230, v231\n\t"
                            "v_smfmac_f32_16x16x64_bf16 v[204:207], v[240:243], %[w17], %[ix]\n\t"
                            "s_waitcnt lgkmcnt(0)\n\t"
                            "s_barrier\n\t"
                            MTA(5)
                            "v_smfmac_f32_16x16x64_bf16 v[204:207], v[244:247], %[w18], %[ix]\n\t"
                            "v_max3_u32 v208, v208, v232, v233\n\t"
                            "v_max3_u32 v208, v208, v234, v235\n\t"
                            "v_smfmac_f32_16x16x64_bf16 v[204:207], v[248:251], %[w19], %[ix]\n\t"
                            "v_max3_u32 v208, v208, v236, v237\n\t"
                            "v_max3_u32 v208, v208, v238, v239\n\t"
                            "v_max3_u32 v208, v208, v240, v241\n\t"
                            "v_max3_u32 v208, v208, v242, v243\n\t"
                            "v_max3_u32 v208, v208, v244, v245\n\t"
                            "v_max3_u32 v208, v208, v246, v247\n\t"
                            "v_max3_u32 v208, v208, v248, v249\n\t"
                            "v_max3_u32 v208, v208, v250, v251\n\t"
                            "v_cmp_lt_u32 vcc, %[lim], v208\n\t"
                            "v_add_f32 v204, v204, v206\n\t"
                            "v_add_f32 v205, v205, v207\n\t"
                            "v_cvt_pk_bf16_f32 v204, v204, v205\n\t"
                            "ds_write_b32 %[vw], v204 offset:256\n\t"
                            MTA(3)
                            "s_cbranch_vccz 1f\n\t"
                            "v_mov_b32 v208, %[it]\n\t"
                            "v_mov_b32 v209, 0\n\t"
                            "ds_write_b32 v209, v208 offset:%[ls]\n\t"
                            "1:\n\t"
                            "s_waitcnt vmcnt(0) lgkmcnt(0)\n\t"
                            "s_barrier\n\t"
                            "s_setprio 0\n\t"
                            :: [mp] "v"(mp0), [vw] "v"(vw), [ix] "v"(sidx), [lim] "s"(lim), [it] "s"(it), [ls] "n"(L_S),
                              [w00] "v"(wt[0][VREV ? 9 - 0 : 0]), [w01] "v"(wt[0][VREV ? 9 - 1 : 1]), [w02] "v"(wt[0][VREV ? 9 - 2 : 2]), [w03] "v"(wt[0][VREV ? 9 - 3 : 3]), [w04] "v"(wt[0][VREV ? 9 - 4 : 4]), [w05] "v"(wt[0][VREV ? 9 - 5 : 5]), [w06] "v"(wt[0][VREV ? 9 - 6 : 6]), [w07] "v"(wt[0][VREV ? 9 - 7 : 7]), [w08] "v"(wt[0][VREV ? 9 - 8 : 8]), [w09] "v"(wt[0][VREV ? 9 - 9 : 9]), [w10] "v"(wt[1][VREV ? 9 - 0 : 0]), [w11] "v"(wt[1][VREV ? 9 - 1 : 1]), [w12] "v"(wt[1][VREV ? 9 - 2 : 2]), [w13] "v"(wt[1][VREV ? 9 - 3 : 3]), [w14] "v"(wt[1][VREV ? 9 - 4 : 4]), [w15] "v"(wt[1][VREV ? 9 - 5 : 5]), [w16] "v"(wt[1][VREV ? 9 - 6 : 6]), [w17] "v"(wt[1][VREV ? 9 - 7 : 7]), [w18] "v"(wt[1][VREV ? 9 - 8 : 8]), [w19] "v"(wt[1][VREV ? 9 - 9 : 9])
                            : "v200", "v201", "v202", "v203", "v204", "v205", "v206", "v207", "v208", "v209", "v212", "v213", "v214", "v215", "v216", "v217", "v218", "v219", "v220", "v221", "v222", "v223", "v224", "v225", "v226", "v227", "v228", "v229", "v230", "v231", "v232", "v233", "v234", "v235", "v236", "v237", "v238", "v239", "v240", "v241", "v242", "v243", "v244", "v245", "v246", "v247", "v248", "v249", "v250", "v251", "vcc", "memory");
                        MT(4);
                        redo = __builtin_amdgcn_readfirstlane(*(volatile LAS unsigned*)(smem + L_S)) == it;
                        if (redo) FC(fc0);
                    } else {
                        f32x16 ca, cb;
                        f32x8 cc;
                        asm volatile("v_mov_b64 v[212:213], 0\n\tv_mov_b64 v[214:215], 0\n\tv_mov_b64 v[216:217], 0\n\tv_mov_b64 v[218:219], 0\n\tv_mov_b64 v[220:221], 0\n\tv_mov_b64 v[222:223], 0\n\tv_mov_b64 v[224:225], 0\n\tv_mov_b64 v[226:227], 0\n\tv_mov_b64 v[228:229], 0\n\tv_mov_b64 v[230:231], 0\n\tv_mov_b64 v[232:233], 0\n\tv_mov_b64 v[234:235], 0\n\tv_mov_b64 v[236:237], 0\n\tv_mov_b64 v[238:239], 0\n\tv_mov_b64 v[240:241], 0\n\tv_mov_b64 v[242:243], 0\n\tv_mov_b64 v[244:245], 0\n\tv_mov_b64 v[246:247], 0\n\tv_mov_b64 v[248:249], 0\n\tv_mov_b64 v[250:251], 0\n\t" : "={v[212:227]}"(ca), "={v[228:243]}"(cb), "={v[244:251]}"(cc));
                        unsigned sr = tro + (unsigned)(s & 3) * 2u * PAIR;
                        asm volatile("" : "+s"(sr));
                        if (TK) fetch(tk, s, !redo, s > 0 ? poll_lim(budget) : ~0u, 0u, 0u, false);
                        else asm volatile(ALN "s_waitcnt lgkmcnt(0)\n\ts_barrier\n\ts_barrier\n\ts_barrier" ::: "memory");
                        unsigned vd, vv, zs = zb;
                        u32x4 af1;
                        asm volatile(
                            "s_setprio 1\n\t"
                            "s_waitcnt lgkmcnt(0)\n\t"
                            "s_barrier\n\t"
                            "ds_read_b64_tr_b16 v[192:193], %[va]\n\t"
                            "ds_read_b64_tr_b16 v[194:195], %[va] offset:32\n\t"
                            "s_waitcnt lgkmcnt(0)\n\t"
                            "v_smfmac_f32_16x16x64_bf16 v[212:215], v[192:195], %[w0], %[ix]\n\t"
                            "v_smfmac_f32_16x16x64_bf16 v[220:223], v[192:195], %[w2], %[ix]\n\t"
                            "v_smfmac_f32_16x16x64_bf16 v[216:219], v[192:195], %[w1], %[ix]\n\t"
                            "v_smfmac_f32_16x16x64_bf16 v[224:227], v[192:195], %[w3], %[ix]\n\t"
                            "s_barrier\n\t"
                            MTA(4)
                            "ds_read_b64_tr_b16 v[196:197], %[va] offset:256\n\t"
                            "v_smfmac_f32_16x16x64_bf16 v[228:231], v[192:195], %[w4], %[ix]\n\t"
                            "ds_read_b64_tr_b16 v[198:199], %[va] offset:288\n\t"
                            "v_smfmac_f32_16x16x64_bf16 v[236:239], v[192:195], %[w6], %[ix]\n\t"
                            "ds_read_b32 v210, %[vz] offset:%[ls]\n\t"
                            "v_smfmac_f32_16x16x64_bf16 v[232:235], v[192:195], %[w5], %[ix]\n\t"
                            "v_smfmac_f32_16x16x64_bf16 v[240:243], v[192:195], %[w7], %[ix]\n\t"
                            "v_smfmac_f32_16x16x64_bf16 v[244:247], v[192:195], %[w8], %[ix]\n\t"
                            "v_smfmac_f32_16x16x64_bf16 v[248:251], v[192:195], %[w9], %[ix]\n\t"
                            "buffer_load_dword %[sf], %[sl], %[rw], %[sr] offen sc1\n\t"
                            : [sf] "=&v"(sf), "+{v[212:227]}"(ca), "+{v[228:243]}"(cb), "+{v[244:251]}"(cc), "=&{v[196:199]}"(af1), "=&{v210}"(vv)
                            : [va] "v"(va), [vz] "v"(0u), [ls] "n"(L_S), [ix] "v"(sidx), [sl] "v"(tk.sl), [sr] "s"(sr), [rw] "s"(rwin),
                              [w0] "v"(WO(0, 0)), [w1] "v"(WO(0, 1)), [w2] "v"(WO(0, 2)), [w3] "v"(WO(0, 3)), [w4] "v"(WO(0, 4)), [w5] "v"(WO(0, 5)), [w6] "v"(WO(0, 6)), [w7] "v"(WO(0, 7)), [w8] "v"(WO(0, 8)), [w9] "v"(WO(0, 9))
                            : "v192", "v193", "v194", "v195", "memory");
                        asm volatile(
                            "s_waitcnt lgkmcnt(0)\n\t"
                            "v_smfmac_f32_16x16x64_bf16 v[212:215], v[196:199], %[w0], %[ix]\n\t"
                            "v_readfirstlane_b32 %[vd], v210\n\t"
                            "v_smfmac_f32_16x16x64_bf16 v[220:223], v[196:199], %[w2], %[ix]\n\t"
                            "s_cmp_eq_u32 %[vd], %[it]\n\t"
                            "s_cselect_b32 %[zs], 2.0, %[zs]\n\t"
                            "v_smfmac_f32_16x16x64_bf16 v[216:219], v[196:199], %[w1], %[ix]\n\t"
                            "s_nop 1\n\t"
                            "v_smfmac_f32_16x16x64_bf16 v[224:227], v[196:199], %[w3], %[ix]\n\t"
                            "v_add_f32 v212, v212, v214\n\t"
                            "v_smfmac_f32_16x16x64_bf16 v[228:231], v[196:199], %[w4], %[ix]\n\t"
                            "v_add_f32 v213, v213, v215\n\t"
                            "v_smfmac_f32_16x16x64_bf16 v[236:239], v[196:199], %[w6], %[ix]\n\t"
                            "v_add_f32 v220, v220, v222\n\t"
                            "v_smfmac_f32_16x16x64_bf16 v[232:235], v[196:199], %[w5], %[ix]\n\t"
                            "v_add_f32 v221, v221, v223\n\t"
                            "v_smfmac_f32_16x16x64_bf16 v[240:243], v[196:199], %[w7], %[ix]\n\t"
                            "v_cvt_pk_bf16_f32 v200, v212, v213\n\t"
                            "v_smfmac_f32_16x16x64_bf16 v[244:247], v[196:199], %[w8], %[ix]\n\t"
                            "v_cvt_pk_bf16_f32 v201, v220, v221\n\t"
                            "v_smfmac_f32_16x16x64_bf16 v[248:251], v[196:199], %[w9], %[ix]\n\t"
                            "v_add_f32 v216, v216, v218\n\t"
                            "v_add_f32 v217, v217, v219\n\t"
                            "v_add_f32 v224, v224, v226\n\t"
                            "v_add_f32 v225, v225, v227\n\t"
                            "v_cvt_pk_bf16_f32 v202, v216, v217\n\t"
                            "v_cvt_pk_bf16_f32 v203, v224, v225\n\t"
                            "v_permlane32_swap_b32 v200, v201\n\t"
                            "v_pk_add_f32 v[228:229], v[228:229], v[230:231]\n\t"
                            "v_permlane32_swap_b32 v202, v203\n\t"
                            "v_min_u32 v201, -2, v201\n\t"
                            "v_pk_add_f32 v[236:237], v[236:237], v[238:239]\n\t"
                            "v_min_u32 v203, -2, v203\n\t"
                            "v_permlane16_swap_b32 v200, v202\n\t"
                            "v_pk_add_f32 v[232:233], v[232:233], v[234:235]\n\t"
                            "v_permlane16_swap_b32 v201, v203\n\t"
                            "v_pk_add_f32 v[240:241], v[240:241], v[242:243]\n\t"
                            MTA(5)
                            "buffer_store_dwordx4 v[200:203], %[z0], %[rw], %[zs] offen sc1\n\t"
                            "v_cvt_pk_bf16_f32 v204, v228, v229\n\t"
                            "v_cvt_pk_bf16_f32 v205, v236, v237\n\t"
                            "v_cvt_pk_bf16_f32 v206, v232, v233\n\t"
                            "v_cvt_pk_bf16_f32 v207, v240, v241\n\t"
                            "v_permlane32_swap_b32 v204, v205\n\t"
                            "v_pk_add_f32 v[244:245], v[244:245], v[246:247]\n\t"
                            "v_permlane32_swap_b32 v206, v207\n\t"
                            "v_min_u32 v205, -2, v205\n\t"
                            "v_pk_add_f32 v[248:249], v[248:249], v[250:251]\n\t"
                            "v_min_u32 v207, -2, v207\n\t"
                            "v_permlane16_swap_b32 v204, v206\n\t"
                            "v_cvt_pk_bf16_f32 v208, v244, v245\n\t"
                            "v_permlane16_swap_b32 v205, v207\n\t"
                            "v_cvt_pk_bf16_f32 v209, v248, v249\n\t"
                            "buffer_store_dwordx4 v[204:207], %[z1], %[rw], %[zs] offen sc1\n\t"
                            "s_nop 0\n\t"
                            "v_permlane32_swap_b32 v208, v209\n\t"
                            "v_min_u32 v209, -2, v209\n\t"
                            "buffer_store_dwordx2 v[208:209], %[z2], %[rw], %[zs] offen sc1\n\t"
                            : [vd] "=&s"(vd), [zs] "+s"(zs), "=&{v[200:203]}"(za), "=&{v[204:207]}"(zc), "=&{v[208:209]}"(ze), "+{v[212:227]}"(ca), "+{v[228:243]}"(cb), "+{v[244:251]}"(cc), "+v"(sf)
                            : [ix] "v"(sidx), [it] "s"(it), [rw] "s"(rwin), [z0] "v"(zo0), [z1] "v"(zo1), [z2] "v"(zo2), "{v[196:199]}"(af1), "{v210}"(vv),
                              [w0] "v"(WO(1, 0)), [w1] "v"(WO(1, 1)), [w2] "v"(WO(1, 2)), [w3] "v"(WO(1, 3)), [w4] "v"(WO(1, 4)), [w5] "v"(WO(1, 5)), [w6] "v"(WO(1, 6)), [w7] "v"(WO(1, 7)), [w8] "v"(WO(1, 8)), [w9] "v"(WO(1, 9))
                            : "scc", "memory");
                        redo = vd == it;
                        RT(4);
                        MT(6);
                        tref = __builtin_amdgcn_s_memtime();
                        __builtin_amdgcn_s_setprio(TKP);
                        asm volatile("s_waitcnt vmcnt(3)" : "+v"(sf) :: "memory");
                    }
                } while (__builtin_expect(redo, 0));
                if (s < 8) SST(8 + s);
                if (C == 0) {
                    if (s > 0 && s + 1 < NSEG) {
                        __builtin_amdgcn_s_sleep(DZR);
                        const unsigned zq = zb == W_ZM ? W_ZM + 2u * NOWN * PAIR : zb - NOWN * PAIR;
                        unsigned s1;
                        asm volatile("v_mov_b32 %0, -1" : "=v"(s1));
                        st128(((u32x4){s1, s1, s1, s1}), rwin, zo0, zq, 16);
                        st128(((u32x4){s1, s1, s1, s1}), rwin, zo1, zq, 16);
                        __builtin_amdgcn_raw_buffer_store_b64((u32x2){s1, s1}, rwin, zo2, zq, 16);
                    }
                } else {
                    if (s > TAILS) __builtin_amdgcn_raw_buffer_store_b32((unsigned)s - 1u, rwin, 0u, W_FZ2 + fo + (unsigned)(hh * 128 + (w & 3) * 4), 16);
                    if (TK && __builtin_expect(s == 0, 0)) { wait_h(rwin, fo, 5, hrdy); clk_init(ck); }
                    if (TK && s + 1 < NSEG) sched2(tk, s, sf);
                    if (!TK) wait_until(tref + DZ2);
                    if (s + 2 < NSEG) put_z2(s);
                }
                if (budget <= 0) set_tmo(rwin);
                zb = zb == W_ZM + 2u * NOWN * PAIR ? W_ZM : zb + NOWN * PAIR;
                RT(5);
                RTS();
                MT(7);
                MTS();
            }
        };
        const int tid = mytid();
        gather_mem0(memL, tid);
        if (tid < 8) *(unsigned*)(smem + L_S + tid * 4) = 0u;
        if (w < 4) steps(IC<0>()); else if (w < 7) steps(IC<1>()); else steps(IC<3>());
    };
    auto vt_role = [&](const u32x8 (&wt)[2][10], int hh, unsigned long long mv) INL {
        constexpr bool vt = true;
        const int jl = 63 - __builtin_clzll(mv);
        auto nextv = [&](int j) { return min(seg_after(mv, j), jl); };
        const int jv1 = nextv(-1), jv2 = nextv(jv1);
        if (w == 0) *(unsigned*)(smem + L_S + (fresh_lane() & 7u) * 4) = 0u;
        stage2(jv1, jv2, true);
        {
            LANES();
            const int ni = __builtin_popcountll(mv) + 1;
            if (w < 4) {
                const LAS char* c0 = (const LAS char*)(hL + hoff + ((fq ^ (fr & 7)) << 4));
                const LAS char* c1 = (const LAS char*)(hL + hoff + (((4 + fq) ^ (fr & 7)) << 4));
                const LAS char* n0 = c0 + 40960; const LAS char* n1 = c1 + 40960;
                u32x4 bb[8];
                project0(bb, c0, c1);
                for (int i = 1; i < ni; ++i) {
                    if (vt) MT(0);
                    f32x4 tc[2][2];
                    project<0>(wt, tc, bb, c0, c1, n0, n1,
                        [&]() {
                            MT(1);
                            if (i == 1) SST(16);
                            asm volatile("s_waitcnt vmcnt(0) lgkmcnt(0)\n\ts_barrier" ::: "memory");
                            if (i == 1) SST(17);
                            MT(2);
                        }, [](auto) {});
                    { const LAS char* x = c0; c0 = n0; n0 = x; x = c1; c1 = n1; n1 = x; }
                    char* tb = tokL + (i & 1) * 32 * QS + (16 * w + 4 * fq) * 2;
#pragma unroll
                    for (int g = 0; g < 2; ++g)
#pragma unroll
                        for (int tt = 0; tt < 2; ++tt) *(u32x2*)(tb + (prow + 8 * tt) * QS + g * 128) = (u32x2){pack2(tc[g][tt][0], tc[g][tt][1]), pack2(tc[g][tt][2], tc[g][tt][3])};
                    asm volatile("" ::: "memory");
                    *(volatile LAS int*)(smem + L_S + 16 + w * 4) = i;
                    if (i == 1) SST(18);
                    if (__builtin_expect(i == 1, 0)) __builtin_amdgcn_s_sleep(VSL);
                    if (vt) { MT(3); const int s = i & 63; MTRT(); MTS(); }
                }
                project_end(bb);
            } else {
                __builtin_amdgcn_s_setprio(OPR);
                const unsigned hv = lane * 16u, fz_ = W_FZ + fo + fz_at(hh, w - 4) + (jv1 ? 4096u : 0u);
                unsigned fp = ~0u;
                unsigned fzp = 0x40000000u;
                int jo = jv1, jh = jv2;
                for (int i = 1; i < ni; ++i, jo = nextv(jo)) {
                    const int b = i + (i >= ZLAG);
                    if (__builtin_expect(i == 1 || i == ZLAG, 0)) {
                        VMWAIT();
                        if (i == ZLAG) {
                            jh = nextv(jh);
                            wait_h(rwin, fo, jh, hrdy);
                            BAR0();
                            for (int t = 0; t < 10; ++t) dma1k(rws, hv, hseg(jh) + (unsigned)((w - 4) * 1024 + t * 4096), (unsigned)(L_H + ((i + 1) & 1) * 40960 + (w - 4) * 1024 + t * 4096));
                            VMWAIT();
                        }
                    }
                    jh = nextv(jh);
                    if (b < ni) BAR0();
                    if (__builtin_expect(jo + layer == 0 && w == 7, 0)) near_init();
                    wait_h(rwin, fo, jh, hrdy);
                    if (i == 1) SST(19);
                    if (vt) MT(1);
                    for (;;) {
                        const u32x4 f = *(volatile LAS u32x4*)(smem + L_S + 16);
                        if (__builtin_amdgcn_readfirstlane(min(min(f[0], f[1]), min(f[2], f[3]))) >= (unsigned)i) break;
                        __builtin_amdgcn_s_sleep(1);
                    }
                    asm volatile("" ::: "memory");
                    if (i == 1) SST(20);
                    if (__builtin_expect(jo == 0 && w == 7, 0)) __builtin_amdgcn_raw_buffer_store_b32((4u * (unsigned)__builtin_amdgcn_s_memrealtime() + (layer ? DVT : DVT0)) & 0xFFFFFFu, rwin, two(), W_T + (unsigned)hh * 128u + 4u, 16);
                    if (vt) MT(0);
                    u32x4 vf[2][4];
#pragma unroll
                    for (int t = 0; t < 2; ++t)
#pragma unroll
                        for (int ks = 0; ks < 4; ++ks) vf[t][ks] = *(const u32x4*)(tokL + (i & 1) * 32 * QS + (16 * t + fr) * QS + ks * 64 + fq * 16);
                    const unsigned ztb = O_Z + (unsigned)((jo - layer) * (int)ZTSEG) * 2u + (unsigned)(10 * (w - 4) * H_ + hh) * 1024u;
                    f32x4 c[2][2];
                    u32x4 r;
                    unsigned hs = hseg(jh) + (unsigned)((w - 4) * 1024), hd = (unsigned)(L_H + ((b + 1) & 1) * 40960 + (w - 4) * 1024);
                    asm volatile("" : "+s"(hs), "+s"(hd));
#pragma unroll
                    for (int nt = 0; nt < 11; ++nt) {
                        auto& cc = c[nt & 1]; auto& cp = c[(nt + 1) & 1];
#pragma unroll
                        for (int ks = 0; ks < 4; ++ks) {
                            if (nt < 10) { cc[0] = mfma32(vf[0][ks], half8(WO(ks >> 1, nt < 10 ? nt : 0), ks & 1), ks ? cc[0] : z4); PIN1(cc[0]); }
                            if (nt > 0 && ks >= 1 && ks < 3) { r[2 * ks - 2] = pack2(cp[ks - 1][0], cp[ks - 1][1]); PIN1(r); }
                            if (nt < 10) { cc[1] = mfma32(vf[1][ks], half8(WO(ks >> 1, nt < 10 ? nt : 0), ks & 1), ks ? cc[1] : z4); PIN1(cc[1]); }
                            if (nt > 0 && ks >= 1 && ks < 3) { r[2 * ks - 1] = nosent1(pack2(cp[ks - 1][2], cp[ks - 1][3])); PIN1(r); }
                            if (nt > 0 && ks == 2) st128(r, rws, lane * 16u, ztb + (unsigned)((nt - 1) * H_) * 1024u, 16);
                            if (nt < 5 && !(ks & 1)) dma1k(rws, hv, hs + (2 * nt + (ks >> 1)) * 4096u, hd + (2 * nt + (ks >> 1)) * 4096u);
                            if (nt == 5 && ks == 0) {
                                asm volatile("s_waitcnt vmcnt(14)" ::: "memory");
                                __builtin_amdgcn_raw_buffer_store_b32(fp, rwin, 0u, fzp, 16);
                            }
                            SB();
                        }
                    }
                    if (vt) MT(2);
                    if (i == 1) SST(21);
                    fp = (unsigned)jo;
                    fzp = fz_;
                    if (__builtin_expect(jo <= ZEARLY || i + 1 == ni, 0)) {
                        VMWAIT();
                        __builtin_amdgcn_raw_buffer_store_b32(fp, rwin, 0u, fz_, 16);
                    } else asm volatile("s_waitcnt vmcnt(7)" ::: "memory");
                    if (jo < 8) SST(8 + jo);
                    if (vt && (!BSPARSE || jo % 6 == 2)) { const int s = jo; RT(0); RTS(); }
                    if (vt) { MT(3); const int s = i & 63; MTRT(); MTS(); }
                }
            }
            __builtin_amdgcn_s_setprio(0);
            VMWAIT();
            BAR();
        }
    };
    auto qt_role = [&](const u32x8 (&wt)[2][10], int hh, auto qq) INL {
        constexpr bool qt = decltype(qq)::v;
        const unsigned long long mq = bid < NQT ? MXQ : ~MXQ;
        const int jl = 63 - __builtin_clzll(mq);
        auto nextq = [&](int j) { return min(seg_after(mq, j), jl); };
        const int jq0 = qt ? nextq(0) : 0, jq1 = qt ? nextq(jq0) : 0;
        stage2(jq0, jq1, !qt);
        auto check2 = [&]() INL { VMWAIT(); check_h<5>(rws, rwin, hseg(jq1) + w * 1024u, L_H + 40960u + w * 1024u, 8192u); };
        {
            LANES();
            const unsigned to0 = (unsigned)((w ^ 4) >> 2) * 8192u + (unsigned)((w & 3) * 64 + (lane ^ ((fq & 1) * (w < 4 ? 8 : 5)))) * 16u, to1 = to0 + 4096u, hv = lane * 16u;
            const LAS char* c0 = (const LAS char*)(hL + hoff + ((fq ^ (fr & 7)) << 4));
            const LAS char* c1 = (const LAS char*)(hL + hoff + (((4 + fq) ^ (fr & 7)) << 4));
            u32x4 bb[8], g0 = SENT, g1 = SENT;
            f32x4 tc[2][2];
            auto pack = [&]() {
                g0 = (u32x4){pack2(tc[0][0][0], tc[0][0][1]), pack2(tc[0][0][2], tc[0][0][3]), pack2(tc[1][0][0], tc[1][0][1]), pack2(tc[1][0][2], tc[1][0][3])};
                g1 = (u32x4){pack2(tc[0][1][0], tc[0][1][1]), pack2(tc[0][1][2], tc[0][1][3]), pack2(tc[1][1][0], tc[1][1][1]), pack2(tc[1][1][2], tc[1][1][3])};
            };
            const LAS char* n0 = c0 + 40960; const LAS char* n1 = c1 + 40960;
            if (!qt) {
                project0(bb, c0, c1);
                project<0>(wt, tc, bb, c0, c1, c0, c1, []() {}, [](auto) {});
                pack();
                *(u32x4*)(tokL + to0) = g0; *(u32x4*)(tokL + to1) = g1;
                project_end(bb);
            } else {
                project0(bb, c0, c1);
                unsigned hs = hseg(jq1) + (unsigned)(w * 1024), hd = (unsigned)(L_H + 40960 + w * 1024), fv = lane == 0 ? 0u : 0x40000000u, fj = 0;
                const unsigned fq_ = W_FQ + fo + (unsigned)(hh * 128 + (jq0 > 1 ? 4096 : 0) + w * 4);
                for (int j = jq0, jn = jq1;; j = jn, jn = nextq(jn)) {
                    MT(0);
                    project<0>(wt, tc, bb, c0, c1, n0, n1,
                        [&]() {
                            if (__builtin_expect(j == jq0, 0)) check2();
                            wait_h(rwin, fo, nextq(jn), hrdy);
                            hs = hseg(nextq(jn)) + (unsigned)(w * 1024); hd = (unsigned)(2 * L_H + 40960 + 2 * w * 1024) - hd;
                            asm volatile("" : "+s"(hs), "+s"(hd));
                            MT(1);
                            VMWAIT();
                            BAR();
                            MT(2);
                        },
                        [&](auto uu) {
                            constexpr int u = decltype(uu)::v;
                            if (u == 34 || u == 38 || u == 2 || u == 6 || u == 10) { constexpr int t = ((u + 6) % 40) / 4; dma1k(rws, hv, hs + t * 8192u, hd + t * 8192u); }
                            if (u == UFQ) { asm volatile("s_waitcnt vmcnt(3)" ::: "memory"); __builtin_amdgcn_raw_buffer_store_b32(fj, rwin, fv, fq_, 16); asm volatile("" ::: "memory"); }
                        });
                    pack();
                    const unsigned qs = O_QK + (unsigned)(j * H_ + hh) * 16384u;
                    st128(g0, rws, to0, qs, 16);
                    st128(g1, rws, to1, qs, 16);
                    { const LAS char* x = c0; c0 = n0; n0 = x; x = c1; c1 = n1; n1 = x; }
                    fj = (unsigned)j;
                    PIN1(fj);
                    if (__builtin_expect(j <= QEARLY || j == jl, 0)) {
                        VMWAIT();
                        __builtin_amdgcn_raw_buffer_store_b32(fj, rwin, fv, fq_, 16);
                        if (j == jl) break;
                    }
                    MT(3);
                    if (j < 8) SST(8 + j);
                    if (!BSPARSE || j % 6 == 2) { const int s = j; RT(0); RTS(); MTRT(); MTS(); }
                }
                project_end(bb);
                VMWAIT();
                BAR0();
            }
        }
        SST(4);
    };
    auto qk_role = [&](const u32x8 (&wt)[2][10]) INL {
        const int hh = bid;
        int qrdy = 0;
        auto steps = [&](auto cc) INL {
        constexpr int C = decltype(cc)::v >> 1, R = decltype(cc)::v & 1;
        LANES();
        const LAS char* ka = (const LAS char*)(smem + L_K + (fr & 1) * TQS + (4 * fq + (fr >> 2)) * 8);
        const LAS char* qa = (const LAS char*)(smem + L_Q + (fr & 1) * 544 + ((fr >> 1) & 1) * 1152 + (16 * fq + 2 * (fr >> 2)) * 4);
        LAS char* qw = (LAS char*)(C < 2 ? smem + L_Q + (fq & 1) * 544 + (fq >> 1) * 1152 + (32 * w + 8 * (fr >> 2) + bs2(fr & 3)) * 4
                                         : smem + L_K + (fq >> 1) * TQS + (32 * (w & 3) + fr) * 8 + (fq & 1) * 4);
        const LAS char* mp0 = (const LAS char*)(memL + ma0);
        const int z = 16 * (fr >> 3) + 4 * (fr & 3) + 2 * ((fr >> 2) & 1);
        const int ko = ((z >> 4) * 256 + fq * 16 + ((z & 15) ^ ((fq & 1) * 5))) * 16;
        unsigned pa0 = 2u * (hh >> 3) * PAIR + (unsigned)(C * 4096 + (hh & 7) * 512 + R * 256 + lane * 4);
        unsigned pa2 = ((8u + (fr >> 3)) * PAIR + C * 4096u + (unsigned)((hh >> 1) * 256 + R * 128 + (hh & 1) * 64 + fq * 16 + (fr & 6) * 2)) | (fr & 1) << 30;
        PIN2(ka, qw); PIN1(mp0);
        constexpr bool TK = C == 2 && R == 1;
        Tk tk = {0u, 0u, 0x40000000u, nullptr};
        if (TK) { tk = tk_init(); __builtin_amdgcn_s_setprio(TKP); }
        if (C < 2) { PIN2(qa, pa0); PIN1(pa2); }
        int qf_ = 0;
        int tbo = 0;
        for (int s = 0; s < NSEG; ++s) {
            RT(0);
            const char* tb = tokL + tbo;
            unsigned pb = __builtin_amdgcn_readfirstlane(s + 1 < NSEG ? p_slot(s) + nh : 0x40000000u);
            asm volatile("" : "+s"(pb));
            bool redo = false;
            unsigned sf = 0;
            if (s <= 1) BAR();
            u32x8 kx[2][2];
            if (C < 2) {
#pragma unroll
                for (int u = 0; u < 4; ++u) kx[u >> 1][u & 1] = cat8(*(const u32x4*)(tb + (ko ^ ((u >> 1) * 16)) + (u & 1) * 2048), *(const u32x4*)(tb + (ko ^ ((u >> 1) * 16)) + (u & 1) * 2048 + 1024));
                PIN2(kx[0][0], kx[0][1]); PIN2(kx[1][0], kx[1][1]);
            }
            do {
                ++it;
                f32x4 a0 = zero4(), a1 = zero4();
                PIN2(a0, a1);
                u32x4 m[10];
                if (C < 2) {
                    __builtin_amdgcn_s_setprio(2);
                    asm volatile(ALN ::: "memory");
                    BAR();
                    MT(0);
#pragma unroll
                    for (int t = 0; t < NB1; ++t) { m[t] = *(const LAS u32x4*)(mp0 + t * 1024); SB(); }
                    BAR0();
#pragma unroll
                    for (int t = 0; t < 10; ++t) {
                        if (t == QB2) { BAR0(); MT(1); }
                        if (t + NB1 < 10) m[t + NB1] = *(const LAS u32x4*)(mp0 + (t + NB1) * 1024);
                        SB();
                        a0 = smfmac(m[t], wt[0][t], a0, sidx); a1 = smfmac(m[t], wt[1][t], a1, sidx);
                        PIN2(a0, a1);
                    }
                } else {
                    unsigned mx = 0;
                    asm volatile("" : "+{v[200:203]}"(a0), "+{v[204:207]}"(a1), "+{v208}"(mx));
                    unsigned lim = s > 0 ? poll_lim(budget) : ~0u, sr = tro + (unsigned)(s & 3) * 2u * PAIR;
                    asm volatile("" : "+s"(lim), "+s"(sr));
                    if (TK) fetch(tk, s, !redo, lim, DQC, (unsigned)hh * 128u, true);
                    else asm volatile(ALN "s_waitcnt lgkmcnt(0)\n\ts_barrier\n\ts_barrier\n\ts_barrier" ::: "memory");
                    asm volatile(
                        "ds_read_b128 v[212:215], %[mp] offset:0\n\t"
                        "ds_read_b128 v[216:219], %[mp] offset:1024\n\t"
                        "ds_read_b128 v[220:223], %[mp] offset:2048\n\t"
                        "ds_read_b128 v[224:227], %[mp] offset:3072\n\t"
                        "ds_read_b128 v[228:231], %[mp] offset:4096\n\t"
                        "ds_read_b128 v[232:235], %[mp] offset:5120\n\t"
                        "ds_read_b128 v[236:239], %[mp] offset:6144\n\t"
                        "ds_read_b128 v[240:243], %[mp] offset:7168\n\t"
                        "ds_read_b128 v[244:247], %[mp] offset:8192\n\t"
                        "ds_read_b128 v[248:251], %[mp] offset:9216\n\t"
                        MTA(4)
                        "s_waitcnt lgkmcnt(9)\n\t"
                        "v_smfmac_f32_16x16x64_bf16 v[200:203], v[212:215], %[w00], %[ix]\n\t"
                        "v_max3_u32 v208, v208, v212, v213\n\t"
                        "v_smfmac_f32_16x16x64_bf16 v[204:207], v[212:215], %[w10], %[ix]\n\t"
                        "s_waitcnt lgkmcnt(8)\n\t"
                        "v_max3_u32 v208, v208, v214, v215\n\t"
                        "v_smfmac_f32_16x16x64_bf16 v[200:203], v[216:219], %[w01], %[ix]\n\t"
                        "v_max3_u32 v208, v208, v216, v217\n\t"
                        "v_smfmac_f32_16x16x64_bf16 v[204:207], v[216:219], %[w11], %[ix]\n\t"
                        "s_waitcnt lgkmcnt(7)\n\t"
                        "v_max3_u32 v208, v208, v218, v219\n\t"
                        "v_smfmac_f32_16x16x64_bf16 v[200:203], v[220:223], %[w02], %[ix]\n\t"
                        "v_max3_u32 v208, v208, v220, v221\n\t"
                        "v_smfmac_f32_16x16x64_bf16 v[204:207], v[220:223], %[w12], %[ix]\n\t"
                        "s_waitcnt lgkmcnt(6)\n\t"
                        "v_max3_u32 v208, v208, v222, v223\n\t"
                        "v_smfmac_f32_16x16x64_bf16 v[200:203], v[224:227], %[w03], %[ix]\n\t"
                        "v_max3_u32 v208, v208, v224, v225\n\t"
                        "v_smfmac_f32_16x16x64_bf16 v[204:207], v[224:227], %[w13], %[ix]\n\t"
                        "s_waitcnt lgkmcnt(0)\n\t"
                        "v_max3_u32 v208, v208, v226, v227\n\t"
                        "v_smfmac_f32_16x16x64_bf16 v[200:203], v[228:231], %[w04], %[ix]\n\t"
                        "v_max3_u32 v208, v208, v228, v229\n\t"
                        "v_smfmac_f32_16x16x64_bf16 v[204:207], v[228:231], %[w14], %[ix]\n\t"
                        "v_max3_u32 v208, v208, v230, v231\n\t"
                        "v_smfmac_f32_16x16x64_bf16 v[200:203], v[232:235], %[w05], %[ix]\n\t"
                        "v_max3_u32 v208, v208, v232, v233\n\t"
                        "v_smfmac_f32_16x16x64_bf16 v[204:207], v[232:235], %[w15], %[ix]\n\t"
                        "v_max3_u32 v208, v208, v234, v235\n\t"
                        "v_smfmac_f32_16x16x64_bf16 v[200:203], v[236:239], %[w06], %[ix]\n\t"
                        "v_max3_u32 v208, v208, v236, v237\n\t"
                        "v_smfmac_f32_16x16x64_bf16 v[204:207], v[236:239], %[w16], %[ix]\n\t"
                        "v_max3_u32 v208, v208, v238, v239\n\t"
                        "v_smfmac_f32_16x16x64_bf16 v[200:203], v[240:243], %[w07], %[ix]\n\t"
                        "v_max3_u32 v208, v208, v240, v241\n\t"
                        "v_smfmac_f32_16x16x64_bf16 v[204:207], v[240:243], %[w17], %[ix]\n\t"
                        "v_max3_u32 v208, v208, v242, v243\n\t"
                        "v_smfmac_f32_16x16x64_bf16 v[200:203], v[244:247], %[w08], %[ix]\n\t"
                        "v_max3_u32 v208, v208, v244, v245\n\t"
                        "v_smfmac_f32_16x16x64_bf16 v[204:207], v[244:247], %[w18], %[ix]\n\t"
                        "v_max3_u32 v208, v208, v246, v247\n\t"
                        "v_smfmac_f32_16x16x64_bf16 v[200:203], v[248:251], %[w09], %[ix]\n\t"
                        "v_max3_u32 v208, v208, v248, v249\n\t"
                        "v_smfmac_f32_16x16x64_bf16 v[204:207], v[248:251], %[w19], %[ix]\n\t"
                        "v_max3_u32 v208, v208, v250, v251\n\t"
                        "buffer_load_dword %[sf], %[sl], %[rw], %[sr] offen sc1\n\t"
                        "v_cmp_lt_u32 vcc, %[lim], v208\n\t"
                        "s_nop 3\n\t"
                        "v_pk_add_f32 v[200:201], v[200:201], v[202:203]\n\t"
                        "v_pk_add_f32 v[204:205], v[204:205], v[206:207]\n\t"
                        "v_cvt_pk_bf16_f32 v200, v200, v201\n\t"
                        "v_cvt_pk_bf16_f32 v204, v204, v205\n\t"
                        "ds_write2_b32 %[qw], v200, v204 offset1:32\n\t"
                        MTA(2)
                        "s_cbranch_vccz 1f\n\t"
                        "v_mov_b32 v208, %[it]\n\t"
                        "v_mov_b32 v209, 0\n\t"
                        "ds_write_b32 v209, v208 offset:%[ls]\n\t"
                        "1:\n\t"
                        "s_waitcnt vmcnt(%[nv]) lgkmcnt(0)\n\t"
                        "s_barrier\n\t"
                        "s_waitcnt vmcnt(0)\n\t"
                        : [sf] "=&v"(sf), "+v"(qf_), "+{v[200:203]}"(a0), "+{v[204:207]}"(a1), "+{v208}"(mx)
                        : [mp] "v"(mp0), [qw] "v"(qw), [ix] "v"(sidx), [lim] "s"(lim), [it] "s"(it), [ls] "n"(L_S), [sl] "v"(tk.sl), [sr] "s"(sr), [rw] "s"(rwin), [nv] "n"(TK ? 2 : 1),
                          [w00] "v"(wt[0][0]), [w01] "v"(wt[0][1]), [w02] "v"(wt[0][2]), [w03] "v"(wt[0][3]), [w04] "v"(wt[0][4]), [w05] "v"(wt[0][5]), [w06] "v"(wt[0][6]), [w07] "v"(wt[0][7]), [w08] "v"(wt[0][8]), [w09] "v"(wt[0][9]), [w10] "v"(wt[1][0]), [w11] "v"(wt[1][1]), [w12] "v"(wt[1][2]), [w13] "v"(wt[1][3]), [w14] "v"(wt[1][4]), [w15] "v"(wt[1][5]), [w16] "v"(wt[1][6]), [w17] "v"(wt[1][7]), [w18] "v"(wt[1][8]), [w19] "v"(wt[1][9])
                        : "v209", "v212", "v213", "v214", "v215", "v216", "v217", "v218", "v219", "v220", "v221", "v222", "v223", "v224", "v225", "v226", "v227", "v228", "v229", "v230", "v231", "v232", "v233", "v234", "v235", "v236", "v237", "v238", "v239", "v240", "v241", "v242", "v243", "v244", "v245", "v246", "v247", "v248", "v249", "v250", "v251", "vcc", "memory");
                }
                if (C < 2) {
                    *(LAS unsigned*)qw = hpack(a0); *(LAS unsigned*)(qw + 16) = hpack(a1);
                    SB();
                    MT(2);
                    unsigned vd;
#define SC0 "v[212:215]"
#define SC1 "v[216:219]"
#define SC2 "v[220:223]"
#define DPP(op, d, ctl) op " " d ", " d ", " d " " ctl " row_mask:0xf bank_mask:0xf\n\t"
#define TAIL(A0, A2, B0, B2, C0, C2) \
                        "v_mov_b64 v[212:213], 0\n\tv_mov_b64 v[214:215], 0\n\tv_mov_b64 v[216:217], 0\n\tv_mov_b64 v[218:219], 0\n\tv_mov_b64 v[220:221], 0\n\tv_mov_b64 v[222:223], 0\n\t" \
                        "s_setprio 3\n\t" \
                        "s_waitcnt lgkmcnt(0)\n\t" \
                        "s_barrier\n\t" MTA(3) \
                        "ds_read_b64_tr_b16 v[224:225], %[qa]\n\t" \
                        "ds_read_b64_tr_b16 v[226:227], %[qa] offset:32\n\t" \
                        "ds_read_b64_tr_b16 v[228:229], %[qa] offset:256\n\t" \
                        "ds_read_b64_tr_b16 v[230:231], %[qa] offset:288\n\t" \
                        "ds_read_b64_tr_b16 v[232:233], %[ka]\n\t" \
                        "ds_read_b64_tr_b16 v[234:235], %[ka] offset:128\n\t" \
                        "ds_read_b64_tr_b16 v[236:237], %[ka] offset:256\n\t" \
                        "ds_read_b64_tr_b16 v[238:239], %[ka] offset:384\n\t" \
                        "ds_read_b64_tr_b16 v[240:241], %[ka] offset:512\n\t" \
                        "ds_read_b64_tr_b16 v[242:243], %[ka] offset:640\n\t" \
                        "s_waitcnt lgkmcnt(8)\n\t" \
                        "v_smfmac_f32_16x16x64_bf16 " SC0 ", v[224:227], %[k00], %[ix]\n\t" \
                        "ds_read_b64_tr_b16 v[244:245], %[ka] offset:768\n\t" \
                        "v_smfmac_f32_16x16x64_bf16 " SC1 ", v[224:227], %[k10], %[ix]\n\t" \
                        "ds_read_b64_tr_b16 v[246:247], %[ka] offset:896\n\t" \
                        "s_waitcnt lgkmcnt(8)\n\t" \
                        "v_smfmac_f32_16x16x64_bf16 " SC0 ", v[228:231], %[k01], %[ix]\n\t" \
                        "ds_read_b32 v248, %[vz] offset:%[ls]\n\t" \
                        "v_smfmac_f32_16x16x64_bf16 " SC1 ", v[228:231], %[k11], %[ix]\n\t" \
                        "s_waitcnt lgkmcnt(5)\n\t" \
                        "v_smfmac_f32_16x16x64_bf16 " SC2 ", v[224:227], v[232:239], %[ix]\n\t" \
                        "s_waitcnt lgkmcnt(1)\n\t" \
                        "v_smfmac_f32_16x16x64_bf16 " SC2 ", v[228:231], v[240:247], %[ix]\n\t" \
                        "s_waitcnt lgkmcnt(0)\n\t" \
                        MTA(4) "v_readfirstlane_b32 %[vd], v248\n\t" \
                        "v_add_f32 v232, " A0 ", " A2 "\n\t" \
                        "s_nop 0\n\t" \
                        "v_add_f32 v233, " B0 ", " B2 "\n\t" \
                        "v_max_f32 v235, v232, v233\n\t" \
                        "s_cmp_eq_u32 %[vd], %[it]\n\t" \
                        "s_cselect_b32 %[p0], 2.0, %[p0]\n\t" \
                        DPP("v_max_f32_dpp", "v235", "row_ror:8") \
                        "v_add_f32 v234, " C0 ", " C2 "\n\t" \
                        "v_max_f32 v235, v235, v234\n\t" \
                        "s_add_u32 %[p1], %[p0], 0x4000\n\t" \
                        "s_nop 0\n\t" DPP("v_max_f32_dpp", "v235", "row_ror:4") \
                        "s_nop 1\n\t" DPP("v_max_f32_dpp", "v235", "row_ror:2") \
                        "s_nop 1\n\t" DPP("v_max_f32_dpp", "v235", "row_ror:1") \
                        "v_mul_f32 v235, 0xbe0293ee, v235\n\t" \
                        "v_fmamk_f32 v232, v232, 0x3e0293ee, v235\n\t" \
                        "v_fmamk_f32 v233, v233, 0x3e0293ee, v235\n\t" \
                        "v_exp_f32 v232, v232\n\t" \
                        "v_exp_f32 v233, v233\n\t" \
                        "v_fmamk_f32 v234, v234, 0x3e0293ee, v235\n\t" \
                        "v_add_f32 v235, v232, v233\n\t" \
                        "v_exp_f32 v234, v234\n\t" \
                        "s_nop 0\n\t" DPP("v_add_f32_dpp", "v235", "row_ror:8") \
                        "v_add_f32 v235, v235, v234\n\t" \
                        "v_mov_b32_dpp v236, v234 quad_perm:[1,1,3,3] row_mask:0xf bank_mask:0xf\n\t" \
                        "s_nop 0\n\t" DPP("v_add_f32_dpp", "v235", "row_ror:4") \
                        "s_nop 1\n\t" DPP("v_add_f32_dpp", "v235", "row_ror:2") \
                        "s_nop 1\n\t" DPP("v_add_f32_dpp", "v235", "row_ror:1") \
                        "v_rcp_f32 v235, v235\n\t" \
                        "s_nop 0\n\t" \
                        "v_mul_f32_e64 v232, v232, v235 clamp\n\t" \
                        "v_mul_f32_e64 v233, v233, v235 clamp\n\t" \
                        "v_cvt_pk_bf16_f32 v232, v232, v233\n\t" \
                        MTA(5) "buffer_store_dword v232, %[pa], %[rw], %[p0] offen sc1\n\t" \
                        "v_mul_f32_e64 v234, v234, v235 clamp\n\t" \
                        "v_mul_f32_e64 v236, v236, v235 clamp\n\t" \
                        "buffer_store_dword v232, %[pa], %[rw], %[p1] offen sc1\n\t" \
                        "v_cvt_pk_bf16_f32 v234, v234, v236\n\t" \
                        "buffer_store_dword v234, %[pm], %[rw], %[p0] offen sc1\n\t" \
                        "s_setprio 0"
#define TAILIO : [vd] "=&s"(vd), [p0] "+s"(ps), [p1] "=&s"(ps1) \
                        : [qa] "v"(qa), [ka] "v"(ka), [vz] "v"(0u), [ls] "n"(L_S), [ix] "v"(sidx), [it] "s"(it), [pa] "v"(pa0), [pm] "v"(pa2), [rw] "s"(rwin), \
                          [k00] "v"(kx[0][0]), [k01] "v"(kx[0][1]), [k10] "v"(kx[1][0]), [k11] "v"(kx[1][1]) \
                        : "v212", "v213", "v214", "v215", "v216", "v217", "v218", "v219", "v220", "v221", "v222", "v223", "v224", "v225", "v226", "v227", "v228", "v229", "v230", "v231", "v232", \
                          "v233", "v234", "v235", "v236", "v237", "v238", "v239", "v240", "v241", "v242", "v243", "v244", "v245", "v246", "v247", "v248", "scc", "memory"
                    unsigned ps = pb, ps1;
                    if (R == 0) asm volatile(TAIL("v212", "v214", "v216", "v218", "v220", "v222") TAILIO);
                    else asm volatile(TAIL("v213", "v215", "v217", "v219", "v221", "v223") TAILIO);
                    redo = vd == it;
                    RT(4);
                    MT(6);
                } else {
                    MT(3);
                    if (TK) { __builtin_amdgcn_s_setprio(TKP); if (TKS) __builtin_amdgcn_s_sleep(TKS); }
                    redo = __builtin_amdgcn_readfirstlane(*(volatile LAS unsigned*)(smem + L_S)) == it;
                    if (redo) FC(fc0);
                }
            } while (__builtin_expect(redo, 0));
            if (s < 8) SST(8 + s);
            if (s >= 56) SST(s - 40);
            if (C + R == 1) {
                if (s + layer > 0) {
                    __builtin_amdgcn_s_sleep(DDMA);
                    unsigned l2 = fresh_lane(), s1;
                    asm volatile("v_mov_b32 %0, -1" : "=v"(s1));
                    const unsigned base = p_slot(s - 1) + nh;
                    asm volatile("s_waitcnt vmcnt(3)" ::: "memory");
                    st128(((u32x4){s1, s1, s1, s1}), rwin, (l2 >> 5) * PAIR + (l2 & 31u) * 16u, base + (unsigned)(2 * (hh >> 3)) * PAIR + (unsigned)(R * 4096 + (hh & 7) * 512), 16);
                    if (R) __builtin_amdgcn_raw_buffer_store_b64((u32x2){s1, s1}, rwin, (l2 >> 5) * PAIR + (l2 & 16u) * 256u + (l2 & 8u) * 16u + (l2 & 7u) * 8u, base + 8u * PAIR + (unsigned)((hh >> 1) * 256 + (hh & 1) * 64), 16);
                    else __builtin_amdgcn_raw_buffer_store_b64((u32x2){s1, s1}, rwin, two(), W_T + (unsigned)((s + 2) & 3) * 2u * PAIR + (unsigned)hh * 128u, 16);
                }
            } else if (TK) {
                if (s + 1 < NSEG) sched2(tk, s, sf);
            } else if (C == 2 && w == 4) {
                if (s + 2 < NSEG) {
                    auto done = [&](int f) INL {
                        f = min(f, __builtin_amdgcn_update_dpp(f, f, 0xB1, 0xF, 0xF, false)); f = min(f, __builtin_amdgcn_update_dpp(f, f, 0x4E, 0xF, 0xF, false));
                        f = min(f, __builtin_amdgcn_update_dpp(f, f, 0x141, 0xF, 0xF, false));
                        return seg_done(MXQ, __builtin_amdgcn_readlane(f, 0), __builtin_amdgcn_readlane(f, 8));
                    };
                    qrdy = max(qrdy, done(qf_));
                    const int sq = s + 2;
                    if (__builtin_expect(s == 0, 0)) __builtin_amdgcn_s_sleep(DQ0);
                    if (__builtin_expect(qrdy < sq, 0)) for (unsigned spins = 0;;) {
                        asm volatile("" ::: "memory");
                        const int f = (int)__builtin_amdgcn_raw_buffer_load_b32(rwin, (unsigned)((lane & 8) * 512 + (lane & 7) * 4), W_FQ + fo + (unsigned)hh * 128u, 16);
                        qrdy = done(f);
                        if (qrdy >= sq) break;
                        FC(fc2);
                        if (spin_fail(spins, rwin)) break;
                        __builtin_amdgcn_s_sleep(QSL);
                    }
                    const unsigned so = O_QK + (unsigned)(((s + 2) * H_ + hh) * 16384), lo = (unsigned)(L_TOK + (tbo ? tbo - 16384 : 32768)), l2 = fresh_lane();
                    if (__builtin_expect(s == 0, 0)) {
                        for (int i = 0; i < 4; ++i) dma4k(rws, l2 * 16u, so - H_ * 16384u + i * 4096u, lo - 16384u + i * 4096u);
                    }
#pragma unroll
                    for (int i = 0; i < 4; ++i) dma4k(rws, l2 * 16u, so + i * 4096u, lo + i * 4096u);
                    if (__builtin_expect(s == 0, 0)) asm volatile("s_waitcnt vmcnt(16)" ::: "memory");
                    if (qrdy < min(s + 5, NSEG - 1))
                        asm volatile("buffer_load_dword %0, %1, %2, %3 offen sc1" : "+v"(qf_) : "v"((l2 & 8u) * 512u + (l2 & 7u) * 4u), "s"(rwin), "s"(W_FQ + fo + (unsigned)hh * 128u) : "memory");
                }
            } else {
                asm volatile("s_waitcnt vmcnt(3)" ::: "memory");
                if (s > TAILS) __builtin_amdgcn_raw_buffer_store_b32((unsigned)s - 1u, rwin, 0u, W_FPT + fo + (unsigned)(hh * 128 + (w & 3) * 4), 16);
                if (C == 2 && s + 1 < NSEG) __builtin_amdgcn_s_sleep(DPT);
                const float scale = 0.08838834764831845f, l2e = 1.44269504088896340736f * (NORND ? scale : 1.f);
                const int tq = (w >> 1) & 1, qh = w & 1;
                f32x4 sc[3] = {z4, z4, z4};
                u32x4 kt[2][4], qf[4], kf[4];
                const unsigned l2 = fresh_lane(), kto = ((l2 & 8u) * 512u + (l2 & 51u) * 16u + (l2 & 4u) * 32u) ^ ((l2 & 16u) * 5u), qto = ((l2 & 55u) * 16u + qh * 128u) ^ ((l2 & 16u) * 8u);
#pragma unroll
                for (int ks = 0; ks < 4; ++ks) {
                    qf[ks] = *(const u32x4*)(tb + 8192 + (tq * 4 + ks) * 1024 + qto);
                    kt[0][ks] = *(const u32x4*)(tb + ks * 1024 + kto);
                    kt[1][ks] = *(const u32x4*)(tb + ks * 1024 + (kto ^ 64u));
                    SB();
                }
#pragma unroll
                for (int ks = 0; ks < 4; ++ks) {
                    sc[0] = mfma32(kt[0][ks], qf[ks], sc[0]); PIN1(sc[0]);
                    kf[ks] = ldtr2(ka + ks * 256, ka + ks * 256 + 128);
                    SB();
                    sc[1] = mfma32(kt[1][ks], qf[ks], sc[1]); PIN1(sc[1]);
                }
#pragma unroll
                for (int ks = 0; ks < 4; ++ks) sc[2] = mfma32(kf[ks], qf[ks], sc[2]);
                const bool hi = (l2 & 8u) != 0u;
                float v[6];
#pragma unroll
                for (int r = 0; r < 4; ++r) v[r] = hi ? sc[1][r] : sc[0][r];
                v[4] = hi ? sc[2][2] : sc[2][0]; v[5] = hi ? sc[2][3] : sc[2][1];
#pragma unroll
                for (int r = 0; r < 6; r += 2) {
                    if (NORND) break;
                    const unsigned b0 = pack2(v[r], v[r + 1]);
                    const unsigned b1 = pack2(lo2f(b0) * scale, hi2f(b0) * scale);
                    v[r] = lo2f(b1); v[r + 1] = hi2f(b1);
                }
                const int lim = 8 * (w & 3) + (int)(l2 & 7u) - 8 * (int)(l2 >> 4) - (int)((l2 & 8u) >> 1);
#pragma unroll
                for (int r = 0; r < 4; ++r) if (r > lim) v[r] = NORND ? -1000000000.0f / scale : bfr(-1000000000.0f);
                float mx = fmx(fmx(fmx(fmx(fmx(v[0], v[1]), v[2]), v[3]), v[4]), v[5]);
                mx = xmax3216(fmx(mx, ror8f(mx)));
                const float mb = -mx * l2e;
#pragma unroll
                for (int r = 0; r < 6; ++r) v[r] = __builtin_amdgcn_exp2f(fmaf(v[r], l2e, mb));
                float sum = fmaf(v[4] + v[5], 0.5f, (v[0] + v[1]) + (v[2] + v[3]));
                sum = xsum3216(sum + ror8f(sum));
                const float inv = __builtin_amdgcn_rcpf(sum);
                const unsigned p0 = pack2(v[0] * inv, v[1] * inv), p1 = nosent1(pack2(v[2] * inv, v[3] * inv)), p2 = nosent1(pack2(v[4] * inv, v[5] * inv));
                const unsigned base = (s + 2 < NSEG ? O_PT : W_PL + fo - (NSEG - 2) * PTSEG * 2u) + (unsigned)(s * PTSEG) * 2u;
                const __amdgpu_buffer_rsrc_t rp = s + 2 < NSEG ? rws : rwin;
                const unsigned oob = (l2 & 8u) << 27;
                st128(((u32x4){p0, p1, dppu<0x128>(p0), dppu<0x128>(p1)}), rp, (l2 * 16u) | oob, base + (unsigned)((hh * 2 + tq) * 1024 + qh * 128), 16);
                __builtin_amdgcn_raw_buffer_store_b64((u32x2){p2, dppu<0x128>(p2)}, rp, ((l2 & 7u) * 16u + (l2 >> 4) * 8u) | oob | (l2 & 32u) << 25, base + 65536u + (unsigned)((tq * 32 + hh) * 256 + qh * 128), 16);
            }
            tbo = tbo == 32768 ? 0 : tbo + 16384;
            if (C == 2 && __builtin_expect(budget <= 0, 0)) set_tmo(rwin);
            RT(5);
            RTS();
            MT(7);
            MTS();
        }
        };
        if (w) gather_mem0(memL, mytid() - 64, 448);
        else *(unsigned*)(smem + L_S) = 0u;
        if (layer == 0) near_init();
        qt_role(wt, hh, IC<0>());
        if (w == 7) { ck.ta = 4u * (unsigned)__builtin_amdgcn_s_memrealtime() + 8u; ck.ma = (unsigned)__builtin_amdgcn_s_memtime(); }
        if (w == 0) steps(IC<0>()); else if (w == 1) steps(IC<1>()); else if (w == 2) steps(IC<2>()); else if (w == 3) steps(IC<3>()); else if (w < 7) steps(IC<4>()); else steps(IC<5>());
    };
    auto own_role = [&]() INL {
        const unsigned long long tstart = __builtin_amdgcn_s_memtime();
        if (layer == 0) near_init();
        LANES();
        const int o = bid - 96, hg = (w + ROT * o) & 3, ot = tile_of(o);
        const unsigned ztoff = (unsigned)((o * H_ + 8 * hg) * 64 + lane) * 16u;
        const unsigned zoff = zpair(o / 10, o % 10) + (unsigned)(lane * 16);
        const unsigned poff = (2u * hg + (mc & 1u)) * PAIR + nh + (mc >> 1) * 4096u + (unsigned)((fq >> 1) * 512 + (fr >> 2) * 128 + (fr & 1) * 64 + (fq & 1) * 32 + (fr & 2) * 8);
        const unsigned pmoff = (8u + (mc & 1u)) * PAIR + nh + (mc >> 1) * 4096u + (unsigned)(fq * 256 + (fr >> 3) * 128 + (fr & 2) * 32 + (fr & 4) * 8 + (fr & 1) * 16);
        const unsigned moff = (unsigned)(ot >> 4) * 2u * PAIR + nh + (unsigned)((ot & 15) * 256 + lane * 4);
        constexpr unsigned mc2 = 4096u;
        char* op = smem + 8192;
        if (w == 4 && lane < 2) *(volatile LAS int*)(smem + 4096 + lane * 4) = -1;
        BAR();
        SST(2);
        if (w == 7) clk_init(ck);
        wait_until(tstart + DO0);
        if (w < 4) {
            u32x8 zf[4];
            int zr = -1;
            unsigned zb = W_ZM + (unsigned)layer * (NOWN * PAIR), zso = O_Z - (unsigned)layer * (unsigned)(ZTSEG * 2);
            const unsigned ra = (unsigned)((ot >> 4) * 2 + (fq & 1)) * PAIR + nh + (unsigned)((fq >> 1) * 4096 + (ot & 15) * 256 + fr * 16);
            int np = 0;
            auto piece = [&]() INL {
                asm volatile("s_mov_b32 m0, %3\n\ts_nop 3\n\tbuffer_load_dwordx4 %0, %1, %2 offen sc1 lds\n\tbuffer_load_dwordx4 %0, %1, %2 offen offset:1024 sc1 lds"
                             :: "v"(ztoff), "s"(rws), "s"(O_Z + (unsigned)((NSEG - 1 - layer) * (int)ZTSEG) * 2u + np * 2048u), "s"(32768 + hg * 8192 + np * 2048) : "m0", "memory");
                ++np;
            };
            for (int s = 0; s + 1 < NSEG; ++s) {
                const unsigned pb = p_slot(s);
                const int sz = s;
                while (__builtin_expect(zr < sz, 0)) {
                    __builtin_amdgcn_s_sleep(1);
                    zr = __builtin_amdgcn_readfirstlane(*(volatile LAS int*)(smem + 4096));
                }
                if (s == 0) SST(1);
                asm volatile(
                    "1:\n\t"
                    "buffer_load_dwordx4 v[160:163], %[zt], %[rs], %[so] offen sc1\n\t"
                    "buffer_load_dwordx4 v[164:167], %[zt], %[rs], %[so] offen offset:1024 sc1\n\t"
                    "buffer_load_dwordx4 v[168:171], %[zt], %[rs], %[so] offen offset:2048 sc1\n\t"
                    "buffer_load_dwordx4 v[172:175], %[zt], %[rs], %[so] offen offset:3072 sc1\n\t"
                    "buffer_load_dwordx4 v[176:179], %[zt], %[rs], %[s4] offen sc1\n\t"
                    "buffer_load_dwordx4 v[180:183], %[zt], %[rs], %[s4] offen offset:1024 sc1\n\t"
                    "buffer_load_dwordx4 v[184:187], %[zt], %[rs], %[s4] offen offset:2048 sc1\n\t"
                    "buffer_load_dwordx4 v[188:191], %[zt], %[rs], %[s4] offen offset:3072 sc1\n\t"
                    "s_cmp_lg_u32 %[s], 0\n\t"
                    "s_cbranch_scc1 2f\n\t"
                    "s_waitcnt vmcnt(0)\n\t"
                    "v_max3_u32 v100, v161, v163, v165\n\t"
                    "v_max3_u32 v100, v100, v167, v169\n\t"
                    "v_max3_u32 v100, v100, v171, v173\n\t"
                    "v_max3_u32 v100, v100, v175, v177\n\t"
                    "v_max3_u32 v100, v100, v179, v181\n\t"
                    "v_max3_u32 v100, v100, v183, v185\n\t"
                    "v_max3_u32 v100, v100, v187, v189\n\t"
                    "v_max_u32 v100, v100, v191\n\t"
                    "v_cmp_eq_u32 vcc, -1, v100\n\t"
                    "s_sub_i32 %[tr], %[tr], 1\n\t"
                    "s_cbranch_vccz 2f\n\t"
                    "s_cmp_gt_i32 %[tr], 0\n\t"
                    "s_cbranch_scc1 1b\n\t"
                    "2:"
                    : "=&{v[160:167]}"(zf[0]), "=&{v[168:175]}"(zf[1]), "=&{v[176:183]}"(zf[2]), "=&{v[184:191]}"(zf[3]), [tr] "+s"(budget)
                    : [zt] "v"(ztoff), [rs] "s"(rws), [so] "s"(zso), [s4] "s"(zso + 4096u), [s] "s"(s) : "v100", "vcc", "scc", "memory");
                if (s == 0) SST(3);
                zso += (unsigned)(ZTSEG * 2);
                if (zr == NSEG - 1 && np < 4) piece();
                if (__builtin_expect(budget <= 0, 0)) set_tmo(rwin);
                RT(6);
                const unsigned spa = (unsigned)((s & 1) * 2048 + (w < 3 ? w * 512 : 0) + lane * 8);
#define OST(r)
                unsigned long long t0_, t1_, t2_, t3_, t4_ = 0, bad;
                int tries = budget;
                const unsigned sw = (unsigned)(((s & 3) + 1) & 4) * 1024u;
                unsigned po = poff ^ sw, pm = pmoff ^ sw, x0 = 4096u, x1 = 4096u;
                const unsigned mb = W_MEM + (unsigned)((s + 1) & 3) * 6u * PAIR;
                if (w < 3) asm volatile(
                    ALN
                    "s_barrier\n\t"
                    OST("%[t0]")
                    "v_mov_b64 v[96:97], 0\n\t"
                    "v_mov_b64 v[98:99], 0\n\t"
                    "v_mov_b32 v100, 0\n\t"
                    "s_barrier\n\t"
                    "1:\n\t"
                    "buffer_load_dwordx4 v[208:211], %[po], %[rw], %[pb] offen sc1\n\t"
                    "buffer_load_dwordx4 v[212:215], %[po], %[rw], %[pb] offen offset:1024 sc1\n\t"
                    "buffer_load_dwordx4 v[216:219], %[po], %[rw], %[pb] offen offset:2048 sc1\n\t"
                    "buffer_load_dwordx4 v[220:223], %[po], %[rw], %[pb] offen offset:3072 sc1\n\t"
                    OST("%[t1]")
                    "s_waitcnt vmcnt(3)\n\t"
                    OST("%[t3]")
                    "v_smfmac_f32_16x16x64_bf16 v[96:99], v[208:211], v[160:167], %[ix]\n\t"
                    "v_max3_u32 v100, v100, v208, v209\n\t"
                    "v_max3_u32 v100, v100, v210, v211\n\t"
                    "s_waitcnt vmcnt(2)\n\t"
                    "v_smfmac_f32_16x16x64_bf16 v[96:99], v[212:215], v[168:175], %[ix]\n\t"
                    "v_max3_u32 v100, v100, v212, v213\n\t"
                    "v_max3_u32 v100, v100, v214, v215\n\t"
                    "s_waitcnt vmcnt(1)\n\t"
                    "v_smfmac_f32_16x16x64_bf16 v[96:99], v[216:219], v[176:183], %[ix]\n\t"
                    "v_max3_u32 v100, v100, v216, v217\n\t"
                    "v_max3_u32 v100, v100, v218, v219\n\t"
                    "s_waitcnt vmcnt(0)\n\t"
                    "v_smfmac_f32_16x16x64_bf16 v[96:99], v[220:223], v[184:191], %[ix]\n\t"
                    "v_max3_u32 v100, v100, v220, v221\n\t"
                    "v_max3_u32 v100, v100, v222, v223\n\t"
                    "v_cmp_eq_u32 vcc, -1, v100\n\t"
                    OST("%[t2]")
                    "s_nop 4\n\t"
                    "v_pk_add_f32 v[96:97], v[96:97], v[98:99]\n\t"
                    "ds_write_b64 %[sp], v[96:97]\n\t"
                    "s_cbranch_vccz 2f\n\t"
                    "s_sub_i32 %[tr], %[tr], 1\n\t"
                    "s_cmp_lt_i32 %[tr], 1\n\t"
                    "s_cbranch_scc1 2f\n\t"
                    "v_xor_b32 %[po], %[x0], %[po]\n\t"
                    "s_xor_b32 %[x0], %[x0], 0x5000\n\t"
                    "s_waitcnt lgkmcnt(0)\n\t"
                    "v_mov_b64 v[96:97], 0\n\t"
                    "v_mov_b64 v[98:99], 0\n\t"
                    "v_mov_b32 v100, 0\n\t"
                    "s_branch 1b\n\t"
                    "2:\n\t"
                    "s_waitcnt lgkmcnt(0)\n\t"
                    "s_barrier\n\t"
                    "s_sleep 3\n\t"
                    : [tr] "+s"(tries), [po] "+v"(po), [x0] "+s"(x0), [t0] "=&s"(t0_), [t1] "=&s"(t1_), [t2] "=&s"(t2_), [t3] "=&s"(t3_)
                    : [sp] "v"(spa), [ix] "v"(sidx), [rw] "s"(rwin), [pb] "s"(pb), "{v[160:167]}"(zf[0]), "{v[168:175]}"(zf[1]), "{v[176:183]}"(zf[2]), "{v[184:191]}"(zf[3])
                    : "v96", "v97", "v98", "v99", "v100", "v208", "v209", "v210", "v211", "v212", "v213", "v214", "v215", "v216", "v217", "v218", "v219", "v220", "v221", "v222", "v223", "vcc", "scc", "memory");
                else asm volatile(
                    ALN
                    "s_barrier\n\t"
                    "buffer_load_dwordx4 v[128:131], %[zo], %[rw], %[zb] offen sc1\n\t"
                    "buffer_load_dwordx4 v[132:135], %[zo], %[rw], %[zb] offen offset:1024 sc1\n\t"
                    "buffer_load_dwordx4 v[136:139], %[zo], %[rw], %[zb] offen offset:2048 sc1\n\t"
                    "buffer_load_dwordx4 v[140:143], %[zo], %[rw], %[zb] offen offset:3072 sc1\n\t"
                    "buffer_load_dwordx4 v[144:147], %[zo], %[rw], %[z4] offen sc1\n\t"
                    "buffer_load_dwordx4 v[148:151], %[zo], %[rw], %[z4] offen offset:1024 sc1\n\t"
                    "buffer_load_dwordx4 v[152:155], %[zo], %[rw], %[z4] offen offset:2048 sc1\n\t"
                    "buffer_load_dwordx4 v[156:159], %[zo], %[rw], %[z4] offen offset:3072 sc1\n\t"
                    OST("%[t0]")
                    "v_mov_b64 v[96:97], 0\n\t"
                    "v_mov_b64 v[98:99], 0\n\t"
                    "v_mov_b32 v100, 0\n\t"
                    "s_barrier\n\t"
                    "s_sleep %[fd]\n\t"
                    "buffer_load_dwordx4 v[208:211], %[po], %[rw], %[pb] offen sc1\n\t"
                    "buffer_load_dwordx4 v[212:215], %[po], %[rw], %[pb] offen offset:1024 sc1\n\t"
                    "buffer_load_dwordx4 v[216:219], %[po], %[rw], %[pb] offen offset:2048 sc1\n\t"
                    "buffer_load_dwordx4 v[220:223], %[po], %[rw], %[pb] offen offset:3072 sc1\n\t"
                    "buffer_load_dwordx4 v[224:227], %[pm], %[rw], %[pb] offen sc1\n\t"
                    "buffer_load_dwordx4 v[228:231], %[pm], %[rw], %[pb] offen offset:1024 sc1\n\t"
                    "buffer_load_dwordx4 v[232:235], %[pm], %[rw], %[pb] offen offset:2048 sc1\n\t"
                    "buffer_load_dwordx4 v[236:239], %[pm], %[rw], %[pb] offen offset:3072 sc1\n\t"
                    OST("%[t1]")
                    "s_waitcnt vmcnt(8)\n\t"
                    "v_max3_u32 v101, v129, v131, v133\n\t"
                    "v_max3_u32 v101, v101, v135, v137\n\t"
                    "v_max3_u32 v101, v101, v139, v141\n\t"
                    "v_max3_u32 v101, v101, v143, v145\n\t"
                    "v_max3_u32 v101, v101, v147, v149\n\t"
                    "v_max3_u32 v101, v101, v151, v153\n\t"
                    "v_max3_u32 v101, v101, v155, v157\n\t"
                    "v_max_u32 v101, v101, v159\n\t"
                    "v_cmp_eq_u32 vcc, -1, v101\n\t"
                    "s_cbranch_vccz 3f\n\t"
                    "buffer_load_dwordx4 v[128:131], %[zo], %[rw], %[zb] offen sc1\n\t"
                    "buffer_load_dwordx4 v[132:135], %[zo], %[rw], %[zb] offen offset:1024 sc1\n\t"
                    "buffer_load_dwordx4 v[136:139], %[zo], %[rw], %[zb] offen offset:2048 sc1\n\t"
                    "buffer_load_dwordx4 v[140:143], %[zo], %[rw], %[zb] offen offset:3072 sc1\n\t"
                    "buffer_load_dwordx4 v[144:147], %[zo], %[rw], %[z4] offen sc1\n\t"
                    "buffer_load_dwordx4 v[148:151], %[zo], %[rw], %[z4] offen offset:1024 sc1\n\t"
                    "buffer_load_dwordx4 v[152:155], %[zo], %[rw], %[z4] offen offset:2048 sc1\n\t"
                    "buffer_load_dwordx4 v[156:159], %[zo], %[rw], %[z4] offen offset:3072 sc1\n\t"
                    "s_waitcnt vmcnt(0)\n\t"
                    "v_max3_u32 v101, v129, v131, v133\n\t"
                    "v_max3_u32 v101, v101, v135, v137\n\t"
                    "v_max3_u32 v101, v101, v139, v141\n\t"
                    "v_max3_u32 v101, v101, v143, v145\n\t"
                    "v_max3_u32 v101, v101, v147, v149\n\t"
                    "v_max3_u32 v101, v101, v151, v153\n\t"
                    "v_max3_u32 v101, v101, v155, v157\n\t"
                    "v_max_u32 v101, v101, v159\n\t"
                    "v_mov_b32 v100, v101\n\t"
                    "3:\n\t"
                    "v_permlane16_swap_b32 v128, v129\n\t"
                    "v_permlane16_swap_b32 v130, v131\n\t"
                    "v_permlane16_swap_b32 v132, v133\n\t"
                    "v_permlane16_swap_b32 v134, v135\n\t"
                    "v_permlane16_swap_b32 v136, v137\n\t"
                    "v_permlane16_swap_b32 v138, v139\n\t"
                    "v_permlane16_swap_b32 v140, v141\n\t"
                    "v_permlane16_swap_b32 v142, v143\n\t"
                    "v_permlane16_swap_b32 v144, v145\n\t"
                    "v_permlane16_swap_b32 v146, v147\n\t"
                    "v_permlane16_swap_b32 v148, v149\n\t"
                    "v_permlane16_swap_b32 v150, v151\n\t"
                    "v_permlane16_swap_b32 v152, v153\n\t"
                    "v_permlane16_swap_b32 v154, v155\n\t"
                    "v_permlane16_swap_b32 v156, v157\n\t"
                    "v_permlane16_swap_b32 v158, v159\n\t"
                    "s_mov_b32 vcc_lo, 0x00ff00ff\n\t"
                    "s_mov_b32 vcc_hi, 0x00ff00ff\n\t"
                    "s_waitcnt vmcnt(7)\n\t"
                    OST("%[t3]")
                    "v_smfmac_f32_16x16x64_bf16 v[96:99], v[208:211], v[160:167], %[ix]\n\t"
                    "v_max3_u32 v100, v100, v208, v209\n\t"
                    "v_max3_u32 v100, v100, v210, v211\n\t"
                    "s_waitcnt vmcnt(6)\n\t"
                    "v_smfmac_f32_16x16x64_bf16 v[96:99], v[212:215], v[168:175], %[ix]\n\t"
                    "v_max3_u32 v100, v100, v212, v213\n\t"
                    "v_max3_u32 v100, v100, v214, v215\n\t"
                    "s_waitcnt vmcnt(5)\n\t"
                    "v_smfmac_f32_16x16x64_bf16 v[96:99], v[216:219], v[176:183], %[ix]\n\t"
                    "v_max3_u32 v100, v100, v216, v217\n\t"
                    "v_max3_u32 v100, v100, v218, v219\n\t"
                    "s_waitcnt vmcnt(4)\n\t"
                    "v_smfmac_f32_16x16x64_bf16 v[96:99], v[220:223], v[184:191], %[ix]\n\t"
                    "v_max3_u32 v100, v100, v220, v221\n\t"
                    "v_max3_u32 v100, v100, v222, v223\n\t"
                    "s_waitcnt vmcnt(3)\n\t"
                    "v_smfmac_f32_16x16x64_bf16 v[96:99], v[224:227], v[128:135], %[ix]\n\t"
                    "v_max3_u32 v100, v100, v224, v225\n\t"
                    "v_max3_u32 v100, v100, v226, v227\n\t"
                    "s_waitcnt vmcnt(2)\n\t"
                    "v_smfmac_f32_16x16x64_bf16 v[96:99], v[228:231], v[136:143], %[ix]\n\t"
                    "s_barrier\n\t"
                    "ds_read_b64 v[102:103], %[sp]\n\t"
                    "ds_read_b64 v[104:105], %[sp] offset:512\n\t"
                    "ds_read_b64 v[106:107], %[sp] offset:1024\n\t"
                    "v_max3_u32 v100, v100, v228, v229\n\t"
                    "v_max3_u32 v100, v100, v230, v231\n\t"
                    "s_waitcnt vmcnt(1)\n\t"
                    "v_smfmac_f32_16x16x64_bf16 v[96:99], v[232:235], v[144:151], %[ix]\n\t"
                    "v_max3_u32 v100, v100, v232, v233\n\t"
                    "v_max3_u32 v100, v100, v234, v235\n\t"
                    "s_waitcnt vmcnt(0)\n\t"
                    "v_smfmac_f32_16x16x64_bf16 v[96:99], v[236:239], v[152:159], %[ix]\n\t"
                    "v_max3_u32 v100, v100, v236, v237\n\t"
                    "v_max3_u32 v100, v100, v238, v239\n\t"
                    OST("%[t2]")
                    "v_cmp_eq_u32 %[bad], -1, v100\n\t"
                    "s_waitcnt lgkmcnt(0)\n\t"
                    "v_add_f32 v102, v102, v104\n\t"
                    "v_add_f32 v103, v103, v105\n\t"
                    "v_add_f32 v102, v102, v106\n\t"
                    "v_add_f32 v103, v103, v107\n\t"
                    "v_add_f32 v96, v96, v102\n\t"
                    "v_add_f32 v97, v97, v103\n\t"
                    "v_add_f32 v96, v96, v98\n\t"
                    "v_add_f32 v97, v97, v99\n\t"
                    "s_cmp_lg_u64 %[bad], 0\n\t"
                    "s_cbranch_scc1 4f\n\t"
                    "2:\n\t"
                    "v_cndmask_b32_dpp v98, v97, v96, vcc row_ror:8 row_mask:0xf bank_mask:0xf\n\t"
                    "s_not_b64 vcc, vcc\n\t"
                    "v_cndmask_b32_dpp v99, v96, v97, vcc row_ror:8 row_mask:0xf bank_mask:0xf\n\t"
                    "v_cvt_pk_bf16_f32 v98, v98, v99\n\t"
                    "v_min_u32 v98, -2, v98\n\t"
                    "buffer_store_dword v98, %[mo], %[rw], %[m0] offen sc1\n\t"
                    "buffer_store_dword v98, %[mo], %[rw], %[m1] offen sc1\n\t"
                    "buffer_store_dword v98, %[mo], %[rw], %[m2] offen sc1\n\t"
                    "buffer_store_dword v98, %[mo], %[rw], %[m3] offen sc1\n\t"
                    OST("%[t4]")
                    "s_branch 5f\n\t"
                    "4:\n\t"
                    "s_sub_i32 %[tr], %[tr], 1\n\t"
                    "s_cmp_lt_i32 %[tr], 1\n\t"
                    "s_cbranch_scc1 2b\n\t"
                    "v_xor_b32 %[po], %[x0], %[po]\n\t"
                    "s_xor_b32 %[x0], %[x0], 0x5000\n\t"
                    "v_xor_b32 %[pm], %[x1], %[pm]\n\t"
                    "s_xor_b32 %[x1], %[x1], 0x5000\n\t"
                    "buffer_load_dwordx4 v[128:131], %[zo], %[rw], %[zb] offen sc1\n\t"
                    "buffer_load_dwordx4 v[132:135], %[zo], %[rw], %[zb] offen offset:1024 sc1\n\t"
                    "buffer_load_dwordx4 v[136:139], %[zo], %[rw], %[zb] offen offset:2048 sc1\n\t"
                    "buffer_load_dwordx4 v[140:143], %[zo], %[rw], %[zb] offen offset:3072 sc1\n\t"
                    "buffer_load_dwordx4 v[144:147], %[zo], %[rw], %[z4] offen sc1\n\t"
                    "buffer_load_dwordx4 v[148:151], %[zo], %[rw], %[z4] offen offset:1024 sc1\n\t"
                    "buffer_load_dwordx4 v[152:155], %[zo], %[rw], %[z4] offen offset:2048 sc1\n\t"
                    "buffer_load_dwordx4 v[156:159], %[zo], %[rw], %[z4] offen offset:3072 sc1\n\t"
                    "buffer_load_dwordx4 v[208:211], %[po], %[rw], %[pb] offen sc1\n\t"
                    "buffer_load_dwordx4 v[212:215], %[po], %[rw], %[pb] offen offset:1024 sc1\n\t"
                    "buffer_load_dwordx4 v[216:219], %[po], %[rw], %[pb] offen offset:2048 sc1\n\t"
                    "buffer_load_dwordx4 v[220:223], %[po], %[rw], %[pb] offen offset:3072 sc1\n\t"
                    "buffer_load_dwordx4 v[224:227], %[pm], %[rw], %[pb] offen sc1\n\t"
                    "buffer_load_dwordx4 v[228:231], %[pm], %[rw], %[pb] offen offset:1024 sc1\n\t"
                    "buffer_load_dwordx4 v[232:235], %[pm], %[rw], %[pb] offen offset:2048 sc1\n\t"
                    "buffer_load_dwordx4 v[236:239], %[pm], %[rw], %[pb] offen offset:3072 sc1\n\t"
                    "v_mov_b64 v[96:97], 0\n\t"
                    "v_mov_b64 v[98:99], 0\n\t"
                    "v_mov_b32 v100, 0\n\t"
                    "s_waitcnt vmcnt(0)\n\t"
                    "v_max3_u32 v101, v129, v131, v133\n\t"
                    "v_max3_u32 v101, v101, v135, v137\n\t"
                    "v_max3_u32 v101, v101, v139, v141\n\t"
                    "v_max3_u32 v101, v101, v143, v145\n\t"
                    "v_max3_u32 v101, v101, v147, v149\n\t"
                    "v_max3_u32 v101, v101, v151, v153\n\t"
                    "v_max3_u32 v101, v101, v155, v157\n\t"
                    "v_max_u32 v101, v101, v159\n\t"
                    "v_mov_b32 v100, v101\n\t"
                    "v_permlane16_swap_b32 v128, v129\n\t"
                    "v_permlane16_swap_b32 v130, v131\n\t"
                    "v_permlane16_swap_b32 v132, v133\n\t"
                    "v_permlane16_swap_b32 v134, v135\n\t"
                    "v_permlane16_swap_b32 v136, v137\n\t"
                    "v_permlane16_swap_b32 v138, v139\n\t"
                    "v_permlane16_swap_b32 v140, v141\n\t"
                    "v_permlane16_swap_b32 v142, v143\n\t"
                    "v_permlane16_swap_b32 v144, v145\n\t"
                    "v_permlane16_swap_b32 v146, v147\n\t"
                    "v_permlane16_swap_b32 v148, v149\n\t"
                    "v_permlane16_swap_b32 v150, v151\n\t"
                    "v_permlane16_swap_b32 v152, v153\n\t"
                    "v_permlane16_swap_b32 v154, v155\n\t"
                    "v_permlane16_swap_b32 v156, v157\n\t"
                    "v_permlane16_swap_b32 v158, v159\n\t"
                    "s_nop 1\n\t"
                    "v_smfmac_f32_16x16x64_bf16 v[96:99], v[208:211], v[160:167], %[ix]\n\t"
                    "v_max3_u32 v100, v100, v208, v209\n\t"
                    "v_max3_u32 v100, v100, v210, v211\n\t"
                    "v_smfmac_f32_16x16x64_bf16 v[96:99], v[212:215], v[168:175], %[ix]\n\t"
                    "v_max3_u32 v100, v100, v212, v213\n\t"
                    "v_max3_u32 v100, v100, v214, v215\n\t"
                    "v_smfmac_f32_16x16x64_bf16 v[96:99], v[216:219], v[176:183], %[ix]\n\t"
                    "v_max3_u32 v100, v100, v216, v217\n\t"
                    "v_max3_u32 v100, v100, v218, v219\n\t"
                    "v_smfmac_f32_16x16x64_bf16 v[96:99], v[220:223], v[184:191], %[ix]\n\t"
                    "v_max3_u32 v100, v100, v220, v221\n\t"
                    "v_max3_u32 v100, v100, v222, v223\n\t"
                    "v_smfmac_f32_16x16x64_bf16 v[96:99], v[224:227], v[128:135], %[ix]\n\t"
                    "v_max3_u32 v100, v100, v224, v225\n\t"
                    "v_max3_u32 v100, v100, v226, v227\n\t"
                    "v_smfmac_f32_16x16x64_bf16 v[96:99], v[228:231], v[136:143], %[ix]\n\t"
                    "v_max3_u32 v100, v100, v228, v229\n\t"
                    "v_max3_u32 v100, v100, v230, v231\n\t"
                    "v_smfmac_f32_16x16x64_bf16 v[96:99], v[232:235], v[144:151], %[ix]\n\t"
                    "v_max3_u32 v100, v100, v232, v233\n\t"
                    "v_max3_u32 v100, v100, v234, v235\n\t"
                    "v_smfmac_f32_16x16x64_bf16 v[96:99], v[236:239], v[152:159], %[ix]\n\t"
                    "v_max3_u32 v100, v100, v236, v237\n\t"
                    "v_max3_u32 v100, v100, v238, v239\n\t"
                    "v_cmp_eq_u32 %[bad], -1, v100\n\t"
                    "s_nop 5\n\t"
                    "v_add_f32 v96, v96, v102\n\t"
                    "v_add_f32 v97, v97, v103\n\t"
                    "v_add_f32 v96, v96, v98\n\t"
                    "v_add_f32 v97, v97, v99\n\t"
                    "s_cmp_lg_u64 %[bad], 0\n\t"
                    "s_cbranch_scc1 4b\n\t"
                    "s_branch 2b\n\t"
                    "5:\n\t"
                    : [tr] "+s"(tries), [po] "+v"(po), [pm] "+v"(pm), [x0] "+s"(x0), [x1] "+s"(x1), [bad] "=&s"(bad), [t0] "=&s"(t0_), [t1] "=&s"(t1_), [t2] "=&s"(t2_), [t3] "=&s"(t3_), [t4] "=&s"(t4_)
                    : [zo] "v"(zoff), [sp] "v"(spa), [ix] "v"(sidx), [rw] "s"(rwin), [zb] "s"(zb), [z4] "s"(zb + 4096u), [pb] "s"(pb), [mo] "v"(moff), [fd] "n"(FDL),
                      [m0] "s"(mb), [m1] "s"(mb + PAIR), [m2] "s"(mb + mc2), [m3] "s"(mb + mc2 + PAIR), "{v[160:167]}"(zf[0]), "{v[168:175]}"(zf[1]), "{v[176:183]}"(zf[2]), "{v[184:191]}"(zf[3])
                    : "v96", "v97", "v98", "v99", "v100", "v101", "v102", "v103", "v104", "v105", "v106", "v107", "v128", "v129", "v130", "v131", "v132", "v133", "v134", "v135", "v136", "v137", "v138", "v139", "v140", "v141", "v142", "v143", "v144", "v145", "v146", "v147", "v148", "v149", "v150", "v151", "v152", "v153", "v154", "v155", "v156", "v157", "v158", "v159", "v208", "v209", "v210", "v211", "v212", "v213", "v214", "v215", "v216", "v217", "v218", "v219", "v220", "v221", "v222", "v223", "v224", "v225", "v226", "v227", "v228", "v229", "v230", "v231", "v232", "v233", "v234", "v235", "v236", "v237", "v238", "v239", "vcc", "scc", "memory");
                budget = tries;
                if (w == 3) *(volatile LAS int*)(smem + 4100) = s;
                __builtin_amdgcn_s_sleep(DPS - 4);
                if (w == 3) {
                    st128(SENT, rwin, ra, W_MEM + (unsigned)(s & 3) * 6u * PAIR, 16);
                    if (lane == 0 && s > 1) __builtin_amdgcn_raw_buffer_store_b32((unsigned)s - 2u, rwin, 0u, W_GO + fo + (unsigned)o * 128u, 16);
                }
                for (;;) {
                    const u32x2 x = *(volatile LAS u32x2*)(smem + 4096);
                    zr = __builtin_amdgcn_readfirstlane(x[0]);
                    if (__builtin_amdgcn_readfirstlane(x[1]) == s) break;
                    __builtin_amdgcn_s_sleep(2);
                }
                zb = zb == W_ZM + 2u * NOWN * PAIR ? W_ZM : zb + NOWN * PAIR;
            }
            const unsigned zq = zb == W_ZM ? W_ZM + 2u * NOWN * PAIR : zb - NOWN * PAIR;
            while (np < 4) {
                while (zr < NSEG - 1) { __builtin_amdgcn_s_sleep(1); zr = __builtin_amdgcn_readfirstlane(*(volatile LAS int*)(smem + 4096)); }
                piece();
            }
            __builtin_amdgcn_s_sleep(DTK);
            for (int e = 0; e < 2; ++e) {
                f32x4 c[2];
                SST(16 + 3 * e);
                for (unsigned spins = 0;;) {
                    asm volatile("" ::: "memory");
                    SST(17 + 3 * e);
                    u32x4 pt[16], ptm[2][2], zm[2];
                    const unsigned pl = W_PL + fo + (unsigned)e * (PTSEG * 2u);
#pragma unroll
                    for (int q = 0; q < 2; ++q) zm[q] = __builtin_amdgcn_raw_buffer_load_b128(rwin, zoff + hg * 2048u + q * 1024u, e ? zb : zq, 16);
#pragma unroll
                    for (int q = 0; q < 16; ++q) pt[q] = __builtin_amdgcn_raw_buffer_load_b128(rwin, ((unsigned)(16 * hg) * 1024u + lane * 16u) | (q & 1 ? 0u : (lane & 32u) << 25), pl + q * 1024u, 16);
#pragma unroll
                    for (int q = 0; q < 4; ++q) ptm[q >> 1][q & 1] = __builtin_amdgcn_raw_buffer_load_b128(rwin, (unsigned)(hg * 2048 + lane * 16), pl + 65536u + (q >> 1) * 8192u + (q & 1) * 1024u, 16);
                    unsigned m = max(smax(zm[0]), smax(zm[1]));
#pragma unroll
                    for (int t = 0; t < 2; ++t) {
                        c[t] = z4;
#pragma unroll
                        for (int q = 0; q < 8; ++q) { c[t] = mfma32(half8(zf[q >> 1], q & 1), pt[2 * q + t], c[t]); m = max(m, smax(pt[2 * q + t])); }
#pragma unroll
                        for (int q = 0; q < 2; ++q) { c[t] = mfma32((u32x4){zm[q][0], zm[q][2], zm[q][1], zm[q][3]}, ptm[t][q], c[t]); m = max(m, smax(ptm[t][q])); }
                    }
                    if (!__any(m == 0xFFFFFFFFu)) break;
                    if (spin_fail(spins, rwin)) break;
                }
                SST(18 + 3 * e);
#pragma unroll
                for (int t = 0; t < 2; ++t) *(f32x4*)(op + e * 8192 + hg * 2048 + (16 * t + fr) * 64 + fq * 16) = c[t];
                BAR();
                VMWAIT();
#pragma unroll
                for (int q = 0; q < 4; ++q) zf[q] = cat8(*(const u32x4*)(smem + 32768 + hg * 8192 + q * 2048 + lane * 16), *(const u32x4*)(smem + 33792 + hg * 8192 + q * 2048 + lane * 16));
                if (e == 0) BAR0();
            }
            if (layer == 0) {
#pragma unroll
                for (int q = 0; q < 4; ++q) st128(SENT, rwin, zoff + hg * 2048u + (q & 1) * 1024u, q < 2 ? zb : zq, 16);
                if (w == 3) st128(SENT, rwin, ra, W_MEM + 3u * 6u * PAIR, 16);
            }
        } else if (w == 4) {
            const unsigned fl = fz_at(lane & 31, o / 10) + (lane & 32) * 128u;
            int zk = -1;
            auto note = [&](int f) INL {
                f = min(f, __builtin_amdgcn_update_dpp(f, f, 0xB1, 0xF, 0xF, false)); f = min(f, __builtin_amdgcn_update_dpp(f, f, 0x4E, 0xF, 0xF, false));
                f = min(f, __builtin_amdgcn_update_dpp(f, f, 0x141, 0xF, 0xF, false)); f = min(f, __builtin_amdgcn_update_dpp(f, f, 0x140, 0xF, 0xF, false));
                zk = seg_done(MXV, min(__builtin_amdgcn_readlane(f, 0), __builtin_amdgcn_readlane(f, 16)), min(__builtin_amdgcn_readlane(f, 32), __builtin_amdgcn_readlane(f, 48)), MXA);
                *(volatile LAS int*)(smem + 4096) = zk;
            };
            for (int s = 0; s < NSEG; ++s) {
                for (unsigned spins = 0; __builtin_expect(zk < s && s > 0, 0);) {
                    if (spins == 0) __builtin_amdgcn_s_sleep(DW4);
                    asm volatile("" ::: "memory");
                    note((int)__builtin_amdgcn_raw_buffer_load_b32(rwin, fl, W_FZ + fo, 16));
                    FC(fc2);
                    if (spin_fail(spins, rwin)) note(NSEG);
                    if (zk < s) __builtin_amdgcn_s_sleep(ZPS);
                }
                if (LKE && s > 0 && zk < (s + 8 >= NSEG ? NSEG - 1 : s + ZLA)) { __builtin_amdgcn_s_sleep(LKE); note((int)__builtin_amdgcn_raw_buffer_load_b32(rwin, fl, W_FZ + fo, 16)); }
                asm volatile("s_waitcnt lgkmcnt(0)\n\ts_barrier\n\ts_barrier" ::: "memory");
                int f = 0;
                const bool look = !LKE && zk < (s + 8 >= NSEG ? NSEG - 1 : s + ZLA);
                if (look) { __builtin_amdgcn_s_sleep(6); f = (int)__builtin_amdgcn_raw_buffer_load_b32(rwin, fl, W_FZ + fo, 16); }
                SB();
                BAR0();
                if (look) note(f);
            }
        } else if (w < 7) {
            const unsigned ooff = (unsigned)((S_ - (7 - w) * SEG + (lane >> 1)) * D_ + ot * 16 + (lane & 1) * 8) * 2u;
            for (int s = 0; s < 3 * NSEG - 3 + (w - 5); ++s) BAR0();
            if (w == 5) __builtin_amdgcn_s_sleep(DPS);
            const u32x4 xr = __builtin_amdgcn_raw_buffer_load_b128(rws, ooff, O_X1, 16);
            BAR0();
            BAR0();
            const char* src = op + (w - 5) * 8192 + lane * 32;
            f32x4 x = *(const f32x4*)src, y = *(const f32x4*)(src + 16);
#pragma unroll
            for (int ww = 1; ww < 4; ++ww) { x += *(const f32x4*)(src + ww * 2048); y += *(const f32x4*)(src + ww * 2048 + 16); }
            const u32x4 r = {pack2(lo2f(xr[0]) + bfr(x[0]), hi2f(xr[0]) + bfr(x[1])), pack2(lo2f(xr[1]) + bfr(x[2]), hi2f(xr[1]) + bfr(x[3])),
                             pack2(lo2f(xr[2]) + bfr(y[0]), hi2f(xr[2]) + bfr(y[1])), pack2(lo2f(xr[3]) + bfr(y[2]), hi2f(xr[3]) + bfr(y[3]))};
            st128(nosent4(r), rout(), ooff, 0u, 16);
            RST(19 + w, layer, w, w & 3);
            if (w == 5) BAR0();
        } else {
            Tk tk = tk_init();
            __builtin_amdgcn_s_setprio(3);
            for (int s = 0; s < NSEG; ++s) {
                const unsigned ta0 = ck.ta, mm0 = ck.ma;
                unsigned tp = tpp + lt + xs + M1;
                if (s < SAD) {
                    if (s) wait_fine(tpp + CF, ck);
                    tp = M2 + xs + (s ? 0 : PEN1);
                    sched(tk, s);
                    tp = tpp - tp;
                    if (s == 0) { wait_fine(tp - DZ0, ck); *(volatile LAS int*)(smem + 4096) = 0; }
                }
                wait_fine_bar(tp - DZP, ck);
                wait_cyc_bar(cyc_at(ck, sx24(tp + (s + 1 < NSEG ? 0 : ML) - ck.ta)));
                clk_rate(ck, ta0, mm0);
                asm volatile("s_barrier ; 7" ::: "memory");
                if (s >= SAD && s + 1 < NSEG) sched(tk, s);
                RTS();
            }
        }
        __builtin_amdgcn_s_setprio(0);
        SST(23);
    };
    static_assert(NSEG % 3 == 1, "ring");
    auto out_role = [&]() INL {
        LANES();
        const int o = bid - NREC, cg = o % 10, send = NSEG - 2;
        const unsigned ztoff = (unsigned)((4 * cg * H_ + 4 * w) * 64 + lane) * 16u;
        const unsigned ptoff = (unsigned)(8 * w * 64 + lane) * 16u, ptmoff = 65536u + (unsigned)(4 * w * 16 + lane) * 16u;
        const unsigned ptoff0 = ptoff | (lane & 32u) << 25;
        const unsigned ooff = (unsigned)(((w & 1) * 16 + (lane >> 2)) * D_ + tile_of(4 * cg + (w & 2)) * 16 + (lane & 3) * 8) * 2u;
        const unsigned fg = fo + (unsigned)((4 * w + (lane & 3)) * 128), gw = (unsigned)((4 * cg + ((lane >> 2) & 1) * 3) / 10) * 4u;
        const unsigned tfoff = fg + (lane < 16 ? W_FPT + (lane >> 2) * 4u : W_FZ2 + gw), zfoff = fo + W_FZ + fz_at(4 * w + (lane & 3), gw / 4u) + (lane & 32u) * 128u;
        int rdy = -1, go = -1, zrdy = -1;
        auto wait_rdy = [&](int s) INL {
            for (unsigned spins = 0; rdy < s;) {
                asm volatile("" ::: "memory");
                if (s < TAILS || go < s - 2) {
                    go = (int)__builtin_amdgcn_readfirstlane(__builtin_amdgcn_raw_buffer_load_b32(rwin, 0u, W_GO + fo + (unsigned)o * 128u, 16));
                    rdy = max(rdy, go);
                    if (rdy < s) { if (s < TAILS || go < s - 3) __builtin_amdgcn_s_sleep(OSLP); else __builtin_amdgcn_s_sleep(OSLP3); }
                } else {
                    if (!__any((int)__builtin_amdgcn_raw_buffer_load_b32(rwin, tfoff, 0u, 16) < s)) rdy = s;
                    else __builtin_amdgcn_s_sleep(OSLP2);
                }
                if (rdy < s && spin_fail(spins, rwin)) break;
            }
        };
        for (int i = 0; i < o / 10; ++i) __builtin_amdgcn_s_sleep(OST0);
        for (int s = o / 10; s < send; s += 4) {
            u32x4 xr, zf[4][4], zx[4], pt[8], ptm[2];
            RT(0);
            for (unsigned spins = 0; zrdy < s;) {
                asm volatile("" ::: "memory");
                int f = (int)__builtin_amdgcn_raw_buffer_load_b32(rwin, zfoff, 0u, 16);
                f = min(f, __builtin_amdgcn_update_dpp(f, f, 0xB1, 0xF, 0xF, false)); f = min(f, __builtin_amdgcn_update_dpp(f, f, 0x4E, 0xF, 0xF, false));
                f = min(f, __builtin_amdgcn_update_dpp(f, f, 0x141, 0xF, 0xF, false));
                zrdy = seg_done(MXV, __builtin_amdgcn_readlane(f, 0), __builtin_amdgcn_readlane(f, 32));
                if (zrdy >= s || spin_fail(spins, rwin)) break;
                __builtin_amdgcn_s_sleep(OSLP);
            }
            if (w >= 4) xr = __builtin_amdgcn_raw_buffer_load_b128(rws, ooff, O_X1 + (unsigned)(s * SEG * D_ * 2), 16);
#pragma unroll
            for (int q = 0; q < 16; ++q) zf[q >> 2][q & 3] = __builtin_amdgcn_raw_buffer_load_b128(rws, ztoff + (q & 3) * 1024u, O_Z + (unsigned)((s - layer) * (int)ZTSEG) * 2u + (q >> 2) * (H_ * 1024u), 16);
            if (s == 61) SST(27);
            wait_rdy(s);
            if (s == 61) SST(28);
            RT(1);
            const unsigned ptb = O_PT + (unsigned)(s * PTSEG) * 2u;
#pragma unroll
            for (int q = 0; q < 8; ++q) pt[q] = __builtin_amdgcn_raw_buffer_load_b128(rws, (q & 1 ? ptoff : ptoff0) + q * 1024u, ptb, 16);
#pragma unroll
            for (int q = 0; q < 2; ++q) ptm[q] = __builtin_amdgcn_raw_buffer_load_b128(rws, ptmoff + q * 8192u, ptb, 16);
#pragma unroll
            for (int q = 0; q < 4; ++q) zx[q] = __builtin_amdgcn_raw_buffer_load_b128(rws, tid * 16u, O_Z2 + (unsigned)(s * NOWN + 4 * cg + q) * 8192u, 16);
            char* op = smem + ((s >> 2) & 1) * 65536;
            f32x4 c[8];
#pragma unroll
            for (int u = 0; u < 8; ++u) {
                c[u] = z4;
#pragma unroll
                for (int q = 0; q < 4; ++q) c[u] = mfma32(zf[u >> 1][q], pt[2 * q + (u & 1)], c[u]);
            }
#pragma unroll
            for (int u = 0; u < 8; ++u) {
                const int tq = u & 1, t = u >> 1;
                c[u] = mfma32((u32x4){zx[t][0], zx[t][2], zx[t][1], zx[t][3]}, ptm[tq], c[u]);
                *(f32x4*)(op + w * 8192 + (t >> 1) * 4096 + (16 * tq + fr) * 128 + ((((t & 1) * 4 + fq) ^ (fr & 7)) << 4)) = c[u];
            }
            BAR();
            if (s == 61) SST(30);
            if (w >= 4) {
                const int tk = (w & 1) * 16 + (lane >> 2);
                const char* s0 = op + (w & 2) * 2048 + tk * 128 + (((2 * (lane & 3)) ^ (tk & 7)) << 4);
                const char* s1 = op + (w & 2) * 2048 + tk * 128 + (((2 * (lane & 3) + 1) ^ (tk & 7)) << 4);
                f32x4 x = *(const f32x4*)s0, y = *(const f32x4*)s1;
#pragma unroll
                for (int ww = 1; ww < 8; ++ww) { x += *(const f32x4*)(s0 + ww * 8192); y += *(const f32x4*)(s1 + ww * 8192); }
                const u32x4 r = {pack2(lo2f(xr[0]) + bfr(x[0]), hi2f(xr[0]) + bfr(x[1])), pack2(lo2f(xr[1]) + bfr(x[2]), hi2f(xr[1]) + bfr(x[3])),
                                 pack2(lo2f(xr[2]) + bfr(y[0]), hi2f(xr[2]) + bfr(y[1])), pack2(lo2f(xr[3]) + bfr(y[2]), hi2f(xr[3]) + bfr(y[3]))};
                if (layer) st128(r, rout(), ooff, (unsigned)(s * SEG * D_ * 2), OAUX);
                else {
                    st128(nosent4(r), rout(), ooff, (unsigned)(s * SEG * D_ * 2), 16);
                    if (s < 56) {
                        VMWAIT();
                        __builtin_amdgcn_raw_buffer_store_b32(0u, rwin, LANE0, W_OUT + (unsigned)(s * 64 + 4 * cg + (w & 3)) * 4u, 16);
                    }
                }
            } else __builtin_amdgcn_s_sleep(OSL4);
            if (s < 8) SST(8 + s);
            if (s >= 56) SST(s - 40);
            RT(2);
            RTS();
        }
    };
    auto a1_units = [&](auto late) INL {
        constexpr bool LATE = decltype(late)::v;
        int tid = mytid();
        RowTw tw;
        if (LATE) row_tw(tw, tid);
        for (int u = LATE ? bid - NWRK + 224 : ((bid - NXT) >> 3) + 4 * (bid & 7); u < (LATE ? 256 : 224); u += LATE ? 16 : 24) {
            if (!LATE) tid = mytid();
            if (w == 4) {
                for (unsigned spins = 0;;) {
                    asm volatile("" ::: "memory");
                    if (LATE) {
                        const int go = __builtin_amdgcn_readfirstlane(__builtin_amdgcn_raw_buffer_load_b32(rwin, 0u, W_GO + (unsigned)(u & 31) * 128u, 16)) - min((u >> 2) - 2, NSEG - 4);
                        if (go >= 0) break;
                        if (go < -3) __builtin_amdgcn_s_sleep(127);
                    } else if (!__any(__builtin_amdgcn_raw_buffer_load_b32(rwin, min(fresh_lane(), (unsigned)NOWN - 1u) * 4u, W_OUT + (unsigned)(u >> 2) * 256u, 16) != 0u)) break;
                    if (spin_fail(spins, rwin)) break;
                    if (u < 200) __builtin_amdgcn_s_sleep(WSLP); else __builtin_amdgcn_s_sleep(16);
                }
            }
            BAR();
            SST(24);
            a1_unit<4, LATE>(mkrsrc(ws + OFF_X2, S_ * D_ * 2), u * 8, rws, rwin, tw, tid, fs);
            if (!LATE) VMWAIT();
            FST(8);
            BAR0();
            FST(9);
            if (!LATE && w == 0) __builtin_amdgcn_raw_buffer_store_b64(__builtin_bit_cast(u32x2, tag + 9ull), rwin, LANE0, W_A1 + (unsigned)u * 128u, 16);
            SST(25);
            FSW(7);
            if (!LATE && u < 176) __builtin_amdgcn_s_sleep(A1S);
        }
    };
    u32x8 wt[2][10];
#define PINW() asm volatile("" : "+{v[0:7]}"(wt[0][0]), "+{v[8:15]}"(wt[1][0]), "+{v[16:23]}"(wt[0][1]), "+{v[24:31]}"(wt[1][1]), "+{v[32:39]}"(wt[0][2]), "+{v[40:47]}"(wt[1][2]), "+{v[48:55]}"(wt[0][3]), "+{v[56:63]}"(wt[1][3]), "+{v[64:71]}"(wt[0][4]), "+{v[72:79]}"(wt[1][4]), "+{v[80:87]}"(wt[0][5]), "+{v[88:95]}"(wt[1][5]), "+{v[96:103]}"(wt[0][6]), "+{v[104:111]}"(wt[1][6]), "+{v[112:119]}"(wt[0][7]), "+{v[120:127]}"(wt[1][7]), "+{v[128:135]}"(wt[0][8]), "+{v[136:143]}"(wt[1][8]), "+{v[144:151]}"(wt[0][9]), "+{v[152:159]}"(wt[1][9]))
#define KILLW() asm volatile("" : "={v[0:7]}"(wt[0][0]), "={v[8:15]}"(wt[1][0]), "={v[16:23]}"(wt[0][1]), "={v[24:31]}"(wt[1][1]), "={v[32:39]}"(wt[0][2]), "={v[40:47]}"(wt[1][2]), "={v[48:55]}"(wt[0][3]), "={v[56:63]}"(wt[1][3]), "={v[64:71]}"(wt[0][4]), "={v[72:79]}"(wt[1][4]), "={v[80:87]}"(wt[0][5]), "={v[88:95]}"(wt[1][5]), "={v[96:103]}"(wt[0][6]), "={v[104:111]}"(wt[1][6]), "={v[112:119]}"(wt[0][7]), "={v[120:127]}"(wt[1][7]), "={v[128:135]}"(wt[0][8]), "={v[136:143]}"(wt[1][8]), "={v[144:151]}"(wt[0][9]), "={v[152:159]}"(wt[1][9]))
    if (blockIdx.x < 64) {
        for (;;) {
            load_qv(wt, bid & 31, bid >= 32, [&]() INL {
                rot0 = __builtin_amdgcn_raw_buffer_load_b64(rws, 0u, 128u, 16);
                if (w == 0) wait_tags(rwin, 0, 1, 64u, tag + 8ull, true);
                if (w == 7) clk_init(ck);
                if (w == 1) { const unsigned x = __builtin_amdgcn_raw_buffer_load_b32(mkrsrc(arg(A_MEM0), NM * D_ * 2), fresh_lane() * 160u, 0, 0); asm volatile("" :: "v"(x)); }
            });
            const unsigned x = get_rot();
            if (__builtin_expect(x == vx, 1)) break;
            set_role(x);
        }
        SST(1);
        PINW();
    } else {
        if (bid >= NWRK) probe();
        KILLW();
    }
    for (; layer < 2; ++layer) {
        new_layer();
        PINW();
        BAR();
        if (qx || (layer ? bid < 96 : (bid >= 32 && bid < 64) || (bid >= 104 && bid < NREC) || xq)) {
            const int ci = qx ? bid - 80 : bid < 96 ? bid : bid < NREC ? bid - 104 : 64 + (bid - NREC) / 10 * 8 + (bid - NREC) % 10;
            SST(26);
            a2_unit(ci >= 100 ? ci - 100 : ci + 60, layer, rws, rwin, mytid(), [&](int k) INL {
                if (k == 2) SST(28);
                else if (w == 0) {
                    if (layer && bid >= 64) for (unsigned spins = 0;;) {
                        asm volatile("" ::: "memory");
                        if (__all(__builtin_bit_cast(u64, __builtin_amdgcn_raw_buffer_load_b64(rwin, 0u, W_A1 + (unsigned)(200 + (bid & 15)) * 128u, 16)) == tag + 9ull) || spin_fail(spins, rwin)) break;
                        __builtin_amdgcn_s_sleep(100);
                    }
                    if (layer && bid >= 64) rec_end(GATE);
                    wait_tags(rwin, 0, 4, layer ? 32u : 64u, tag + 8ull + layer, !layer);
                    SST(27);
                }
            }, fs);
            SST(29);
            VMWAIT();
            FST(8);
            BAR0();
            FST(9);
            if (w == 0) {
                __builtin_amdgcn_raw_buffer_store_b32(0u, rwin, LANE0, W_COL + fo + (unsigned)ci * 128u, 16);
                if (bid == NQT && layer == 1) __builtin_amdgcn_raw_buffer_store_b64(__builtin_bit_cast(u32x2, tag), rws, LANE0, 0u, 16);
            }
            SST(30);
            FSW(1 + layer);
        }
#define NEXTB() do { PINW(); asm volatile("" : "+s"(bid), "+s"(w)); } while (0)
        NEXTB();
        if (bid < 32) { fresh_rsrc(); qk_role(wt); PINW(); }
        NEXTB();
        const int hh = bid < 96 ? bid & 31 : bid < NQT ? (bid - NREC) / 10 * 8 + (bid - NREC) % 10 : (bid - NQT) & 31;
        if (((qx || (bid >= 32 && bid < 64)) && layer == 0) || xq) { if (layer) { if (w == 0) rec_end(GATX); BAR0(); } load_qv(wt, hh, xt || bid < 64, []() {}); SST(1); PINW(); }
        NEXTB();
        if (bid >= 32 && bid < 64) { if (layer == 0) near_init(); fresh_rsrc(); vo_role(wt); PINW(); }
        NEXTB();
        if ((bid >= 64 && bid < 96) || xt) {
            fresh_rsrc();
            vt_role(wt, hh, xt ? MXV : ~MXV);
            if (xt) {
                const unsigned g0 = (unsigned)(hh * 512 + mytid()) * 16u;
                if (layer == 0) for (unsigned i = 0; i < 13; ++i) {
                    st128(SENT, i < 7 ? mkrsrc(ws + O_ARM, O_ARM1 - O_ARM) : i < 12 ? mkrsrc(ws + O_Z - ZTSEG * 2, ZTSEG * 2) : mkrsrc(arg(1) + (W_ARM - W_LAYER) / 2, W_LAYER), g0, (i < 7 ? i : i < 12 ? i - 7 : 0) * 262144u, 16);
                    __builtin_amdgcn_s_sleep(40);
                }
                static_assert(O_ARM + 7u * 262144u >= O_ARM1 && ZTSEG * 2 <= 5u * 262144u && W_LAYER <= 262144u && !(MXV >> 40), "arming");
                VMWAIT();
                BAR0();
                if (w >= 4) __builtin_amdgcn_raw_buffer_store_b32(NSEG - 1, rwin, LANE0, W_FZ + fo + fz_at(hh, w - 4) + 4096u, 16);
                if (layer == 0 && (bid & 7) < 6) a1_units(IC<0>());
            }
            PINW();
        }
        NEXTB();
        if ((qx && !xt) || xq) { fresh_rsrc(); qt_role(wt, hh, IC<1>()); PINW(); }
        NEXTB();
        if (bid >= 96 && !qx) {
            const int fi = xq ? 256 : bid >= NWRK ? 255 - bid : bid >= NREC ? 8 + (bid - NREC) / 10 * 2 + (bid - NREC) % 10 : bid - 72, tid = mytid();
            RowTw tw;
            row_tw(tw, tid);
            if (layer == 0 && fi >= 16 && fi < 32) { if (w == 0) wait_tags(rwin, 0, 1, 64u, tag + 8ull, true); BAR0(); }
            if (layer && fi < 64) {
                for (unsigned spins = 0;;) {
                    asm volatile("" ::: "memory");
                    if (__all(__builtin_bit_cast(u64, __builtin_amdgcn_raw_buffer_load_b64(rwin, 0u, W_A1 + (unsigned)fi * 128u, 16)) == tag + 9ull) || spin_fail(spins, rwin)) break;
                    __builtin_amdgcn_s_sleep(8);
                }
            }
            bool cols = false;
            for (int u = fi; u < 256; u += 16) {
                a3_unit(mkrsrc(layer ? (const u16*)(ws + OFF_X2) : arg(A_X), S_ * D_ * 2), u * 8, layer, rws, rwin, tw, tid, [&]() INL {
                    if (cols) return;
                    cols = true;
                    if (w == 7) {
                        const unsigned l = fresh_lane();
                        unsigned o[3] = {l * 128u, l * 128u + 8192u, l < 32u ? l * 128u + 16384u : 0x40000000u};
                        for (unsigned spins = 0;;) {
                            asm volatile("" ::: "memory");
                            unsigned v[3];
#pragma unroll
                            for (int g = 0; g < 3; ++g) v[g] = __builtin_amdgcn_raw_buffer_load_b32(rwin, o[g], W_COL + fo, 16);
#pragma unroll
                            for (int g = 0; g < 3; ++g) if (v[g] == 0u) o[g] = 0x40000000u;
                            if (!__any((v[0] | v[1] | v[2]) != 0u) || spin_fail(spins, rwin)) break;
                            __builtin_amdgcn_s_sleep(CSLP);
                        }
                    }
                    BAR0();
                    SST(6);
                }, fs);
                VMWAIT();
                FST(8);
                BAR0();
                FST(9);
                if (w == 0) __builtin_amdgcn_raw_buffer_store_b32(0u, rwin, LANE0, W_H + fo + (unsigned)((u >> 2) * 128 + (u & 3) * 4), 16);
                if (u < 64) SST(7);
                FSW(u < 64 ? 3 + layer : 5);
                if (fi >= 16) break;
                if (u < 64) u += 48;
            }
            if (bid < NREC) { fresh_rsrc(); own_role(); }
            else if (bid < NQT) out_role();
            else if (layer == 0) a1_units(IC<1>());
            KILLW();
        }
        NEXTB();
        SST(31);
    }
}

constexpr int L_DBG = 0;
extern "C" size_t dbg_offset(int i) {
    const size_t o[] = {OFF_WIN, W_TMO, W_NEAR, OFF_F, OFF_X1, OFF_X2, OFF_H, OFF_WFT};
    return o[i];
}

extern "C" void kernel_launch(void* const* d_in, const int* in_sizes, int n_in, void* d_out, int out_size, void* d_ws, size_t ws_size,
                              hipStream_t stream) {
    char* ws = (char*)d_ws;
    hipFuncSetAttribute((const void*)mega_kernel, hipFuncAttributeMaxDynamicSharedMemorySize, L_END + L_DBG);
    char* win = (char*)(((uintptr_t)(ws + OFF_WIN) + 0x1FFFFFu) & ~(uintptr_t)0x1FFFFFu);
    Args a{ws, win, (const u16*)d_in[0], (const u16*)d_in[1], (const u16*)d_in[2], (const u16*)d_in[4], (const u16*)d_in[5], (const u16*)d_in[6], (const u16*)d_in[7], (const u16*)d_in[8], (u16*)d_out};
    mega_kernel<<<256, 512, L_END + L_DBG, stream>>>(a);
}
```

```cpp
#include <hip/hip_runtime.h>
#include <hip/hip_bf16.h>
#include <stdint.h>

typedef unsigned short u16;
typedef unsigned long long u64;
typedef __attribute__((ext_vector_type(8))) __bf16 bf16x8;
typedef __attribute__((ext_vector_type(4))) short s16x4;
typedef __attribute__((ext_vector_type(4))) float f32x4;
typedef __attribute__((ext_vector_type(8))) float f32x8;
typedef __attribute__((ext_vector_type(16))) float f32x16;
typedef __attribute__((ext_vector_type(4))) unsigned u32x4;
typedef __attribute__((ext_vector_type(2))) unsigned u32x2;
typedef __attribute__((ext_vector_type(8))) unsigned u32x8;
typedef __attribute__((ext_vector_type(16))) __bf16 bf16x16;
typedef __attribute__((ext_vector_type(2))) unsigned short u16x2;
#define LAS __attribute__((address_space(3)))
#define INL __attribute__((always_inline))

constexpr int S_ = 2048, D_ = 640, DF = 321, H_ = 32, DH = 128, HD = 4096, NM = 8, SEG = 32, NSEG = 64;
constexpr int NOWN = 40;
constexpr int NCOL = 160;
constexpr size_t ZTSEG = (size_t)NOWN * H_ * 512;
constexpr unsigned PAIR = 16384, HALF = 8192;
constexpr unsigned W_MEM = 0, W_T = W_MEM + 24 * PAIR, W_P = 33 * PAIR, W_PROBE = 74 * PAIR, W_GO = W_PROBE + 2 * PAIR, W_FZ = W_GO + 5120, W_FPT = W_FZ + 8192, W_FZ2 = W_FPT + 4096,
                   W_FQ = W_FZ2 + 4096, W_H = W_FQ + 8192, W_COL = W_H + HALF, W_PL = W_COL + 20480, W_LAYER = W_PL + 2 * 81920 - W_GO, W_OUT = 32 * PAIR, W_MISC = W_OUT + 14336,
                   W_ARM = W_GO + 2 * W_LAYER, W_A1 = 106 * PAIR, W_ZM = 2u * 1024 * 1024, W_END = W_ZM + 120 * PAIR;
static_assert(W_ARM <= W_A1 && W_ARM % 1024 == 0, "window");
__device__ __forceinline__ unsigned p_slot(int s) { return W_P + (unsigned)((s & 3) * 10 + (((s & 3) + 1) >> 2)) * PAIR; }
constexpr unsigned W_TMO = W_MISC, W_NEAR = W_MISC + 128;
constexpr int PTSEG = 32768 + 8192;

#define FPAD 256
constexpr unsigned FST_ = S_ * 16 + FPAD, GST = 4096 + FPAD, WST = S_ * 8 + FPAD;
constexpr size_t al(size_t x) { return (x + 255) & ~(size_t)255; }
constexpr size_t OFF_CTL  = 0;
constexpr size_t OFF_WFT  = 4096 + 4 * 1024 * 1024;
constexpr size_t OFF_F    = OFF_WFT + al((size_t)(NCOL + 2) * WST);
constexpr size_t OFF_X1   = OFF_F + al((size_t)NCOL * FST_);
constexpr size_t OFF_X2   = OFF_X1 + al((size_t)S_ * D_ * 2);
constexpr size_t OFF_FL   = OFF_X2 + al((size_t)S_ * D_ * 2);
constexpr int NHF = 5;
constexpr size_t OFF_H    = OFF_FL + 3 * (size_t)NCOL * GST + NHF * 40960;
constexpr size_t OFF_Z    = OFF_H + al((size_t)S_ * D_ * 2) + ZTSEG * 2;
constexpr size_t OFF_Z2   = OFF_Z + al(ZTSEG * NSEG * 2);
constexpr size_t OFF_PT   = OFF_Z2 + al((size_t)NSEG * NOWN * 8192);
constexpr size_t OFF_QK   = OFF_PT + al((size_t)NSEG * PTSEG * 2);
constexpr size_t OFF_WIN  = OFF_QK + (size_t)NSEG * H_ * 16384;
constexpr size_t OFF_END  = OFF_WIN + (size_t)6 * 1024 * 1024;
static_assert(OFF_END <= (size_t)256 * 1024 * 1024, "workspace");
constexpr unsigned O_FL = (unsigned)OFF_FL, O_ARM = O_FL - 256 * D_ * 2, O_ARM1 = O_FL + 2 * NCOL * GST, O_WFT = (unsigned)OFF_WFT, O_F = (unsigned)OFF_F, O_H = (unsigned)OFF_H, O_X1 = (unsigned)OFF_X1, O_Z = (unsigned)OFF_Z, O_Z2 = (unsigned)OFF_Z2, O_PT = (unsigned)OFF_PT, O_QK = (unsigned)OFF_QK;

constexpr double PI_ = 3.14159265358979323846;
constexpr double tay(double x, int s) {
    double t = s ? x : 1.0, r = t;
    for (int i = s + 1; i < 18; i += 2) { t *= -x * x / (double)(i * (i + 1)); r += t; }
    return r;
}
struct Cs { float c, s; };
constexpr Cs cis(int m, int n) {
    m %= n;
    const int q = (m * 8 / n + 1) / 2;
    const double x = 2.0 * PI_ * (double)(4 * m - q * n) / (double)(4 * n), c = tay(x, 0), s = tay(x, 1);
    return (q & 3) == 0 ? Cs{(float)c, (float)-s} : (q & 3) == 1 ? Cs{(float)-s, (float)-c} : (q & 3) == 2 ? Cs{(float)-c, (float)s} : Cs{(float)s, (float)c};
}
template <int NI> struct alignas(16) Tw7 { Cs v[4][NI][2]; };
template <int NI, int N, int KIND> constexpr Tw7<NI> mk_tw() {
    Tw7<NI> r{};
    for (int i = 0; i < NI; ++i)
        for (int q = 1; q <= 8; ++q) r.v[(q - 1) >> 1][i][(q - 1) & 1] = cis((KIND == 0 ? i : KIND == 1 ? 8 * (i >> 3) : (i >> 3) + 10 * (i & 7)) * q, N);
    return r;
}
struct TwCol { Tw7<256> a; Tw7<32> b; Tw7<4> c; };
static __device__ const TwCol TWC = {mk_tw<256, 2048, 0>(), mk_tw<32, 256, 0>(), mk_tw<4, 32, 0>()};
static __device__ const Tw7<80> TWR2 = mk_tw<80, 640, 1>();
static __device__ const Tw7<80> TWR3 = mk_tw<80, 640, 2>();

#define st128(v, r, vo, so, aux) do { u32x4 d_ = (v); asm volatile("" : "+v"(d_)); __builtin_amdgcn_raw_buffer_store_b128(d_, r, vo, so, aux); asm volatile("s_nop 1" :: "v"(d_)); } while (0)
__device__ __forceinline__ float bf2f(u16 v) { return __uint_as_float((unsigned)v << 16); }
__device__ __forceinline__ u16 f2bf(float f) { __bf16 b = (__bf16)f; return __builtin_bit_cast(u16, b); }
__device__ __forceinline__ float bfr(float f) { return (float)(__bf16)f; }
typedef __attribute__((ext_vector_type(2))) float f32x2;
typedef __attribute__((ext_vector_type(2))) __bf16 bf16x2;
__device__ __forceinline__ unsigned pack2(float a, float b) { return __builtin_bit_cast(unsigned, __builtin_convertvector((f32x2){a, b}, bf16x2)); }
__device__ __forceinline__ float lo2f(unsigned v) { return __uint_as_float(v << 16); }
__device__ __forceinline__ float hi2f(unsigned v) { return __uint_as_float(v & 0xFFFF0000u); }
__device__ __forceinline__ float fmx(float a, float b) { return __builtin_elementwise_maximum(a, b); }
__device__ __forceinline__ f32x4 mfma32(u32x4 a, u32x4 b, f32x4 c) {
    return __builtin_amdgcn_mfma_f32_16x16x32_bf16(__builtin_bit_cast(bf16x8, a), __builtin_bit_cast(bf16x8, b), c, 0, 0, 0);
}
__device__ __forceinline__ f32x4 mfma16(u32x2 a, u32x2 b, f32x4 c) {
    return __builtin_amdgcn_mfma_f32_16x16x16bf16_1k(__builtin_bit_cast(s16x4, a), __builtin_bit_cast(s16x4, b), c, 0, 0, 0);
}
__device__ __forceinline__ f32x4 smfmac(u32x4 a, u32x8 b, f32x4 c, int idx) {
    return __builtin_amdgcn_smfmac_f32_16x16x64_bf16(__builtin_bit_cast(bf16x8, a), __builtin_bit_cast(bf16x16, b), c, idx, 0, 0);
}
__device__ __forceinline__ int pi16(int k) { return ((k & 3) << 2) | ((k >> 1) & 2) | (k >> 3); }
__device__ __forceinline__ float addswap32(float a, float b) {
    auto r = __builtin_amdgcn_permlane32_swap(__float_as_uint(a), __float_as_uint(b), false, false);
    return __uint_as_float(r[0]) + __uint_as_float(r[1]);
}
__device__ __forceinline__ float xmax3216(float v) {
    auto r = __builtin_amdgcn_permlane32_swap(__float_as_uint(v), __float_as_uint(v), false, false);
    v = fmx(__uint_as_float(r[0]), __uint_as_float(r[1]));
    auto q = __builtin_amdgcn_permlane16_swap(__float_as_uint(v), __float_as_uint(v), false, false);
    return fmx(__uint_as_float(q[0]), __uint_as_float(q[1]));
}
__device__ __forceinline__ float xsum3216(float v) {
    auto r = __builtin_amdgcn_permlane32_swap(__float_as_uint(v), __float_as_uint(v), false, false);
    v = __uint_as_float(r[0]) + __uint_as_float(r[1]);
    auto q = __builtin_amdgcn_permlane16_swap(__float_as_uint(v), __float_as_uint(v), false, false);
    return __uint_as_float(q[0]) + __uint_as_float(q[1]);
}
template <int CTRL> __device__ __forceinline__ float dppf(float v) { return __uint_as_float((unsigned)__builtin_amdgcn_update_dpp(0, (int)__float_as_uint(v), CTRL, 0xF, 0xF, false)); }
__device__ __forceinline__ float wsum(float v) {
    v += dppf<0xB1>(v); v += dppf<0x4E>(v); v += dppf<0x141>(v); v += dppf<0x140>(v);
    return xsum3216(v);
}
__device__ __forceinline__ unsigned ror8(unsigned v) { return (unsigned)__builtin_amdgcn_update_dpp(0, (int)v, 0x128, 0xF, 0xF, false); }
__device__ __forceinline__ float ror8f(float v) { return __uint_as_float(ror8(__float_as_uint(v))); }
__device__ __forceinline__ unsigned nosent(unsigned v) {
    u16x2 lim = {0xFFFE, 0xFFFE};
    return __builtin_bit_cast(unsigned, __builtin_elementwise_min(__builtin_bit_cast(u16x2, v), lim));
}
__device__ __forceinline__ unsigned pkmax(unsigned a, unsigned b) {
    return __builtin_bit_cast(unsigned, __builtin_elementwise_max(__builtin_bit_cast(u16x2, a), __builtin_bit_cast(u16x2, b)));
}
__device__ __forceinline__ bool has_sent(u32x4 v) {
    unsigned m = pkmax(pkmax(v.x, v.y), pkmax(v.z, v.w));
    return (m & 0xFFFFu) == 0xFFFFu || (m >> 16) == 0xFFFFu;
}

__device__ __forceinline__ unsigned nosent1(unsigned v) { return min(v, 0xFFFFFFFEu); }
__device__ __forceinline__ unsigned smax(u32x4 v) { return max(v[1], v[3]); }
__device__ __forceinline__ u32x4 sent4() { unsigned x; asm volatile("v_mov_b32 %0, -1" : "=v"(x)); return (u32x4){x, x, x, x}; }
#define SENT sent4()
__device__ __forceinline__ f32x4 zero4() { f32x2 x, y; asm volatile("v_mov_b64 %0, 0\n\tv_mov_b64 %1, 0" : "=v"(x), "=v"(y)); return (f32x4){x[0], x[1], y[0], y[1]}; }
__device__ __forceinline__ u32x4 nosent4(u32x4 v) { return (u32x4){nosent1(v[0]), nosent1(v[1]), nosent1(v[2]), nosent1(v[3])}; }
#define SPIN_CAP 400000u
__device__ __forceinline__ void set_tmo(__amdgpu_buffer_rsrc_t rwin) { __builtin_amdgcn_raw_buffer_store_b32(0u, rwin, 0u, W_TMO, 16); }
__device__ __forceinline__ bool spin_fail(unsigned& spins, __amdgpu_buffer_rsrc_t rwin) {
    if (++spins > SPIN_CAP) { set_tmo(rwin); return true; }
    return (spins & 1023u) == 0u && __builtin_amdgcn_readfirstlane(__builtin_amdgcn_raw_buffer_load_b32(rwin, 0u, W_TMO, 16)) == 0u;
}
#define GATE 2
#define GATX 2
__device__ __forceinline__ void tell_at(__amdgpu_buffer_rsrc_t rwin, unsigned so, unsigned d) {
    __builtin_amdgcn_raw_buffer_store_b32(((unsigned)__builtin_amdgcn_s_memrealtime() + d) & 0xFFFFFFu, rwin, 0u, so, 16);
}
template <int N> __device__ __forceinline__ void wait_told(__amdgpu_buffer_rsrc_t rwin, const unsigned (&vo)[N], unsigned so, int slp) {
    unsigned v[N];
#pragma unroll
    for (int g = 0; g < N; ++g) v[g] = ~0u;
    for (unsigned spins = 0;;) {
        asm volatile("" ::: "memory");
        unsigned x[N], m = 0;
#pragma unroll
        for (int g = 0; g < N; ++g) x[g] = __builtin_amdgcn_raw_buffer_load_b32(rwin, v[g] == ~0u ? vo[g] : 0x40000000u, so, 16);
#pragma unroll
        for (int g = 0; g < N; ++g) { if (v[g] == ~0u) v[g] = x[g]; m = max(m, v[g]); }
        if (!__any(m == ~0u) || spin_fail(spins, rwin)) break;
        if (slp) __builtin_amdgcn_s_sleep(8); else __builtin_amdgcn_s_sleep(3);
    }
    for (int i = 0; i < 400; ++i) {
        const unsigned t = (unsigned)__builtin_amdgcn_s_memrealtime();
        bool e = false;
#pragma unroll
        for (int g = 0; g < N; ++g) e |= ((v[g] - t) << 8) - 1u < 0x7FFFFFFFu;
        if (!__any(e)) break;
    }
}
#define TG 250
#define TH 155
#define TH0 185
#define DVT0 1000
#define A2P 0
#define TWG 1
#define PS1 3
#define PS2 3
#define PS3 2

typedef f32x2 c2;
__device__ __forceinline__ c2 swp(c2 a) { return __builtin_shufflevector(a, a, 1, 0); }
__device__ __forceinline__ c2 fm(c2 a, c2 b, c2 c) { return __builtin_elementwise_fma(a, b, c); }
__device__ __forceinline__ c2 bc(float x) { return (c2){x, x}; }
__device__ __forceinline__ c2 sgn() { c2 s = {-1.f, 1.f}; asm("" : "+s"(s)); return s; }
template <int SG> __device__ __forceinline__ c2 addi(c2 a, c2 b) { return fm(swp(b), SG > 0 ? sgn() : swp(sgn()), a); }
template <int SG> __device__ __forceinline__ c2 cfin(c2 a, c2 w, c2 t) {
    c2 d;
    if (SG < 0) asm("v_pk_fma_f32 %0, %1, %2, %3 op_sel:[1,1,0] op_sel_hi:[0,1,1] neg_lo:[1,0,0]" : "=v"(d) : "v"(a), "v"(w), "v"(t));
    else asm("v_pk_fma_f32 %0, %1, %2, %3 op_sel:[1,1,0] op_sel_hi:[0,1,1] neg_hi:[1,0,0]" : "=v"(d) : "v"(a), "v"(w), "v"(t));
    return d;
}
struct Tw { f32x4 v[4]; };
template <int NI> __device__ __forceinline__ void ldtw(Tw& t, const Tw7<NI>& tab, int i) {
#pragma unroll
    for (int j = 0; j < 4; ++j) t.v[j] = *(const f32x4*)tab.v[j][i];
}
template <int NI> __device__ __forceinline__ void ldtw(Tw& t, const LAS f32x4* p) {
#pragma unroll
    for (int j = 0; j < 4; ++j) t.v[j] = p[j * NI];
}
template <int SG> __device__ __forceinline__ void twid(c2 (&u)[8], const Tw& t) {
    c2 w[8], m[8];
#pragma unroll
    for (int r = 1; r < 8; ++r) {
        const f32x4 x = t.v[(r - 1) >> 1];
        w[r] = r & 1 ? (c2){x[0], x[1]} : (c2){x[2], x[3]};
        m[r] = u[r] * bc(w[r].x);
    }
    if (TWG) asm("" : "+v"(m[1]), "+v"(m[2]), "+v"(m[3]), "+v"(m[4]), "+v"(m[5]), "+v"(m[6]), "+v"(m[7]));
#pragma unroll
    for (int r = 1; r < 8; ++r) u[r] = cfin<SG>(u[r], w[r], m[r]);
}
template <int SG> __device__ __forceinline__ void dft4(c2& a, c2& b, c2& c, c2& d) {
    const c2 t0 = a + c, t1 = a - c, t2 = b + d, t3 = b - d;
    a = t0 + t2; c = t0 - t2; b = addi<SG>(t1, t3); d = addi<-SG>(t1, t3);
}
template <int SG> __device__ __forceinline__ void dft8(c2 (&u)[8]) {
    c2 e0 = u[0], e1 = u[2], e2 = u[4], e3 = u[6], o0 = u[1], o1 = u[3], o2 = u[5], o3 = u[7];
    dft4<SG>(e0, e1, e2, e3); dft4<SG>(o0, o1, o2, o3);
    const c2 r = bc(0.70710678118654752440f);
    o1 = addi<SG>(o1, o1); o3 = addi<-SG>(o3, o3);
    u[0] = e0 + o0; u[4] = e0 - o0;
    u[1] = fm(o1, r, e1); u[5] = fm(o1, -r, e1);
    u[2] = addi<SG>(e2, o2); u[6] = addi<-SG>(e2, o2);
    u[3] = fm(o3, -r, e3); u[7] = fm(o3, r, e3);
}
template <int SG> __device__ __forceinline__ void dft5(c2& x0, c2& x1, c2& x2, c2& x3, c2& x4) {
    const c2 c1 = bc(0.30901699437494742410f), c2_ = bc(-0.80901699437494742410f), s1 = bc(0.95105651629515357212f), s2 = bc(0.58778525229247312917f);
    const c2 t1 = x1 + x4, t2 = x2 + x3, t3 = x1 - x4, t4 = x2 - x3;
    const c2 a1 = fm(c2_, t2, fm(c1, t1, x0)), a2 = fm(c1, t2, fm(c2_, t1, x0));
    const c2 b1 = fm(s2, t4, s1 * t3), b2 = fm(-s1, t4, s2 * t3);
    x0 = x0 + t1 + t2;
    x1 = addi<SG>(a1, b1); x4 = addi<-SG>(a1, b1); x2 = addi<SG>(a2, b2); x3 = addi<-SG>(a2, b2);
}
template <int SG> __device__ __forceinline__ void dft10(c2 (&u)[10]) {
    c2 e0 = u[0], e1 = u[2], e2 = u[4], e3 = u[6], e4 = u[8], o0 = u[5], o1 = u[7], o2 = u[9], o3 = u[1], o4 = u[3];
    dft5<SG>(e0, e1, e2, e3, e4); dft5<SG>(o0, o1, o2, o3, o4);
    u[0] = e0 + o0; u[5] = e0 - o0; u[6] = e1 + o1; u[1] = e1 - o1; u[2] = e2 + o2; u[7] = e2 - o2; u[8] = e3 + o3; u[3] = e3 - o3; u[4] = e4 + o4; u[9] = e4 - o4;
}
__device__ __forceinline__ void swap32(c2& v) { auto r = __builtin_amdgcn_permlane32_swap(__float_as_uint(v.x), __float_as_uint(v.y), false, false); v = (c2){__uint_as_float(r[0]), __uint_as_float(r[1])}; }
template <int B> __device__ __forceinline__ void swapl(c2& a, c2& b) {
#pragma unroll
    for (int i = 0; i < 2; ++i) {
        auto r = B == 5 ? __builtin_amdgcn_permlane32_swap(__float_as_uint(a[i]), __float_as_uint(b[i]), false, false) : __builtin_amdgcn_permlane16_swap(__float_as_uint(a[i]), __float_as_uint(b[i]), false, false);
        a[i] = __uint_as_float(r[0]); b[i] = __uint_as_float(r[1]);
    }
}
__device__ __forceinline__ c2 unpk(unsigned v) { return (c2){lo2f(v), hi2f(v)}; }
__device__ __forceinline__ float hsum32(c2 s) {
    float v = s.x + s.y;
    v += dppf<0xB1>(v); v += dppf<0x4E>(v); v += dppf<0x141>(v); v += dppf<0x140>(v);
    auto q = __builtin_amdgcn_permlane16_swap(__float_as_uint(v), __float_as_uint(v), false, false);
    return __uint_as_float(q[0]) + __uint_as_float(q[1]);
}
__device__ __forceinline__ void ln_row(const c2 (&v)[10], unsigned (&o)[10]) {
    const c2 s = ((v[0] + v[1]) + (v[2] + v[3])) + ((v[4] + v[5]) + (v[6] + v[7])) + (v[8] + v[9]);
    const float mean = hsum32(s) * (1.f / 640.f);
    c2 q0 = bc(0.f), q1 = bc(0.f);
#pragma unroll
    for (int r = 0; r < 10; r += 2) { const c2 d = v[r] - bc(mean), e = v[r + 1] - bc(mean); q0 = fm(d, d, q0); q1 = fm(e, e, q1); }
    const float mb = bfr(mean);
    c2 d[10];
#pragma unroll
    for (int r = 0; r < 10; ++r) { const c2 x = v[r] - bc(mb); d[r] = unpk(pack2(x.x, x.y)); }
    const float var = hsum32(q0 + q1) * (1.f / 640.f);
    const float rs = __builtin_amdgcn_rcpf(bfr(sqrtf(bfr(bfr(var) + 1e-5f))));
#pragma unroll
    for (int r = 0; r < 10; ++r) { const c2 x = d[r] * bc(rs); o[r] = pack2(x.x, x.y); }
}

__device__ __forceinline__ __amdgpu_buffer_rsrc_t mkrsrc(const void* p, unsigned bytes) {
    return __builtin_amdgcn_make_buffer_rsrc((void*)p, 0, bytes, 0x00020000);
}
__device__ __forceinline__ unsigned xcc_id() { unsigned x; asm volatile("s_getreg_b32 %0, hwreg(HW_REG_XCC_ID)" : "=s"(x)); return x & 15u; }
#define VMWAIT() do { asm volatile("" ::: "memory"); __builtin_amdgcn_s_waitcnt(0x0F70); asm volatile("" ::: "memory"); } while (0)
#define BAR() asm volatile("s_waitcnt lgkmcnt(0)\n\ts_barrier" ::: "memory")
#define BAR0() asm volatile("s_barrier" ::: "memory")

#define FST(k) do {} while (0)
#define FSW(k) do {} while (0)
extern __shared__ __attribute__((aligned(16))) char smem[];
#define RST(k, lay, wv, sel) do {} while (0)
constexpr int R_C = 8 * 10 * 72 * 8;
constexpr float FSC = 0.5f / ((float)S_ * (float)D_);
__device__ __forceinline__ c2 ldv(const LAS void* p) { return *(const volatile LAS c2*)p; }
struct RowTw { Tw a, b; };
__device__ __forceinline__ void row_tw(RowTw& t, int tid) { const int i = tid % 80; ldtw(t.a, TWR2, i); ldtw(t.b, TWR3, i); }
template <int NF, bool LATE> __device__ __forceinline__ void a1_unit(__amdgpu_buffer_rsrc_t rx, int row0, __amdgpu_buffer_rsrc_t rws, __amdgpu_buffer_rsrc_t rwin, RowTw& tw, int tid, unsigned (&fs)[12]) {
    const int wave = __builtin_amdgcn_readfirstlane(tid >> 6), lane = tid & 63;
    LAS c2* A = (LAS c2*)smem; LAS c2* C = (LAS c2*)(smem + R_C);
    FST(0);
    if (wave < NF) {
        unsigned d[10];
        for (unsigned spins = 0;;) {
            asm volatile("" ::: "memory");
#pragma unroll
            for (int r = 0; r < 10; ++r) d[r] = __builtin_amdgcn_raw_buffer_load_b32(rx, (lane & 31) * 4u + (lane >> 5) * (D_ * 2u) + r * 128u, (unsigned)(row0 + 2 * wave) * (D_ * 2), 16);
            if (!LATE) break;
            unsigned m = d[0];
#pragma unroll
            for (int r = 1; r < 10; ++r) m = max(m, d[r]);
            if (!__any(m == ~0u) || spin_fail(spins, rwin)) break;
            __builtin_amdgcn_s_sleep(PS1);
        }
        c2 u[10];
#pragma unroll
        for (int r = 0; r < 10; ++r) u[r] = unpk(d[r]);
        FST(1);
        if (LATE) RST(16, 0, wave, 0);
        ln_row(u, d);
        FST(2);
#pragma unroll
        for (int r = 0; r < 10; ++r) { u[r] = unpk(d[r]); swap32(u[r]); }
        dft10<-1>(u);
#pragma unroll
        for (int r = 0; r < 10; ++r) A[(wave * 10 + r) * 72 + (lane & 31) + 36 * (lane >> 5)] = u[r];
        FST(3);
    }
    BAR();
    if (NF == 4 && !LATE) row_tw(tw, tid);
    FST(4);
#pragma unroll
    for (int c = 0; c < (NF > 4 ? 2 : 1); ++c) {
        if (tid < (NF > 4 ? (c ? 160 : 480) : 80 * NF)) {
            const int K = (tid >> 3) + 60 * c, m = tid & 7;
            LAS c2* a = A + K * 72;
            c2 u[8];
#pragma unroll
            for (int q = 0; q < 8; ++q) u[q] = ldv(a + (m >> 1) + 36 * (m & 1) + 4 * q);
            twid<-1>(u, tw.a);
            dft8<-1>(u);
#pragma unroll
            for (int p = 0; p < 8; ++p) a[9 * p + m] = u[p];
            asm volatile("" ::: "memory");
#pragma unroll
            for (int q = 0; q < 8; ++q) u[q] = ldv(a + 9 * m + q);
            twid<-1>(u, tw.b);
            dft8<-1>(u);
            LAS c2* o = C + (K / 10) * 638 + K + 10 * m;
#pragma unroll
            for (int p = 0; p < 8; ++p) o[80 * p] = u[p];
        }
    }
    FST(5);
    BAR();
    FST(6);
#pragma unroll
    for (int c = 0; c < (NF > 4 ? 5 : 3); ++c) {
        const int it = tid + 512 * c, kp = it / (2 * NF), row = it % (2 * NF);
        if (NF > 4 || c < 2 || tid < 256) {
            const LAS c2* z = C + (row >> 1) * 648;
            const f32x4 zk = *(const LAS f32x4*)(z + 2 * kp);
            const c2 n0 = ldv(z + (kp ? 640 - 2 * kp : 320)), n1 = ldv(z + 639 - 2 * kp);
            const bool odd = it & 1;
            f32x4 o = odd ? (f32x4){zk[1] + n0.y, n0.x - zk[0], zk[3] + n1.y, n1.x - zk[2]} : (f32x4){zk[0] + n0.x, zk[1] - n0.y, zk[2] + n1.x, zk[3] - n1.y};
            if (c == 0 && kp == 0) { o[0] = 2.f * (odd ? zk[1] : zk[0]); o[1] = 2.f * (odd ? n0.y : n0.x); }
            u32x4 ob = __builtin_bit_cast(u32x4, o * FSC);
            if (LATE) { ob[1] = nosent1(ob[1]); ob[3] = nosent1(ob[3]); }
            st128(ob, rws, kp * (LATE ? GST : FST_) + row * 16u, (LATE ? O_FL - (S_ - 256) * 16u : O_F) + (unsigned)row0 * 16u, 16);
        }
    }
    FST(7);
}
template <class FG> __device__ __forceinline__ void a3_unit(__amdgpu_buffer_rsrc_t rx, int row0, int layer, __amdgpu_buffer_rsrc_t rws, __amdgpu_buffer_rsrc_t rwin, const RowTw& tw, int tid, FG go, unsigned (&fs)[12]) {
    const int wave = __builtin_amdgcn_readfirstlane(tid >> 6), lane = tid & 63;
    LAS c2* A = (LAS c2*)smem; LAS c2* C = (LAS c2*)(smem + R_C);
    const unsigned n0 = (lane & 7) | ((lane & 24) << 1);
    c2 x[10];
    if (wave < 4) {
        const unsigned base = (unsigned)(row0 + 2 * wave) * (D_ * 2), vo = n0 * 2u + (lane >> 5) * (D_ * 2u);
#pragma unroll
        for (int r = 0; r < 10; ++r) x[r] = (c2){bf2f(__builtin_amdgcn_raw_buffer_load_b16(rx, vo + 128u * r, base, 16)), bf2f(__builtin_amdgcn_raw_buffer_load_b16(rx, vo + 16u + 128u * r, base, 16))};
    }
    const bool early = row0 < 256;
    if (!early) go();
    else if (TG) {
        if (wave == 7) { const unsigned vo[3] = {lane * 128u, lane * 128u + 8192u, min(lane, 31) * 128u + 16384u}; wait_told(rwin, vo, W_COL + (unsigned)layer * W_LAYER + 4u, 1); }
        BAR0();
    }
    FST(0);
    u32x4 fv[3];
    {
        const unsigned vo = (tid >> 3) * (early ? GST : FST_) + (tid & 7) * 16u, st = (early ? GST : FST_) * 64u, so = (early ? O_FL + (unsigned)(2 - layer) * (NCOL * GST) : O_F) + (unsigned)row0 * 16u;
#pragma unroll
        for (int c = 0; c < 3; ++c) fv[c] = __builtin_amdgcn_raw_buffer_load_b128(rws, vo, c < 2 || wave < 4 ? so + c * st : 0x40000000u, 16);
        if (early) for (unsigned spins = 0;;) {
            if (!__any(max(max(smax(fv[0]), smax(fv[1])), smax(fv[2])) == ~0u) || spin_fail(spins, rwin)) break;
            __builtin_amdgcn_s_sleep(PS3);
            asm volatile("" ::: "memory");
            u32x4 x[3];
#pragma unroll
            for (int c = 0; c < 3; ++c) x[c] = __builtin_amdgcn_raw_buffer_load_b128(rws, smax(fv[c]) == ~0u ? vo : 0x40000000u, so + c * st, 16);
#pragma unroll
            for (int c = 0; c < 3; ++c) if (smax(fv[c]) == ~0u) fv[c] = x[c];
        }
    }
#pragma unroll
    for (int c = 0; c < 3; ++c) {
        const int it = tid + 512 * c, kp = it >> 3;
        if (c < 2 || tid < 256) {
            const bool odd = it & 1;
            const f32x4 v = __builtin_bit_cast(f32x4, fv[c]), p = {dppf<0xB1>(v[0]), dppf<0xB1>(v[1]), dppf<0xB1>(v[2]), dppf<0xB1>(v[3])};
            c2 z0 = odd ? (c2){p[0] + v[1], v[0] - p[1]} : (c2){v[0] - p[1], p[0] + v[1]};
            const c2 z1 = odd ? (c2){p[2] + v[3], v[2] - p[3]} : (c2){v[2] - p[3], p[2] + v[3]};
            int i0 = odd ? 640 - 2 * kp : 2 * kp;
            if (c == 0 && kp == 0) { z0 = odd ? (c2){p[1], v[1]} : (c2){v[0], p[0]}; i0 >>= 1; }
            LAS c2* z = C + ((it >> 1) & 3) * 648;
            z[i0] = z0; z[odd ? 639 - 2 * kp : 2 * kp + 1] = z1;
        }
    }
    FST(1);
    BAR();
    if (early && TH && wave == 7) tell_at(rwin, W_H + (unsigned)layer * W_LAYER + (unsigned)((row0 >> 5) * 128 + 16 + ((row0 >> 3) & 3) * 4), layer ? TH : TH0);
    if (early) RST(3, layer, wave, 0);
    FST(2);
    if (tid < 320) {
        const int K = tid >> 3, m = tid & 7;
        LAS c2* a = A + K * 72;
        const LAS c2* o = C + (K / 10) * 638 + K + 10 * m;
        c2 u[8];
#pragma unroll
        for (int p = 0; p < 8; ++p) u[p] = ldv(o + 80 * p);
        dft8<1>(u);
        twid<1>(u, tw.b);
#pragma unroll
        for (int q = 0; q < 8; ++q) a[9 * q + m] = u[q];
        asm volatile("" ::: "memory");
#pragma unroll
        for (int p = 0; p < 8; ++p) u[p] = ldv(a + 9 * m + p);
        dft8<1>(u);
        twid<1>(u, tw.a);
#pragma unroll
        for (int q = 0; q < 8; ++q) a[m + 8 * (q >> 1) + 32 * (q & 1)] = u[q];
    }
    FST(3);
    BAR();
    FST(4);
    if (wave < 4) {
        c2 u[10];
#pragma unroll
        for (int r = 0; r < 10; ++r) u[r] = ldv(A + (wave * 10 + r) * 72 + lane);
        dft10<1>(u);
        unsigned x1[10], h[10];
#pragma unroll
        for (int r = 0; r < 10; ++r) {
            swap32(u[r]);
            const c2 y = x[r] + unpk(pack2(u[r].x, u[r].y));
            x1[r] = pack2(y.x, y.y);
            u[r] = unpk(x1[r]);
        }
        FST(5);
        ln_row(u, h);
        FST(6);
        LAS char* st = (LAS char*)C + wave * 5120;
        LAS char* hp = st + (lane >> 5) * 1280 + (n0 & 48) * 2 + pi16(n0 & 15) * 2;
#pragma unroll
        for (int r = 0; r < 10; ++r) *(LAS unsigned*)(hp + 128 * r) = nosent1(h[r]);
#pragma unroll
        for (int c = 0; c < 3; ++c) {
            if (c < 2 || lane < 32) {
                const int id = c * 64 + lane, rr = id >= 80, ch = id - 80 * rr, row = (row0 + 2 * wave + rr) & 31;
                st128(*(const LAS u32x4*)(st + id * 16), rws, (unsigned)((ch >> 3) * 4096 + row * 128 + (((ch & 7) ^ (row & 7)) << 4)), O_H + (unsigned)((row0 >> 5) - NHF * layer) * 40960u, 16);
            }
        }
        if (early) RST(4, layer, wave, 0);
        LAS char* xp = st + 2560 + (lane >> 5) * 1280 + n0 * 2;
#pragma unroll
        for (int r = 0; r < 10; ++r) { *(LAS u16*)(xp + 128 * r) = (u16)x1[r]; *(LAS u16*)(xp + 128 * r + 16) = (u16)(x1[r] >> 16); }
#pragma unroll
        for (int c = 0; c < 3; ++c)
            if (c < 2 || lane < 32) st128(*(const LAS u32x4*)(st + 2560 + (c * 64 + lane) * 16), rws, (c * 64 + lane) * 16u, O_X1 + (unsigned)(row0 + 2 * wave) * (D_ * 2), 16);
        FST(7);
    }
}

constexpr int C_T = 8 * 544 * 8, C_B = C_T + (int)sizeof(TwCol), C_W = C_B + 32768, C_END = C_W + 8 * 516 * 4;
template <class FG> __device__ __forceinline__ void a2_unit(int kp, int late, __amdgpu_buffer_rsrc_t rws, __amdgpu_buffer_rsrc_t rwin, int tid, FG go, unsigned (&fs)[12]) {
    const int w = __builtin_amdgcn_readfirstlane(tid >> 6), l = tid & 63;
    const unsigned fb = O_F + (unsigned)kp * FST_, wb = O_WFT + (unsigned)kp * WST;
    const int b = (l >> 1) & 7;
    const int k0 = w + 8 * b + 128 * (l >> 4);
    c2 u[8];
    u32x4 wv[2];
    {
        f32x4 tv[3];
#pragma unroll
        for (int i = 0; i < 3; ++i) tv[i] = ((const f32x4*)&TWC)[min(tid + 512 * i, (int)sizeof(TwCol) / 16 - 1)];
        go(0);
#pragma unroll
        for (int i = 0; i < 3; ++i) ((LAS f32x4*)(smem + C_T))[min(tid + 512 * i, (int)sizeof(TwCol) / 16 - 1)] = tv[i];
        BAR();
        FST(0);
        u32x4 x[4];
#pragma unroll
        for (int r = 0; r < 4; ++r) x[r] = __builtin_amdgcn_raw_buffer_load_b128(rws, (tid >> 1) * 16u + (tid & 1) * 4096u, fb + r * 8192u, 16);
#pragma unroll
        for (int r = 0; r < 2; ++r) wv[r] = __builtin_amdgcn_raw_buffer_load_b128(rws, tid * 16u, wb + r * 8192u, 16);
#pragma unroll
        for (int r = 0; r < 4; ++r) {
            const f32x4 y = __builtin_bit_cast(f32x4, x[r]);
            const c2 z = {dppf<0xB1>(l & 1 ? y[0] : y[2]), dppf<0xB1>(l & 1 ? y[1] : y[3])};
            u[2 * r] = l & 1 ? z : (c2){y[0], y[1]};
            u[2 * r + 1] = l & 1 ? (c2){y[2], y[3]} : z;
        }
    }
    const LAS f32x4* t1 = (const LAS f32x4*)(smem + C_T) + (tid >> 1);
    const LAS f32x4* t2 = (const LAS f32x4*)(smem + C_T + sizeof(Tw7<256>)) + (l >> 1);
    const LAS f32x4* t3 = (const LAS f32x4*)(smem + C_T + sizeof(Tw7<256>) + sizeof(Tw7<32>)) + (l >> 4);
    LAS c2* e0 = (LAS c2*)smem + tid;
    LAS c2* e1 = (LAS c2*)smem + w * 544 + l;
    LAS c2* e2 = (LAS c2*)smem + w * 544 + 2 * (l >> 4) + (l & 1);
    Tw tw;
    ldtw<256>(tw, t1);
    if (late) {
        go(2);
        FST(1);
        for (unsigned spins = 0;;) {
            asm volatile("" ::: "memory");
            const u32x2 x = __builtin_amdgcn_raw_buffer_load_b64(rws, tid * 8u, O_FL + (unsigned)kp * GST, 16);
            u[7] = __builtin_bit_cast(c2, x);
            if (!__any(x[1] == ~0u) || spin_fail(spins, rwin)) break;
            if (w < 3) __builtin_amdgcn_s_sleep(16); else __builtin_amdgcn_s_sleep(PS2);
        }
    }
    FST(2);
    dft8<-1>(u);
    twid<-1>(u, tw);
    if (kp == 0) {
#pragma unroll
        for (int i = 0; i < 4; ++i)
            asm volatile("s_mov_b32 m0, %3\n\ts_nop 3\n\tbuffer_load_dwordx4 %0, %1, %2 offen sc1 lds" :: "v"(l * 16u), "s"(rws), "s"(O_WFT + NCOL * WST + (w * 4 + i) * 1024u), "s"(C_B + (w * 4 + i) * 1024u), "v"(u[7]) : "m0", "memory");
    }
#pragma unroll
    for (int r = 0; r < 8; ++r) e0[544 * r] = u[r];
    ldtw<32>(tw, t2);
    BAR();
    if (A2P == 1 && w < 4) __builtin_amdgcn_s_setprio(2);
    if (A2P == 2 && (w & 1)) __builtin_amdgcn_s_setprio(2);
    if (A2P == 3 && w >= 4) __builtin_amdgcn_s_sleep(4);
    if (TG && w == 0) tell_at(rwin, W_COL + (unsigned)late * W_LAYER + (unsigned)kp * 128u + 4u, kp ? TG : TG + 30);
#pragma unroll
    for (int r = 0; r < 8; ++r) u[r] = ldv(e1 + 64 * r);
    dft8<-1>(u);
    twid<-1>(u, tw);
#pragma unroll
    for (int r = 0; r < 8; ++r) e1[68 * r] = u[r];
    asm volatile("" ::: "memory");
#pragma unroll
    for (int r = 0; r < 8; ++r) u[r] = ldv(e2 + 68 * b + 8 * r);
    ldtw<4>(tw, t3);
    dft8<-1>(u);
    twid<-1>(u, tw);
    c2 f[8];
#pragma unroll
    for (int r = 0; r < 8; ++r) f[r] = unpk(wv[r >> 2][r & 3]);
    asm volatile("s_nop 1" : "+v"(u[1]), "+v"(u[2]), "+v"(u[3]), "+v"(u[4]), "+v"(u[5]), "+v"(u[6]), "+v"(u[7]));
#pragma unroll
    for (int r = 0; r < 4; ++r) swapl<5>(u[r], u[r + 4]);
#pragma unroll
    for (int r = 0; r < 4; ++r) swapl<4>(u[(r & 1) + 2 * (r & 2)], u[(r & 1) + 2 * (r & 2) + 2]);
    dft4<-1>(u[0], u[2], u[4], u[6]); dft4<-1>(u[1], u[3], u[5], u[7]);
    FST(3);
    if (kp == 0) {
        LAS c2* z = (LAS c2*)(smem + C_W);
#pragma unroll
        for (int r = 0; r < 8; ++r) if (!(l & 1)) z[w * 258 + b + 16 * (l >> 4) + 8 * (r & 1) + 64 * (r >> 1)] = u[r];
        VMWAIT();
        BAR();
#pragma unroll
        for (int r = 0; r < 8; ++r) {
            const int kn = (S_ - k0 - 64 * (r & 1) - 512 * (r >> 1)) & (S_ - 1);
            const f32x4 ab = ((const LAS f32x4*)(smem + C_B))[256 * r + (tid >> 1)];
            const c2 zn = z[(kn & 7) * 258 + (kn >> 3)], g = {ab[2], ab[3]}, h = l & 1 ? f[r] : (c2){ab[0], ab[1]};
            const c2 y = cfin<1>(g, zn, g * bc(zn.x));
            u[r] = cfin<-1>(u[r], h, fm(u[r], bc(h.x), l & 1 ? bc(0.f) : y));
        }
    } else {
#pragma unroll
        for (int r = 0; r < 8; ++r) u[r] = cfin<-1>(u[r], f[r], u[r] * bc(f[r].x));
    }
    FST(4);
    dft4<1>(u[0], u[2], u[4], u[6]); dft4<1>(u[1], u[3], u[5], u[7]);
#pragma unroll
    for (int r = 0; r < 4; ++r) swapl<4>(u[(r & 1) + 2 * (r & 2)], u[(r & 1) + 2 * (r & 2) + 2]);
#pragma unroll
    for (int r = 0; r < 4; ++r) swapl<5>(u[r], u[r + 4]);
    twid<1>(u, tw);
    dft8<1>(u);
#pragma unroll
    for (int r = 0; r < 8; ++r) e2[66 * b + 8 * r] = u[r];
    asm volatile("" ::: "memory");
    ldtw<32>(tw, t2);
#pragma unroll
    for (int r = 0; r < 8; ++r) u[r] = ldv(e1 + 66 * r);
    twid<1>(u, tw);
    dft8<1>(u);
#pragma unroll
    for (int r = 0; r < 8; ++r) e1[64 * r] = u[r];
    ldtw<256>(tw, t1);
    __builtin_amdgcn_s_setprio(0);
    FST(7);
    BAR();
    FST(10);
#pragma unroll
    for (int r = 0; r < 8; ++r) u[r] = ldv(e0 + 544 * r);
    twid<1>(u, tw);
    dft8<1>(u);
    FST(5);
    __builtin_amdgcn_raw_buffer_store_b64((u32x2){__float_as_uint(u[0].x), nosent1(__float_as_uint(u[0].y))}, rws, tid * 8u, O_FL + (unsigned)((2 - late) * NCOL + kp) * GST, 16);
#pragma unroll
    for (int r = 1; r < 8; ++r) __builtin_amdgcn_raw_buffer_store_b64(__builtin_bit_cast(u32x2, u[r]), rws, tid * 8u, fb + r * 4096u, 16);
    FST(6);
}

struct Args { char* ws; char* win; const u16 *x, *wr, *wi, *mem0, *wq, *wk, *wv, *wo; u16* out; };
__device__ __forceinline__ const u16* arg(int i) {
    const __attribute__((address_space(4))) u64* p = (const __attribute__((address_space(4))) u64*)__builtin_amdgcn_kernarg_segment_ptr();
    asm volatile("" : "+s"(p));
    return (const u16*)p[i];
}
enum { A_X = 2, A_WR, A_WI, A_MEM0, A_WQ, A_WK, A_WV, A_WO, A_OUT };
#define MXV 0x5556ull
#define MXQ 0x1554ull
#define NTAIL 7
static_assert(NTAIL >= 3, "the owners release segments up to NSEG - 4");
constexpr int TAILS = NSEG - NTAIL;
static_assert((MXV & 31) == 22 && (MXQ & 31) == 20, "segment sets");
constexpr int NREC = 96 + NOWN, NQT = NREC + NOWN, NXT = NQT + 32, NWRK = NXT + 32;
#define SST(k) do {} while (0)
#define LSEL 0
#define RT(k) do {} while (0)
#define RTS() do {} while (0)
#define B3ATTR
#define MTA(k)
#define MTRT() do {} while (0)
#define MT(k) do {} while (0)
#define MTS() do {} while (0)
#define FC(x) do {} while (0)
#define DQC 1200
#define DVC 1200
#define M1 44
#define M2 226
#define PEN 280
#define DZP 55
#define LT0 342
#define SAD 1
#define PEN1 230
#define PEN0 330
#define TSLP 1
#define OPR 2
#define BSPARSE 0
#define ZLAG 3
#define TKP 0
#define DPS 12
#define DXT 3000
#define LTM 12
#define XS0 10
#define XSU 8
#define XSM 32
#define LTU 4
#define LTS 3
#define XSS 3
#define TKS 3
#define LT1 450
#define FDL 2
#define ZLA 3
#define NORND 0
#define LTX 40
#define TKE 200
#define DVO 4000
#define ZPS 2
#define HLA 8
#define QB2 2
#define NB2 6
#define DZR 24
#define ML 150
#define NB1 3
#define DPT 1
#define DDMA 8
#define DZ2 800
#define QEARLY 2
#define OSLP 32
#define OSLP2 1
#define WSLP 64
#define DO0 4000
#define DW0 1200
#define DQT 2000
#define ZEARLY 4
#define HSLP 4
#define ASLP 4
#define CSLP 4
#define A1S 0
#define OSLP3 12
#define DVT 860
#define VSL 0
#define WARM 1
constexpr unsigned CODE_BYTES = 96000;
#define DTK 4
#define OSL4 10
#define OAUX 0
#define TSL0 6
#define SE 6
#define XE 20
#define PENS 20
#define CF 190
#define HSL2 16
#define DH1 10
#define DZ0 400
#define FZP 1
__device__ __forceinline__ unsigned fz_at(unsigned h, unsigned g) { return FZP ? g * 1024u + (h >> 2) * 128u + (h & 3u) * 4u : h * 128u + g * 4u; }
#define LKE 0
#define DW4 6
#define DBIG 580
#define PENH 280
#define ROT 0
#define VREV 0
#define DGV 0
#define VOFF0 "0"
#define VOFF1 "1024"
#define VOFF2 "2048"
#define VOFF3 "3072"
#define VOFF4 "4096"
#define VOFF5 "5120"
#define VOFF6 "6144"
#define VOFF7 "7168"
#define VOFF8 "8192"
#define VOFF9 "9216"
#define STK 1
#define STX 0
#define UFQ 14
#define DQ0 36
#define QSL 6
#define OST0 40
#define NPR 3
#define DIE 0
#define ALN ".p2align 6\n\t"
#define SB() __builtin_amdgcn_sched_barrier(0)
#define PIN1(a) asm volatile("" : "+v"(a) :: "memory")
#define PIN2(a, b) asm volatile("" : "+v"(a), "+v"(b) :: "memory")
__device__ __forceinline__ unsigned fresh_lane() { unsigned l; asm volatile("v_mbcnt_lo_u32_b32 %0, -1, 0\n\tv_mbcnt_hi_u32_b32 %0, -1, %0" : "=v"(l)); return l; }
#define LANE0 (min(fresh_lane(), 1u) << 30)
__device__ __forceinline__ void wait_until(unsigned long long t) { while ((long long)(__builtin_amdgcn_s_memtime() - t) < 0) __builtin_amdgcn_s_sleep(1); }
struct Clk { unsigned ta, ma, cpq, qpc; };
__device__ __forceinline__ int sx24(unsigned v) { return (int)(v << 8) >> 8; }
#define WAIT_FINE ALN \
        "s_memrealtime vcc\n\t" \
        "s_memtime s[100:101]\n\t" \
        "s_brev_b32 %[fi], -2\n\t" \
        "s_waitcnt lgkmcnt(0)\n\t" \
        "s_mov_b32 %[rp], vcc_lo\n\t" \
        "s_lshl_b32 vcc_hi, vcc_lo, 2\n\t" \
        "s_sub_u32 %[x], %[t], vcc_hi\n\t" \
        "s_bfe_i32 %[x], %[x], 0x180000\n\t" \
        "s_min_i32 %[x], %[x], 4000\n\t" \
        "s_max_i32 %[x], %[x], -4000\n\t" \
        "s_add_u32 %[t], vcc_hi, %[x]\n\t" \
        "s_mul_i32 %[x], %[x], %[cq]\n\t" \
        "s_ashr_i32 %[x], %[x], 8\n\t" \
        "s_sub_u32 %[tm], %[t], %[ta]\n\t" \
        "s_bfe_i32 %[tm], %[tm], 0x180000\n\t" \
        "s_min_i32 %[tm], %[tm], 1000000\n\t" \
        "s_max_i32 %[tm], %[tm], -1000000\n\t" \
        "s_mul_i32 %[tm], %[tm], %[cq]\n\t" \
        "s_ashr_i32 %[tm], %[tm], 8\n\t" \
        "s_add_u32 %[tm], %[tm], %[ma]\n\t" \
        "s_sub_u32 %[tm], %[tm], s100\n\t" \
        "s_lshr_b32 vcc_hi, %[cq], 4\n\t" \
        "s_sub_u32 vcc_hi, %[x], vcc_hi\n\t" \
        "s_max_i32 %[tm], %[tm], vcc_hi\n\t" \
        "s_min_i32 %[tm], %[tm], %[x]\n\t" \
        "s_add_u32 %[tm], %[tm], s100\n" \
        "1:\n\t" \
        "s_memrealtime vcc\n\t" \
        "s_memtime s[100:101]\n\t" \
        "s_waitcnt lgkmcnt(0)\n\t" \
        "s_cmp_eq_u32 vcc_lo, %[rp]\n\t" \
        "s_cbranch_scc1 2f\n\t" \
        "s_mov_b32 %[rp], vcc_lo\n\t" \
        "s_lshl_b32 %[x], vcc_lo, 2\n\t" \
        "s_sub_u32 %[x], %[t], %[x]\n\t" \
        "s_bfe_i32 %[x], %[x], 0x180000\n\t" \
        "s_mul_i32 %[x], %[x], %[cq]\n\t" \
        "s_ashr_i32 %[x], %[x], 8\n\t" \
        "s_add_u32 %[x], %[x], s100\n\t" \
        "s_sub_u32 s101, %[x], %[tm]\n\t" \
        "s_cmp_lt_i32 s101, %[fi]\n\t" \
        "s_cselect_b32 %[tm], %[x], %[tm]\n\t" \
        "s_mov_b32 %[fi], 0\n" \
        "2:\n\t" \
        "s_sub_u32 %[x], %[tm], s100\n\t" \
        "s_cmp_gt_i32 %[x], 260\n\t" \
        "s_cbranch_scc1 1b\n\t" \
        "s_cmp_eq_u32 %[fi], 0\n\t" \
        "s_cselect_b32 %[ta], %[t], %[ta]\n\t" \
        "s_cselect_b32 %[ma], %[tm], %[ma]\n" \
        "3:\n\t" \
        "s_memtime s[100:101]\n\t" \
        "s_waitcnt lgkmcnt(0)\n\t" \
        "s_sub_u32 %[x], %[tm], s100\n\t" \
        "s_cmp_gt_i32 %[x], 92\n\t" \
        "s_cbranch_scc1 3b\n\t" \
        "s_max_i32 %[x], %[x], 0\n\t" \
        "s_andn2_b32 %[x], %[x], 3\n\t" \
        "s_sub_u32 %[x], 104, %[x]\n\t" \
        "s_getpc_b64 vcc\n\t" \
        "s_add_u32 vcc_lo, vcc_lo, %[x]\n\t" \
        "s_addc_u32 vcc_hi, vcc_hi, 0\n\t" \
        "s_setpc_b64 vcc\n\t" \
        ".rept 23\n\t" \
        "s_nop 0\n\t" \
        ".endr\n\t"
#define WAIT_FINE_OUT [tm] "=&s"(tm), [rp] "=&s"(rp), [x] "=&s"(x), [fi] "=&s"(fi), [t] "+s"(t), [ta] "+s"(c.ta), [ma] "+s"(c.ma)
__device__ __forceinline__ void wait_fine(unsigned t, Clk& c) {
    unsigned tm, rp, x, fi;
    asm volatile(WAIT_FINE : WAIT_FINE_OUT : [cq] "s"(c.cpq) : "vcc", "scc", "s100", "s101", "memory");
}
__device__ __forceinline__ void wait_fine_bar(unsigned t, Clk& c) {
    unsigned tm, rp, x, fi;
    asm volatile(WAIT_FINE "s_barrier" : WAIT_FINE_OUT : [cq] "s"(c.cpq) : "vcc", "scc", "s100", "s101", "memory");
}
__device__ __forceinline__ void wait_cyc_bar(unsigned tm) {
    unsigned x;
    asm volatile(
        ALN
        "3:\n\t"
        "s_memtime s[100:101]\n\t"
        "s_waitcnt lgkmcnt(0)\n\t"
        "s_sub_u32 %0, %1, s100\n\t"
        "s_cmp_gt_i32 %0, 92\n\t"
        "s_cbranch_scc1 3b\n\t"
        "s_max_i32 %0, %0, 0\n\t"
        "s_andn2_b32 %0, %0, 3\n\t"
        "s_sub_u32 %0, 104, %0\n\t"
        "s_getpc_b64 vcc\n\t"
        "s_add_u32 vcc_lo, vcc_lo, %0\n\t"
        "s_addc_u32 vcc_hi, vcc_hi, 0\n\t"
        "s_setpc_b64 vcc\n\t"
        ".rept 23\n\t"
        "s_nop 0\n\t"
        ".endr\n\t"
        "s_barrier"
        : "=&s"(x) : "s"(tm) : "vcc", "scc", "s100", "s101", "memory");
}
__device__ __forceinline__ unsigned cyc_at(const Clk& c, int d) {
    d = min(d, 20000);
    asm volatile("" : "+s"(d));
    return c.ma + (unsigned)((max(d, -4000) * (int)c.cpq) >> 8);
}
__device__ __forceinline__ void clk_rate(Clk& c, unsigned ta0, unsigned ma0) {
    const int dq = sx24(c.ta - ta0);
    if (dq > 200) {
        const float r = fminf(fmaxf((float)(c.ma - ma0) * __builtin_amdgcn_rcpf((float)dq), 1.f), 8.f);
        asm volatile("s_nop 1\n\tv_readfirstlane_b32 %0, %2\n\tv_readfirstlane_b32 %1, %3\n\ts_nop 3" : "=s"(c.cpq), "=s"(c.qpc)
                     : "v"((unsigned)(256.f * r + 0.5f)), "v"((unsigned)(65536.f * __builtin_amdgcn_rcpf(r))));
    }
}
__device__ __forceinline__ void clk_init(Clk& c) {
    c.ta = 4u * (unsigned)__builtin_amdgcn_s_memrealtime(); c.ma = (unsigned)__builtin_amdgcn_s_memtime();
    for (int i = 0; i < 2; ++i) {
        const unsigned ta0 = c.ta, ma0 = c.ma;
        wait_fine(c.ta + (i ? 360u : 60u), c);
        clk_rate(c, ta0, ma0);
    }
}
__device__ __forceinline__ unsigned sched_lane(int s) { const unsigned l = fresh_lane(); return (l & 31u) * 128u + (l >> 5) * 4u; }
__device__ __forceinline__ int read_sched(__amdgpu_buffer_rsrc_t rwin, int s, unsigned vo, unsigned to, unsigned f, unsigned g0, int& lt, int& xs, unsigned& fc) {
    int d = (int)((f - g0) & 0xFFFFFFu);
    if (__builtin_expect(__any((f >> 24) != (unsigned)s) || s == 0, 0)) {
        for (unsigned spins = 0; __any((f >> 24) != (unsigned)s);) {
            FC(fc);
            if (spin_fail(spins, rwin)) break;
            if (s == 0) __builtin_amdgcn_s_sleep(TSL0); else __builtin_amdgcn_s_sleep(TSLP);
            asm volatile("" ::: "memory");
            if ((f >> 24) != (unsigned)s) f = __builtin_amdgcn_raw_buffer_load_b32(rwin, vo, to, 16);
        }
        const unsigned f0 = __builtin_amdgcn_readfirstlane(f) - 0x100000u;
        d = (int)((f - f0) & 0xFFFFFFu) + (int)((f0 - g0) & 0xFFFFFFu);
    }
    d = max(d, __builtin_amdgcn_update_dpp(0, d, 0xB1, 0xF, 0xF, true));
    d = max(d, __builtin_amdgcn_update_dpp(0, d, 0x4E, 0xF, 0xF, true));
    d = max(d, __builtin_amdgcn_update_dpp(0, d, 0x141, 0xF, 0xF, true));
    d = max(d, __builtin_amdgcn_update_dpp(0, d, 0x140, 0xF, 0xF, true));
    d = max(d, __builtin_amdgcn_update_dpp(0, d, 0x142, 0xA, 0xF, true));
    d = max(__builtin_amdgcn_readlane(d, 16), __builtin_amdgcn_readlane(d, 48));
    const int l0 = lt, x0 = xs;
    d -= 0x400000;
    const bool miss = d > l0 + min(x0 + LTM, LTX);
    if (s >= SAD) { lt = miss ? l0 : min(max(d, l0 - 1 - ((l0 - d) >> LTS)), l0 + LTU); xs = miss ? min(x0 + XSU, XSM) : max(x0 - 1 - (x0 >> XSS), 0); }
    if (s == 1) lt = LT0;
    if (s < SE) xs = max(xs, XE);
    return 0x400000 + (s < SAD ? d + (s ? PENS : PEN0) : miss ? d + (d < DBIG ? PENH : PEN) : l0 + x0) + M1 + M2 + x0;
}
constexpr int QS = 272;
constexpr int TQS = 1152;
constexpr int L_Q = 10240, L_K = L_Q + 2 * TQS, L_S = L_K + 2 * TQS, L_TOK = L_S + 256, L_H = L_TOK + 49152, L_END = L_H + 2 * 40960;
static_assert(C_END <= L_END && L_END == 146176, "LDS");
typedef __attribute__((ext_vector_type(4))) short v4i16_t;
__device__ __forceinline__ u32x2 ldtr(const char* p) { return __builtin_bit_cast(u32x2, __builtin_amdgcn_ds_read_tr16_b64_v4i16((LAS v4i16_t*)p)); }
__device__ __forceinline__ u32x2 ldtr1(const LAS char* p) { return __builtin_bit_cast(u32x2, __builtin_amdgcn_ds_read_tr16_b64_v4i16((LAS v4i16_t*)p)); }
__device__ __forceinline__ u32x4 ldtr2(const LAS char* p0, const LAS char* p1) {
    const u32x2 x = __builtin_bit_cast(u32x2, __builtin_amdgcn_ds_read_tr16_b64_v4i16((LAS v4i16_t*)p0)), y = __builtin_bit_cast(u32x2, __builtin_amdgcn_ds_read_tr16_b64_v4i16((LAS v4i16_t*)p1));
    return (u32x4){x[0], x[1], y[0], y[1]};
}
__device__ __forceinline__ u32x4 ldtr2(const char* p0, const char* p1) { const u32x2 x = ldtr(p0), y = ldtr(p1); return (u32x4){x[0], x[1], y[0], y[1]}; }

__device__ __forceinline__ unsigned poll_lim(int& budget) {
    --budget;
    unsigned lim = budget > 0 ? 0xFFFFFFFEu : 0xFFFFFFFFu;
    asm volatile("" : "+s"(lim));
    return lim;
}
#define GATHER \
        "buffer_load_dwordx4 %[vo], %[rw], %[s0] offen sc1 lds\n\t" \
        "buffer_load_dwordx4 %[vo], %[rw], %[s0] offen offset:1024 sc1 lds\n\t" \
        "buffer_load_dwordx4 %[vo], %[rw], %[s0] offen offset:2048 sc1 lds\n\t" \
        "buffer_load_dwordx4 %[vo], %[rw], %[s0] offen offset:3072 sc1 lds\n\t" \
        "s_movk_i32 m0, 0x1000\n\t" \
        "s_nop 0\n\t" \
        "buffer_load_dwordx4 %[vo], %[rw], %[s4] offen sc1 lds\n\t" \
        "buffer_load_dwordx4 %[vo], %[rw], %[s4] offen offset:1024 sc1 lds\n\t" \
        "buffer_load_dwordx4 %[vo], %[rw], %[s4] offen offset:2048 sc1 lds\n\t" \
        "buffer_load_dwordx4 %[vo], %[rw], %[s4] offen offset:3072 sc1 lds\n\t" \
        "s_movk_i32 m0, 0x2000\n\t" \
        "s_nop 0\n\t" \
        "buffer_load_dwordx4 %[vo], %[rw], %[s1] offen sc1 lds\n\t" \
        "buffer_load_dwordx4 %[vo], %[rw], %[s1] offen offset:1024 sc1 lds"
#define GATHER_R \
        "buffer_load_dwordx4 %[vo], %[rw], %[s1] offen offset:1024 sc1 lds\n\t" \
        "buffer_load_dwordx4 %[vo], %[rw], %[s1] offen sc1 lds\n\t" \
        "s_movk_i32 m0, 0x1000\n\t" \
        "s_nop 0\n\t" \
        "buffer_load_dwordx4 %[vo], %[rw], %[s4] offen offset:3072 sc1 lds\n\t" \
        "buffer_load_dwordx4 %[vo], %[rw], %[s4] offen offset:2048 sc1 lds\n\t" \
        "buffer_load_dwordx4 %[vo], %[rw], %[s4] offen offset:1024 sc1 lds\n\t" \
        "buffer_load_dwordx4 %[vo], %[rw], %[s4] offen sc1 lds\n\t" \
        "s_movk_i32 m0, 0\n\t" \
        "s_nop 0\n\t" \
        "buffer_load_dwordx4 %[vo], %[rw], %[s0] offen offset:3072 sc1 lds\n\t" \
        "buffer_load_dwordx4 %[vo], %[rw], %[s0] offen offset:2048 sc1 lds\n\t" \
        "buffer_load_dwordx4 %[vo], %[rw], %[s0] offen offset:1024 sc1 lds\n\t" \
        "buffer_load_dwordx4 %[vo], %[rw], %[s0] offen sc1 lds"
#define GATHER_IN [vo] "v"(vo), [rw] "s"(rwin), [s0] "s"(so0), [s4] "s"(so0 + 2u * PAIR), [s1] "s"(so0 + 4u * PAIR)
__device__ __forceinline__ void gather_dma(__amdgpu_buffer_rsrc_t rwin, unsigned vo, unsigned so0, bool rev) {
    if (rev) asm volatile("s_movk_i32 m0, 0x2000\n\ts_nop 0\n\t" GATHER_R :: GATHER_IN : "m0", "memory");
    else asm volatile("s_mov_b32 m0, 0\n\ts_nop 0\n\t" GATHER :: GATHER_IN : "m0", "memory");
}
__device__ __forceinline__ void gather_at(unsigned t, Clk& c, __amdgpu_buffer_rsrc_t rwin, unsigned vo, unsigned so0, bool rev) {
    unsigned tm, rp, x, fi;
    if (rev) asm volatile("s_movk_i32 m0, 0x2000\n\t" WAIT_FINE GATHER_R : WAIT_FINE_OUT : [cq] "s"(c.cpq), GATHER_IN : "vcc", "scc", "s100", "s101", "m0", "memory");
    else asm volatile("s_mov_b32 m0, 0\n\t" WAIT_FINE GATHER : WAIT_FINE_OUT : [cq] "s"(c.cpq), GATHER_IN : "vcc", "scc", "s100", "s101", "m0", "memory");
}
__device__ __forceinline__ void dma4k(__amdgpu_buffer_rsrc_t r, unsigned vo, unsigned so, unsigned lds) {
    asm volatile(
        "s_mov_b32 m0, %3\n\t"
        "s_nop 0\n\t"
        "buffer_load_dwordx4 %0, %1, %2 offen sc1 lds\n\t"
        "buffer_load_dwordx4 %0, %1, %2 offen offset:1024 sc1 lds\n\t"
        "buffer_load_dwordx4 %0, %1, %2 offen offset:2048 sc1 lds\n\t"
        "buffer_load_dwordx4 %0, %1, %2 offen offset:3072 sc1 lds"
        :: "v"(vo), "s"(r), "s"(so), "s"(lds) : "m0", "memory");
}
__device__ __forceinline__ int bs2(int c) { return (c & ~3) | ((c & 1) << 1) | ((c >> 1) & 1); }
__device__ __forceinline__ void gather_mem0(char* memL, int tid, int nt = 512) {
    const u16* mem0 = arg(A_MEM0);
    for (int c = tid; c < 640; c += nt) {
        const u32x2* src = (const u32x2*)(mem0 + ((c & 6) + ((c >> 3) & 1)) * D_ + (c >> 4) * 16 + (c & 1) * 4);
        const u32x2 x = src[0], y = src[2];
        *(u32x4*)(memL + c * 16) = (u32x4){(x[0] & 0xFFFFu) | (y[0] << 16), (x[0] >> 16) | (y[0] & 0xFFFF0000u), (x[1] & 0xFFFFu) | (y[1] << 16), (x[1] >> 16) | (y[1] & 0xFFFF0000u)};
    }
}
__device__ __forceinline__ f32x2 hadd(f32x4 c) { return (f32x2){c[0], c[1]} + (f32x2){c[2], c[3]}; }
__device__ __forceinline__ unsigned hpack(f32x4 c) { const f32x2 y = hadd(c); return pack2(y[0], y[1]); }
__device__ __forceinline__ u32x8 cat8(u32x4 lo, u32x4 hi) { return (u32x8){lo[0], lo[1], lo[2], lo[3], hi[0], hi[1], hi[2], hi[3]}; }
__device__ __forceinline__ u32x4 half8(const u32x8& v, int u) { return u ? (u32x4){v[4], v[5], v[6], v[7]} : (u32x4){v[0], v[1], v[2], v[3]}; }
__device__ __forceinline__ void dma1k(__amdgpu_buffer_rsrc_t r, unsigned vo, unsigned so, unsigned lds) {
    asm volatile("s_mov_b32 m0, %3\n\ts_nop 0\n\tbuffer_load_dwordx4 %0, %1, %2 offen sc1 lds" :: "v"(vo), "s"(r), "s"(so), "s"(lds) : "m0", "memory");
}
template <class FM> __device__ __forceinline__ void load_w(u32x8 (&wt)[2][10], const u16* src, unsigned rs, bool perm, unsigned b, unsigned c, int w, FM mid) {
    const __amdgpu_buffer_rsrc_t r = mkrsrc(src, 0x7FFFFFFFu);
    const unsigned l = fresh_lane(), q = l >> 4, lds = (unsigned)(w >> 2) * 65536u, v = w & 3;
    const unsigned vo0 = (perm ? ((q & 1u) << 3) | ((q & 2u) << 1) : q) * rs + ((l & 15u) ^ (q << 1)) * 16u, vo1 = vo0 ^ 128u, a = perm ? rs : 4u * rs;
    const LAS char* p0 = (const LAS char*)(size_t)(lds + q * 2048u + ((l >> 2) & 3u) * 256u + ((2u * v) ^ ((l >> 2) & 7u)) * 32u + (l & 3u) * 8u);
    const LAS char* p1 = (const LAS char*)((size_t)p0 ^ 32u);
    auto issue = [&](int g) {
#pragma unroll
        for (int i = 0; i < 4; ++i)
            asm volatile("s_mov_b32 m0, %3\n\ts_nop 3\n\tbuffer_load_dwordx4 %0, %1, %2 offen lds" :: "v"(i < 2 ? vo0 : vo1), "s"(r), "s"(i * a + (g & 1) * b + (g >> 1) * c), "s"(lds + (g & 3) * 16384u + v * 4096u + i * 1024u) : "m0", "memory");
    };
#pragma unroll
    for (int g = 0; g < 3; ++g) issue(g);
#pragma unroll
    for (int g = 0; g < 10; ++g) {
        asm volatile("s_waitcnt vmcnt(%0)\n\ts_barrier" :: "n"(g < 8 ? 8 : 4 * (9 - g)) : "memory");
        if (g + 3 < 10) issue(g + 3);
        const int o = (g & 3) * 16384;
        wt[0][g] = cat8(ldtr2(p0 + o, p0 + o + 1024), ldtr2(p0 + o + 8192, p0 + o + 9216));
        wt[1][g] = cat8(ldtr2(p1 + o, p1 + o + 1024), ldtr2(p1 + o + 8192, p1 + o + 9216));
        asm volatile("s_waitcnt lgkmcnt(0)" : "+v"(wt[0][g]), "+v"(wt[1][g]) :: "memory");
        if (g == 6) mid();
    }
    BAR0();
}
__device__ __forceinline__ int tile_of(int o) { return 8 * ((o % 10) >> 1) + 2 * (o / 10) + (o & 1); }
__device__ __forceinline__ void wait_tags(__amdgpu_buffer_rsrc_t rwin, int g0, int g1, unsigned n1, u64 val, bool boot) {
    const unsigned l = fresh_lane();
    unsigned o[4];
#pragma unroll
    for (int g = 0; g < 4; ++g) o[g] = g >= g0 && g < g1 && (g + 1 < g1 || l < n1) ? l * 128u + g * 8192u : 0x40000000u;
    for (unsigned spins = 0;;) {
        asm volatile("" ::: "memory");
        u32x2 v[4];
#pragma unroll
        for (int g = 0; g < 4; ++g) v[g] = __builtin_amdgcn_raw_buffer_load_b64(rwin, o[g], W_A1, 16);
#pragma unroll
        for (int g = 0; g < 4; ++g) if (__builtin_bit_cast(u64, v[g]) == val) o[g] = 0x40000000u;
        if (!__any((o[0] & o[1] & o[2] & o[3]) != 0x40000000u)) break;
        if (boot ? ++spins > SPIN_CAP : spin_fail(spins, rwin)) break;
        __builtin_amdgcn_s_sleep(ASLP);
    }
}
template <int N> __device__ __forceinline__ void stage_h(__amdgpu_buffer_rsrc_t rws, unsigned src, unsigned lds, unsigned st) {
    const unsigned l = fresh_lane() * 16u;
#pragma unroll
    for (int i = 0; i < N; ++i) dma1k(rws, l, src + i * st, lds + i * st);
}
template <int N> __device__ __forceinline__ void check_h(__amdgpu_buffer_rsrc_t rws, __amdgpu_buffer_rsrc_t rwin, unsigned src, unsigned lds, unsigned st) {
    const LAS char* p = (const LAS char*)smem + lds + fresh_lane() * 16u;
    for (unsigned spins = 0;;) {
        unsigned m = 0;
#pragma unroll
        for (int i = 0; i < N; ++i) { const u32x4 x = *(const volatile LAS u32x4*)(p + i * st); m = max(max(m, max(x[0], x[1])), max(x[2], x[3])); }
        if (__builtin_expect(!__any(m == ~0u), 1) || spin_fail(spins, rwin)) break;
        stage_h<N>(rws, src, lds, st);
        VMWAIT();
    }
}
__device__ __forceinline__ void wait_h(__amdgpu_buffer_rsrc_t rwin, unsigned fo, int seg, int& hrdy) {
    unsigned lim = 63u;
    if (__builtin_expect(hrdy <= seg, 0)) for (unsigned spins = 0;;) {
        asm volatile("" ::: "memory");
        const u32x4 f = __builtin_amdgcn_raw_buffer_load_b128(rwin, min(fresh_lane(), lim) * 128u, W_H + fo, 16);
        const unsigned long long miss = __ballot((f[0] | f[1] | f[2] | f[3]) != 0u);
        hrdy = miss ? __builtin_ctzll(miss) : (int)lim + 1;
        if (hrdy > seg) break;
        if (spin_fail(spins, rwin)) break;
        lim = (unsigned)min(seg + HLA, 63);
        if (seg < 8) __builtin_amdgcn_s_sleep(HSLP); else __builtin_amdgcn_s_sleep(HSL2);
    }
}
template <int CTRL> __device__ __forceinline__ unsigned dppu(unsigned v) { return (unsigned)__builtin_amdgcn_mov_dpp((int)v, CTRL, 0xF, 0xF, true); }
template <int CTRL> __device__ __forceinline__ u32x4 ror4(u32x4 v) { return (u32x4){dppu<CTRL>(v[0]), dppu<CTRL>(v[1]), dppu<CTRL>(v[2]), dppu<CTRL>(v[3])}; }
template <int N> struct IC { static constexpr int v = N; };
__device__ __forceinline__ float xmax16(float v) {
    auto q = __builtin_amdgcn_permlane16_swap(__float_as_uint(v), __float_as_uint(v), false, false);
    return fmx(__uint_as_float(q[0]), __uint_as_float(q[1]));
}
__device__ __forceinline__ float xsum16(float v) {
    auto q = __builtin_amdgcn_permlane16_swap(__float_as_uint(v), __float_as_uint(v), false, false);
    return __uint_as_float(q[0]) + __uint_as_float(q[1]);
}
__device__ __forceinline__ float addswap16(float a, float b) {
    auto r = __builtin_amdgcn_permlane16_swap(__float_as_uint(a), __float_as_uint(b), false, false);
    return __uint_as_float(r[0]) + __uint_as_float(r[1]);
}
#define LANES() \
    const int lane = (int)fresh_lane(), tid = w * 64 + lane, fr = lane & 15, fq = lane >> 4; \
    const int sidx = (lane & 2) ? 0xEEEE : 0x4444; \
    const int ma0 = fq * 256 + (fr & 1) * 128 + (fr >> 1) * 16; \
    const int prow = 16 * ((fr >> 2) & 1) + 4 * (fr >> 3) + (fr & 3), hoff = fr * 128; \
    (void)tid; (void)sidx; (void)ma0; (void)prow; (void)hoff

__device__ __forceinline__ int seg_after(unsigned long long m, int j) { m = j >= 63 ? 0ull : m >> (j + 1); return m ? j + 1 + __builtin_ctzll(m) : NSEG; }
__device__ __forceinline__ int seg_done(unsigned long long m, int f0, int f1, unsigned long long x = 0) { return min(seg_after(~m, f0), seg_after(m | x, f1)) - 1; }
constexpr unsigned long long MXA = 1ull << 40;
template <int OFF> __device__ __forceinline__ void ldfrag(u32x4& d, const LAS char* p) { asm volatile("ds_read_b128 %0, %1 offset:%2" : "=v"(d) : "v"(p), "n"(OFF) : "memory"); }
template <int U, class FM, class FV> __device__ __forceinline__ void project(const u32x8 (&wt)[2][10], f32x4 (&tc)[2][2], u32x4 (&bb)[8], const LAS char* c0, const LAS char* c1, const LAS char* n0, const LAS char* n1, FM mid, FV vm) {
    constexpr int t = U >> 2, k = U & 3, v = (U + 8) % 40;
    if (U == 32) mid();
    vm(IC<U>());
    const LAS char* p = U < 32 ? (k >> 1 ? c1 : c0) : (k >> 1 ? n1 : n0);
    if (U < 2) asm volatile("s_nop 0\n\ts_waitcnt lgkmcnt(7)\n\tv_mfma_f32_16x16x32_bf16 %0, %3, %2, 0\n\tv_mfma_f32_16x16x32_bf16 %1, %4, %2, 0\n\tds_read_b128 %2, %5 offset:%6"
                            : "=&v"(tc[0][k & 1]), "=&v"(tc[1][k & 1]), "+v"(bb[U & 7]) : "v"(half8(wt[0][t], k >> 1)), "v"(half8(wt[1][t], k >> 1)), "v"(p), "n"((v >> 2) * 4096 + (k & 1) * 2048) : "memory");
    else asm volatile(".rept %7\n\ts_nop 0\n\t.endr\n\ts_waitcnt lgkmcnt(7)\n\tv_mfma_f32_16x16x32_bf16 %0, %3, %2, %0\n\tv_mfma_f32_16x16x32_bf16 %1, %4, %2, %1\n\tds_read_b128 %2, %5 offset:%6"
                      : "+v"(tc[0][k & 1]), "+v"(tc[1][k & 1]), "+v"(bb[U & 7]) : "v"(half8(wt[0][t], k >> 1)), "v"(half8(wt[1][t], k >> 1)), "v"(p), "n"((v >> 2) * 4096 + (k & 1) * 2048), "n"(U < 8 ? 1 : 0) : "memory");
    if constexpr (U < 39) project<U + 1>(wt, tc, bb, c0, c1, n0, n1, mid, vm);
    else asm volatile("s_nop 7" : "+v"(tc[0][0]), "+v"(tc[0][1]), "+v"(tc[1][0]), "+v"(tc[1][1]));
}
__device__ __forceinline__ void project_end(u32x4 (&bb)[8]) {
    asm volatile("s_waitcnt lgkmcnt(0)" : "+v"(bb[0]), "+v"(bb[1]), "+v"(bb[2]), "+v"(bb[3]), "+v"(bb[4]), "+v"(bb[5]), "+v"(bb[6]), "+v"(bb[7]) :: "memory");
}
__device__ __forceinline__ void project0(u32x4 (&bb)[8], const LAS char* c0, const LAS char* c1) {
    ldfrag<0>(bb[0], c0); ldfrag<2048>(bb[1], c0); ldfrag<0>(bb[2], c1); ldfrag<2048>(bb[3], c1);
    ldfrag<4096>(bb[4], c0); ldfrag<6144>(bb[5], c0); ldfrag<4096>(bb[6], c1); ldfrag<6144>(bb[7], c1);
    project_end(bb);
}

__global__ __launch_bounds__(512, 2) B3ATTR void mega_kernel(Args a) {
    char* memL = smem;
    char* tokL = smem + L_TOK;
    char* hL = smem + L_H;
    int w = __builtin_amdgcn_readfirstlane(threadIdx.x >> 6);
    __amdgpu_buffer_rsrc_t rwin = mkrsrc(a.win, W_END);
    char* ws = a.ws;
    __amdgpu_buffer_rsrc_t rws = mkrsrc(ws, (unsigned)OFF_WIN);
    auto fresh_rsrc = [&]() INL { ws = (char*)arg(0); rws = mkrsrc(ws, (unsigned)OFF_WIN); rwin = mkrsrc(arg(1), W_END); };
    const char* pc;
    asm volatile("s_getpc_b64 %0" : "=s"(pc));
    unsigned fs[12] = {};
    const u32x2 tg0 = __builtin_amdgcn_raw_buffer_load_b64(rws, 0u, 0u, 16);
    u64 tag;
    u32x2 rot0;
    {
        const int tid = threadIdx.x;
        if (blockIdx.x < 64) {
            const unsigned j = blockIdx.x;
            if (j == 0 && w == 0) __builtin_amdgcn_raw_buffer_store_b64(__builtin_bit_cast(u32x2, (__builtin_bit_cast(u64, tg0) & ~15ull) + 16ull + (xcc_id() & 7u)), rws, LANE0, 128u, 16);
            const unsigned g0 = (j * 512u + tid) * 16u;
#pragma unroll
            for (int i = 0; i < 3; ++i) st128(SENT, mkrsrc(ws + O_ARM1, O_H + NHF * 40960u - O_ARM1), g0 + i * 524288u, 0, 16);
#pragma unroll
            for (int i = 0; i < 7; ++i) {
                const unsigned g = g0 + i * 524288u;
                st128(SENT, mkrsrc(a.win, W_END + 2 * PAIR), g + (g < W_ARM - W_LAYER ? 0u : W_ZM - (W_ARM - W_LAYER)), 0, 16);
            }
#pragma unroll
            for (int i = 0; i < 3; ++i) st128(SENT, mkrsrc(ws + O_Z, ZTSEG * 2), g0 + i * 524288u, 0, 16);
            static_assert(W_ARM - W_LAYER + W_END + 2 * PAIR - W_ZM <= 7u * 524288u && O_ARM1 + 3u * 524288u >= O_H + NHF * 40960u && ZTSEG * 2 <= 3u * 524288u, "arming");
        } else if (blockIdx.x >= 128) {
            RowTw tw;
            row_tw(tw, tid);
            a1_unit<8, false>(mkrsrc(a.x, S_ * D_ * 2), (blockIdx.x - 128) * 16, rws, rwin, tw, tid, fs);
        } else {
            const unsigned j = blockIdx.x - 64;
            FST(0);
            auto row = [&](unsigned i) { return (j & 7u) + 8u * (i & 15u) + 128u * ((j >> 3) & 3u) + 512u * (((j >> 4) & 2u) + ((i >> 4) & 1u)); };
            u32x4 v[6];
#pragma unroll
            for (int i = 0; i < 6; ++i) {
                const unsigned id = min(tid + 512 * i, 64 * 41 - 1), r = id / 41u;
                v[i] = ((const u32x4*)(r < 32u ? a.wr : a.wi))[row(r) * (DF * 2) / 16u + id - r * 41u];
            }
            u16 n[4];
            if (w == 7) {
                const size_t kn = (size_t)((S_ - row(tid)) & (S_ - 1)) * DF;
                n[0] = a.wr[kn]; n[1] = a.wi[kn]; n[2] = a.wr[kn + 320]; n[3] = a.wi[kn + 320];
            }
            const unsigned wo = WARM ? (w * 8u + (j >> 3)) * 8192u : 0x40000000u;
            const unsigned x0 = __builtin_amdgcn_raw_buffer_load_b32(mkrsrc(pc, CODE_BYTES), (tid & 63) * 128u, wo, 0), x1 = __builtin_amdgcn_raw_buffer_load_b32(mkrsrc(&TWC, sizeof(TwCol)), (tid & 63) * 128u, wo, 0);
#pragma unroll
            for (int i = 0; i < 6; ++i) ((u32x4*)smem)[min(tid + 512 * i, 64 * 41 - 1)] = v[i];
            FST(1);
            BAR();
            FST(2);
            const u16* q0 = (const u16*)smem + (j & 7u);
            u32x4 o[5];
#pragma unroll
            for (int i = 0; i < 5; ++i) {
                const u16* q = q0 + ((tid >> 1) & 7) * 328 + (tid >> 4) * 2 + (tid & 1) + 64 * i;
#pragma unroll
                for (int e = 0; e < 4; ++e) o[i][e] = (unsigned)q[e * 8 * 328] | ((unsigned)q[(32 + e * 8) * 328] << 16);
            }
#pragma unroll
            for (int i = 0; i < 5; ++i) st128(o[i], rws, (tid >> 4) * WST + (tid & 15) * 16u, O_WFT + i * 32u * WST + (j >> 5) * 8192u + (j & 7u) * 1024u + ((j >> 3) & 3u) * 256u, 16);
            if (w == 7) {
                const u16* q = q0 + (tid & 31) * 328;
                const unsigned k = row(tid);
                const c2 h0 = (c2){bf2f(q[0]) + bf2f(n[0]), bf2f(q[32 * 328]) - bf2f(n[1])} * bc(0.25f), h1 = (c2){bf2f(q[320]) + bf2f(n[2]), bf2f(q[32 * 328 + 320]) - bf2f(n[3])} * bc(0.25f);
                st128(__builtin_bit_cast(u32x4, (f32x4){h0.x + h1.x, h0.y + h1.y, h0.x - h1.x, h0.y - h1.y}), rws, (((k >> 8) & 6u) * 256u + (k & 64u) * 4u + (k & 7u) * 32u + ((k >> 4) & 24u) + ((k >> 3) & 7u)) * 16u, O_WFT + NCOL * WST, 16);
            }
            FST(3);
            asm volatile("" :: "v"(x0), "v"(x1));
            FST(4);
        }
        rot0 = __builtin_amdgcn_raw_buffer_load_b64(rws, 0u, 128u, 16);
        VMWAIT();
        FST(8);
        BAR0();
        FST(9);
    }
    tag = (((u64)__builtin_amdgcn_readfirstlane(tg0[1]) << 32) | (__builtin_amdgcn_readfirstlane(tg0[0]) & ~15u)) + 16ull;
    if (w == 0) __builtin_amdgcn_raw_buffer_store_b64(__builtin_bit_cast(u32x2, tag + 8ull), rwin, LANE0, W_A1 + blockIdx.x * 128u, 16);
    if (blockIdx.x >= 128) FSW(0); else if (blockIdx.x >= 64) FSW(6);
    unsigned vx;
    int bid;
    auto set_role = [&](unsigned x0) INL {
        vx = x0;
        const int j = blockIdx.x >> 3, x = vx & 3;
        if (!(vx & 4)) bid = j < 8 ? 8 * x + j : j < 16 ? 24 + 8 * x + j : j < 26 ? 80 + 10 * x + j : j < 30 ? NWRK + 4 * (j - 26) + x : NXT - 24 + 8 * x + j;
        else bid = j < 8 ? 64 + 8 * x + j : j < 16 ? NQT - 8 + 8 * x + j : j < 26 ? NREC - 16 + 10 * x + j : NXT - 26 + 8 * x + j;
    };
    auto get_rot = [&]() INL {
        unsigned r = 0;
        for (unsigned spins = 0; spins < SPIN_CAP; ++spins) {
            const u64 x = __builtin_bit_cast(u64, rot0) - tag;
            r = __builtin_amdgcn_readfirstlane((unsigned)x);
            if (__all(x < 8ull)) break;
            __builtin_amdgcn_s_sleep(2);
            asm volatile("" ::: "memory");
            rot0 = __builtin_amdgcn_raw_buffer_load_b64(rws, 0u, 128u, 16);
        }
        return (r + blockIdx.x + 4u * DIE) & 7u;
    };
    set_role(blockIdx.x < 64 ? (xcc_id() ^ (4u * DIE)) & 7u : get_rot());
    unsigned long long tref = 0;
    int layer = 0;
    unsigned fo = 0;
    unsigned nh = 0, nz = 0, mc = 0, qz = 0, mo = 0, tro = 0;
    auto near_init = [&]() INL {
        unsigned f;
        for (unsigned spins = 0;;) {
            asm volatile("" ::: "memory");
            f = __builtin_amdgcn_raw_buffer_load_b32(rwin, (fresh_lane() & 15u) * 4u, W_NEAR, 16);
            if (!__any(f == ~0u) || spin_fail(spins, rwin)) break;
            __builtin_amdgcn_s_sleep(8);
        }
        const unsigned h = (unsigned)__ballot(f & 1u), q = (unsigned)__ballot(f & 2u) ^ 0xAAAAu;
        auto most = [&](unsigned m) { return (__builtin_popcount(m & 0x0F0Fu) + 3u) >> 3; };
        nh = most(h) * HALF; nz = most(h >> 4) * HALF;
        mc = (vx & 3u) ^ most(q) ^ (STK ? 2u * most((unsigned)__ballot(f & 4u) ^ 0x3333u) : STX);
        qz = most(q >> 4);
        mo = W_MEM + (mc & 1u) * PAIR + nh + (mc >> 1) * 4096u;
        tro = mo - W_MEM + W_T;
    };
    auto zpair = [&](unsigned x, unsigned k) { return (((x >> 1) * 10u + k) * 2u + ((x & 1u) ^ qz)) * PAIR + nz; };
    auto two = [&]() { const unsigned l = fresh_lane(); return l < 4 ? (l >> 1) * PAIR + (l & 1) * 4096u + nh : 0x40000000u; };
    const f32x4 z4 = {0.f, 0.f, 0.f, 0.f};
    int hrdy = 0, budget = (int)SPIN_CAP;
    unsigned fc0 = 0, fc1 = 0, fc2 = 0;
    Clk ck = {0u, 0u, 1600u, 10486u};
    SST(5);
#define xt (bid >= NXT && bid < NWRK)
#define qx (bid >= NQT && bid < NWRK)
#define xq (bid >= NREC && bid < NQT && (bid - NREC) % 10 < 8)
    unsigned tpp = 0;
    int lt = LT1, xs = XS0;
    struct Tk { unsigned vo, tw, sl; const LAS char* sp; };
    auto tk_init = [&]() {
        const unsigned l = fresh_lane();
        Tk k = {l * 16u, two(), (l & 31u) * 128u + (l >> 5) * 4u, (const LAS char*)(memL + l * 64 + 4 + ((l >> 1) & 7) * 8)};
        PIN2(k.vo, k.tw); PIN2(k.sl, k.sp);
        return k;
    };
    auto sched2 = [&](Tk& k, int s, unsigned f) INL {
        const unsigned g0 = tpp - 0x400000u;
        tpp = g0 + (unsigned)read_sched(rwin, s, k.sl, tro + (unsigned)(s & 3) * 2u * PAIR, f, g0, lt, xs, fc1);
        k.sl &= ~4u;
        RT(6);
    };
    auto sched = [&](Tk& k, int s) INL { sched2(k, s, __builtin_amdgcn_raw_buffer_load_b32(rwin, k.sl, tro + (unsigned)(s & 3) * 2u * PAIR, 16)); };
    unsigned it = 0;
    auto fetch = [&](const Tk& k, int s, bool first, unsigned lim, unsigned dc, unsigned fo, bool tell) INL {
        const unsigned sb = (unsigned)(s & 3) * 6u * PAIR, ta0 = ck.ta, mm0 = ck.ma;
        if (__builtin_expect(s > 0, 1)) {
            if (__builtin_expect(first, 1)) { const unsigned te = cyc_at(ck, sx24(tpp - TKE - ck.ta)); while ((int)((unsigned)__builtin_amdgcn_s_memtime() - te) < 0) __builtin_amdgcn_s_sleep(1); }
            if (__builtin_expect(first, 1)) gather_at(tpp + (tell ? 0 : DGV), ck, rwin, k.vo, mo + sb, VREV && !tell); else { const unsigned c = (mc + 1u + (it & 1u)) & 3u; gather_dma(rwin, k.vo, W_MEM + (c & 1u) * PAIR + (c >> 1) * 4096u + nh + sb, VREV && !tell); }
        }
        __builtin_amdgcn_s_setprio(0);
        RT(1);
        clk_rate(ck, ta0, mm0);
        unsigned tb = ck.ma - dc, tg = (unsigned)s << 24, so = W_T + (unsigned)(s & 3) * 2u * PAIR + fo;
        asm volatile("" : "+s"(tb), "+s"(tg), "+s"(so));
        asm volatile(ALN "s_waitcnt vmcnt(%1) lgkmcnt(0)\n\ts_barrier\n\ts_waitcnt vmcnt(%2)\n\ts_barrier\n\ts_waitcnt vmcnt(0)\n\ts_memtime %0\n\ts_barrier"
                     : "=s"(tref) : "n"(10 - NB1), "n"(10 - NB2) : "memory");
        MT(1);
        if (!tell) { RT(3); return; }
        unsigned x0 = *(const LAS unsigned*)k.sp, x1 = *(const LAS unsigned*)(k.sp + 4096), x2 = *(const LAS unsigned*)(k.sp + 6144);
        __builtin_amdgcn_s_waitcnt(0xC07F);
        asm volatile("" : "+s"(tref) :: "memory");
        const unsigned fw = ((ck.ta + ((((unsigned)tref - tb) * ck.qpc) >> 16)) & 0xFFFFFFu) | tg;
        __builtin_amdgcn_raw_buffer_store_b32(fw, rwin, k.tw, __any(max(max(x0, x1), x2) > lim) ? 0x40000000u : so, 16);
        RT(3);
    };
    auto rout = [&]() { return mkrsrc(layer ? arg(A_OUT) : (const u16*)(ws + OFF_X2), S_ * D_ * 2); };
    auto rec_end = [&](unsigned g) INL {
        if (g) for (unsigned spins = 0;;) {
            asm volatile("" ::: "memory");
            if (__builtin_amdgcn_readfirstlane(__builtin_amdgcn_raw_buffer_load_b32(rws, 0u, g == 3 ? O_FL + (unsigned)(bid & 127) * GST + 4092u : O_FL - g * SEG * D_ * 2u + (unsigned)bid * 128u, 16)) != -1 || spin_fail(spins, rwin)) break;
            __builtin_amdgcn_s_sleep(16);
        }
    };
    auto mytid = [&]() { return w * 64 + (int)fresh_lane(); };
    auto new_layer = [&]() { fo = (unsigned)layer * W_LAYER; asm volatile("" : "+s"(fo), "+s"(w), "+s"(bid), "+s"(nh), "+s"(nz), "+s"(mc), "+s"(qz), "+s"(tag)); hrdy = 0; tpp = 0; lt = LT1; xs = XS0; };
    auto load_qv = [&](u32x8 (&wt)[2][10], int hh, bool v, auto mid) INL {
        const bool o = v && w >= 4;
        load_w(wt, arg(A_WQ + 2 * v + (w >> 2)) + (o ? (hh * 128 + (w & 3) * 32) * D_ : hh * 128 + (w & 3) * 16 * HD), o ? D_ * 2 : HD * 2, !o, o ? 16 * D_ * 2 : 64 * HD * 2, o ? 256 : 128 * HD * 2, w, mid);
    };
    auto hseg = [&](int j) { return O_H + (unsigned)(j - NHF * layer) * 40960u; };
    auto stage2 = [&](int j0, int j1, bool vt) INL {
        if (w == 0) {
            if (j1 >= NHF) wait_h(rwin, fo, j1, hrdy);
            else if (TH) { const unsigned vo[1] = {(fresh_lane() & 3u) * 4u}; wait_told(rwin, vo, W_H + fo + (unsigned)j0 * 128u + 16u, 1); }
            else for (unsigned spins = 0;;) {
                asm volatile("" ::: "memory");
                const unsigned l = fresh_lane();
                if (!__any(__builtin_amdgcn_raw_buffer_load_b32(rws, (l & 31u) * 128u + (l >> 5) * (unsigned)(j1 - j0) * 40960u, hseg(j0) + 36864u, 16) == ~0u) || spin_fail(spins, rwin)) break;
                __builtin_amdgcn_s_sleep(HSLP);
            }
            *(volatile LAS int*)(smem + L_S + 32) = hrdy;
        }
        BAR();
        hrdy = __builtin_amdgcn_readfirstlane(*(volatile LAS int*)(smem + L_S + 32));
        SST(2);
        if (j0 == 1 && vt) __builtin_amdgcn_s_sleep(DH1);
        stage_h<5>(rws, hseg(j0) + w * 1024u, L_H + w * 1024u, 8192u);
        if (!vt) {
            stage_h<5>(rws, hseg(j1) + w * 1024u, L_H + 40960u + w * 1024u, 8192u);
            asm volatile("s_waitcnt vmcnt(5)" ::: "memory");
        } else if (w >= 4 && j1 != j0) {
            stage_h<10>(rws, hseg(j1) + (w - 4) * 1024u, L_H + 40960u + (w - 4) * 1024u, 4096u);
            asm volatile("s_waitcnt vmcnt(10)" ::: "memory");
        } else VMWAIT();
        check_h<5>(rws, rwin, hseg(j0) + w * 1024u, L_H + w * 1024u, 8192u);
        BAR0();
        SST(3);
        if (vt && w >= 4 && j1 != j0) {
            VMWAIT();
            check_h<10>(rws, rwin, hseg(j1) + (w - 4) * 1024u, L_H + 40960u + (w - 4) * 1024u, 4096u);
        }
    };
#define WO(eb, nt) wt[(nt) & 1][((nt) & ~1) + (eb)]
    unsigned zb = W_ZM;
    auto probe = [&]() INL {
        {
            unsigned* best = (unsigned*)smem;
            const int tid = mytid();
            const __amdgpu_buffer_rsrc_t rw = mkrsrc(a.win + (bid & 4 ? W_END : W_PROBE), 2 * PAIR);
            const unsigned base = (unsigned)(bid & 8) * 64u + w * 1024u;
            if (tid < 8) best[tid] = 100000u;
            if (w == 0) wait_tags(rwin, 0, 1, 64u, tag + 8ull, true);
            BAR();
            unsigned mn[4] = {~0u, ~0u, ~0u, ~0u};
            for (int r = 0; r < NPR; ++r) {
#pragma unroll
                for (int i = 0; i < 4; ++i) {
                    const unsigned long long t1 = __builtin_amdgcn_s_memtime();
                    asm volatile("s_waitcnt lgkmcnt(0)" ::: "memory");
                    unsigned v = __builtin_amdgcn_raw_buffer_load_b32(rw, base + r * 128u + i * HALF, 0, 16);
                    asm volatile("s_waitcnt vmcnt(0)" : "+v"(v) :: "memory");
                    const unsigned long long t2 = __builtin_amdgcn_s_memtime();
                    mn[i] = min(mn[i], (unsigned)(t2 - t1));
                }
            }
            if ((tid & 63) == 0) {
#pragma unroll
                for (int i = 0; i < 4; ++i) atomicMin(&best[i + (w & 4)], mn[i]);
            }
            BAR();
            const unsigned h = min(min(best[1], best[3]), min(best[5], best[7])) < min(min(best[0], best[2]), min(best[4], best[6])), q = best[h + 2] + best[h + 6] < best[h] + best[h + 4], k = best[h + 4] + best[h + 6] < best[h] + best[h + 2];
            if (w == 0) __builtin_amdgcn_raw_buffer_store_b32(h + 2u * q + 4u * k, rwin, LANE0, W_NEAR + (unsigned)(bid & 15) * 4u, 16);
            BAR();
        }
    };
    auto vo_role = [&](const u32x8 (&wt)[2][10]) INL {
        const int hh = bid & 31;
        auto steps = [&](auto cc) INL {
            constexpr int C = decltype(cc)::v & 1;
            constexpr bool TK = decltype(cc)::v == 3;
            LANES();
            Tk tk = {0u, 0u, 0x40000000u, nullptr};
            if (TK) { tk = tk_init(); __builtin_amdgcn_s_setprio(TKP); }
            const unsigned zi = (unsigned)(hh * 128 + fr * 8) * 2u;
            unsigned zo0 = zpair(w & 3, fq) + zi, zo1 = zo0 + 8 * PAIR, zo2 = zpair(w & 3, 8 + (fq >> 1)) + zi + (fq & 1) * 8;
            const LAS char* va = (const LAS char*)(smem + L_Q + (fr & 1) * 544 + ((fr >> 1) & 1) * 1152 + (16 * fq + 2 * (fr >> 2)) * 4);
            LAS char* vw = (LAS char*)(smem + L_Q + (fq & 1) * 544 + (fq >> 1) * 1152 + (16 * w + bs2(fr)) * 4);
            const LAS char* mp0 = (const LAS char*)(memL + ma0);
            PIN2(zo0, zo1); PIN1(zo2);
            if (C == 0) { PIN2(mp0, vw); } else PIN1(va);
            u32x4 za = SENT, zc = SENT;
            u32x2 ze = {0u, 0u};
            auto put_z2 = [&](int s) {
                const unsigned z2b = s < NSEG ? O_Z2 + (unsigned)s * (NOWN * 8192u) + (unsigned)(10 * (w & 3) * 8192 + hh * 256) : 0x40000000u, l2 = fresh_lane();
                st128(za, rws, (l2 >> 4) * 8192u + (l2 & 15u) * 16u, z2b, 16);
                st128(zc, rws, (l2 >> 4) * 8192u + (l2 & 15u) * 16u, z2b + 4u * 8192u, 16);
                __builtin_amdgcn_raw_buffer_store_b64(ze, rws, (l2 >> 5) * 8192u + (l2 & 15u) * 16u + ((l2 >> 4) & 1u) * 8u, z2b + 8u * 8192u, 16);
            };
            for (int s = 0; s < NSEG; ++s) {
                RT(0);
                bool redo = false;
                unsigned sf = 0;
                do {
                    ++it;
                    if (C == 0) {
                        const unsigned lim = s > 0 ? poll_lim(budget) : ~0u;
                        asm volatile(
                            "v_mov_b64 v[200:201], 0\n\t"
                            "v_mov_b64 v[202:203], 0\n\t"
                            "v_mov_b64 v[204:205], 0\n\t"
                            "v_mov_b64 v[206:207], 0\n\t"
                            "v_mov_b32 v208, 0\n\t"
                            "s_setprio 2\n\t"
                            ALN
                            "s_waitcnt lgkmcnt(0)\n\t"
                            "s_barrier\n\t"
                            MTA(0)
                            "ds_read_b128 v[212:215], %[mp] offset:" VOFF0 "\n\t"
                            "ds_read_b128 v[216:219], %[mp] offset:" VOFF1 "\n\t"
                            "ds_read_b128 v[220:223], %[mp] offset:" VOFF2 "\n\t"
                            "s_barrier\n\t"
                            "ds_read_b128 v[224:227], %[mp] offset:" VOFF3 "\n\t"
                            "ds_read_b128 v[228:231], %[mp] offset:" VOFF4 "\n\t"
                            "ds_read_b128 v[232:235], %[mp] offset:" VOFF5 "\n\t"
                            "s_waitcnt lgkmcnt(5)\n\t"
                            "v_smfmac_f32_16x16x64_bf16 v[200:203], v[212:215], %[w00], %[ix]\n\t"
                            "s_waitcnt lgkmcnt(4)\n\t"
                            "v_smfmac_f32_16x16x64_bf16 v[200:203], v[216:219], %[w01], %[ix]\n\t"
                            "s_barrier\n\t"
                            MTA(1)
                            "ds_read_b128 v[236:239], %[mp] offset:" VOFF6 "\n\t"
                            "s_waitcnt lgkmcnt(4)\n\t"
                            "v_smfmac_f32_16x16x64_bf16 v[200:203], v[220:223], %[w02], %[ix]\n\t"
                            "ds_read_b128 v[240:243], %[mp] offset:" VOFF7 "\n\t"
                            "s_waitcnt lgkmcnt(4)\n\t"
                            "v_smfmac_f32_16x16x64_bf16 v[200:203], v[224:227], %[w03], %[ix]\n\t"
                            "ds_read_b128 v[244:247], %[mp] offset:" VOFF8 "\n\t"
                            "s_waitcnt lgkmcnt(4)\n\t"
                            "v_smfmac_f32_16x16x64_bf16 v[200:203], v[228:231], %[w04], %[ix]\n\t"
                            "ds_read_b128 v[248:251], %[mp] offset:" VOFF9 "\n\t"
                            "s_waitcnt lgkmcnt(4)\n\t"
                            "v_smfmac_f32_16x16x64_bf16 v[200:203], v[232:235], %[w05], %[ix]\n\t"
                            "s_waitcnt lgkmcnt(3)\n\t"
                            "v_smfmac_f32_16x16x64_bf16 v[200:203], v[236:239], %[w06], %[ix]\n\t"
                            "s_waitcnt lgkmcnt(2)\n\t"
                            "v_smfmac_f32_16x16x64_bf16 v[200:203], v[240:243], %[w07], %[ix]\n\t"
                            "s_waitcnt lgkmcnt(1)\n\t"
                            "v_smfmac_f32_16x16x64_bf16 v[200:203], v[244:247], %[w08], %[ix]\n\t"
                            "s_waitcnt lgkmcnt(0)\n\t"
                            "v_smfmac_f32_16x16x64_bf16 v[200:203], v[248:251], %[w09], %[ix]\n\t"
                            "s_nop 2\n\t"
                            "v_smfmac_f32_16x16x64_bf16 v[204:207], v[212:215], %[w10], %[ix]\n\t"
                            "s_nop 2\n\t"
                            "v_smfmac_f32_16x16x64_bf16 v[204:207], v[216:219], %[w11], %[ix]\n\t"
                            "v_add_f32 v200, v200, v202\n\t"
                            "v_add_f32 v201, v201, v203\n\t"
                            "v_cvt_pk_bf16_f32 v200, v200, v201\n\t"
                            "ds_write_b32 %[vw], v200\n\t"
                            MTA(2)
                            "v_smfmac_f32_16x16x64_bf16 v[204:207], v[220:223], %[w12], %[ix]\n\t"
                            "v_max3_u32 v208, v208, v212, v213\n\t"
                            "v_max3_u32 v208, v208, v214, v215\n\t"
                            "v_smfmac_f32_16x16x64_bf16 v[204:207], v[224:227], %[w13], %[ix]\n\t"
                            "v_max3_u32 v208, v208, v216, v217\n\t"
                            "v_max3_u32 v208, v208, v218, v219\n\t"
                            "v_smfmac_f32_16x16x64_bf16 v[204:207], v[228:231], %[w14], %[ix]\n\t"
                            "v_max3_u32 v208, v208, v220, v221\n\t"
                            "v_max3_u32 v208, v208, v222, v223\n\t"
                            "v_smfmac_f32_16x16x64_bf16 v[204:207], v[232:235], %[w15], %[ix]\n\t"
                            "v_max3_u32 v208, v208, v224, v225\n\t"
                            "v_max3_u32 v208, v208, v226, v227\n\t"
                            "v_smfmac_f32_16x16x64_bf16 v[204:207], v[236:239], %[w16], %[ix]\n\t"
                            "v_max3_u32 v208, v208, v228, v229\n\t"
                            "v_max3_u32 v208, v208, v230, v231\n\t"
                            "v_smfmac_f32_16x16x64_bf16 v[204:207], v[240:243], %[w17], %[ix]\n\t"
                            "s_waitcnt lgkmcnt(0)\n\t"
                            "s_barrier\n\t"
                            MTA(5)
                            "v_smfmac_f32_16x16x64_bf16 v[204:207], v[244:247], %[w18], %[ix]\n\t"
                            "v_max3_u32 v208, v208, v232, v233\n\t"
                            "v_max3_u32 v208, v208, v234, v235\n\t"
                            "v_smfmac_f32_16x16x64_bf16 v[204:207], v[248:251], %[w19], %[ix]\n\t"
                            "v_max3_u32 v208, v208, v236, v237\n\t"
                            "v_max3_u32 v208, v208, v238, v239\n\t"
                            "v_max3_u32 v208, v208, v240, v241\n\t"
                            "v_max3_u32 v208, v208, v242, v243\n\t"
                            "v_max3_u32 v208, v208, v244, v245\n\t"
                            "v_max3_u32 v208, v208, v246, v247\n\t"
                            "v_max3_u32 v208, v208, v248, v249\n\t"
                            "v_max3_u32 v208, v208, v250, v251\n\t"
                            "v_cmp_lt_u32 vcc, %[lim], v208\n\t"
                            "v_add_f32 v204, v204, v206\n\t"
                            "v_add_f32 v205, v205, v207\n\t"
                            "v_cvt_pk_bf16_f32 v204, v204, v205\n\t"
                            "ds_write_b32 %[vw], v204 offset:256\n\t"
                            MTA(3)
                            "s_cbranch_vccz 1f\n\t"
                            "v_mov_b32 v208, %[it]\n\t"
                            "v_mov_b32 v209, 0\n\t"
                            "ds_write_b32 v209, v208 offset:%[ls]\n\t"
                            "1:\n\t"
                            "s_waitcnt vmcnt(0) lgkmcnt(0)\n\t"
                            "s_barrier\n\t"
                            "s_setprio 0\n\t"
                            :: [mp] "v"(mp0), [vw] "v"(vw), [ix] "v"(sidx), [lim] "s"(lim), [it] "s"(it), [ls] "n"(L_S),
                              [w00] "v"(wt[0][VREV ? 9 - 0 : 0]), [w01] "v"(wt[0][VREV ? 9 - 1 : 1]), [w02] "v"(wt[0][VREV ? 9 - 2 : 2]), [w03] "v"(wt[0][VREV ? 9 - 3 : 3]), [w04] "v"(wt[0][VREV ? 9 - 4 : 4]), [w05] "v"(wt[0][VREV ? 9 - 5 : 5]), [w06] "v"(wt[0][VREV ? 9 - 6 : 6]), [w07] "v"(wt[0][VREV ? 9 - 7 : 7]), [w08] "v"(wt[0][VREV ? 9 - 8 : 8]), [w09] "v"(wt[0][VREV ? 9 - 9 : 9]), [w10] "v"(wt[1][VREV ? 9 - 0 : 0]), [w11] "v"(wt[1][VREV ? 9 - 1 : 1]), [w12] "v"(wt[1][VREV ? 9 - 2 : 2]), [w13] "v"(wt[1][VREV ? 9 - 3 : 3]), [w14] "v"(wt[1][VREV ? 9 - 4 : 4]), [w15] "v"(wt[1][VREV ? 9 - 5 : 5]), [w16] "v"(wt[1][VREV ? 9 - 6 : 6]), [w17] "v"(wt[1][VREV ? 9 - 7 : 7]), [w18] "v"(wt[1][VREV ? 9 - 8 : 8]), [w19] "v"(wt[1][VREV ? 9 - 9 : 9])
                            : "v200", "v201", "v202", "v203", "v204", "v205", "v206", "v207", "v208", "v209", "v212", "v213", "v214", "v215", "v216", "v217", "v218", "v219", "v220", "v221", "v222", "v223", "v224", "v225", "v226", "v227", "v228", "v229", "v230", "v231", "v232", "v233", "v234", "v235", "v236", "v237", "v238", "v239", "v240", "v241", "v242", "v243", "v244", "v245", "v246", "v247", "v248", "v249", "v250", "v251", "vcc", "memory");
                        MT(4);
                        redo = __builtin_amdgcn_readfirstlane(*(volatile LAS unsigned*)(smem + L_S)) == it;
                        if (redo) FC(fc0);
                    } else {
                        f32x16 ca, cb;
                        f32x8 cc;
                        asm volatile("v_mov_b64 v[212:213], 0\n\tv_mov_b64 v[214:215], 0\n\tv_mov_b64 v[216:217], 0\n\tv_mov_b64 v[218:219], 0\n\tv_mov_b64 v[220:221], 0\n\tv_mov_b64 v[222:223], 0\n\tv_mov_b64 v[224:225], 0\n\tv_mov_b64 v[226:227], 0\n\tv_mov_b64 v[228:229], 0\n\tv_mov_b64 v[230:231], 0\n\tv_mov_b64 v[232:233], 0\n\tv_mov_b64 v[234:235], 0\n\tv_mov_b64 v[236:237], 0\n\tv_mov_b64 v[238:239], 0\n\tv_mov_b64 v[240:241], 0\n\tv_mov_b64 v[242:243], 0\n\tv_mov_b64 v[244:245], 0\n\tv_mov_b64 v[246:247], 0\n\tv_mov_b64 v[248:249], 0\n\tv_mov_b64 v[250:251], 0\n\t" : "={v[212:227]}"(ca), "={v[228:243]}"(cb), "={v[244:251]}"(cc));
                        unsigned sr = tro + (unsigned)(s & 3) * 2u * PAIR;
                        asm volatile("" : "+s"(sr));
                        if (TK) fetch(tk, s, !redo, s > 0 ? poll_lim(budget) : ~0u, 0u, 0u, false);
                        else asm volatile(ALN "s_waitcnt lgkmcnt(0)\n\ts_barrier\n\ts_barrier\n\ts_barrier" ::: "memory");
                        unsigned vd, vv, zs = zb;
                        u32x4 af1;
                        asm volatile(
                            "s_setprio 1\n\t"
                            "s_waitcnt lgkmcnt(0)\n\t"
                            "s_barrier\n\t"
                            "ds_read_b64_tr_b16 v[192:193], %[va]\n\t"
                            "ds_read_b64_tr_b16 v[194:195], %[va] offset:32\n\t"
                            "s_waitcnt lgkmcnt(0)\n\t"
                            "v_smfmac_f32_16x16x64_bf16 v[212:215], v[192:195], %[w0], %[ix]\n\t"
                            "v_smfmac_f32_16x16x64_bf16 v[220:223], v[192:195], %[w2], %[ix]\n\t"
                            "v_smfmac_f32_16x16x64_bf16 v[216:219], v[192:195], %[w1], %[ix]\n\t"
                            "v_smfmac_f32_16x16x64_bf16 v[224:227], v[192:195], %[w3], %[ix]\n\t"
                            "s_barrier\n\t"
                            MTA(4)
                            "ds_read_b64_tr_b16 v[196:197], %[va] offset:256\n\t"
                            "v_smfmac_f32_16x16x64_bf16 v[228:231], v[192:195], %[w4], %[ix]\n\t"
                            "ds_read_b64_tr_b16 v[198:199], %[va] offset:288\n\t"
                            "v_smfmac_f32_16x16x64_bf16 v[236:239], v[192:195], %[w6], %[ix]\n\t"
                            "ds_read_b32 v210, %[vz] offset:%[ls]\n\t"
                            "v_smfmac_f32_16x16x64_bf16 v[232:235], v[192:195], %[w5], %[ix]\n\t"
                            "v_smfmac_f32_16x16x64_bf16 v[240:243], v[192:195], %[w7], %[ix]\n\t"
                            "v_smfmac_f32_16x16x64_bf16 v[244:247], v[192:195], %[w8], %[ix]\n\t"
                            "v_smfmac_f32_16x16x64_bf16 v[248:251], v[192:195], %[w9], %[ix]\n\t"
                            "buffer_load_dword %[sf], %[sl], %[rw], %[sr] offen sc1\n\t"
                            : [sf] "=&v"(sf), "+{v[212:227]}"(ca), "+{v[228:243]}"(cb), "+{v[244:251]}"(cc), "=&{v[196:199]}"(af1), "=&{v210}"(vv)
                            : [va] "v"(va), [vz] "v"(0u), [ls] "n"(L_S), [ix] "v"(sidx), [sl] "v"(tk.sl), [sr] "s"(sr), [rw] "s"(rwin),
                              [w0] "v"(WO(0, 0)), [w1] "v"(WO(0, 1)), [w2] "v"(WO(0, 2)), [w3] "v"(WO(0, 3)), [w4] "v"(WO(0, 4)), [w5] "v"(WO(0, 5)), [w6] "v"(WO(0, 6)), [w7] "v"(WO(0, 7)), [w8] "v"(WO(0, 8)), [w9] "v"(WO(0, 9))
                            : "v192", "v193", "v194", "v195", "memory");
                        asm volatile(
                            "s_waitcnt lgkmcnt(0)\n\t"
                            "v_smfmac_f32_16x16x64_bf16 v[212:215], v[196:199], %[w0], %[ix]\n\t"
                            "v_readfirstlane_b32 %[vd], v210\n\t"
                            "v_smfmac_f32_16x16x64_bf16 v[220:223], v[196:199], %[w2], %[ix]\n\t"
                            "s_cmp_eq_u32 %[vd], %[it]\n\t"
                            "s_cselect_b32 %[zs], 2.0, %[zs]\n\t"
                            "v_smfmac_f32_16x16x64_bf16 v[216:219], v[196:199], %[w1], %[ix]\n\t"
                            "s_nop 1\n\t"
                            "v_smfmac_f32_16x16x64_bf16 v[224:227], v[196:199], %[w3], %[ix]\n\t"
                            "v_add_f32 v212, v212, v214\n\t"
                            "v_smfmac_f32_16x16x64_bf16 v[228:231], v[196:199], %[w4], %[ix]\n\t"
                            "v_add_f32 v213, v213, v215\n\t"
                            "v_smfmac_f32_16x16x64_bf16 v[236:239], v[196:199], %[w6], %[ix]\n\t"
                            "v_add_f32 v220, v220, v222\n\t"
                            "v_smfmac_f32_16x16x64_bf16 v[232:235], v[196:199], %[w5], %[ix]\n\t"
                            "v_add_f32 v221, v221, v223\n\t"
                            "v_smfmac_f32_16x16x64_bf16 v[240:243], v[196:199], %[w7], %[ix]\n\t"
                            "v_cvt_pk_bf16_f32 v200, v212, v213\n\t"
                            "v_smfmac_f32_16x16x64_bf16 v[244:247], v[196:199], %[w8], %[ix]\n\t"
                            "v_cvt_pk_bf16_f32 v201, v220, v221\n\t"
                            "v_smfmac_f32_16x16x64_bf16 v[248:251], v[196:199], %[w9], %[ix]\n\t"
                            "v_add_f32 v216, v216, v218\n\t"
                            "v_add_f32 v217, v217, v219\n\t"
                            "v_add_f32 v224, v224, v226\n\t"
                            "v_add_f32 v225, v225, v227\n\t"
                            "v_cvt_pk_bf16_f32 v202, v216, v217\n\t"
                            "v_cvt_pk_bf16_f32 v203, v224, v225\n\t"
                            "v_permlane32_swap_b32 v200, v201\n\t"
                            "v_pk_add_f32 v[228:229], v[228:229], v[230:231]\n\t"
                            "v_permlane32_swap_b32 v202, v203\n\t"
                            "v_min_u32 v201, -2, v201\n\t"
                            "v_pk_add_f32 v[236:237], v[236:237], v[238:239]\n\t"
                            "v_min_u32 v203, -2, v203\n\t"
                            "v_permlane16_swap_b32 v200, v202\n\t"
                            "v_pk_add_f32 v[232:233], v[232:233], v[234:235]\n\t"
                            "v_permlane16_swap_b32 v201, v203\n\t"
                            "v_pk_add_f32 v[240:241], v[240:241], v[242:243]\n\t"
                            MTA(5)
                            "buffer_store_dwordx4 v[200:203], %[z0], %[rw], %[zs] offen sc1\n\t"
                            "v_cvt_pk_bf16_f32 v204, v228, v229\n\t"
                            "v_cvt_pk_bf16_f32 v205, v236, v237\n\t"
                            "v_cvt_pk_bf16_f32 v206, v232, v233\n\t"
                            "v_cvt_pk_bf16_f32 v207, v240, v241\n\t"
                            "v_permlane32_swap_b32 v204, v205\n\t"
                            "v_pk_add_f32 v[244:245], v[244:245], v[246:247]\n\t"
                            "v_permlane32_swap_b32 v206, v207\n\t"
                            "v_min_u32 v205, -2, v205\n\t"
                            "v_pk_add_f32 v[248:249], v[248:249], v[250:251]\n\t"
                            "v_min_u32 v207, -2, v207\n\t"
                            "v_permlane16_swap_b32 v204, v206\n\t"
                            "v_cvt_pk_bf16_f32 v208, v244, v245\n\t"
                            "v_permlane16_swap_b32 v205, v207\n\t"
                            "v_cvt_pk_bf16_f32 v209, v248, v249\n\t"
                            "buffer_store_dwordx4 v[204:207], %[z1], %[rw], %[zs] offen sc1\n\t"
                            "s_nop 0\n\t"
                            "v_permlane32_swap_b32 v208, v209\n\t"
                            "v_min_u32 v209, -2, v209\n\t"
                            "buffer_store_dwordx2 v[208:209], %[z2], %[rw], %[zs] offen sc1\n\t"
                            : [vd] "=&s"(vd), [zs] "+s"(zs), "=&{v[200:203]}"(za), "=&{v[204:207]}"(zc), "=&{v[208:209]}"(ze), "+{v[212:227]}"(ca), "+{v[228:243]}"(cb), "+{v[244:251]}"(cc), "+v"(sf)
                            : [ix] "v"(sidx), [it] "s"(it), [rw] "s"(rwin), [z0] "v"(zo0), [z1] "v"(zo1), [z2] "v"(zo2), "{v[196:199]}"(af1), "{v210}"(vv),
                              [w0] "v"(WO(1, 0)), [w1] "v"(WO(1, 1)), [w2] "v"(WO(1, 2)), [w3] "v"(WO(1, 3)), [w4] "v"(WO(1, 4)), [w5] "v"(WO(1, 5)), [w6] "v"(WO(1, 6)), [w7] "v"(WO(1, 7)), [w8] "v"(WO(1, 8)), [w9] "v"(WO(1, 9))
                            : "scc", "memory");
                        redo = vd == it;
                        RT(4);
                        MT(6);
                        tref = __builtin_amdgcn_s_memtime();
                        __builtin_amdgcn_s_setprio(TKP);
                        asm volatile("s_waitcnt vmcnt(3)" : "+v"(sf) :: "memory");
                    }
                } while (__builtin_expect(redo, 0));
                if (s < 8) SST(8 + s);
                if (C == 0) {
                    if (s > 0 && s + 1 < NSEG) {
                        __builtin_amdgcn_s_sleep(DZR);
                        const unsigned zq = zb == W_ZM ? W_ZM + 2u * NOWN * PAIR : zb - NOWN * PAIR;
                        unsigned s1;
                        asm volatile("v_mov_b32 %0, -1" : "=v"(s1));
                        st128(((u32x4){s1, s1, s1, s1}), rwin, zo0, zq, 16);
                        st128(((u32x4){s1, s1, s1, s1}), rwin, zo1, zq, 16);
                        __builtin_amdgcn_raw_buffer_store_b64((u32x2){s1, s1}, rwin, zo2, zq, 16);
                    }
                } else {
                    if (s > TAILS) __builtin_amdgcn_raw_buffer_store_b32((unsigned)s - 1u, rwin, 0u, W_FZ2 + fo + (unsigned)(hh * 128 + (w & 3) * 4), 16);
                    if (TK && __builtin_expect(s == 0, 0)) { wait_h(rwin, fo, 5, hrdy); clk_init(ck); }
                    if (TK && s + 1 < NSEG) sched2(tk, s, sf);
                    if (!TK) wait_until(tref + DZ2);
                    if (s + 2 < NSEG) put_z2(s);
                }
                if (budget <= 0) set_tmo(rwin);
                zb = zb == W_ZM + 2u * NOWN * PAIR ? W_ZM : zb + NOWN * PAIR;
                RT(5);
                RTS();
                MT(7);
                MTS();
            }
        };
        const int tid = mytid();
        gather_mem0(memL, tid);
        if (tid < 8) *(unsigned*)(smem + L_S + tid * 4) = 0u;
        if (w < 4) steps(IC<0>()); else if (w < 7) steps(IC<1>()); else steps(IC<3>());
    };
    auto vt_role = [&](const u32x8 (&wt)[2][10], int hh, unsigned long long mv) INL {
        constexpr bool vt = true;
        const int jl = 63 - __builtin_clzll(mv);
        auto nextv = [&](int j) { return min(seg_after(mv, j), jl); };
        const int jv1 = nextv(-1), jv2 = nextv(jv1);
        if (w == 0) *(unsigned*)(smem + L_S + (fresh_lane() & 7u) * 4) = 0u;
        stage2(jv1, jv2, true);
        {
            LANES();
            const int ni = __builtin_popcountll(mv) + 1;
            if (w < 4) {
                const LAS char* c0 = (const LAS char*)(hL + hoff + ((fq ^ (fr & 7)) << 4));
                const LAS char* c1 = (const LAS char*)(hL + hoff + (((4 + fq) ^ (fr & 7)) << 4));
                const LAS char* n0 = c0 + 40960; const LAS char* n1 = c1 + 40960;
                u32x4 bb[8];
                project0(bb, c0, c1);
                for (int i = 1; i < ni; ++i) {
                    if (vt) MT(0);
                    f32x4 tc[2][2];
                    project<0>(wt, tc, bb, c0, c1, n0, n1,
                        [&]() {
                            MT(1);
                            if (i == 1) SST(16);
                            asm volatile("s_waitcnt vmcnt(0) lgkmcnt(0)\n\ts_barrier" ::: "memory");
                            if (i == 1) SST(17);
                            MT(2);
                        }, [](auto) {});
                    { const LAS char* x = c0; c0 = n0; n0 = x; x = c1; c1 = n1; n1 = x; }
                    char* tb = tokL + (i & 1) * 32 * QS + (16 * w + 4 * fq) * 2;
#pragma unroll
                    for (int g = 0; g < 2; ++g)
#pragma unroll
                        for (int tt = 0; tt < 2; ++tt) *(u32x2*)(tb + (prow + 8 * tt) * QS + g * 128) = (u32x2){pack2(tc[g][tt][0], tc[g][tt][1]), pack2(tc[g][tt][2], tc[g][tt][3])};
                    asm volatile("" ::: "memory");
                    *(volatile LAS int*)(smem + L_S + 16 + w * 4) = i;
                    if (i == 1) SST(18);
                    if (__builtin_expect(i == 1, 0)) __builtin_amdgcn_s_sleep(VSL);
                    if (vt) { MT(3); const int s = i & 63; MTRT(); MTS(); }
                }
                project_end(bb);
            } else {
                __builtin_amdgcn_s_setprio(OPR);
                const unsigned hv = lane * 16u, fz_ = W_FZ + fo + fz_at(hh, w - 4) + (jv1 ? 4096u : 0u);
                unsigned fp = ~0u;
                unsigned fzp = 0x40000000u;
                int jo = jv1, jh = jv2;
                for (int i = 1; i < ni; ++i, jo = nextv(jo)) {
                    const int b = i + (i >= ZLAG);
                    if (__builtin_expect(i == 1 || i == ZLAG, 0)) {
                        VMWAIT();
                        if (i == ZLAG) {
                            jh = nextv(jh);
                            wait_h(rwin, fo, jh, hrdy);
                            BAR0();
                            for (int t = 0; t < 10; ++t) dma1k(rws, hv, hseg(jh) + (unsigned)((w - 4) * 1024 + t * 4096), (unsigned)(L_H + ((i + 1) & 1) * 40960 + (w - 4) * 1024 + t * 4096));
                            VMWAIT();
                        }
                    }
                    jh = nextv(jh);
                    if (b < ni) BAR0();
                    if (__builtin_expect(jo + layer == 0 && w == 7, 0)) near_init();
                    wait_h(rwin, fo, jh, hrdy);
                    if (i == 1) SST(19);
                    if (vt) MT(1);
                    for (;;) {
                        const u32x4 f = *(volatile LAS u32x4*)(smem + L_S + 16);
                        if (__builtin_amdgcn_readfirstlane(min(min(f[0], f[1]), min(f[2], f[3]))) >= (unsigned)i) break;
                        __builtin_amdgcn_s_sleep(1);
                    }
                    asm volatile("" ::: "memory");
                    if (i == 1) SST(20);
                    if (__builtin_expect(jo == 0 && w == 7, 0)) __builtin_amdgcn_raw_buffer_store_b32((4u * (unsigned)__builtin_amdgcn_s_memrealtime() + (layer ? DVT : DVT0)) & 0xFFFFFFu, rwin, two(), W_T + (unsigned)hh * 128u + 4u, 16);
                    if (vt) MT(0);
                    u32x4 vf[2][4];
#pragma unroll
                    for (int t = 0; t < 2; ++t)
#pragma unroll
                        for (int ks = 0; ks < 4; ++ks) vf[t][ks] = *(const u32x4*)(tokL + (i & 1) * 32 * QS + (16 * t + fr) * QS + ks * 64 + fq * 16);
                    const unsigned ztb = O_Z + (unsigned)((jo - layer) * (int)ZTSEG) * 2u + (unsigned)(10 * (w - 4) * H_ + hh) * 1024u;
                    f32x4 c[2][2];
                    u32x4 r;
                    unsigned hs = hseg(jh) + (unsigned)((w - 4) * 1024), hd = (unsigned)(L_H + ((b + 1) & 1) * 40960 + (w - 4) * 1024);
                    asm volatile("" : "+s"(hs), "+s"(hd));
#pragma unroll
                    for (int nt = 0; nt < 11; ++nt) {
                        auto& cc = c[nt & 1]; auto& cp = c[(nt + 1) & 1];
#pragma unroll
                        for (int ks = 0; ks < 4; ++ks) {
                            if (nt < 10) { cc[0] = mfma32(vf[0][ks], half8(WO(ks >> 1, nt < 10 ? nt : 0), ks & 1), ks ? cc[0] : z4); PIN1(cc[0]); }
                            if (nt > 0 && ks >= 1 && ks < 3) { r[2 * ks - 2] = pack2(cp[ks - 1][0], cp[ks - 1][1]); PIN1(r); }
                            if (nt < 10) { cc[1] = mfma32(vf[1][ks], half8(WO(ks >> 1, nt < 10 ? nt : 0), ks & 1), ks ? cc[1] : z4); PIN1(cc[1]); }
                            if (nt > 0 && ks >= 1 && ks < 3) { r[2 * ks - 1] = nosent1(pack2(cp[ks - 1][2], cp[ks - 1][3])); PIN1(r); }
                            if (nt > 0 && ks == 2) st128(r, rws, lane * 16u, ztb + (unsigned)((nt - 1) * H_) * 1024u, 16);
                            if (nt < 5 && !(ks & 1)) dma1k(rws, hv, hs + (2 * nt + (ks >> 1)) * 4096u, hd + (2 * nt + (ks >> 1)) * 4096u);
                            if (nt == 5 && ks == 0) {
                                asm volatile("s_waitcnt vmcnt(14)" ::: "memory");
                                __builtin_amdgcn_raw_buffer_store_b32(fp, rwin, 0u, fzp, 16);
                            }
                            SB();
                        }
                    }
                    if (vt) MT(2);
                    if (i == 1) SST(21);
                    fp = (unsigned)jo;
                    fzp = fz_;
                    if (__builtin_expect(jo <= ZEARLY || i + 1 == ni, 0)) {
                        VMWAIT();
                        __builtin_amdgcn_raw_buffer_store_b32(fp, rwin, 0u, fz_, 16);
                    } else asm volatile("s_waitcnt vmcnt(7)" ::: "memory");
                    if (jo < 8) SST(8 + jo);
                    if (vt && (!BSPARSE || jo % 6 == 2)) { const int s = jo; RT(0); RTS(); }
                    if (vt) { MT(3); const int s = i & 63; MTRT(); MTS(); }
                }
            }
            __builtin_amdgcn_s_setprio(0);
            VMWAIT();
            BAR();
        }
    };
    auto qt_role = [&](const u32x8 (&wt)[2][10], int hh, auto qq) INL {
        constexpr bool qt = decltype(qq)::v;
        const unsigned long long mq = bid < NQT ? MXQ : ~MXQ;
        const int jl = 63 - __builtin_clzll(mq);
        auto nextq = [&](int j) { return min(seg_after(mq, j), jl); };
        const int jq0 = qt ? nextq(0) : 0, jq1 = qt ? nextq(jq0) : 0;
        stage2(jq0, jq1, !qt);
        auto check2 = [&]() INL { VMWAIT(); check_h<5>(rws, rwin, hseg(jq1) + w * 1024u, L_H + 40960u + w * 1024u, 8192u); };
        {
            LANES();
            const unsigned to0 = (unsigned)((w ^ 4) >> 2) * 8192u + (unsigned)((w & 3) * 64 + (lane ^ ((fq & 1) * (w < 4 ? 8 : 5)))) * 16u, to1 = to0 + 4096u, hv = lane * 16u;
            const LAS char* c0 = (const LAS char*)(hL + hoff + ((fq ^ (fr & 7)) << 4));
            const LAS char* c1 = (const LAS char*)(hL + hoff + (((4 + fq) ^ (fr & 7)) << 4));
            u32x4 bb[8], g0 = SENT, g1 = SENT;
            f32x4 tc[2][2];
            auto pack = [&]() {
                g0 = (u32x4){pack2(tc[0][0][0], tc[0][0][1]), pack2(tc[0][0][2], tc[0][0][3]), pack2(tc[1][0][0], tc[1][0][1]), pack2(tc[1][0][2], tc[1][0][3])};
                g1 = (u32x4){pack2(tc[0][1][0], tc[0][1][1]), pack2(tc[0][1][2], tc[0][1][3]), pack2(tc[1][1][0], tc[1][1][1]), pack2(tc[1][1][2], tc[1][1][3])};
            };
            const LAS char* n0 = c0 + 40960; const LAS char* n1 = c1 + 40960;
            if (!qt) {
                project0(bb, c0, c1);
                project<0>(wt, tc, bb, c0, c1, c0, c1, []() {}, [](auto) {});
                pack();
                *(u32x4*)(tokL + to0) = g0; *(u32x4*)(tokL + to1) = g1;
                project_end(bb);
            } else {
                project0(bb, c0, c1);
                unsigned hs = hseg(jq1) + (unsigned)(w * 1024), hd = (unsigned)(L_H + 40960 + w * 1024), fv = lane == 0 ? 0u : 0x40000000u, fj = 0;
                const unsigned fq_ = W_FQ + fo + (unsigned)(hh * 128 + (jq0 > 1 ? 4096 : 0) + w * 4);
                for (int j = jq0, jn = jq1;; j = jn, jn = nextq(jn)) {
                    MT(0);
                    project<0>(wt, tc, bb, c0, c1, n0, n1,
                        [&]() {
                            if (__builtin_expect(j == jq0, 0)) check2();
                            wait_h(rwin, fo, nextq(jn), hrdy);
                            hs = hseg(nextq(jn)) + (unsigned)(w * 1024); hd = (unsigned)(2 * L_H + 40960 + 2 * w * 1024) - hd;
                            asm volatile("" : "+s"(hs), "+s"(hd));
                            MT(1);
                            VMWAIT();
                            BAR();
                            MT(2);
                        },
                        [&](auto uu) {
                            constexpr int u = decltype(uu)::v;
                            if (u == 34 || u == 38 || u == 2 || u == 6 || u == 10) { constexpr int t = ((u + 6) % 40) / 4; dma1k(rws, hv, hs + t * 8192u, hd + t * 8192u); }
                            if (u == UFQ) { asm volatile("s_waitcnt vmcnt(3)" ::: "memory"); __builtin_amdgcn_raw_buffer_store_b32(fj, rwin, fv, fq_, 16); asm volatile("" ::: "memory"); }
                        });
                    pack();
                    const unsigned qs = O_QK + (unsigned)(j * H_ + hh) * 16384u;
                    st128(g0, rws, to0, qs, 16);
                    st128(g1, rws, to1, qs, 16);
                    { const LAS char* x = c0; c0 = n0; n0 = x; x = c1; c1 = n1; n1 = x; }
                    fj = (unsigned)j;
                    PIN1(fj);
                    if (__builtin_expect(j <= QEARLY || j == jl, 0)) {
                        VMWAIT();
                        __builtin_amdgcn_raw_buffer_store_b32(fj, rwin, fv, fq_, 16);
                        if (j == jl) break;
                    }
                    MT(3);
                    if (j < 8) SST(8 + j);
                    if (!BSPARSE || j % 6 == 2) { const int s = j; RT(0); RTS(); MTRT(); MTS(); }
                }
                project_end(bb);
                VMWAIT();
                BAR0();
            }
        }
        SST(4);
    };
    auto qk_role = [&](const u32x8 (&wt)[2][10]) INL {
        const int hh = bid;
        int qrdy = 0;
        auto steps = [&](auto cc) INL {
        constexpr int C = decltype(cc)::v >> 1, R = decltype(cc)::v & 1;
        LANES();
        const LAS char* ka = (const LAS char*)(smem + L_K + (fr & 1) * TQS + (4 * fq + (fr >> 2)) * 8);
        const LAS char* qa = (const LAS char*)(smem + L_Q + (fr & 1) * 544 + ((fr >> 1) & 1) * 1152 + (16 * fq + 2 * (fr >> 2)) * 4);
        LAS char* qw = (LAS char*)(C < 2 ? smem + L_Q + (fq & 1) * 544 + (fq >> 1) * 1152 + (32 * w + 8 * (fr >> 2) + bs2(fr & 3)) * 4
                                         : smem + L_K + (fq >> 1) * TQS + (32 * (w & 3) + fr) * 8 + (fq & 1) * 4);
        const LAS char* mp0 = (const LAS char*)(memL + ma0);
        const int z = 16 * (fr >> 3) + 4 * (fr & 3) + 2 * ((fr >> 2) & 1);
        const int ko = ((z >> 4) * 256 + fq * 16 + ((z & 15) ^ ((fq & 1) * 5))) * 16;
        unsigned pa0 = 2u * (hh >> 3) * PAIR + (unsigned)(C * 4096 + (hh & 7) * 512 + R * 256 + lane * 4);
        unsigned pa2 = ((8u + (fr >> 3)) * PAIR + C * 4096u + (unsigned)((hh >> 1) * 256 + R * 128 + (hh & 1) * 64 + fq * 16 + (fr & 6) * 2)) | (fr & 1) << 30;
        PIN2(ka, qw); PIN1(mp0);
        constexpr bool TK = C == 2 && R == 1;
        Tk tk = {0u, 0u, 0x40000000u, nullptr};
        if (TK) { tk = tk_init(); __builtin_amdgcn_s_setprio(TKP); }
        if (C < 2) { PIN2(qa, pa0); PIN1(pa2); }
        int qf_ = 0;
        int tbo = 0;
        for (int s = 0; s < NSEG; ++s) {
            RT(0);
            const char* tb = tokL + tbo;
            unsigned pb = __builtin_amdgcn_readfirstlane(s + 1 < NSEG ? p_slot(s) + nh : 0x40000000u);
            asm volatile("" : "+s"(pb));
            bool redo = false;
            unsigned sf = 0;
            if (s <= 1) BAR();
            u32x8 kx[2][2];
            if (C < 2) {
#pragma unroll
                for (int u = 0; u < 4; ++u) kx[u >> 1][u & 1] = cat8(*(const u32x4*)(tb + (ko ^ ((u >> 1) * 16)) + (u & 1) * 2048), *(const u32x4*)(tb + (ko ^ ((u >> 1) * 16)) + (u & 1) * 2048 + 1024));
                PIN2(kx[0][0], kx[0][1]); PIN2(kx[1][0], kx[1][1]);
            }
            do {
                ++it;
                f32x4 a0 = zero4(), a1 = zero4();
                PIN2(a0, a1);
                u32x4 m[10];
                if (C < 2) {
                    __builtin_amdgcn_s_setprio(2);
                    asm volatile(ALN ::: "memory");
                    BAR();
                    MT(0);
#pragma unroll
                    for (int t = 0; t < NB1; ++t) { m[t] = *(const LAS u32x4*)(mp0 + t * 1024); SB(); }
                    BAR0();
#pragma unroll
                    for (int t = 0; t < 10; ++t) {
                        if (t == QB2) { BAR0(); MT(1); }
                        if (t + NB1 < 10) m[t + NB1] = *(const LAS u32x4*)(mp0 + (t + NB1) * 1024);
                        SB();
                        a0 = smfmac(m[t], wt[0][t], a0, sidx); a1 = smfmac(m[t], wt[1][t], a1, sidx);
                        PIN2(a0, a1);
                    }
                } else {
                    unsigned mx = 0;
                    asm volatile("" : "+{v[200:203]}"(a0), "+{v[204:207]}"(a1), "+{v208}"(mx));
                    unsigned lim = s > 0 ? poll_lim(budget) : ~0u, sr = tro + (unsigned)(s & 3) * 2u * PAIR;
                    asm volatile("" : "+s"(lim), "+s"(sr));
                    if (TK) fetch(tk, s, !redo, lim, DQC, (unsigned)hh * 128u, true);
                    else asm volatile(ALN "s_waitcnt lgkmcnt(0)\n\ts_barrier\n\ts_barrier\n\ts_barrier" ::: "memory");
                    asm volatile(
                        "ds_read_b128 v[212:215], %[mp] offset:0\n\t"
                        "ds_read_b128 v[216:219], %[mp] offset:1024\n\t"
                        "ds_read_b128 v[220:223], %[mp] offset:2048\n\t"
                        "ds_read_b128 v[224:227], %[mp] offset:3072\n\t"
                        "ds_read_b128 v[228:231], %[mp] offset:4096\n\t"
                        "ds_read_b128 v[232:235], %[mp] offset:5120\n\t"
                        "ds_read_b128 v[236:239], %[mp] offset:6144\n\t"
                        "ds_read_b128 v[240:243], %[mp] offset:7168\n\t"
                        "ds_read_b128 v[244:247], %[mp] offset:8192\n\t"
                        "ds_read_b128 v[248:251], %[mp] offset:9216\n\t"
                        MTA(4)
                        "s_waitcnt lgkmcnt(9)\n\t"
                        "v_smfmac_f32_16x16x64_bf16 v[200:203], v[212:215], %[w00], %[ix]\n\t"
                        "v_max3_u32 v208, v208, v212, v213\n\t"
                        "v_smfmac_f32_16x16x64_bf16 v[204:207], v[212:215], %[w10], %[ix]\n\t"
                        "s_waitcnt lgkmcnt(8)\n\t"
                        "v_max3_u32 v208, v208, v214, v215\n\t"
                        "v_smfmac_f32_16x16x64_bf16 v[200:203], v[216:219], %[w01], %[ix]\n\t"
                        "v_max3_u32 v208, v208, v216, v217\n\t"
                        "v_smfmac_f32_16x16x64_bf16 v[204:207], v[216:219], %[w11], %[ix]\n\t"
                        "s_waitcnt lgkmcnt(7)\n\t"
                        "v_max3_u32 v208, v208, v218, v219\n\t"
                        "v_smfmac_f32_16x16x64_bf16 v[200:203], v[220:223], %[w02], %[ix]\n\t"
                        "v_max3_u32 v208, v208, v220, v221\n\t"
                        "v_smfmac_f32_16x16x64_bf16 v[204:207], v[220:223], %[w12], %[ix]\n\t"
                        "s_waitcnt lgkmcnt(6)\n\t"
                        "v_max3_u32 v208, v208, v222, v223\n\t"
                        "v_smfmac_f32_16x16x64_bf16 v[200:203], v[224:227], %[w03], %[ix]\n\t"
                        "v_max3_u32 v208, v208, v224, v225\n\t"
                        "v_smfmac_f32_16x16x64_bf16 v[204:207], v[224:227], %[w13], %[ix]\n\t"
                        "s_waitcnt lgkmcnt(0)\n\t"
                        "v_max3_u32 v208, v208, v226, v227\n\t"
                        "v_smfmac_f32_16x16x64_bf16 v[200:203], v[228:231], %[w04], %[ix]\n\t"
                        "v_max3_u32 v208, v208, v228, v229\n\t"
                        "v_smfmac_f32_16x16x64_bf16 v[204:207], v[228:231], %[w14], %[ix]\n\t"
                        "v_max3_u32 v208, v208, v230, v231\n\t"
                        "v_smfmac_f32_16x16x64_bf16 v[200:203], v[232:235], %[w05], %[ix]\n\t"
                        "v_max3_u32 v208, v208, v232, v233\n\t"
                        "v_smfmac_f32_16x16x64_bf16 v[204:207], v[232:235], %[w15], %[ix]\n\t"
                        "v_max3_u32 v208, v208, v234, v235\n\t"
                        "v_smfmac_f32_16x16x64_bf16 v[200:203], v[236:239], %[w06], %[ix]\n\t"
                        "v_max3_u32 v208, v208, v236, v237\n\t"
                        "v_smfmac_f32_16x16x64_bf16 v[204:207], v[236:239], %[w16], %[ix]\n\t"
                        "v_max3_u32 v208, v208, v238, v239\n\t"
                        "v_smfmac_f32_16x16x64_bf16 v[200:203], v[240:243], %[w07], %[ix]\n\t"
                        "v_max3_u32 v208, v208, v240, v241\n\t"
                        "v_smfmac_f32_16x16x64_bf16 v[204:207], v[240:243], %[w17], %[ix]\n\t"
                        "v_max3_u32 v208, v208, v242, v243\n\t"
                        "v_smfmac_f32_16x16x64_bf16 v[200:203], v[244:247], %[w08], %[ix]\n\t"
                        "v_max3_u32 v208, v208, v244, v245\n\t"
                        "v_smfmac_f32_16x16x64_bf16 v[204:207], v[244:247], %[w18], %[ix]\n\t"
                        "v_max3_u32 v208, v208, v246, v247\n\t"
                        "v_smfmac_f32_16x16x64_bf16 v[200:203], v[248:251], %[w09], %[ix]\n\t"
                        "v_max3_u32 v208, v208, v248, v249\n\t"
                        "v_smfmac_f32_16x16x64_bf16 v[204:207], v[248:251], %[w19], %[ix]\n\t"
                        "v_max3_u32 v208, v208, v250, v251\n\t"
                        "buffer_load_dword %[sf], %[sl], %[rw], %[sr] offen sc1\n\t"
                        "v_cmp_lt_u32 vcc, %[lim], v208\n\t"
                        "s_nop 3\n\t"
                        "v_pk_add_f32 v[200:201], v[200:201], v[202:203]\n\t"
                        "v_pk_add_f32 v[204:205], v[204:205], v[206:207]\n\t"
                        "v_cvt_pk_bf16_f32 v200, v200, v201\n\t"
                        "v_cvt_pk_bf16_f32 v204, v204, v205\n\t"
                        "ds_write2_b32 %[qw], v200, v204 offset1:32\n\t"
                        MTA(2)
                        "s_cbranch_vccz 1f\n\t"
                        "v_mov_b32 v208, %[it]\n\t"
                        "v_mov_b32 v209, 0\n\t"
                        "ds_write_b32 v209, v208 offset:%[ls]\n\t"
                        "1:\n\t"
                        "s_waitcnt vmcnt(%[nv]) lgkmcnt(0)\n\t"
                        "s_barrier\n\t"
                        "s_waitcnt vmcnt(0)\n\t"
                        : [sf] "=&v"(sf), "+v"(qf_), "+{v[200:203]}"(a0), "+{v[204:207]}"(a1), "+{v208}"(mx)
                        : [mp] "v"(mp0), [qw] "v"(qw), [ix] "v"(sidx), [lim] "s"(lim), [it] "s"(it), [ls] "n"(L_S), [sl] "v"(tk.sl), [sr] "s"(sr), [rw] "s"(rwin), [nv] "n"(TK ? 2 : 1),
                          [w00] "v"(wt[0][0]), [w01] "v"(wt[0][1]), [w02] "v"(wt[0][2]), [w03] "v"(wt[0][3]), [w04] "v"(wt[0][4]), [w05] "v"(wt[0][5]), [w06] "v"(wt[0][6]), [w07] "v"(wt[0][7]), [w08] "v"(wt[0][8]), [w09] "v"(wt[0][9]), [w10] "v"(wt[1][0]), [w11] "v"(wt[1][1]), [w12] "v"(wt[1][2]), [w13] "v"(wt[1][3]), [w14] "v"(wt[1][4]), [w15] "v"(wt[1][5]), [w16] "v"(wt[1][6]), [w17] "v"(wt[1][7]), [w18] "v"(wt[1][8]), [w19] "v"(wt[1][9])
                        : "v209", "v212", "v213", "v214", "v215", "v216", "v217", "v218", "v219", "v220", "v221", "v222", "v223", "v224", "v225", "v226", "v227", "v228", "v229", "v230", "v231", "v232", "v233", "v234", "v235", "v236", "v237", "v238", "v239", "v240", "v241", "v242", "v243", "v244", "v245", "v246", "v247", "v248", "v249", "v250", "v251", "vcc", "memory");
                }
                if (C < 2) {
                    *(LAS unsigned*)qw = hpack(a0); *(LAS unsigned*)(qw + 16) = hpack(a1);
                    SB();
                    MT(2);
                    unsigned vd;
#define SC0 "v[212:215]"
#define SC1 "v[216:219]"
#define SC2 "v[220:223]"
#define DPP(op, d, ctl) op " " d ", " d ", " d " " ctl " row_mask:0xf bank_mask:0xf\n\t"
#define TAIL(A0, A2, B0, B2, C0, C2) \
                        "v_mov_b64 v[212:213], 0\n\tv_mov_b64 v[214:215], 0\n\tv_mov_b64 v[216:217], 0\n\tv_mov_b64 v[218:219], 0\n\tv_mov_b64 v[220:221], 0\n\tv_mov_b64 v[222:223], 0\n\t" \
                        "s_setprio 3\n\t" \
                        "s_waitcnt lgkmcnt(0)\n\t" \
                        "s_barrier\n\t" MTA(3) \
                        "ds_read_b64_tr_b16 v[224:225], %[qa]\n\t" \
                        "ds_read_b64_tr_b16 v[226:227], %[qa] offset:32\n\t" \
                        "ds_read_b64_tr_b16 v[228:229], %[qa] offset:256\n\t" \
                        "ds_read_b64_tr_b16 v[230:231], %[qa] offset:288\n\t" \
                        "ds_read_b64_tr_b16 v[232:233], %[ka]\n\t" \
                        "ds_read_b64_tr_b16 v[234:235], %[ka] offset:128\n\t" \
                        "ds_read_b64_tr_b16 v[236:237], %[ka] offset:256\n\t" \
                        "ds_read_b64_tr_b16 v[238:239], %[ka] offset:384\n\t" \
                        "ds_read_b64_tr_b16 v[240:241], %[ka] offset:512\n\t" \
                        "ds_read_b64_tr_b16 v[242:243], %[ka] offset:640\n\t" \
                        "s_waitcnt lgkmcnt(8)\n\t" \
                        "v_smfmac_f32_16x16x64_bf16 " SC0 ", v[224:227], %[k00], %[ix]\n\t" \
                        "ds_read_b64_tr_b16 v[244:245], %[ka] offset:768\n\t" \
                        "v_smfmac_f32_16x16x64_bf16 " SC1 ", v[224:227], %[k10], %[ix]\n\t" \
                        "ds_read_b64_tr_b16 v[246:247], %[ka] offset:896\n\t" \
                        "s_waitcnt lgkmcnt(8)\n\t" \
                        "v_smfmac_f32_16x16x64_bf16 " SC0 ", v[228:231], %[k01], %[ix]\n\t" \
                        "ds_read_b32 v248, %[vz] offset:%[ls]\n\t" \
                        "v_smfmac_f32_16x16x64_bf16 " SC1 ", v[228:231], %[k11], %[ix]\n\t" \
                        "s_waitcnt lgkmcnt(5)\n\t" \
                        "v_smfmac_f32_16x16x64_bf16 " SC2 ", v[224:227], v[232:239], %[ix]\n\t" \
                        "s_waitcnt lgkmcnt(1)\n\t" \
                        "v_smfmac_f32_16x16x64_bf16 " SC2 ", v[228:231], v[240:247], %[ix]\n\t" \
                        "s_waitcnt lgkmcnt(0)\n\t" \
                        MTA(4) "v_readfirstlane_b32 %[vd], v248\n\t" \
                        "v_add_f32 v232, " A0 ", " A2 "\n\t" \
                        "s_nop 0\n\t" \
                        "v_add_f32 v233, " B0 ", " B2 "\n\t" \
                        "v_max_f32 v235, v232, v233\n\t" \
                        "s_cmp_eq_u32 %[vd], %[it]\n\t" \
                        "s_cselect_b32 %[p0], 2.0, %[p0]\n\t" \
                        DPP("v_max_f32_dpp", "v235", "row_ror:8") \
                        "v_add_f32 v234, " C0 ", " C2 "\n\t" \
                        "v_max_f32 v235, v235, v234\n\t" \
                        "s_add_u32 %[p1], %[p0], 0x4000\n\t" \
                        "s_nop 0\n\t" DPP("v_max_f32_dpp", "v235", "row_ror:4") \
                        "s_nop 1\n\t" DPP("v_max_f32_dpp", "v235", "row_ror:2") \
                        "s_nop 1\n\t" DPP("v_max_f32_dpp", "v235", "row_ror:1") \
                        "v_mul_f32 v235, 0xbe0293ee, v235\n\t" \
                        "v_fmamk_f32 v232, v232, 0x3e0293ee, v235\n\t" \
                        "v_fmamk_f32 v233, v233, 0x3e0293ee, v235\n\t" \
                        "v_exp_f32 v232, v232\n\t" \
                        "v_exp_f32 v233, v233\n\t" \
                        "v_fmamk_f32 v234, v234, 0x3e0293ee, v235\n\t" \
                        "v_add_f32 v235, v232, v233\n\t" \
                        "v_exp_f32 v234, v234\n\t" \
                        "s_nop 0\n\t" DPP("v_add_f32_dpp", "v235", "row_ror:8") \
                        "v_add_f32 v235, v235, v234\n\t" \
                        "v_mov_b32_dpp v236, v234 quad_perm:[1,1,3,3] row_mask:0xf bank_mask:0xf\n\t" \
                        "s_nop 0\n\t" DPP("v_add_f32_dpp", "v235", "row_ror:4") \
                        "s_nop 1\n\t" DPP("v_add_f32_dpp", "v235", "row_ror:2") \
                        "s_nop 1\n\t" DPP("v_add_f32_dpp", "v235", "row_ror:1") \
                        "v_rcp_f32 v235, v235\n\t" \
                        "s_nop 0\n\t" \
                        "v_mul_f32_e64 v232, v232, v235 clamp\n\t" \
                        "v_mul_f32_e64 v233, v233, v235 clamp\n\t" \
                        "v_cvt_pk_bf16_f32 v232, v232, v233\n\t" \
                        MTA(5) "buffer_store_dword v232, %[pa], %[rw], %[p0] offen sc1\n\t" \
                        "v_mul_f32_e64 v234, v234, v235 clamp\n\t" \
                        "v_mul_f32_e64 v236, v236, v235 clamp\n\t" \
                        "buffer_store_dword v232, %[pa], %[rw], %[p1] offen sc1\n\t" \
                        "v_cvt_pk_bf16_f32 v234, v234, v236\n\t" \
                        "buffer_store_dword v234, %[pm], %[rw], %[p0] offen sc1\n\t" \
                        "s_setprio 0"
#define TAILIO : [vd] "=&s"(vd), [p0] "+s"(ps), [p1] "=&s"(ps1) \
                        : [qa] "v"(qa), [ka] "v"(ka), [vz] "v"(0u), [ls] "n"(L_S), [ix] "v"(sidx), [it] "s"(it), [pa] "v"(pa0), [pm] "v"(pa2), [rw] "s"(rwin), \
                          [k00] "v"(kx[0][0]), [k01] "v"(kx[0][1]), [k10] "v"(kx[1][0]), [k11] "v"(kx[1][1]) \
                        : "v212", "v213", "v214", "v215", "v216", "v217", "v218", "v219", "v220", "v221", "v222", "v223", "v224", "v225", "v226", "v227", "v228", "v229", "v230", "v231", "v232", \
                          "v233", "v234", "v235", "v236", "v237", "v238", "v239", "v240", "v241", "v242", "v243", "v244", "v245", "v246", "v247", "v248", "scc", "memory"
                    unsigned ps = pb, ps1;
                    if (R == 0) asm volatile(TAIL("v212", "v214", "v216", "v218", "v220", "v222") TAILIO);
                    else asm volatile(TAIL("v213", "v215", "v217", "v219", "v221", "v223") TAILIO);
                    redo = vd == it;
                    RT(4);
                    MT(6);
                } else {
                    MT(3);
                    if (TK) { __builtin_amdgcn_s_setprio(TKP); if (TKS) __builtin_amdgcn_s_sleep(TKS); }
                    redo = __builtin_amdgcn_readfirstlane(*(volatile LAS unsigned*)(smem + L_S)) == it;
                    if (redo) FC(fc0);
                }
            } while (__builtin_expect(redo, 0));
            if (s < 8) SST(8 + s);
            if (s >= 56) SST(s - 40);
            if (C + R == 1) {
                if (s + layer > 0) {
                    __builtin_amdgcn_s_sleep(DDMA);
                    unsigned l2 = fresh_lane(), s1;
                    asm volatile("v_mov_b32 %0, -1" : "=v"(s1));
                    const unsigned base = p_slot(s - 1) + nh;
                    asm volatile("s_waitcnt vmcnt(3)" ::: "memory");
                    st128(((u32x4){s1, s1, s1, s1}), rwin, (l2 >> 5) * PAIR + (l2 & 31u) * 16u, base + (unsigned)(2 * (hh >> 3)) * PAIR + (unsigned)(R * 4096 + (hh & 7) * 512), 16);
                    if (R) __builtin_amdgcn_raw_buffer_store_b64((u32x2){s1, s1}, rwin, (l2 >> 5) * PAIR + (l2 & 16u) * 256u + (l2 & 8u) * 16u + (l2 & 7u) * 8u, base + 8u * PAIR + (unsigned)((hh >> 1) * 256 + (hh & 1) * 64), 16);
                    else __builtin_amdgcn_raw_buffer_store_b64((u32x2){s1, s1}, rwin, two(), W_T + (unsigned)((s + 2) & 3) * 2u * PAIR + (unsigned)hh * 128u, 16);
                }
            } else if (TK) {
                if (s + 1 < NSEG) sched2(tk, s, sf);
            } else if (C == 2 && w == 4) {
                if (s + 2 < NSEG) {
                    auto done = [&](int f) INL {
                        f = min(f, __builtin_amdgcn_update_dpp(f, f, 0xB1, 0xF, 0xF, false)); f = min(f, __builtin_amdgcn_update_dpp(f, f, 0x4E, 0xF, 0xF, false));
                        f = min(f, __builtin_amdgcn_update_dpp(f, f, 0x141, 0xF, 0xF, false));
                        return seg_done(MXQ, __builtin_amdgcn_readlane(f, 0), __builtin_amdgcn_readlane(f, 8));
                    };
                    qrdy = max(qrdy, done(qf_));
                    const int sq = s + 2;
                    if (__builtin_expect(s == 0, 0)) __builtin_amdgcn_s_sleep(DQ0);
                    if (__builtin_expect(qrdy < sq, 0)) for (unsigned spins = 0;;) {
                        asm volatile("" ::: "memory");
                        const int f = (int)__builtin_amdgcn_raw_buffer_load_b32(rwin, (unsigned)((lane & 8) * 512 + (lane & 7) * 4), W_FQ + fo + (unsigned)hh * 128u, 16);
                        qrdy = done(f);
                        if (qrdy >= sq) break;
                        FC(fc2);
                        if (spin_fail(spins, rwin)) break;
                        __builtin_amdgcn_s_sleep(QSL);
                    }
                    const unsigned so = O_QK + (unsigned)(((s + 2) * H_ + hh) * 16384), lo = (unsigned)(L_TOK + (tbo ? tbo - 16384 : 32768)), l2 = fresh_lane();
                    if (__builtin_expect(s == 0, 0)) {
                        for (int i = 0; i < 4; ++i) dma4k(rws, l2 * 16u, so - H_ * 16384u + i * 4096u, lo - 16384u + i * 4096u);
                    }
#pragma unroll
                    for (int i = 0; i < 4; ++i) dma4k(rws, l2 * 16u, so + i * 4096u, lo + i * 4096u);
                    if (__builtin_expect(s == 0, 0)) asm volatile("s_waitcnt vmcnt(16)" ::: "memory");
                    if (qrdy < min(s + 5, NSEG - 1))
                        asm volatile("buffer_load_dword %0, %1, %2, %3 offen sc1" : "+v"(qf_) : "v"((l2 & 8u) * 512u + (l2 & 7u) * 4u), "s"(rwin), "s"(W_FQ + fo + (unsigned)hh * 128u) : "memory");
                }
            } else {
                asm volatile("s_waitcnt vmcnt(3)" ::: "memory");
                if (s > TAILS) __builtin_amdgcn_raw_buffer_store_b32((unsigned)s - 1u, rwin, 0u, W_FPT + fo + (unsigned)(hh * 128 + (w & 3) * 4), 16);
                if (C == 2 && s + 1 < NSEG) __builtin_amdgcn_s_sleep(DPT);
                const float scale = 0.08838834764831845f, l2e = 1.44269504088896340736f * (NORND ? scale : 1.f);
                const int tq = (w >> 1) & 1, qh = w & 1;
                f32x4 sc[3] = {z4, z4, z4};
                u32x4 kt[2][4], qf[4], kf[4];
                const unsigned l2 = fresh_lane(), kto = ((l2 & 8u) * 512u + (l2 & 51u) * 16u + (l2 & 4u) * 32u) ^ ((l2 & 16u) * 5u), qto = ((l2 & 55u) * 16u + qh * 128u) ^ ((l2 & 16u) * 8u);
#pragma unroll
                for (int ks = 0; ks < 4; ++ks) {
                    qf[ks] = *(const u32x4*)(tb + 8192 + (tq * 4 + ks) * 1024 + qto);
                    kt[0][ks] = *(const u32x4*)(tb + ks * 1024 + kto);
                    kt[1][ks] = *(const u32x4*)(tb + ks * 1024 + (kto ^ 64u));
                    SB();
                }
#pragma unroll
                for (int ks = 0; ks < 4; ++ks) {
                    sc[0] = mfma32(kt[0][ks], qf[ks], sc[0]); PIN1(sc[0]);
                    kf[ks] = ldtr2(ka + ks * 256, ka + ks * 256 + 128);
                    SB();
                    sc[1] = mfma32(kt[1][ks], qf[ks], sc[1]); PIN1(sc[1]);
                }
#pragma unroll
                for (int ks = 0; ks < 4; ++ks) sc[2] = mfma32(kf[ks], qf[ks], sc[2]);
                const bool hi = (l2 & 8u) != 0u;
                float v[6];
#pragma unroll
                for (int r = 0; r < 4; ++r) v[r] = hi ? sc[1][r] : sc[0][r];
                v[4] = hi ? sc[2][2] : sc[2][0]; v[5] = hi ? sc[2][3] : sc[2][1];
#pragma unroll
                for (int r = 0; r < 6; r += 2) {
                    if (NORND) break;
                    const unsigned b0 = pack2(v[r], v[r + 1]);
                    const unsigned b1 = pack2(lo2f(b0) * scale, hi2f(b0) * scale);
                    v[r] = lo2f(b1); v[r + 1] = hi2f(b1);
                }
                const int lim = 8 * (w & 3) + (int)(l2 & 7u) - 8 * (int)(l2 >> 4) - (int)((l2 & 8u) >> 1);
#pragma unroll
                for (int r = 0; r < 4; ++r) if (r > lim) v[r] = NORND ? -1000000000.0f / scale : bfr(-1000000000.0f);
                float mx = fmx(fmx(fmx(fmx(fmx(v[0], v[1]), v[2]), v[3]), v[4]), v[5]);
                mx = xmax3216(fmx(mx, ror8f(mx)));
                const float mb = -mx * l2e;
#pragma unroll
                for (int r = 0; r < 6; ++r) v[r] = __builtin_amdgcn_exp2f(fmaf(v[r], l2e, mb));
                float sum = fmaf(v[4] + v[5], 0.5f, (v[0] + v[1]) + (v[2] + v[3]));
                sum = xsum3216(sum + ror8f(sum));
                const float inv = __builtin_amdgcn_rcpf(sum);
                const unsigned p0 = pack2(v[0] * inv, v[1] * inv), p1 = nosent1(pack2(v[2] * inv, v[3] * inv)), p2 = nosent1(pack2(v[4] * inv, v[5] * inv));
                const unsigned base = (s + 2 < NSEG ? O_PT : W_PL + fo - (NSEG - 2) * PTSEG * 2u) + (unsigned)(s * PTSEG) * 2u;
                const __amdgpu_buffer_rsrc_t rp = s + 2 < NSEG ? rws : rwin;
                const unsigned oob = (l2 & 8u) << 27;
                st128(((u32x4){p0, p1, dppu<0x128>(p0), dppu<0x128>(p1)}), rp, (l2 * 16u) | oob, base + (unsigned)((hh * 2 + tq) * 1024 + qh * 128), 16);
                __builtin_amdgcn_raw_buffer_store_b64((u32x2){p2, dppu<0x128>(p2)}, rp, ((l2 & 7u) * 16u + (l2 >> 4) * 8u) | oob | (l2 & 32u) << 25, base + 65536u + (unsigned)((tq * 32 + hh) * 256 + qh * 128), 16);
            }
            tbo = tbo == 32768 ? 0 : tbo + 16384;
            if (C == 2 && __builtin_expect(budget <= 0, 0)) set_tmo(rwin);
            RT(5);
            RTS();
            MT(7);
            MTS();
        }
        };
        if (w) gather_mem0(memL, mytid() - 64, 448);
        else *(unsigned*)(smem + L_S) = 0u;
        if (layer == 0) near_init();
        qt_role(wt, hh, IC<0>());
        if (w == 7) { ck.ta = 4u * (unsigned)__builtin_amdgcn_s_memrealtime() + 8u; ck.ma = (unsigned)__builtin_amdgcn_s_memtime(); }
        if (w == 0) steps(IC<0>()); else if (w == 1) steps(IC<1>()); else if (w == 2) steps(IC<2>()); else if (w == 3) steps(IC<3>()); else if (w < 7) steps(IC<4>()); else steps(IC<5>());
    };
    auto own_role = [&]() INL {
        const unsigned long long tstart = __builtin_amdgcn_s_memtime();
        if (layer == 0) near_init();
        LANES();
        const int o = bid - 96, hg = (w + ROT * o) & 3, ot = tile_of(o);
        const unsigned ztoff = (unsigned)((o * H_ + 8 * hg) * 64 + lane) * 16u;
        const unsigned zoff = zpair(o / 10, o % 10) + (unsigned)(lane * 16);
        const unsigned poff = (2u * hg + (mc & 1u)) * PAIR + nh + (mc >> 1) * 4096u + (unsigned)((fq >> 1) * 512 + (fr >> 2) * 128 + (fr & 1) * 64 + (fq & 1) * 32 + (fr & 2) * 8);
        const unsigned pmoff = (8u + (mc & 1u)) * PAIR + nh + (mc >> 1) * 4096u + (unsigned)(fq * 256 + (fr >> 3) * 128 + (fr & 2) * 32 + (fr & 4) * 8 + (fr & 1) * 16);
        const unsigned moff = (unsigned)(ot >> 4) * 2u * PAIR + nh + (unsigned)((ot & 15) * 256 + lane * 4);
        constexpr unsigned mc2 = 4096u;
        char* op = smem + 8192;
        if (w == 4 && lane < 2) *(volatile LAS int*)(smem + 4096 + lane * 4) = -1;
        BAR();
        SST(2);
        if (w == 7) clk_init(ck);
        wait_until(tstart + DO0);
        if (w < 4) {
            u32x8 zf[4];
            int zr = -1;
            unsigned zb = W_ZM + (unsigned)layer * (NOWN * PAIR), zso = O_Z - (unsigned)layer * (unsigned)(ZTSEG * 2);
            const unsigned ra = (unsigned)((ot >> 4) * 2 + (fq & 1)) * PAIR + nh + (unsigned)((fq >> 1) * 4096 + (ot & 15) * 256 + fr * 16);
            int np = 0;
            auto piece = [&]() INL {
                asm volatile("s_mov_b32 m0, %3\n\ts_nop 3\n\tbuffer_load_dwordx4 %0, %1, %2 offen sc1 lds\n\tbuffer_load_dwordx4 %0, %1, %2 offen offset:1024 sc1 lds"
                             :: "v"(ztoff), "s"(rws), "s"(O_Z + (unsigned)((NSEG - 1 - layer) * (int)ZTSEG) * 2u + np * 2048u), "s"(32768 + hg * 8192 + np * 2048) : "m0", "memory");
                ++np;
            };
            for (int s = 0; s + 1 < NSEG; ++s) {
                const unsigned pb = p_slot(s);
                const int sz = s;
                while (__builtin_expect(zr < sz, 0)) {
                    __builtin_amdgcn_s_sleep(1);
                    zr = __builtin_amdgcn_readfirstlane(*(volatile LAS int*)(smem + 4096));
                }
                if (s == 0) SST(1);
                asm volatile(
                    "1:\n\t"
                    "buffer_load_dwordx4 v[160:163], %[zt], %[rs], %[so] offen sc1\n\t"
                    "buffer_load_dwordx4 v[164:167], %[zt], %[rs], %[so] offen offset:1024 sc1\n\t"
                    "buffer_load_dwordx4 v[168:171], %[zt], %[rs], %[so] offen offset:2048 sc1\n\t"
                    "buffer_load_dwordx4 v[172:175], %[zt], %[rs], %[so] offen offset:3072 sc1\n\t"
                    "buffer_load_dwordx4 v[176:179], %[zt], %[rs], %[s4] offen sc1\n\t"
                    "buffer_load_dwordx4 v[180:183], %[zt], %[rs], %[s4] offen offset:1024 sc1\n\t"
                    "buffer_load_dwordx4 v[184:187], %[zt], %[rs], %[s4] offen offset:2048 sc1\n\t"
                    "buffer_load_dwordx4 v[188:191], %[zt], %[rs], %[s4] offen offset:3072 sc1\n\t"
                    "s_cmp_lg_u32 %[s], 0\n\t"
                    "s_cbranch_scc1 2f\n\t"
                    "s_waitcnt vmcnt(0)\n\t"
                    "v_max3_u32 v100, v161, v163, v165\n\t"
                    "v_max3_u32 v100, v100, v167, v169\n\t"
                    "v_max3_u32 v100, v100, v171, v173\n\t"
                    "v_max3_u32 v100, v100, v175, v177\n\t"
                    "v_max3_u32 v100, v100, v179, v181\n\t"
                    "v_max3_u32 v100, v100, v183, v185\n\t"
                    "v_max3_u32 v100, v100, v187, v189\n\t"
                    "v_max_u32 v100, v100, v191\n\t"
                    "v_cmp_eq_u32 vcc, -1, v100\n\t"
                    "s_sub_i32 %[tr], %[tr], 1\n\t"
                    "s_cbranch_vccz 2f\n\t"
                    "s_cmp_gt_i32 %[tr], 0\n\t"
                    "s_cbranch_scc1 1b\n\t"
                    "2:"
                    : "=&{v[160:167]}"(zf[0]), "=&{v[168:175]}"(zf[1]), "=&{v[176:183]}"(zf[2]), "=&{v[184:191]}"(zf[3]), [tr] "+s"(budget)
                    : [zt] "v"(ztoff), [rs] "s"(rws), [so] "s"(zso), [s4] "s"(zso + 4096u), [s] "s"(s) : "v100", "vcc", "scc", "memory");
                if (s == 0) SST(3);
                zso += (unsigned)(ZTSEG * 2);
                if (zr == NSEG - 1 && np < 4) piece();
                if (__builtin_expect(budget <= 0, 0)) set_tmo(rwin);
                RT(6);
                const unsigned spa = (unsigned)((s & 1) * 2048 + (w < 3 ? w * 512 : 0) + lane * 8);
#define OST(r)
                unsigned long long t0_, t1_, t2_, t3_, t4_ = 0, bad;
                int tries = budget;
                const unsigned sw = (unsigned)(((s & 3) + 1) & 4) * 1024u;
                unsigned po = poff ^ sw, pm = pmoff ^ sw, x0 = 4096u, x1 = 4096u;
                const unsigned mb = W_MEM + (unsigned)((s + 1) & 3) * 6u * PAIR;
                if (w < 3) asm volatile(
                    ALN
                    "s_barrier\n\t"
                    OST("%[t0]")
                    "v_mov_b64 v[96:97], 0\n\t"
                    "v_mov_b64 v[98:99], 0\n\t"
                    "v_mov_b32 v100, 0\n\t"
                    "s_barrier\n\t"
                    "1:\n\t"
                    "buffer_load_dwordx4 v[208:211], %[po], %[rw], %[pb] offen sc1\n\t"
                    "buffer_load_dwordx4 v[212:215], %[po], %[rw], %[pb] offen offset:1024 sc1\n\t"
                    "buffer_load_dwordx4 v[216:219], %[po], %[rw], %[pb] offen offset:2048 sc1\n\t"
                    "buffer_load_dwordx4 v[220:223], %[po], %[rw], %[pb] offen offset:3072 sc1\n\t"
                    OST("%[t1]")
                    "s_waitcnt vmcnt(3)\n\t"
                    OST("%[t3]")
                    "v_smfmac_f32_16x16x64_bf16 v[96:99], v[208:211], v[160:167], %[ix]\n\t"
                    "v_max3_u32 v100, v100, v208, v209\n\t"
                    "v_max3_u32 v100, v100, v210, v211\n\t"
                    "s_waitcnt vmcnt(2)\n\t"
                    "v_smfmac_f32_16x16x64_bf16 v[96:99], v[212:215], v[168:175], %[ix]\n\t"
                    "v_max3_u32 v100, v100, v212, v213\n\t"
                    "v_max3_u32 v100, v100, v214, v215\n\t"
                    "s_waitcnt vmcnt(1)\n\t"
                    "v_smfmac_f32_16x16x64_bf16 v[96:99], v[216:219], v[176:183], %[ix]\n\t"
                    "v_max3_u32 v100, v100, v216, v217\n\t"
                    "v_max3_u32 v100, v100, v218, v219\n\t"
                    "s_waitcnt vmcnt(0)\n\t"
                    "v_smfmac_f32_16x16x64_bf16 v[96:99], v[220:223], v[184:191], %[ix]\n\t"
                    "v_max3_u32 v100, v100, v220, v221\n\t"
                    "v_max3_u32 v100, v100, v222, v223\n\t"
                    "v_cmp_eq_u32 vcc, -1, v100\n\t"
                    OST("%[t2]")
                    "s_nop 4\n\t"
                    "v_pk_add_f32 v[96:97], v[96:97], v[98:99]\n\t"
                    "ds_write_b64 %[sp], v[96:97]\n\t"
                    "s_cbranch_vccz 2f\n\t"
                    "s_sub_i32 %[tr], %[tr], 1\n\t"
                    "s_cmp_lt_i32 %[tr], 1\n\t"
                    "s_cbranch_scc1 2f\n\t"
                    "v_xor_b32 %[po], %[x0], %[po]\n\t"
                    "s_xor_b32 %[x0], %[x0], 0x5000\n\t"
                    "s_waitcnt lgkmcnt(0)\n\t"
                    "v_mov_b64 v[96:97], 0\n\t"
                    "v_mov_b64 v[98:99], 0\n\t"
                    "v_mov_b32 v100, 0\n\t"
                    "s_branch 1b\n\t"
                    "2:\n\t"
                    "s_waitcnt lgkmcnt(0)\n\t"
                    "s_barrier\n\t"
                    "s_sleep 3\n\t"
                    : [tr] "+s"(tries), [po] "+v"(po), [x0] "+s"(x0), [t0] "=&s"(t0_), [t1] "=&s"(t1_), [t2] "=&s"(t2_), [t3] "=&s"(t3_)
                    : [sp] "v"(spa), [ix] "v"(sidx), [rw] "s"(rwin), [pb] "s"(pb), "{v[160:167]}"(zf[0]), "{v[168:175]}"(zf[1]), "{v[176:183]}"(zf[2]), "{v[184:191]}"(zf[3])
                    : "v96", "v97", "v98", "v99", "v100", "v208", "v209", "v210", "v211", "v212", "v213", "v214", "v215", "v216", "v217", "v218", "v219", "v220", "v221", "v222", "v223", "vcc", "scc", "memory");
                else asm volatile(
                    ALN
                    "s_barrier\n\t"
                    "buffer_load_dwordx4 v[128:131], %[zo], %[rw], %[zb] offen sc1\n\t"
                    "buffer_load_dwordx4 v[132:135], %[zo], %[rw], %[zb] offen offset:1024 sc1\n\t"
                    "buffer_load_dwordx4 v[136:139], %[zo], %[rw], %[zb] offen offset:2048 sc1\n\t"
                    "buffer_load_dwordx4 v[140:143], %[zo], %[rw], %[zb] offen offset:3072 sc1\n\t"
                    "buffer_load_dwordx4 v[144:147], %[zo], %[rw], %[z4] offen sc1\n\t"
                    "buffer_load_dwordx4 v[148:151], %[zo], %[rw], %[z4] offen offset:1024 sc1\n\t"
                    "buffer_load_dwordx4 v[152:155], %[zo], %[rw], %[z4] offen offset:2048 sc1\n\t"
                    "buffer_load_dwordx4 v[156:159], %[zo], %[rw], %[z4] offen offset:3072 sc1\n\t"
                    OST("%[t0]")
                    "v_mov_b64 v[96:97], 0\n\t"
                    "v_mov_b64 v[98:99], 0\n\t"
                    "v_mov_b32 v100, 0\n\t"
                    "s_barrier\n\t"
                    "s_sleep %[fd]\n\t"
                    "buffer_load_dwordx4 v[208:211], %[po], %[rw], %[pb] offen sc1\n\t"
                    "buffer_load_dwordx4 v[212:215], %[po], %[rw], %[pb] offen offset:1024 sc1\n\t"
                    "buffer_load_dwordx4 v[216:219], %[po], %[rw], %[pb] offen offset:2048 sc1\n\t"
                    "buffer_load_dwordx4 v[220:223], %[po], %[rw], %[pb] offen offset:3072 sc1\n\t"
                    "buffer_load_dwordx4 v[224:227], %[pm], %[rw], %[pb] offen sc1\n\t"
                    "buffer_load_dwordx4 v[228:231], %[pm], %[rw], %[pb] offen offset:1024 sc1\n\t"
                    "buffer_load_dwordx4 v[232:235], %[pm], %[rw], %[pb] offen offset:2048 sc1\n\t"
                    "buffer_load_dwordx4 v[236:239], %[pm], %[rw], %[pb] offen offset:3072 sc1\n\t"
                    OST("%[t1]")
                    "s_waitcnt vmcnt(8)\n\t"
                    "v_max3_u32 v101, v129, v131, v133\n\t"
                    "v_max3_u32 v101, v101, v135, v137\n\t"
                    "v_max3_u32 v101, v101, v139, v141\n\t"
                    "v_max3_u32 v101, v101, v143, v145\n\t"
                    "v_max3_u32 v101, v101, v147, v149\n\t"
                    "v_max3_u32 v101, v101, v151, v153\n\t"
                    "v_max3_u32 v101, v101, v155, v157\n\t"
                    "v_max_u32 v101, v101, v159\n\t"
                    "v_cmp_eq_u32 vcc, -1, v101\n\t"
                    "s_cbranch_vccz 3f\n\t"
                    "buffer_load_dwordx4 v[128:131], %[zo], %[rw], %[zb] offen sc1\n\t"
                    "buffer_load_dwordx4 v[132:135], %[zo], %[rw], %[zb] offen offset:1024 sc1\n\t"
                    "buffer_load_dwordx4 v[136:139], %[zo], %[rw], %[zb] offen offset:2048 sc1\n\t"
                    "buffer_load_dwordx4 v[140:143], %[zo], %[rw], %[zb] offen offset:3072 sc1\n\t"
                    "buffer_load_dwordx4 v[144:147], %[zo], %[rw], %[z4] offen sc1\n\t"
                    "buffer_load_dwordx4 v[148:151], %[zo], %[rw], %[z4] offen offset:1024 sc1\n\t"
                    "buffer_load_dwordx4 v[152:155], %[zo], %[rw], %[z4] offen offset:2048 sc1\n\t"
                    "buffer_load_dwordx4 v[156:159], %[zo], %[rw], %[z4] offen offset:3072 sc1\n\t"
                    "s_waitcnt vmcnt(0)\n\t"
                    "v_max3_u32 v101, v129, v131, v133\n\t"
                    "v_max3_u32 v101, v101, v135, v137\n\t"
                    "v_max3_u32 v101, v101, v139, v141\n\t"
                    "v_max3_u32 v101, v101, v143, v145\n\t"
                    "v_max3_u32 v101, v101, v147, v149\n\t"
                    "v_max3_u32 v101, v101, v151, v153\n\t"
                    "v_max3_u32 v101, v101, v155, v157\n\t"
                    "v_max_u32 v101, v101, v159\n\t"
                    "v_mov_b32 v100, v101\n\t"
                    "3:\n\t"
                    "v_permlane16_swap_b32 v128, v129\n\t"
                    "v_permlane16_swap_b32 v130, v131\n\t"
                    "v_permlane16_swap_b32 v132, v133\n\t"
                    "v_permlane16_swap_b32 v134, v135\n\t"
                    "v_permlane16_swap_b32 v136, v137\n\t"
                    "v_permlane16_swap_b32 v138, v139\n\t"
                    "v_permlane16_swap_b32 v140, v141\n\t"
                    "v_permlane16_swap_b32 v142, v143\n\t"
                    "v_permlane16_swap_b32 v144, v145\n\t"
                    "v_permlane16_swap_b32 v146, v147\n\t"
                    "v_permlane16_swap_b32 v148, v149\n\t"
                    "v_permlane16_swap_b32 v150, v151\n\t"
                    "v_permlane16_swap_b32 v152, v153\n\t"
                    "v_permlane16_swap_b32 v154, v155\n\t"
                    "v_permlane16_swap_b32 v156, v157\n\t"
                    "v_permlane16_swap_b32 v158, v159\n\t"
                    "s_mov_b32 vcc_lo, 0x00ff00ff\n\t"
                    "s_mov_b32 vcc_hi, 0x00ff00ff\n\t"
                    "s_waitcnt vmcnt(7)\n\t"
                    OST("%[t3]")
                    "v_smfmac_f32_16x16x64_bf16 v[96:99], v[208:211], v[160:167], %[ix]\n\t"
                    "v_max3_u32 v100, v100, v208, v209\n\t"
                    "v_max3_u32 v100, v100, v210, v211\n\t"
                    "s_waitcnt vmcnt(6)\n\t"
                    "v_smfmac_f32_16x16x64_bf16 v[96:99], v[212:215], v[168:175], %[ix]\n\t"
                    "v_max3_u32 v100, v100, v212, v213\n\t"
                    "v_max3_u32 v100, v100, v214, v215\n\t"
                    "s_waitcnt vmcnt(5)\n\t"
                    "v_smfmac_f32_16x16x64_bf16 v[96:99], v[216:219], v[176:183], %[ix]\n\t"
                    "v_max3_u32 v100, v100, v216, v217\n\t"
                    "v_max3_u32 v100, v100, v218, v219\n\t"
                    "s_waitcnt vmcnt(4)\n\t"
                    "v_smfmac_f32_16x16x64_bf16 v[96:99], v[220:223], v[184:191], %[ix]\n\t"
                    "v_max3_u32 v100, v100, v220, v221\n\t"
                    "v_max3_u32 v100, v100, v222, v223\n\t"
                    "s_waitcnt vmcnt(3)\n\t"
                    "v_smfmac_f32_16x16x64_bf16 v[96:99], v[224:227], v[128:135], %[ix]\n\t"
                    "v_max3_u32 v100, v100, v224, v225\n\t"
                    "v_max3_u32 v100, v100, v226, v227\n\t"
                    "s_waitcnt vmcnt(2)\n\t"
                    "v_smfmac_f32_16x16x64_bf16 v[96:99], v[228:231], v[136:143], %[ix]\n\t"
                    "s_barrier\n\t"
                    "ds_read_b64 v[102:103], %[sp]\n\t"
                    "ds_read_b64 v[104:105], %[sp] offset:512\n\t"
                    "ds_read_b64 v[106:107], %[sp] offset:1024\n\t"
                    "v_max3_u32 v100, v100, v228, v229\n\t"
                    "v_max3_u32 v100, v100, v230, v231\n\t"
                    "s_waitcnt vmcnt(1)\n\t"
                    "v_smfmac_f32_16x16x64_bf16 v[96:99], v[232:235], v[144:151], %[ix]\n\t"
                    "v_max3_u32 v100, v100, v232, v233\n\t"
                    "v_max3_u32 v100, v100, v234, v235\n\t"
                    "s_waitcnt vmcnt(0)\n\t"
                    "v_smfmac_f32_16x16x64_bf16 v[96:99], v[236:239], v[152:159], %[ix]\n\t"
                    "v_max3_u32 v100, v100, v236, v237\n\t"
                    "v_max3_u32 v100, v100, v238, v239\n\t"
                    OST("%[t2]")
                    "v_cmp_eq_u32 %[bad], -1, v100\n\t"
                    "s_waitcnt lgkmcnt(0)\n\t"
                    "v_add_f32 v102, v102, v104\n\t"
                    "v_add_f32 v103, v103, v105\n\t"
                    "v_add_f32 v102, v102, v106\n\t"
                    "v_add_f32 v103, v103, v107\n\t"
                    "v_add_f32 v96, v96, v102\n\t"
                    "v_add_f32 v97, v97, v103\n\t"
                    "v_add_f32 v96, v96, v98\n\t"
                    "v_add_f32 v97, v97, v99\n\t"
                    "s_cmp_lg_u64 %[bad], 0\n\t"
                    "s_cbranch_scc1 4f\n\t"
                    "2:\n\t"
                    "v_cndmask_b32_dpp v98, v97, v96, vcc row_ror:8 row_mask:0xf bank_mask:0xf\n\t"
                    "s_not_b64 vcc, vcc\n\t"
                    "v_cndmask_b32_dpp v99, v96, v97, vcc row_ror:8 row_mask:0xf bank_mask:0xf\n\t"
                    "v_cvt_pk_bf16_f32 v98, v98, v99\n\t"
                    "v_min_u32 v98, -2, v98\n\t"
                    "buffer_store_dword v98, %[mo], %[rw], %[m0] offen sc1\n\t"
                    "buffer_store_dword v98, %[mo], %[rw], %[m1] offen sc1\n\t"
                    "buffer_store_dword v98, %[mo], %[rw], %[m2] offen sc1\n\t"
                    "buffer_store_dword v98, %[mo], %[rw], %[m3] offen sc1\n\t"
                    OST("%[t4]")
                    "s_branch 5f\n\t"
                    "4:\n\t"
                    "s_sub_i32 %[tr], %[tr], 1\n\t"
                    "s_cmp_lt_i32 %[tr], 1\n\t"
                    "s_cbranch_scc1 2b\n\t"
                    "v_xor_b32 %[po], %[x0], %[po]\n\t"
                    "s_xor_b32 %[x0], %[x0], 0x5000\n\t"
                    "v_xor_b32 %[pm], %[x1], %[pm]\n\t"
                    "s_xor_b32 %[x1], %[x1], 0x5000\n\t"
                    "buffer_load_dwordx4 v[128:131], %[zo], %[rw], %[zb] offen sc1\n\t"
                    "buffer_load_dwordx4 v[132:135], %[zo], %[rw], %[zb] offen offset:1024 sc1\n\t"
                    "buffer_load_dwordx4 v[136:139], %[zo], %[rw], %[zb] offen offset:2048 sc1\n\t"
                    "buffer_load_dwordx4 v[140:143], %[zo], %[rw], %[zb] offen offset:3072 sc1\n\t"
                    "buffer_load_dwordx4 v[144:147], %[zo], %[rw], %[z4] offen sc1\n\t"
                    "buffer_load_dwordx4 v[148:151], %[zo], %[rw], %[z4] offen offset:1024 sc1\n\t"
                    "buffer_load_dwordx4 v[152:155], %[zo], %[rw], %[z4] offen offset:2048 sc1\n\t"
                    "buffer_load_dwordx4 v[156:159], %[zo], %[rw], %[z4] offen offset:3072 sc1\n\t"
                    "buffer_load_dwordx4 v[208:211], %[po], %[rw], %[pb] offen sc1\n\t"
                    "buffer_load_dwordx4 v[212:215], %[po], %[rw], %[pb] offen offset:1024 sc1\n\t"
                    "buffer_load_dwordx4 v[216:219], %[po], %[rw], %[pb] offen offset:2048 sc1\n\t"
                    "buffer_load_dwordx4 v[220:223], %[po], %[rw], %[pb] offen offset:3072 sc1\n\t"
                    "buffer_load_dwordx4 v[224:227], %[pm], %[rw], %[pb] offen sc1\n\t"
                    "buffer_load_dwordx4 v[228:231], %[pm], %[rw], %[pb] offen offset:1024 sc1\n\t"
                    "buffer_load_dwordx4 v[232:235], %[pm], %[rw], %[pb] offen offset:2048 sc1\n\t"
                    "buffer_load_dwordx4 v[236:239], %[pm], %[rw], %[pb] offen offset:3072 sc1\n\t"
                    "v_mov_b64 v[96:97], 0\n\t"
                    "v_mov_b64 v[98:99], 0\n\t"
                    "v_mov_b32 v100, 0\n\t"
                    "s_waitcnt vmcnt(0)\n\t"
                    "v_max3_u32 v101, v129, v131, v133\n\t"
                    "v_max3_u32 v101, v101, v135, v137\n\t"
                    "v_max3_u32 v101, v101, v139, v141\n\t"
                    "v_max3_u32 v101, v101, v143, v145\n\t"
                    "v_max3_u32 v101, v101, v147, v149\n\t"
                    "v_max3_u32 v101, v101, v151, v153\n\t"
                    "v_max3_u32 v101, v101, v155, v157\n\t"
                    "v_max_u32 v101, v101, v159\n\t"
                    "v_mov_b32 v100, v101\n\t"
                    "v_permlane16_swap_b32 v128, v129\n\t"
                    "v_permlane16_swap_b32 v130, v131\n\t"
                    "v_permlane16_swap_b32 v132, v133\n\t"
                    "v_permlane16_swap_b32 v134, v135\n\t"
                    "v_permlane16_swap_b32 v136, v137\n\t"
                    "v_permlane16_swap_b32 v138, v139\n\t"
                    "v_permlane16_swap_b32 v140, v141\n\t"
                    "v_permlane16_swap_b32 v142, v143\n\t"
                    "v_permlane16_swap_b32 v144, v145\n\t"
                    "v_permlane16_swap_b32 v146, v147\n\t"
                    "v_permlane16_swap_b32 v148, v149\n\t"
                    "v_permlane16_swap_b32 v150, v151\n\t"
                    "v_permlane16_swap_b32 v152, v153\n\t"
                    "v_permlane16_swap_b32 v154, v155\n\t"
                    "v_permlane16_swap_b32 v156, v157\n\t"
                    "v_permlane16_swap_b32 v158, v159\n\t"
                    "s_nop 1\n\t"
                    "v_smfmac_f32_16x16x64_bf16 v[96:99], v[208:211], v[160:167], %[ix]\n\t"
                    "v_max3_u32 v100, v100, v208, v209\n\t"
                    "v_max3_u32 v100, v100, v210, v211\n\t"
                    "v_smfmac_f32_16x16x64_bf16 v[96:99], v[212:215], v[168:175], %[ix]\n\t"
                    "v_max3_u32 v100, v100, v212, v213\n\t"
                    "v_max3_u32 v100, v100, v214, v215\n\t"
                    "v_smfmac_f32_16x16x64_bf16 v[96:99], v[216:219], v[176:183], %[ix]\n\t"
                    "v_max3_u32 v100, v100, v216, v217\n\t"
                    "v_max3_u32 v100, v100, v218, v219\n\t"
                    "v_smfmac_f32_16x16x64_bf16 v[96:99], v[220:223], v[184:191], %[ix]\n\t"
                    "v_max3_u32 v100, v100, v220, v221\n\t"
                    "v_max3_u32 v100, v100, v222, v223\n\t"
                    "v_smfmac_f32_16x16x64_bf16 v[96:99], v[224:227], v[128:135], %[ix]\n\t"
                    "v_max3_u32 v100, v100, v224, v225\n\t"
                    "v_max3_u32 v100, v100, v226, v227\n\t"
                    "v_smfmac_f32_16x16x64_bf16 v[96:99], v[228:231], v[136:143], %[ix]\n\t"
                    "v_max3_u32 v100, v100, v228, v229\n\t"
                    "v_max3_u32 v100, v100, v230, v231\n\t"
                    "v_smfmac_f32_16x16x64_bf16 v[96:99], v[232:235], v[144:151], %[ix]\n\t"
                    "v_max3_u32 v100, v100, v232, v233\n\t"
                    "v_max3_u32 v100, v100, v234, v235\n\t"
                    "v_smfmac_f32_16x16x64_bf16 v[96:99], v[236:239], v[152:159], %[ix]\n\t"
                    "v_max3_u32 v100, v100, v236, v237\n\t"
                    "v_max3_u32 v100, v100, v238, v239\n\t"
                    "v_cmp_eq_u32 %[bad], -1, v100\n\t"
                    "s_nop 5\n\t"
                    "v_add_f32 v96, v96, v102\n\t"
                    "v_add_f32 v97, v97, v103\n\t"
                    "v_add_f32 v96, v96, v98\n\t"
                    "v_add_f32 v97, v97, v99\n\t"
                    "s_cmp_lg_u64 %[bad], 0\n\t"
                    "s_cbranch_scc1 4b\n\t"
                    "s_branch 2b\n\t"
                    "5:\n\t"
                    : [tr] "+s"(tries), [po] "+v"(po), [pm] "+v"(pm), [x0] "+s"(x0), [x1] "+s"(x1), [bad] "=&s"(bad), [t0] "=&s"(t0_), [t1] "=&s"(t1_), [t2] "=&s"(t2_), [t3] "=&s"(t3_), [t4] "=&s"(t4_)
                    : [zo] "v"(zoff), [sp] "v"(spa), [ix] "v"(sidx), [rw] "s"(rwin), [zb] "s"(zb), [z4] "s"(zb + 4096u), [pb] "s"(pb), [mo] "v"(moff), [fd] "n"(FDL),
                      [m0] "s"(mb), [m1] "s"(mb + PAIR), [m2] "s"(mb + mc2), [m3] "s"(mb + mc2 + PAIR), "{v[160:167]}"(zf[0]), "{v[168:175]}"(zf[1]), "{v[176:183]}"(zf[2]), "{v[184:191]}"(zf[3])
                    : "v96", "v97", "v98", "v99", "v100", "v101", "v102", "v103", "v104", "v105", "v106", "v107", "v128", "v129", "v130", "v131", "v132", "v133", "v134", "v135", "v136", "v137", "v138", "v139", "v140", "v141", "v142", "v143", "v144", "v145", "v146", "v147", "v148", "v149", "v150", "v151", "v152", "v153", "v154", "v155", "v156", "v157", "v158", "v159", "v208", "v209", "v210", "v211", "v212", "v213", "v214", "v215", "v216", "v217", "v218", "v219", "v220", "v221", "v222", "v223", "v224", "v225", "v226", "v227", "v228", "v229", "v230", "v231", "v232", "v233", "v234", "v235", "v236", "v237", "v238", "v239", "vcc", "scc", "memory");
                budget = tries;
                if (w == 3) *(volatile LAS int*)(smem + 4100) = s;
                __builtin_amdgcn_s_sleep(DPS - 4);
                if (w == 3) {
                    st128(SENT, rwin, ra, W_MEM + (unsigned)(s & 3) * 6u * PAIR, 16);
                    if (lane == 0 && s > 1) __builtin_amdgcn_raw_buffer_store_b32((unsigned)s - 2u, rwin, 0u, W_GO + fo + (unsigned)o * 128u, 16);
                }
                for (;;) {
                    const u32x2 x = *(volatile LAS u32x2*)(smem + 4096);
                    zr = __builtin_amdgcn_readfirstlane(x[0]);
                    if (__builtin_amdgcn_readfirstlane(x[1]) == s) break;
                    __builtin_amdgcn_s_sleep(2);
                }
                zb = zb == W_ZM + 2u * NOWN * PAIR ? W_ZM : zb + NOWN * PAIR;
            }
            const unsigned zq = zb == W_ZM ? W_ZM + 2u * NOWN * PAIR : zb - NOWN * PAIR;
            while (np < 4) {
                while (zr < NSEG - 1) { __builtin_amdgcn_s_sleep(1); zr = __builtin_amdgcn_readfirstlane(*(volatile LAS int*)(smem + 4096)); }
                piece();
            }
            __builtin_amdgcn_s_sleep(DTK);
            for (int e = 0; e < 2; ++e) {
                f32x4 c[2];
                SST(16 + 3 * e);
                for (unsigned spins = 0;;) {
                    asm volatile("" ::: "memory");
                    SST(17 + 3 * e);
                    u32x4 pt[16], ptm[2][2], zm[2];
                    const unsigned pl = W_PL + fo + (unsigned)e * (PTSEG * 2u);
#pragma unroll
                    for (int q = 0; q < 2; ++q) zm[q] = __builtin_amdgcn_raw_buffer_load_b128(rwin, zoff + hg * 2048u + q * 1024u, e ? zb : zq, 16);
#pragma unroll
                    for (int q = 0; q < 16; ++q) pt[q] = __builtin_amdgcn_raw_buffer_load_b128(rwin, ((unsigned)(16 * hg) * 1024u + lane * 16u) | (q & 1 ? 0u : (lane & 32u) << 25), pl + q * 1024u, 16);
#pragma unroll
                    for (int q = 0; q < 4; ++q) ptm[q >> 1][q & 1] = __builtin_amdgcn_raw_buffer_load_b128(rwin, (unsigned)(hg * 2048 + lane * 16), pl + 65536u + (q >> 1) * 8192u + (q & 1) * 1024u, 16);
                    unsigned m = max(smax(zm[0]), smax(zm[1]));
#pragma unroll
                    for (int t = 0; t < 2; ++t) {
                        c[t] = z4;
#pragma unroll
                        for (int q = 0; q < 8; ++q) { c[t] = mfma32(half8(zf[q >> 1], q & 1), pt[2 * q + t], c[t]); m = max(m, smax(pt[2 * q + t])); }
#pragma unroll
                        for (int q = 0; q < 2; ++q) { c[t] = mfma32((u32x4){zm[q][0], zm[q][2], zm[q][1], zm[q][3]}, ptm[t][q], c[t]); m = max(m, smax(ptm[t][q])); }
                    }
                    if (!__any(m == 0xFFFFFFFFu)) break;
                    if (spin_fail(spins, rwin)) break;
                }
                SST(18 + 3 * e);
#pragma unroll
                for (int t = 0; t < 2; ++t) *(f32x4*)(op + e * 8192 + hg * 2048 + (16 * t + fr) * 64 + fq * 16) = c[t];
                BAR();
                VMWAIT();
#pragma unroll
                for (int q = 0; q < 4; ++q) zf[q] = cat8(*(const u32x4*)(smem + 32768 + hg * 8192 + q * 2048 + lane * 16), *(const u32x4*)(smem + 33792 + hg * 8192 + q * 2048 + lane * 16));
                if (e == 0) BAR0();
            }
            if (layer == 0) {
#pragma unroll
                for (int q = 0; q < 4; ++q) st128(SENT, rwin, zoff + hg * 2048u + (q & 1) * 1024u, q < 2 ? zb : zq, 16);
                if (w == 3) st128(SENT, rwin, ra, W_MEM + 3u * 6u * PAIR, 16);
            }
        } else if (w == 4) {
            const unsigned fl = fz_at(lane & 31, o / 10) + (lane & 32) * 128u;
            int zk = -1;
            auto note = [&](int f) INL {
                f = min(f, __builtin_amdgcn_update_dpp(f, f, 0xB1, 0xF, 0xF, false)); f = min(f, __builtin_amdgcn_update_dpp(f, f, 0x4E, 0xF, 0xF, false));
                f = min(f, __builtin_amdgcn_update_dpp(f, f, 0x141, 0xF, 0xF, false)); f = min(f, __builtin_amdgcn_update_dpp(f, f, 0x140, 0xF, 0xF, false));
                zk = seg_done(MXV, min(__builtin_amdgcn_readlane(f, 0), __builtin_amdgcn_readlane(f, 16)), min(__builtin_amdgcn_readlane(f, 32), __builtin_amdgcn_readlane(f, 48)), MXA);
                *(volatile LAS int*)(smem + 4096) = zk;
            };
            for (int s = 0; s < NSEG; ++s) {
                for (unsigned spins = 0; __builtin_expect(zk < s && s > 0, 0);) {
                    if (spins == 0) __builtin_amdgcn_s_sleep(DW4);
                    asm volatile("" ::: "memory");
                    note((int)__builtin_amdgcn_raw_buffer_load_b32(rwin, fl, W_FZ + fo, 16));
                    FC(fc2);
                    if (spin_fail(spins, rwin)) note(NSEG);
                    if (zk < s) __builtin_amdgcn_s_sleep(ZPS);
                }
                if (LKE && s > 0 && zk < (s + 8 >= NSEG ? NSEG - 1 : s + ZLA)) { __builtin_amdgcn_s_sleep(LKE); note((int)__builtin_amdgcn_raw_buffer_load_b32(rwin, fl, W_FZ + fo, 16)); }
                asm volatile("s_waitcnt lgkmcnt(0)\n\ts_barrier\n\ts_barrier" ::: "memory");
                int f = 0;
                const bool look = !LKE && zk < (s + 8 >= NSEG ? NSEG - 1 : s + ZLA);
                if (look) { __builtin_amdgcn_s_sleep(6); f = (int)__builtin_amdgcn_raw_buffer_load_b32(rwin, fl, W_FZ + fo, 16); }
                SB();
                BAR0();
                if (look) note(f);
            }
        } else if (w < 7) {
            const unsigned ooff = (unsigned)((S_ - (7 - w) * SEG + (lane >> 1)) * D_ + ot * 16 + (lane & 1) * 8) * 2u;
            const __amdgpu_buffer_rsrc_t ro = rout();
            for (int s = 0; s < 3 * NSEG - 3 + (w - 5); ++s) BAR0();
            if (w == 5) __builtin_amdgcn_s_sleep(DPS);
            const u32x4 xr = __builtin_amdgcn_raw_buffer_load_b128(rws, ooff, O_X1, 16);
            BAR0();
            BAR0();
            const char* src = op + (w - 5) * 8192 + lane * 32;
            f32x4 x = *(const f32x4*)src, y = *(const f32x4*)(src + 16);
#pragma unroll
            for (int ww = 1; ww < 4; ++ww) { x += *(const f32x4*)(src + ww * 2048); y += *(const f32x4*)(src + ww * 2048 + 16); }
            const u32x4 r = {pack2(lo2f(xr[0]) + bfr(x[0]), hi2f(xr[0]) + bfr(x[1])), pack2(lo2f(xr[1]) + bfr(x[2]), hi2f(xr[1]) + bfr(x[3])),
                             pack2(lo2f(xr[2]) + bfr(y[0]), hi2f(xr[2]) + bfr(y[1])), pack2(lo2f(xr[3]) + bfr(y[2]), hi2f(xr[3]) + bfr(y[3]))};
            st128(nosent4(r), ro, ooff, 0u, 16);
            RST(19 + w, layer, w, w & 3);
            if (w == 5) BAR0();
        } else {
            Tk tk = tk_init();
            __builtin_amdgcn_s_setprio(3);
            for (int s = 0; s < NSEG; ++s) {
                const unsigned ta0 = ck.ta, mm0 = ck.ma;
                unsigned tp = tpp + lt + xs + M1;
                if (s < SAD) {
                    if (s) wait_fine(tpp + CF, ck);
                    tp = M2 + xs + (s ? 0 : PEN1);
                    sched(tk, s);
                    tp = tpp - tp;
                    if (s == 0) { wait_fine(tp - DZ0, ck); *(volatile LAS int*)(smem + 4096) = 0; }
                }
                wait_fine_bar(tp - DZP, ck);
                wait_cyc_bar(cyc_at(ck, sx24(tp + (s + 1 < NSEG ? 0 : ML) - ck.ta)));
                clk_rate(ck, ta0, mm0);
                asm volatile("s_barrier ; 7" ::: "memory");
                if (s >= SAD && s + 1 < NSEG) sched(tk, s);
                RTS();
            }
        }
        __builtin_amdgcn_s_setprio(0);
        SST(23);
    };
    static_assert(NSEG % 3 == 1, "ring");
    auto out_role = [&]() INL {
        LANES();
        const int o = bid - NREC, cg = o % 10, send = NSEG - 2;
        const unsigned ztoff = (unsigned)((4 * cg * H_ + 4 * w) * 64 + lane) * 16u;
        const unsigned ptoff = (unsigned)(8 * w * 64 + lane) * 16u, ptmoff = 65536u + (unsigned)(4 * w * 16 + lane) * 16u;
        const unsigned ptoff0 = ptoff | (lane & 32u) << 25;
        const unsigned ooff = (unsigned)(((w & 1) * 16 + (lane >> 2)) * D_ + tile_of(4 * cg + (w & 2)) * 16 + (lane & 3) * 8) * 2u;
        const unsigned fg = fo + (unsigned)((4 * w + (lane & 3)) * 128), gw = (unsigned)((4 * cg + ((lane >> 2) & 1) * 3) / 10) * 4u;
        const unsigned tfoff = fg + (lane < 16 ? W_FPT + (lane >> 2) * 4u : W_FZ2 + gw), zfoff = fo + W_FZ + fz_at(4 * w + (lane & 3), gw / 4u) + (lane & 32u) * 128u;
        int rdy = -1, go = -1, zrdy = -1;
        auto wait_rdy = [&](int s) INL {
            for (unsigned spins = 0; rdy < s;) {
                asm volatile("" ::: "memory");
                if (s < TAILS || go < s - 2) {
                    go = (int)__builtin_amdgcn_readfirstlane(__builtin_amdgcn_raw_buffer_load_b32(rwin, 0u, W_GO + fo + (unsigned)o * 128u, 16));
                    rdy = max(rdy, go);
                    if (rdy < s) { if (s < TAILS || go < s - 3) __builtin_amdgcn_s_sleep(OSLP); else __builtin_amdgcn_s_sleep(OSLP3); }
                } else {
                    if (!__any((int)__builtin_amdgcn_raw_buffer_load_b32(rwin, tfoff, 0u, 16) < s)) rdy = s;
                    else __builtin_amdgcn_s_sleep(OSLP2);
                }
                if (rdy < s && spin_fail(spins, rwin)) break;
            }
        };
        const __amdgpu_buffer_rsrc_t ro = rout();
        for (int i = 0; i < o / 10; ++i) __builtin_amdgcn_s_sleep(OST0);
        for (int s = o / 10; s < send; s += 4) {
            u32x4 xr, zf[4][4], zx[4], pt[8], ptm[2];
            RT(0);
            for (unsigned spins = 0; zrdy < s;) {
                asm volatile("" ::: "memory");
                int f = (int)__builtin_amdgcn_raw_buffer_load_b32(rwin, zfoff, 0u, 16);
                f = min(f, __builtin_amdgcn_update_dpp(f, f, 0xB1, 0xF, 0xF, false)); f = min(f, __builtin_amdgcn_update_dpp(f, f, 0x4E, 0xF, 0xF, false));
                f = min(f, __builtin_amdgcn_update_dpp(f, f, 0x141, 0xF, 0xF, false));
                zrdy = seg_done(MXV, __builtin_amdgcn_readlane(f, 0), __builtin_amdgcn_readlane(f, 32));
                if (zrdy >= s || spin_fail(spins, rwin)) break;
                __builtin_amdgcn_s_sleep(OSLP);
            }
            if (w >= 4) xr = __builtin_amdgcn_raw_buffer_load_b128(rws, ooff, O_X1 + (unsigned)(s * SEG * D_ * 2), 16);
#pragma unroll
            for (int q = 0; q < 16; ++q) zf[q >> 2][q & 3] = __builtin_amdgcn_raw_buffer_load_b128(rws, ztoff + (q & 3) * 1024u, O_Z + (unsigned)((s - layer) * (int)ZTSEG) * 2u + (q >> 2) * (H_ * 1024u), 16);
            if (s == 61) SST(27);
            wait_rdy(s);
            if (s == 61) SST(28);
            RT(1);
            const unsigned ptb = O_PT + (unsigned)(s * PTSEG) * 2u;
#pragma unroll
            for (int q = 0; q < 8; ++q) pt[q] = __builtin_amdgcn_raw_buffer_load_b128(rws, (q & 1 ? ptoff : ptoff0) + q * 1024u, ptb, 16);
#pragma unroll
            for (int q = 0; q < 2; ++q) ptm[q] = __builtin_amdgcn_raw_buffer_load_b128(rws, ptmoff + q * 8192u, ptb, 16);
#pragma unroll
            for (int q = 0; q < 4; ++q) zx[q] = __builtin_amdgcn_raw_buffer_load_b128(rws, tid * 16u, O_Z2 + (unsigned)(s * NOWN + 4 * cg + q) * 8192u, 16);
            char* op = smem + ((s >> 2) & 1) * 65536;
            f32x4 c[8];
#pragma unroll
            for (int q = 0; q < 4; ++q) {
#pragma unroll
                for (int u = 0; u < 8; ++u) c[u] = mfma32(zf[u >> 1][q], pt[2 * q + (u & 1)], q ? c[u] : z4);
                SB();
            }
#pragma unroll
            for (int u = 0; u < 8; ++u) {
                const int tq = u & 1, t = u >> 1;
                c[u] = mfma32((u32x4){zx[t][0], zx[t][2], zx[t][1], zx[t][3]}, ptm[tq], c[u]);
                *(f32x4*)(op + w * 8192 + (t >> 1) * 4096 + (16 * tq + fr) * 128 + ((((t & 1) * 4 + fq) ^ (fr & 7)) << 4)) = c[u];
            }
            BAR();
            if (s == 61) SST(30);
            if (w >= 4) {
                const int tk = (w & 1) * 16 + (lane >> 2);
                const char* s0 = op + (w & 2) * 2048 + tk * 128 + (((2 * (lane & 3)) ^ (tk & 7)) << 4);
                const char* s1 = op + (w & 2) * 2048 + tk * 128 + (((2 * (lane & 3) + 1) ^ (tk & 7)) << 4);
                f32x4 x = *(const f32x4*)s0, y = *(const f32x4*)s1;
#pragma unroll
                for (int ww = 1; ww < 8; ++ww) { x += *(const f32x4*)(s0 + ww * 8192); y += *(const f32x4*)(s1 + ww * 8192); }
                const u32x4 r = {pack2(lo2f(xr[0]) + bfr(x[0]), hi2f(xr[0]) + bfr(x[1])), pack2(lo2f(xr[1]) + bfr(x[2]), hi2f(xr[1]) + bfr(x[3])),
                                 pack2(lo2f(xr[2]) + bfr(y[0]), hi2f(xr[2]) + bfr(y[1])), pack2(lo2f(xr[3]) + bfr(y[2]), hi2f(xr[3]) + bfr(y[3]))};
                if (layer) st128(r, ro, ooff, (unsigned)(s * SEG * D_ * 2), OAUX);
                else {
                    st128(nosent4(r), ro, ooff, (unsigned)(s * SEG * D_ * 2), 16);
                    if (s < 56) {
                        VMWAIT();
                        __builtin_amdgcn_raw_buffer_store_b32(0u, rwin, LANE0, W_OUT + (unsigned)(s * 64 + 4 * cg + (w & 3)) * 4u, 16);
                    }
                }
            } else __builtin_amdgcn_s_sleep(OSL4);
            if (s < 8) SST(8 + s);
            if (s >= 56) SST(s - 40);
            RT(2);
            RTS();
        }
    };
    auto a1_units = [&](auto late) INL {
        constexpr bool LATE = decltype(late)::v;
        int tid = mytid();
        RowTw tw;
        if (LATE) row_tw(tw, tid);
        for (int u = LATE ? bid - NWRK + 224 : ((bid - NXT) >> 3) + 4 * (bid & 7); u < (LATE ? 256 : 224); u += LATE ? 16 : 24) {
            if (!LATE) tid = mytid();
            if (w == 4) {
                for (unsigned spins = 0;;) {
                    asm volatile("" ::: "memory");
                    if (LATE) {
                        const int go = __builtin_amdgcn_readfirstlane(__builtin_amdgcn_raw_buffer_load_b32(rwin, 0u, W_GO + (unsigned)(u & 31) * 128u, 16)) - min((u >> 2) - 2, NSEG - 4);
                        if (go >= 0) break;
                        if (go < -3) __builtin_amdgcn_s_sleep(127);
                    } else if (!__any(__builtin_amdgcn_raw_buffer_load_b32(rwin, min(fresh_lane(), (unsigned)NOWN - 1u) * 4u, W_OUT + (unsigned)(u >> 2) * 256u, 16) != 0u)) break;
                    if (spin_fail(spins, rwin)) break;
                    if (u < 200) __builtin_amdgcn_s_sleep(WSLP); else __builtin_amdgcn_s_sleep(16);
                }
            }
            BAR();
            SST(24);
            a1_unit<4, LATE>(mkrsrc(ws + OFF_X2, S_ * D_ * 2), u * 8, rws, rwin, tw, tid, fs);
            if (!LATE) VMWAIT();
            FST(8);
            BAR0();
            FST(9);
            if (!LATE && w == 0) __builtin_amdgcn_raw_buffer_store_b64(__builtin_bit_cast(u32x2, tag + 9ull), rwin, LANE0, W_A1 + (unsigned)u * 128u, 16);
            SST(25);
            FSW(7);
            if (!LATE && u < 176) __builtin_amdgcn_s_sleep(A1S);
        }
    };
    u32x8 wt[2][10];
#define PINW() asm volatile("" : "+{v[0:7]}"(wt[0][0]), "+{v[8:15]}"(wt[1][0]), "+{v[16:23]}"(wt[0][1]), "+{v[24:31]}"(wt[1][1]), "+{v[32:39]}"(wt[0][2]), "+{v[40:47]}"(wt[1][2]), "+{v[48:55]}"(wt[0][3]), "+{v[56:63]}"(wt[1][3]), "+{v[64:71]}"(wt[0][4]), "+{v[72:79]}"(wt[1][4]), "+{v[80:87]}"(wt[0][5]), "+{v[88:95]}"(wt[1][5]), "+{v[96:103]}"(wt[0][6]), "+{v[104:111]}"(wt[1][6]), "+{v[112:119]}"(wt[0][7]), "+{v[120:127]}"(wt[1][7]), "+{v[128:135]}"(wt[0][8]), "+{v[136:143]}"(wt[1][8]), "+{v[144:151]}"(wt[0][9]), "+{v[152:159]}"(wt[1][9]))
#define KILLW() asm volatile("" : "={v[0:7]}"(wt[0][0]), "={v[8:15]}"(wt[1][0]), "={v[16:23]}"(wt[0][1]), "={v[24:31]}"(wt[1][1]), "={v[32:39]}"(wt[0][2]), "={v[40:47]}"(wt[1][2]), "={v[48:55]}"(wt[0][3]), "={v[56:63]}"(wt[1][3]), "={v[64:71]}"(wt[0][4]), "={v[72:79]}"(wt[1][4]), "={v[80:87]}"(wt[0][5]), "={v[88:95]}"(wt[1][5]), "={v[96:103]}"(wt[0][6]), "={v[104:111]}"(wt[1][6]), "={v[112:119]}"(wt[0][7]), "={v[120:127]}"(wt[1][7]), "={v[128:135]}"(wt[0][8]), "={v[136:143]}"(wt[1][8]), "={v[144:151]}"(wt[0][9]), "={v[152:159]}"(wt[1][9]))
    if (blockIdx.x < 64) {
        for (;;) {
            load_qv(wt, bid & 31, bid >= 32, [&]() INL {
                rot0 = __builtin_amdgcn_raw_buffer_load_b64(rws, 0u, 128u, 16);
                if (w == 0) wait_tags(rwin, 0, 1, 64u, tag + 8ull, true);
                if (w == 7) clk_init(ck);
                if (w == 1) { const unsigned x = __builtin_amdgcn_raw_buffer_load_b32(mkrsrc(arg(A_MEM0), NM * D_ * 2), fresh_lane() * 160u, 0, 0); asm volatile("" :: "v"(x)); }
            });
            const unsigned x = get_rot();
            if (__builtin_expect(x == vx, 1)) break;
            set_role(x);
        }
        SST(1);
        PINW();
    } else {
        if (bid >= NWRK) probe();
        KILLW();
    }
    for (; layer < 2; ++layer) {
        new_layer();
        PINW();
        BAR();
        if (qx || (layer ? bid < 96 : (bid >= 32 && bid < 64) || (bid >= 104 && bid < NREC) || xq)) {
            const int ci = qx ? bid - 80 : bid < 96 ? bid : bid < NREC ? bid - 104 : 64 + (bid - NREC) / 10 * 8 + (bid - NREC) % 10;
            SST(26);
            a2_unit(ci >= 100 ? ci - 100 : ci + 60, layer, rws, rwin, mytid(), [&](int k) INL {
                if (k == 2) SST(28);
                else if (w == 0) {
                    if (layer && bid >= 64) for (unsigned spins = 0;;) {
                        asm volatile("" ::: "memory");
                        if (__all(__builtin_bit_cast(u64, __builtin_amdgcn_raw_buffer_load_b64(rwin, 0u, W_A1 + (unsigned)(200 + (bid & 15)) * 128u, 16)) == tag + 9ull) || spin_fail(spins, rwin)) break;
                        __builtin_amdgcn_s_sleep(100);
                    }
                    if (layer && bid >= 64) rec_end(GATE);
                    wait_tags(rwin, 0, 4, layer ? 32u : 64u, tag + 8ull + layer, !layer);
                    SST(27);
                }
            }, fs);
            SST(29);
            VMWAIT();
            FST(8);
            BAR0();
            FST(9);
            if (w == 0) {
                __builtin_amdgcn_raw_buffer_store_b32(0u, rwin, LANE0, W_COL + fo + (unsigned)ci * 128u, 16);
                if (bid == NQT && layer == 1) __builtin_amdgcn_raw_buffer_store_b64(__builtin_bit_cast(u32x2, tag), rws, LANE0, 0u, 16);
            }
            SST(30);
            FSW(1 + layer);
        }
#define NEXTB() do { PINW(); asm volatile("" : "+s"(bid), "+s"(w)); } while (0)
        NEXTB();
        if (bid < 32) { fresh_rsrc(); qk_role(wt); PINW(); }
        NEXTB();
        const int hh = bid < 96 ? bid & 31 : bid < NQT ? (bid - NREC) / 10 * 8 + (bid - NREC) % 10 : (bid - NQT) & 31;
        if (((qx || (bid >= 32 && bid < 64)) && layer == 0) || xq) { if (layer) { if (w == 0) rec_end(GATX); BAR0(); } load_qv(wt, hh, xt || bid < 64, []() {}); SST(1); PINW(); }
        NEXTB();
        if (bid >= 32 && bid < 64) { if (layer == 0) near_init(); fresh_rsrc(); vo_role(wt); PINW(); }
        NEXTB();
        if ((bid >= 64 && bid < 96) || xt) {
            fresh_rsrc();
            vt_role(wt, hh, xt ? MXV : ~MXV);
            if (xt) {
                const unsigned g0 = (unsigned)(hh * 512 + mytid()) * 16u;
                if (layer == 0) for (unsigned i = 0; i < 13; ++i) {
                    st128(SENT, i < 7 ? mkrsrc(ws + O_ARM, O_ARM1 - O_ARM) : i < 12 ? mkrsrc(ws + O_Z - ZTSEG * 2, ZTSEG * 2) : mkrsrc(arg(1) + (W_ARM - W_LAYER) / 2, W_LAYER), g0, (i < 7 ? i : i < 12 ? i - 7 : 0) * 262144u, 16);
                    __builtin_amdgcn_s_sleep(40);
                }
                static_assert(O_ARM + 7u * 262144u >= O_ARM1 && ZTSEG * 2 <= 5u * 262144u && W_LAYER <= 262144u && !(MXV >> 40), "arming");
                VMWAIT();
                BAR0();
                if (w >= 4) __builtin_amdgcn_raw_buffer_store_b32(NSEG - 1, rwin, LANE0, W_FZ + fo + fz_at(hh, w - 4) + 4096u, 16);
                if (layer == 0 && (bid & 7) < 6) a1_units(IC<0>());
            }
            PINW();
        }
        NEXTB();
        if ((qx && !xt) || xq) { fresh_rsrc(); qt_role(wt, hh, IC<1>()); PINW(); }
        NEXTB();
        if (bid >= 96 && !qx) {
            const int fi = xq ? 256 : bid >= NWRK ? 255 - bid : bid >= NREC ? 8 + (bid - NREC) / 10 * 2 + (bid - NREC) % 10 : bid - 72, tid = mytid();
            RowTw tw;
            row_tw(tw, tid);
            if (layer == 0 && fi >= 16 && fi < 32) { if (w == 0) wait_tags(rwin, 0, 1, 64u, tag + 8ull, true); BAR0(); }
            if (layer && fi < 64) {
                for (unsigned spins = 0;;) {
                    asm volatile("" ::: "memory");
                    if (__all(__builtin_bit_cast(u64, __builtin_amdgcn_raw_buffer_load_b64(rwin, 0u, W_A1 + (unsigned)fi * 128u, 16)) == tag + 9ull) || spin_fail(spins, rwin)) break;
                    __builtin_amdgcn_s_sleep(8);
                }
            }
            bool cols = false;
            for (int u = fi; u < 256; u += 16) {
                a3_unit(mkrsrc(layer ? (const u16*)(ws + OFF_X2) : arg(A_X), S_ * D_ * 2), u * 8, layer, rws, rwin, tw, tid, [&]() INL {
                    if (cols) return;
                    cols = true;
                    if (w == 7) {
                        const unsigned l = fresh_lane();
                        unsigned o[3] = {l * 128u, l * 128u + 8192u, l < 32u ? l * 128u + 16384u : 0x40000000u};
                        for (unsigned spins = 0;;) {
                            asm volatile("" ::: "memory");
                            unsigned v[3];
#pragma unroll
                            for (int g = 0; g < 3; ++g) v[g] = __builtin_amdgcn_raw_buffer_load_b32(rwin, o[g], W_COL + fo, 16);
#pragma unroll
                            for (int g = 0; g < 3; ++g) if (v[g] == 0u) o[g] = 0x40000000u;
                            if (!__any((v[0] | v[1] | v[2]) != 0u) || spin_fail(spins, rwin)) break;
                            __builtin_amdgcn_s_sleep(CSLP);
                        }
                    }
                    BAR0();
                    SST(6);
                }, fs);
                VMWAIT();
                FST(8);
                BAR0();
                FST(9);
                if (w == 0) __builtin_amdgcn_raw_buffer_store_b32(0u, rwin, LANE0, W_H + fo + (unsigned)((u >> 2) * 128 + (u & 3) * 4), 16);
                if (u < 64) SST(7);
                FSW(u < 64 ? 3 + layer : 5);
                if (fi >= 16) break;
                if (u < 64) u += 48;
            }
            if (bid < NREC) { fresh_rsrc(); own_role(); }
            else if (bid < NQT) out_role();
            else if (layer == 0) a1_units(IC<1>());
            KILLW();
        }
        NEXTB();
        SST(31);
    }
}

constexpr int L_DBG = 0;
extern "C" size_t dbg_offset(int i) {
    const size_t o[] = {OFF_WIN, W_TMO, W_NEAR, OFF_F, OFF_X1, OFF_X2, OFF_H, OFF_WFT};
    return o[i];
}

extern "C" void kernel_launch(void* const* d_in, const int* in_sizes, int n_in, void* d_out, int out_size, void* d_ws, size_t ws_size,
                              hipStream_t stream) {
    char* ws = (char*)d_ws;
    hipFuncSetAttribute((const void*)mega_kernel, hipFuncAttributeMaxDynamicSharedMemorySize, L_END + L_DBG);
    char* win = (char*)(((uintptr_t)(ws + OFF_WIN) + 0x1FFFFFu) & ~(uintptr_t)0x1FFFFFu);
    Args a{ws, win, (const u16*)d_in[0], (const u16*)d_in[1], (const u16*)d_in[2], (const u16*)d_in[4], (const u16*)d_in[5], (const u16*)d_in[6], (const u16*)d_in[7], (const u16*)d_in[8], (u16*)d_out};
    mega_kernel<<<256, 512, L_END + L_DBG, stream>>>(a);
}
```

```cpp
#include <hip/hip_runtime.h>
#include <hip/hip_bf16.h>
#include <stdint.h>

typedef unsigned short u16;
typedef unsigned long long u64;
typedef __attribute__((ext_vector_type(8))) __bf16 bf16x8;
typedef __attribute__((ext_vector_type(4))) short s16x4;
typedef __attribute__((ext_vector_type(4))) float f32x4;
typedef __attribute__((ext_vector_type(8))) float f32x8;
typedef __attribute__((ext_vector_type(16))) float f32x16;
typedef __attribute__((ext_vector_type(4))) unsigned u32x4;
typedef __attribute__((ext_vector_type(2))) unsigned u32x2;
typedef __attribute__((ext_vector_type(8))) unsigned u32x8;
typedef __attribute__((ext_vector_type(16))) __bf16 bf16x16;
typedef __attribute__((ext_vector_type(2))) unsigned short u16x2;
#define LAS __attribute__((address_space(3)))
#define INL __attribute__((always_inline))

constexpr int S_ = 2048, D_ = 640, DF = 321, H_ = 32, DH = 128, HD = 4096, NM = 8, SEG = 32, NSEG = 64;
constexpr int NOWN = 40;
constexpr int NCOL = 160;
constexpr size_t ZTSEG = (size_t)NOWN * H_ * 512;
constexpr unsigned PAIR = 16384, HALF = 8192;
constexpr unsigned W_MEM = 0, W_T = W_MEM + 24 * PAIR, W_P = 33 * PAIR, W_PROBE = 74 * PAIR, W_GO = W_PROBE + 2 * PAIR, W_FZ = W_GO + 5120, W_FPT = W_FZ + 8192, W_FZ2 = W_FPT + 4096,
                   W_FQ = W_FZ2 + 4096, W_H = W_FQ + 8192, W_COL = W_H + HALF, W_PL = W_COL + 20480, W_LAYER = W_PL + 2 * 81920 - W_GO, W_OUT = 32 * PAIR, W_MISC = W_OUT + 14336,
                   W_ARM = W_GO + 2 * W_LAYER, W_A1 = 106 * PAIR, W_ZM = 2u * 1024 * 1024, W_END = W_ZM + 120 * PAIR;
static_assert(W_ARM <= W_A1 && W_ARM % 1024 == 0, "window");
__device__ __forceinline__ unsigned p_slot(int s) { return W_P + (unsigned)((s & 3) * 10 + (((s & 3) + 1) >> 2)) * PAIR; }
constexpr unsigned W_TMO = W_MISC, W_NEAR = W_MISC + 128;
constexpr int PTSEG = 32768 + 8192;

#define FPAD 256
constexpr unsigned FST_ = S_ * 16 + FPAD, GST = 4096 + FPAD, WST = S_ * 8 + FPAD;
constexpr size_t al(size_t x) { return (x + 255) & ~(size_t)255; }
constexpr size_t OFF_CTL  = 0;
constexpr size_t OFF_WFT  = 4096 + 4 * 1024 * 1024;
constexpr size_t OFF_F    = OFF_WFT + al((size_t)(NCOL + 2) * WST);
constexpr size_t OFF_X1   = OFF_F + al((size_t)NCOL * FST_);
constexpr size_t OFF_X2   = OFF_X1 + al((size_t)S_ * D_ * 2);
constexpr size_t OFF_FL   = OFF_X2 + al((size_t)S_ * D_ * 2);
constexpr int NHF = 5;
constexpr size_t OFF_H    = OFF_FL + 3 * (size_t)NCOL * GST + NHF * 40960;
constexpr size_t OFF_Z    = OFF_H + al((size_t)S_ * D_ * 2) + ZTSEG * 2;
constexpr size_t OFF_Z2   = OFF_Z + al(ZTSEG * NSEG * 2);
constexpr size_t OFF_PT   = OFF_Z2 + al((size_t)NSEG * NOWN * 8192);
constexpr size_t OFF_QK   = OFF_PT + al((size_t)NSEG * PTSEG * 2);
constexpr size_t OFF_WIN  = OFF_QK + (size_t)NSEG * H_ * 16384;
constexpr size_t OFF_END  = OFF_WIN + (size_t)6 * 1024 * 1024;
static_assert(OFF_END <= (size_t)256 * 1024 * 1024, "workspace");
constexpr unsigned O_FL = (unsigned)OFF_FL, O_ARM = O_FL - 256 * D_ * 2, O_ARM1 = O_FL + 2 * NCOL * GST, O_WFT = (unsigned)OFF_WFT, O_F = (unsigned)OFF_F, O_H = (unsigned)OFF_H, O_X1 = (unsigned)OFF_X1, O_Z = (unsigned)OFF_Z, O_Z2 = (unsigned)OFF_Z2, O_PT = (unsigned)OFF_PT, O_QK = (unsigned)OFF_QK;

constexpr double PI_ = 3.14159265358979323846;
constexpr double tay(double x, int s) {
    double t = s ? x : 1.0, r = t;
    for (int i = s + 1; i < 18; i += 2) { t *= -x * x / (double)(i * (i + 1)); r += t; }
    return r;
}
struct Cs { float c, s; };
constexpr Cs cis(int m, int n) {
    m %= n;
    const int q = (m * 8 / n + 1) / 2;
    const double x = 2.0 * PI_ * (double)(4 * m - q * n) / (double)(4 * n), c = tay(x, 0), s = tay(x, 1);
    return (q & 3) == 0 ? Cs{(float)c, (float)-s} : (q & 3) == 1 ? Cs{(float)-s, (float)-c} : (q & 3) == 2 ? Cs{(float)-c, (float)s} : Cs{(float)s, (float)c};
}
template <int NI> struct alignas(16) Tw7 { Cs v[4][NI][2]; };
template <int NI, int N, int KIND> constexpr Tw7<NI> mk_tw() {
    Tw7<NI> r{};
    for (int i = 0; i < NI; ++i)
        for (int q = 1; q <= 8; ++q) r.v[(q - 1) >> 1][i][(q - 1) & 1] = cis((KIND == 0 ? i : KIND == 1 ? 8 * (i >> 3) : (i >> 3) + 10 * (i & 7)) * q, N);
    return r;
}
struct TwCol { Tw7<256> a; Tw7<32> b; Tw7<4> c; };
static __device__ const TwCol TWC = {mk_tw<256, 2048, 0>(), mk_tw<32, 256, 0>(), mk_tw<4, 32, 0>()};
static __device__ const Tw7<80> TWR2 = mk_tw<80, 640, 1>();
static __device__ const Tw7<80> TWR3 = mk_tw<80, 640, 2>();

#define st128(v, r, vo, so, aux) do { u32x4 d_ = (v); asm volatile("" : "+v"(d_)); __builtin_amdgcn_raw_buffer_store_b128(d_, r, vo, so, aux); asm volatile("s_nop 1" :: "v"(d_)); } while (0)
__device__ __forceinline__ float bf2f(u16 v) { return __uint_as_float((unsigned)v << 16); }
__device__ __forceinline__ u16 f2bf(float f) { __bf16 b = (__bf16)f; return __builtin_bit_cast(u16, b); }
__device__ __forceinline__ float bfr(float f) { return (float)(__bf16)f; }
typedef __attribute__((ext_vector_type(2))) float f32x2;
typedef __attribute__((ext_vector_type(2))) __bf16 bf16x2;
__device__ __forceinline__ unsigned pack2(float a, float b) { return __builtin_bit_cast(unsigned, __builtin_convertvector((f32x2){a, b}, bf16x2)); }
__device__ __forceinline__ float lo2f(unsigned v) { return __uint_as_float(v << 16); }
__device__ __forceinline__ float hi2f(unsigned v) { return __uint_as_float(v & 0xFFFF0000u); }
__device__ __forceinline__ float fmx(float a, float b) { return __builtin_elementwise_maximum(a, b); }
__device__ __forceinline__ f32x4 mfma32(u32x4 a, u32x4 b, f32x4 c) {
    return __builtin_amdgcn_mfma_f32_16x16x32_bf16(__builtin_bit_cast(bf16x8, a), __builtin_bit_cast(bf16x8, b), c, 0, 0, 0);
}
__device__ __forceinline__ f32x4 mfma16(u32x2 a, u32x2 b, f32x4 c) {
    return __builtin_amdgcn_mfma_f32_16x16x16bf16_1k(__builtin_bit_cast(s16x4, a), __builtin_bit_cast(s16x4, b), c, 0, 0, 0);
}
__device__ __forceinline__ f32x4 smfmac(u32x4 a, u32x8 b, f32x4 c, int idx) {
    return __builtin_amdgcn_smfmac_f32_16x16x64_bf16(__builtin_bit_cast(bf16x8, a), __builtin_bit_cast(bf16x16, b), c, idx, 0, 0);
}
__device__ __forceinline__ int pi16(int k) { return ((k & 3) << 2) | ((k >> 1) & 2) | (k >> 3); }
__device__ __forceinline__ float addswap32(float a, float b) {
    auto r = __builtin_amdgcn_permlane32_swap(__float_as_uint(a), __float_as_uint(b), false, false);
    return __uint_as_float(r[0]) + __uint_as_float(r[1]);
}
__device__ __forceinline__ float xmax3216(float v) {
    auto r = __builtin_amdgcn_permlane32_swap(__float_as_uint(v), __float_as_uint(v), false, false);
    v = fmx(__uint_as_float(r[0]), __uint_as_float(r[1]));
    auto q = __builtin_amdgcn_permlane16_swap(__float_as_uint(v), __float_as_uint(v), false, false);
    return fmx(__uint_as_float(q[0]), __uint_as_float(q[1]));
}
__device__ __forceinline__ float xsum3216(float v) {
    auto r = __builtin_amdgcn_permlane32_swap(__float_as_uint(v), __float_as_uint(v), false, false);
    v = __uint_as_float(r[0]) + __uint_as_float(r[1]);
    auto q = __builtin_amdgcn_permlane16_swap(__float_as_uint(v), __float_as_uint(v), false, false);
    return __uint_as_float(q[0]) + __uint_as_float(q[1]);
}
template <int CTRL> __device__ __forceinline__ float dppf(float v) { return __uint_as_float((unsigned)__builtin_amdgcn_update_dpp(0, (int)__float_as_uint(v), CTRL, 0xF, 0xF, false)); }
__device__ __forceinline__ float wsum(float v) {
    v += dppf<0xB1>(v); v += dppf<0x4E>(v); v += dppf<0x141>(v); v += dppf<0x140>(v);
    return xsum3216(v);
}
__device__ __forceinline__ unsigned ror8(unsigned v) { return (unsigned)__builtin_amdgcn_update_dpp(0, (int)v, 0x128, 0xF, 0xF, false); }
__device__ __forceinline__ float ror8f(float v) { return __uint_as_float(ror8(__float_as_uint(v))); }
__device__ __forceinline__ unsigned nosent(unsigned v) {
    u16x2 lim = {0xFFFE, 0xFFFE};
    return __builtin_bit_cast(unsigned, __builtin_elementwise_min(__builtin_bit_cast(u16x2, v), lim));
}
__device__ __forceinline__ unsigned pkmax(unsigned a, unsigned b) {
    return __builtin_bit_cast(unsigned, __builtin_elementwise_max(__builtin_bit_cast(u16x2, a), __builtin_bit_cast(u16x2, b)));
}
__device__ __forceinline__ bool has_sent(u32x4 v) {
    unsigned m = pkmax(pkmax(v.x, v.y), pkmax(v.z, v.w));
    return (m & 0xFFFFu) == 0xFFFFu || (m >> 16) == 0xFFFFu;
}

__device__ __forceinline__ unsigned nosent1(unsigned v) { return min(v, 0xFFFFFFFEu); }
__device__ __forceinline__ unsigned smax(u32x4 v) { return max(v[1], v[3]); }
__device__ __forceinline__ u32x4 sent4() { unsigned x; asm volatile("v_mov_b32 %0, -1" : "=v"(x)); return (u32x4){x, x, x, x}; }
#define SENT sent4()
__device__ __forceinline__ f32x4 zero4() { f32x2 x, y; asm volatile("v_mov_b64 %0, 0\n\tv_mov_b64 %1, 0" : "=v"(x), "=v"(y)); return (f32x4){x[0], x[1], y[0], y[1]}; }
__device__ __forceinline__ u32x4 nosent4(u32x4 v) { return (u32x4){nosent1(v[0]), nosent1(v[1]), nosent1(v[2]), nosent1(v[3])}; }
#define SPIN_CAP 400000u
__device__ __forceinline__ void set_tmo(__amdgpu_buffer_rsrc_t rwin) { __builtin_amdgcn_raw_buffer_store_b32(0u, rwin, 0u, W_TMO, 16); }
__device__ __forceinline__ bool spin_fail(unsigned& spins, __amdgpu_buffer_rsrc_t rwin) {
    if (++spins > SPIN_CAP) { set_tmo(rwin); return true; }
    return (spins & 1023u) == 0u && __builtin_amdgcn_readfirstlane(__builtin_amdgcn_raw_buffer_load_b32(rwin, 0u, W_TMO, 16)) == 0u;
}
#define GATE 2
#define GATX 2
__device__ __forceinline__ void tell_at(__amdgpu_buffer_rsrc_t rwin, unsigned so, unsigned d) {
    __builtin_amdgcn_raw_buffer_store_b32(((unsigned)__builtin_amdgcn_s_memrealtime() + d) & 0xFFFFFFu, rwin, 0u, so, 16);
}
template <int N> __device__ __forceinline__ void wait_told(__amdgpu_buffer_rsrc_t rwin, const unsigned (&vo)[N], unsigned so, int slp) {
    unsigned v[N];
#pragma unroll
    for (int g = 0; g < N; ++g) v[g] = ~0u;
    for (unsigned spins = 0;;) {
        asm volatile("" ::: "memory");
        unsigned x[N], m = 0;
#pragma unroll
        for (int g = 0; g < N; ++g) x[g] = __builtin_amdgcn_raw_buffer_load_b32(rwin, v[g] == ~0u ? vo[g] : 0x40000000u, so, 16);
#pragma unroll
        for (int g = 0; g < N; ++g) { if (v[g] == ~0u) v[g] = x[g]; m = max(m, v[g]); }
        if (!__any(m == ~0u) || spin_fail(spins, rwin)) break;
        if (slp) __builtin_amdgcn_s_sleep(8); else __builtin_amdgcn_s_sleep(3);
    }
    for (int i = 0; i < 400; ++i) {
        const unsigned t = (unsigned)__builtin_amdgcn_s_memrealtime();
        bool e = false;
#pragma unroll
        for (int g = 0; g < N; ++g) e |= ((v[g] - t) << 8) - 1u < 0x7FFFFFFFu;
        if (!__any(e)) break;
    }
}
#define TG 250
#define TH 155
#define TH0 185
#define DVT0 950
#define A2P 0
#define TWG 1
#define TRK 4
#define PS1 3
#define PS2 3
#define PS3 2

typedef f32x2 c2;
__device__ __forceinline__ c2 swp(c2 a) { return __builtin_shufflevector(a, a, 1, 0); }
__device__ __forceinline__ c2 fm(c2 a, c2 b, c2 c) { return __builtin_elementwise_fma(a, b, c); }
__device__ __forceinline__ c2 bc(float x) { return (c2){x, x}; }
__device__ __forceinline__ c2 sgn() { c2 s = {-1.f, 1.f}; asm("" : "+s"(s)); return s; }
template <int SG> __device__ __forceinline__ c2 addi(c2 a, c2 b) { return fm(swp(b), SG > 0 ? sgn() : swp(sgn()), a); }
template <int SG> __device__ __forceinline__ c2 cfin(c2 a, c2 w, c2 t) {
    c2 d;
    if (SG < 0) asm("v_pk_fma_f32 %0, %1, %2, %3 op_sel:[1,1,0] op_sel_hi:[0,1,1] neg_lo:[1,0,0]" : "=v"(d) : "v"(a), "v"(w), "v"(t));
    else asm("v_pk_fma_f32 %0, %1, %2, %3 op_sel:[1,1,0] op_sel_hi:[0,1,1] neg_hi:[1,0,0]" : "=v"(d) : "v"(a), "v"(w), "v"(t));
    return d;
}
struct Tw { f32x4 v[4]; };
template <int NI> __device__ __forceinline__ void ldtw(Tw& t, const Tw7<NI>& tab, int i) {
#pragma unroll
    for (int j = 0; j < 4; ++j) t.v[j] = *(const f32x4*)tab.v[j][i];
}
template <int NI> __device__ __forceinline__ void ldtw(Tw& t, const LAS f32x4* p) {
#pragma unroll
    for (int j = 0; j < 4; ++j) t.v[j] = p[j * NI];
}
template <int SG> __device__ __forceinline__ void twid(c2 (&u)[8], const Tw& t) {
    c2 w[8], m[8];
#pragma unroll
    for (int r = 1; r < 8; ++r) {
        const f32x4 x = t.v[(r - 1) >> 1];
        w[r] = r & 1 ? (c2){x[0], x[1]} : (c2){x[2], x[3]};
        m[r] = u[r] * bc(w[r].x);
    }
    if (TWG) asm("" : "+v"(m[1]), "+v"(m[2]), "+v"(m[3]), "+v"(m[4]), "+v"(m[5]), "+v"(m[6]), "+v"(m[7]));
#pragma unroll
    for (int r = 1; r < 8; ++r) u[r] = cfin<SG>(u[r], w[r], m[r]);
}
template <int SG> __device__ __forceinline__ void dft4(c2& a, c2& b, c2& c, c2& d) {
    const c2 t0 = a + c, t1 = a - c, t2 = b + d, t3 = b - d;
    a = t0 + t2; c = t0 - t2; b = addi<SG>(t1, t3); d = addi<-SG>(t1, t3);
}
template <int SG> __device__ __forceinline__ void dft8(c2 (&u)[8]) {
    c2 e0 = u[0], e1 = u[2], e2 = u[4], e3 = u[6], o0 = u[1], o1 = u[3], o2 = u[5], o3 = u[7];
    dft4<SG>(e0, e1, e2, e3); dft4<SG>(o0, o1, o2, o3);
    const c2 r = bc(0.70710678118654752440f);
    o1 = addi<SG>(o1, o1); o3 = addi<-SG>(o3, o3);
    u[0] = e0 + o0; u[4] = e0 - o0;
    u[1] = fm(o1, r, e1); u[5] = fm(o1, -r, e1);
    u[2] = addi<SG>(e2, o2); u[6] = addi<-SG>(e2, o2);
    u[3] = fm(o3, -r, e3); u[7] = fm(o3, r, e3);
}
template <int SG> __device__ __forceinline__ void dft5(c2& x0, c2& x1, c2& x2, c2& x3, c2& x4) {
    const c2 c1 = bc(0.30901699437494742410f), c2_ = bc(-0.80901699437494742410f), s1 = bc(0.95105651629515357212f), s2 = bc(0.58778525229247312917f);
    const c2 t1 = x1 + x4, t2 = x2 + x3, t3 = x1 - x4, t4 = x2 - x3;
    const c2 a1 = fm(c2_, t2, fm(c1, t1, x0)), a2 = fm(c1, t2, fm(c2_, t1, x0));
    const c2 b1 = fm(s2, t4, s1 * t3), b2 = fm(-s1, t4, s2 * t3);
    x0 = x0 + t1 + t2;
    x1 = addi<SG>(a1, b1); x4 = addi<-SG>(a1, b1); x2 = addi<SG>(a2, b2); x3 = addi<-SG>(a2, b2);
}
template <int SG> __device__ __forceinline__ void dft10(c2 (&u)[10]) {
    c2 e0 = u[0], e1 = u[2], e2 = u[4], e3 = u[6], e4 = u[8], o0 = u[5], o1 = u[7], o2 = u[9], o3 = u[1], o4 = u[3];
    dft5<SG>(e0, e1, e2, e3, e4); dft5<SG>(o0, o1, o2, o3, o4);
    u[0] = e0 + o0; u[5] = e0 - o0; u[6] = e1 + o1; u[1] = e1 - o1; u[2] = e2 + o2; u[7] = e2 - o2; u[8] = e3 + o3; u[3] = e3 - o3; u[4] = e4 + o4; u[9] = e4 - o4;
}
__device__ __forceinline__ void swap32(c2& v) { auto r = __builtin_amdgcn_permlane32_swap(__float_as_uint(v.x), __float_as_uint(v.y), false, false); v = (c2){__uint_as_float(r[0]), __uint_as_float(r[1])}; }
template <int B> __device__ __forceinline__ void swapl(c2& a, c2& b) {
#pragma unroll
    for (int i = 0; i < 2; ++i) {
        auto r = B == 5 ? __builtin_amdgcn_permlane32_swap(__float_as_uint(a[i]), __float_as_uint(b[i]), false, false) : __builtin_amdgcn_permlane16_swap(__float_as_uint(a[i]), __float_as_uint(b[i]), false, false);
        a[i] = __uint_as_float(r[0]); b[i] = __uint_as_float(r[1]);
    }
}
__device__ __forceinline__ c2 unpk(unsigned v) { return (c2){lo2f(v), hi2f(v)}; }
__device__ __forceinline__ float hsum32(c2 s) {
    float v = s.x + s.y;
    v += dppf<0xB1>(v); v += dppf<0x4E>(v); v += dppf<0x141>(v); v += dppf<0x140>(v);
    auto q = __builtin_amdgcn_permlane16_swap(__float_as_uint(v), __float_as_uint(v), false, false);
    return __uint_as_float(q[0]) + __uint_as_float(q[1]);
}
__device__ __forceinline__ void ln_row(const c2 (&v)[10], unsigned (&o)[10]) {
    const c2 s = ((v[0] + v[1]) + (v[2] + v[3])) + ((v[4] + v[5]) + (v[6] + v[7])) + (v[8] + v[9]);
    const float mean = hsum32(s) * (1.f / 640.f);
    c2 q0 = bc(0.f), q1 = bc(0.f);
#pragma unroll
    for (int r = 0; r < 10; r += 2) { const c2 d = v[r] - bc(mean), e = v[r + 1] - bc(mean); q0 = fm(d, d, q0); q1 = fm(e, e, q1); }
    const float mb = bfr(mean);
    c2 d[10];
#pragma unroll
    for (int r = 0; r < 10; ++r) { const c2 x = v[r] - bc(mb); d[r] = unpk(pack2(x.x, x.y)); }
    const float var = hsum32(q0 + q1) * (1.f / 640.f);
    const float rs = __builtin_amdgcn_rcpf(bfr(sqrtf(bfr(bfr(var) + 1e-5f))));
#pragma unroll
    for (int r = 0; r < 10; ++r) { const c2 x = d[r] * bc(rs); o[r] = pack2(x.x, x.y); }
}

__device__ __forceinline__ __amdgpu_buffer_rsrc_t mkrsrc(const void* p, unsigned bytes) {
    return __builtin_amdgcn_make_buffer_rsrc((void*)p, 0, bytes, 0x00020000);
}
__device__ __forceinline__ unsigned xcc_id() { unsigned x; asm volatile("s_getreg_b32 %0, hwreg(HW_REG_XCC_ID)" : "=s"(x)); return x & 15u; }
#define VMWAIT() do { asm volatile("" ::: "memory"); __builtin_amdgcn_s_waitcnt(0x0F70); asm volatile("" ::: "memory"); } while (0)
#define BAR() asm volatile("s_waitcnt lgkmcnt(0)\n\ts_barrier" ::: "memory")
#define BAR0() asm volatile("s_barrier" ::: "memory")

#define FST(k) do {} while (0)
#define FSW(k) do {} while (0)
extern __shared__ __attribute__((aligned(16))) char smem[];
#define RST(k, lay, wv, sel) do {} while (0)
constexpr int R_C = 8 * 10 * 72 * 8;
constexpr float FSC = 0.5f / ((float)S_ * (float)D_);
__device__ __forceinline__ c2 ldv(const LAS void* p) { return *(const volatile LAS c2*)p; }
struct RowTw { Tw a, b; };
__device__ __forceinline__ void row_tw(RowTw& t, int tid) { const int i = tid % 80; ldtw(t.a, TWR2, i); ldtw(t.b, TWR3, i); }
template <int NF, bool LATE> __device__ __forceinline__ void a1_unit(__amdgpu_buffer_rsrc_t rx, int row0, __amdgpu_buffer_rsrc_t rws, __amdgpu_buffer_rsrc_t rwin, RowTw& tw, int tid, unsigned (&fs)[12]) {
    const int wave = __builtin_amdgcn_readfirstlane(tid >> 6), lane = tid & 63;
    LAS c2* A = (LAS c2*)smem; LAS c2* C = (LAS c2*)(smem + R_C);
    FST(0);
    if (wave < NF) {
        unsigned d[10];
        for (unsigned spins = 0;;) {
            asm volatile("" ::: "memory");
#pragma unroll
            for (int r = 0; r < 10; ++r) d[r] = __builtin_amdgcn_raw_buffer_load_b32(rx, (lane & 31) * 4u + (lane >> 5) * (D_ * 2u) + r * 128u, (unsigned)(row0 + 2 * wave) * (D_ * 2), 16);
            if (!LATE) break;
            unsigned m = d[0];
#pragma unroll
            for (int r = 1; r < 10; ++r) m = max(m, d[r]);
            if (!__any(m == ~0u) || spin_fail(spins, rwin)) break;
            __builtin_amdgcn_s_sleep(PS1);
        }
        c2 u[10];
#pragma unroll
        for (int r = 0; r < 10; ++r) u[r] = unpk(d[r]);
        FST(1);
        if (LATE) RST(16, 0, wave, 0);
        ln_row(u, d);
        FST(2);
#pragma unroll
        for (int r = 0; r < 10; ++r) { u[r] = unpk(d[r]); swap32(u[r]); }
        dft10<-1>(u);
#pragma unroll
        for (int r = 0; r < 10; ++r) A[(wave * 10 + r) * 72 + (lane & 31) + 36 * (lane >> 5)] = u[r];
        FST(3);
    }
    BAR();
    if (NF == 4 && !LATE) row_tw(tw, tid);
    FST(4);
#pragma unroll
    for (int c = 0; c < (NF > 4 ? 2 : 1); ++c) {
        if (tid < (NF > 4 ? (c ? 160 : 480) : 80 * NF)) {
            const int K = (tid >> 3) + 60 * c, m = tid & 7;
            LAS c2* a = A + K * 72;
            c2 u[8];
#pragma unroll
            for (int q = 0; q < 8; ++q) u[q] = ldv(a + (m >> 1) + 36 * (m & 1) + 4 * q);
            twid<-1>(u, tw.a);
            dft8<-1>(u);
#pragma unroll
            for (int p = 0; p < 8; ++p) a[9 * p + m] = u[p];
            asm volatile("" ::: "memory");
#pragma unroll
            for (int q = 0; q < 8; ++q) u[q] = ldv(a + 9 * m + q);
            twid<-1>(u, tw.b);
            dft8<-1>(u);
            LAS c2* o = C + (K / 10) * 638 + K + 10 * m;
#pragma unroll
            for (int p = 0; p < 8; ++p) o[80 * p] = u[p];
        }
    }
    FST(5);
    BAR();
    FST(6);
#pragma unroll
    for (int c = 0; c < (NF > 4 ? 5 : 3); ++c) {
        const int it = tid + 512 * c, kp = it / (2 * NF), row = it % (2 * NF);
        if (NF == 4 && !LATE && row0 < 1600) for (int i = 0; i < (c ? 8 : wave); ++i) __builtin_amdgcn_s_sleep(TRK);
        if (NF > 4 || c < 2 || tid < 256) {
            const LAS c2* z = C + (row >> 1) * 648;
            const f32x4 zk = *(const LAS f32x4*)(z + 2 * kp);
            const c2 n0 = ldv(z + (kp ? 640 - 2 * kp : 320)), n1 = ldv(z + 639 - 2 * kp);
            const bool odd = it & 1;
            f32x4 o = odd ? (f32x4){zk[1] + n0.y, n0.x - zk[0], zk[3] + n1.y, n1.x - zk[2]} : (f32x4){zk[0] + n0.x, zk[1] - n0.y, zk[2] + n1.x, zk[3] - n1.y};
            if (c == 0 && kp == 0) { o[0] = 2.f * (odd ? zk[1] : zk[0]); o[1] = 2.f * (odd ? n0.y : n0.x); }
            u32x4 ob = __builtin_bit_cast(u32x4, o * FSC);
            if (LATE) { ob[1] = nosent1(ob[1]); ob[3] = nosent1(ob[3]); }
            st128(ob, rws, kp * (LATE ? GST : FST_) + row * 16u, (LATE ? O_FL - (S_ - 256) * 16u : O_F) + (unsigned)row0 * 16u, 16);
        }
    }
    FST(7);
}
template <class FG> __device__ __forceinline__ void a3_unit(__amdgpu_buffer_rsrc_t rx, int row0, int layer, __amdgpu_buffer_rsrc_t rws, __amdgpu_buffer_rsrc_t rwin, const RowTw& tw, int tid, FG go, unsigned (&fs)[12]) {
    const int wave = __builtin_amdgcn_readfirstlane(tid >> 6), lane = tid & 63;
    LAS c2* A = (LAS c2*)smem; LAS c2* C = (LAS c2*)(smem + R_C);
    const unsigned n0 = (lane & 7) | ((lane & 24) << 1);
    c2 x[10];
    if (wave < 4) {
        const unsigned base = (unsigned)(row0 + 2 * wave) * (D_ * 2), vo = n0 * 2u + (lane >> 5) * (D_ * 2u);
#pragma unroll
        for (int r = 0; r < 10; ++r) x[r] = (c2){bf2f(__builtin_amdgcn_raw_buffer_load_b16(rx, vo + 128u * r, base, 16)), bf2f(__builtin_amdgcn_raw_buffer_load_b16(rx, vo + 16u + 128u * r, base, 16))};
    }
    const bool early = row0 < 256;
    if (!early) go();
    else if (TG) {
        if (wave == 7) { const unsigned vo[3] = {lane * 128u, lane * 128u + 8192u, min(lane, 31) * 128u + 16384u}; wait_told(rwin, vo, W_COL + (unsigned)layer * W_LAYER + 4u, 1); }
        BAR0();
    }
    FST(0);
    u32x4 fv[3];
    {
        const unsigned vo = (tid >> 3) * (early ? GST : FST_) + (tid & 7) * 16u, st = (early ? GST : FST_) * 64u, so = (early ? O_FL + (unsigned)(2 - layer) * (NCOL * GST) : O_F) + (unsigned)row0 * 16u;
#pragma unroll
        for (int c = 0; c < 3; ++c) fv[c] = __builtin_amdgcn_raw_buffer_load_b128(rws, vo, c < 2 || wave < 4 ? so + c * st : 0x40000000u, 16);
        if (early) for (unsigned spins = 0;;) {
            if (!__any(max(max(smax(fv[0]), smax(fv[1])), smax(fv[2])) == ~0u) || spin_fail(spins, rwin)) break;
            __builtin_amdgcn_s_sleep(PS3);
            asm volatile("" ::: "memory");
            u32x4 x[3];
#pragma unroll
            for (int c = 0; c < 3; ++c) x[c] = __builtin_amdgcn_raw_buffer_load_b128(rws, smax(fv[c]) == ~0u ? vo : 0x40000000u, so + c * st, 16);
#pragma unroll
            for (int c = 0; c < 3; ++c) if (smax(fv[c]) == ~0u) fv[c] = x[c];
        }
    }
#pragma unroll
    for (int c = 0; c < 3; ++c) {
        const int it = tid + 512 * c, kp = it >> 3;
        if (c < 2 || tid < 256) {
            const bool odd = it & 1;
            const f32x4 v = __builtin_bit_cast(f32x4, fv[c]), p = {dppf<0xB1>(v[0]), dppf<0xB1>(v[1]), dppf<0xB1>(v[2]), dppf<0xB1>(v[3])};
            c2 z0 = odd ? (c2){p[0] + v[1], v[0] - p[1]} : (c2){v[0] - p[1], p[0] + v[1]};
            const c2 z1 = odd ? (c2){p[2] + v[3], v[2] - p[3]} : (c2){v[2] - p[3], p[2] + v[3]};
            int i0 = odd ? 640 - 2 * kp : 2 * kp;
            if (c == 0 && kp == 0) { z0 = odd ? (c2){p[1], v[1]} : (c2){v[0], p[0]}; i0 >>= 1; }
            LAS c2* z = C + ((it >> 1) & 3) * 648;
            z[i0] = z0; z[odd ? 639 - 2 * kp : 2 * kp + 1] = z1;
        }
    }
    FST(1);
    BAR();
    if (early && TH && wave == 7) tell_at(rwin, W_H + (unsigned)layer * W_LAYER + (unsigned)((row0 >> 5) * 128 + 16 + ((row0 >> 3) & 3) * 4), layer ? TH : TH0);
    if (early) RST(3, layer, wave, 0);
    FST(2);
    if (tid < 320) {
        const int K = tid >> 3, m = tid & 7;
        LAS c2* a = A + K * 72;
        const LAS c2* o = C + (K / 10) * 638 + K + 10 * m;
        c2 u[8];
#pragma unroll
        for (int p = 0; p < 8; ++p) u[p] = ldv(o + 80 * p);
        dft8<1>(u);
        twid<1>(u, tw.b);
#pragma unroll
        for (int q = 0; q < 8; ++q) a[9 * q + m] = u[q];
        asm volatile("" ::: "memory");
#pragma unroll
        for (int p = 0; p < 8; ++p) u[p] = ldv(a + 9 * m + p);
        dft8<1>(u);
        twid<1>(u, tw.a);
#pragma unroll
        for (int q = 0; q < 8; ++q) a[m + 8 * (q >> 1) + 32 * (q & 1)] = u[q];
    }
    FST(3);
    BAR();
    FST(4);
    if (wave < 4) {
        c2 u[10];
#pragma unroll
        for (int r = 0; r < 10; ++r) u[r] = ldv(A + (wave * 10 + r) * 72 + lane);
        dft10<1>(u);
        unsigned x1[10], h[10];
#pragma unroll
        for (int r = 0; r < 10; ++r) {
            swap32(u[r]);
            const c2 y = x[r] + unpk(pack2(u[r].x, u[r].y));
            x1[r] = pack2(y.x, y.y);
            u[r] = unpk(x1[r]);
        }
        FST(5);
        ln_row(u, h);
        FST(6);
        LAS char* st = (LAS char*)C + wave * 5120;
        LAS char* hp = st + (lane >> 5) * 1280 + (n0 & 48) * 2 + pi16(n0 & 15) * 2;
#pragma unroll
        for (int r = 0; r < 10; ++r) *(LAS unsigned*)(hp + 128 * r) = nosent1(h[r]);
#pragma unroll
        for (int c = 0; c < 3; ++c) {
            if (c < 2 || lane < 32) {
                const int id = c * 64 + lane, rr = id >= 80, ch = id - 80 * rr, row = (row0 + 2 * wave + rr) & 31;
                st128(*(const LAS u32x4*)(st + id * 16), rws, (unsigned)((ch >> 3) * 4096 + row * 128 + (((ch & 7) ^ (row & 7)) << 4)), O_H + (unsigned)((row0 >> 5) - NHF * layer) * 40960u, 16);
            }
        }
        if (early) RST(4, layer, wave, 0);
        LAS char* xp = st + 2560 + (lane >> 5) * 1280 + n0 * 2;
#pragma unroll
        for (int r = 0; r < 10; ++r) { *(LAS u16*)(xp + 128 * r) = (u16)x1[r]; *(LAS u16*)(xp + 128 * r + 16) = (u16)(x1[r] >> 16); }
#pragma unroll
        for (int c = 0; c < 3; ++c)
            if (c < 2 || lane < 32) st128(*(const LAS u32x4*)(st + 2560 + (c * 64 + lane) * 16), rws, (c * 64 + lane) * 16u, O_X1 + (unsigned)(row0 + 2 * wave) * (D_ * 2), 16);
        FST(7);
    }
}

constexpr int C_T = 8 * 544 * 8, C_B = C_T + (int)sizeof(TwCol), C_W = C_B + 32768, C_END = C_W + 8 * 516 * 4;
template <class FG> __device__ __forceinline__ void a2_unit(int kp, int late, __amdgpu_buffer_rsrc_t rws, __amdgpu_buffer_rsrc_t rwin, int tid, FG go, unsigned (&fs)[12]) {
    const int w = __builtin_amdgcn_readfirstlane(tid >> 6), l = tid & 63;
    const unsigned fb = O_F + (unsigned)kp * FST_, wb = O_WFT + (unsigned)kp * WST;
    const int b = (l >> 1) & 7;
    const int k0 = w + 8 * b + 128 * (l >> 4);
    c2 u[8];
    u32x4 wv[2];
    {
        f32x4 tv[3];
#pragma unroll
        for (int i = 0; i < 3; ++i) tv[i] = ((const f32x4*)&TWC)[min(tid + 512 * i, (int)sizeof(TwCol) / 16 - 1)];
        go(0);
#pragma unroll
        for (int i = 0; i < 3; ++i) ((LAS f32x4*)(smem + C_T))[min(tid + 512 * i, (int)sizeof(TwCol) / 16 - 1)] = tv[i];
        BAR();
        FST(0);
        u32x4 x[4];
#pragma unroll
        for (int r = 0; r < 4; ++r) x[r] = __builtin_amdgcn_raw_buffer_load_b128(rws, (tid >> 1) * 16u + (tid & 1) * 4096u, fb + r * 8192u, 16);
#pragma unroll
        for (int r = 0; r < 2; ++r) wv[r] = __builtin_amdgcn_raw_buffer_load_b128(rws, tid * 16u, wb + r * 8192u, 16);
#pragma unroll
        for (int r = 0; r < 4; ++r) {
            const f32x4 y = __builtin_bit_cast(f32x4, x[r]);
            const c2 z = {dppf<0xB1>(l & 1 ? y[0] : y[2]), dppf<0xB1>(l & 1 ? y[1] : y[3])};
            u[2 * r] = l & 1 ? z : (c2){y[0], y[1]};
            u[2 * r + 1] = l & 1 ? (c2){y[2], y[3]} : z;
        }
    }
    const LAS f32x4* t1 = (const LAS f32x4*)(smem + C_T) + (tid >> 1);
    const LAS f32x4* t2 = (const LAS f32x4*)(smem + C_T + sizeof(Tw7<256>)) + (l >> 1);
    const LAS f32x4* t3 = (const LAS f32x4*)(smem + C_T + sizeof(Tw7<256>) + sizeof(Tw7<32>)) + (l >> 4);
    LAS c2* e0 = (LAS c2*)smem + tid;
    LAS c2* e1 = (LAS c2*)smem + w * 544 + l;
    LAS c2* e2 = (LAS c2*)smem + w * 544 + 2 * (l >> 4) + (l & 1);
    Tw tw;
    ldtw<256>(tw, t1);
    if (late) {
        go(2);
        FST(1);
        for (unsigned spins = 0;;) {
            asm volatile("" ::: "memory");
            const u32x2 x = __builtin_amdgcn_raw_buffer_load_b64(rws, tid * 8u, O_FL + (unsigned)kp * GST, 16);
            u[7] = __builtin_bit_cast(c2, x);
            if (!__any(x[1] == ~0u) || spin_fail(spins, rwin)) break;
            if (w < 3) __builtin_amdgcn_s_sleep(16); else __builtin_amdgcn_s_sleep(PS2);
        }
    }
    FST(2);
    dft8<-1>(u);
    twid<-1>(u, tw);
    if (kp == 0) {
#pragma unroll
        for (int i = 0; i < 4; ++i)
            asm volatile("s_mov_b32 m0, %3\n\ts_nop 3\n\tbuffer_load_dwordx4 %0, %1, %2 offen sc1 lds" :: "v"(l * 16u), "s"(rws), "s"(O_WFT + NCOL * WST + (w * 4 + i) * 1024u), "s"(C_B + (w * 4 + i) * 1024u), "v"(u[7]) : "m0", "memory");
    }
#pragma unroll
    for (int r = 0; r < 8; ++r) e0[544 * r] = u[r];
    ldtw<32>(tw, t2);
    BAR();
    if (A2P == 1 && w < 4) __builtin_amdgcn_s_setprio(2);
    if (A2P == 2 && (w & 1)) __builtin_amdgcn_s_setprio(2);
    if (A2P == 3 && w >= 4) __builtin_amdgcn_s_sleep(4);
    if (TG && w == 0) tell_at(rwin, W_COL + (unsigned)late * W_LAYER + (unsigned)kp * 128u + 4u, kp ? TG : TG + 30);
#pragma unroll
    for (int r = 0; r < 8; ++r) u[r] = ldv(e1 + 64 * r);
    dft8<-1>(u);
    twid<-1>(u, tw);
#pragma unroll
    for (int r = 0; r < 8; ++r) e1[68 * r] = u[r];
    asm volatile("" ::: "memory");
#pragma unroll
    for (int r = 0; r < 8; ++r) u[r] = ldv(e2 + 68 * b + 8 * r);
    ldtw<4>(tw, t3);
    dft8<-1>(u);
    twid<-1>(u, tw);
    c2 f[8];
#pragma unroll
    for (int r = 0; r < 8; ++r) f[r] = unpk(wv[r >> 2][r & 3]);
    asm volatile("s_nop 1" : "+v"(u[1]), "+v"(u[2]), "+v"(u[3]), "+v"(u[4]), "+v"(u[5]), "+v"(u[6]), "+v"(u[7]));
#pragma unroll
    for (int r = 0; r < 4; ++r) swapl<5>(u[r], u[r + 4]);
#pragma unroll
    for (int r = 0; r < 4; ++r) swapl<4>(u[(r & 1) + 2 * (r & 2)], u[(r & 1) + 2 * (r & 2) + 2]);
    dft4<-1>(u[0], u[2], u[4], u[6]); dft4<-1>(u[1], u[3], u[5], u[7]);
    FST(3);
    if (kp == 0) {
        LAS c2* z = (LAS c2*)(smem + C_W);
#pragma unroll
        for (int r = 0; r < 8; ++r) if (!(l & 1)) z[w * 258 + b + 16 * (l >> 4) + 8 * (r & 1) + 64 * (r >> 1)] = u[r];
        VMWAIT();
        BAR();
#pragma unroll
        for (int r = 0; r < 8; ++r) {
            const int kn = (S_ - k0 - 64 * (r & 1) - 512 * (r >> 1)) & (S_ - 1);
            const f32x4 ab = ((const LAS f32x4*)(smem + C_B))[256 * r + (tid >> 1)];
            const c2 zn = z[(kn & 7) * 258 + (kn >> 3)], g = {ab[2], ab[3]}, h = l & 1 ? f[r] : (c2){ab[0], ab[1]};
            const c2 y = cfin<1>(g, zn, g * bc(zn.x));
            u[r] = cfin<-1>(u[r], h, fm(u[r], bc(h.x), l & 1 ? bc(0.f) : y));
        }
    } else {
#pragma unroll
        for (int r = 0; r < 8; ++r) u[r] = cfin<-1>(u[r], f[r], u[r] * bc(f[r].x));
    }
    FST(4);
    dft4<1>(u[0], u[2], u[4], u[6]); dft4<1>(u[1], u[3], u[5], u[7]);
#pragma unroll
    for (int r = 0; r < 4; ++r) swapl<4>(u[(r & 1) + 2 * (r & 2)], u[(r & 1) + 2 * (r & 2) + 2]);
#pragma unroll
    for (int r = 0; r < 4; ++r) swapl<5>(u[r], u[r + 4]);
    twid<1>(u, tw);
    dft8<1>(u);
#pragma unroll
    for (int r = 0; r < 8; ++r) e2[66 * b + 8 * r] = u[r];
    asm volatile("" ::: "memory");
    ldtw<32>(tw, t2);
#pragma unroll
    for (int r = 0; r < 8; ++r) u[r] = ldv(e1 + 66 * r);
    twid<1>(u, tw);
    dft8<1>(u);
#pragma unroll
    for (int r = 0; r < 8; ++r) e1[64 * r] = u[r];
    ldtw<256>(tw, t1);
    __builtin_amdgcn_s_setprio(0);
    FST(7);
    BAR();
    FST(10);
#pragma unroll
    for (int r = 0; r < 8; ++r) u[r] = ldv(e0 + 544 * r);
    twid<1>(u, tw);
    dft8<1>(u);
    FST(5);
    __builtin_amdgcn_raw_buffer_store_b64((u32x2){__float_as_uint(u[0].x), nosent1(__float_as_uint(u[0].y))}, rws, tid * 8u, O_FL + (unsigned)((2 - late) * NCOL + kp) * GST, 16);
#pragma unroll
    for (int r = 1; r < 8; ++r) __builtin_amdgcn_raw_buffer_store_b64(__builtin_bit_cast(u32x2, u[r]), rws, tid * 8u, fb + r * 4096u, 16);
    FST(6);
}

struct Args { char* ws; char* win; const u16 *x, *wr, *wi, *mem0, *wq, *wk, *wv, *wo; u16* out; };
__device__ __forceinline__ const u16* arg(int i) {
    const __attribute__((address_space(4))) u64* p = (const __attribute__((address_space(4))) u64*)__builtin_amdgcn_kernarg_segment_ptr();
    asm volatile("" : "+s"(p));
    return (const u16*)p[i];
}
enum { A_X = 2, A_WR, A_WI, A_MEM0, A_WQ, A_WK, A_WV, A_WO, A_OUT };
#define MXV 0x5556ull
#define MXQ 0x1554ull
#define NTAIL 10
static_assert(NTAIL >= 3, "the owners release segments up to NSEG - 4");
constexpr int TAILS = NSEG - NTAIL;
static_assert((MXV & 31) == 22 && (MXQ & 31) == 20, "segment sets");
constexpr int NREC = 96 + NOWN, NQT = NREC + NOWN, NXT = NQT + 32, NWRK = NXT + 32;
#define SST(k) do {} while (0)
#define LSEL 0
#define RT(k) do {} while (0)
#define RTS() do {} while (0)
#define B3ATTR
#define MTA(k)
#define MTRT() do {} while (0)
#define MT(k) do {} while (0)
#define MTS() do {} while (0)
#define FC(x) do {} while (0)
#define DQC 1200
#define DVC 1200
#define M1 44
#define M2 226
#define PEN 280
#define DZP 55
#define LT0 342
#define SAD 1
#define PEN1 230
#define PEN0 330
#define TSLP 1
#define OPR 2
#define BSPARSE 0
#define ZLAG 4
#define TKP 0
#define DPS 12
#define DXT 3000
#define LTM 12
#define XS0 10
#define XSU 8
#define XSM 32
#define LTU 4
#define LTS 3
#define XSS 3
#define TKS 3
#define LT1 450
#define FDL 2
#define ZLA 3
#define NORND 0
#define LTX 40
#define TKE 200
#define DVO 4000
#define ZPS 2
#define HLA 8
#define QB2 2
#define NB2 6
#define DZR 24
#define ML 150
#define NB1 3
#define DPT 1
#define DDMA 8
#define DZ2 800
#define QEARLY 2
#define OSLP 32
#define OSLP2 1
#define WSLP 64
#define DO0 4000
#define DW0 1200
#define DQT 2000
#define ZEARLY 6
#define HSLP 4
#define ASLP 4
#define CSLP 4
#define A1S 0
#define OSLP3 12
#define DVT 860
#define VSL 0
#define WARM 1
constexpr unsigned CODE_BYTES = 96000;
#define DTK 4
#define OSL4 10
#define OAUX 0
#define TSL0 6
#define SE 6
#define XE 12
#define PENS 20
#define CF 190
#define HSL2 16
#define DH1 10
#define DZ0 400
#define FZP 1
__device__ __forceinline__ unsigned fz_at(unsigned h, unsigned g) { return FZP ? g * 1024u + (h >> 2) * 128u + (h & 3u) * 4u : h * 128u + g * 4u; }
#define LKE 0
#define DW4 6
#define DBIG 580
#define PENH 280
#define ROT 0
#define VREV 0
#define DGV 0
#define VOFF0 "0"
#define VOFF1 "1024"
#define VOFF2 "2048"
#define VOFF3 "3072"
#define VOFF4 "4096"
#define VOFF5 "5120"
#define VOFF6 "6144"
#define VOFF7 "7168"
#define VOFF8 "8192"
#define VOFF9 "9216"
#define STK 1
#define STX 0
#define UFQ 14
#define DQ0 36
#define QSL 6
#define OST0 40
#define NPR 3
#define DQA 1300
#define DQA0 1400
#define DIE 0
#define ALN ".p2align 6\n\t"
#define SB() __builtin_amdgcn_sched_barrier(0)
#define PIN1(a) asm volatile("" : "+v"(a) :: "memory")
#define PIN2(a, b) asm volatile("" : "+v"(a), "+v"(b) :: "memory")
__device__ __forceinline__ unsigned fresh_lane() { unsigned l; asm volatile("v_mbcnt_lo_u32_b32 %0, -1, 0\n\tv_mbcnt_hi_u32_b32 %0, -1, %0" : "=v"(l)); return l; }
#define LANE0 (min(fresh_lane(), 1u) << 30)
__device__ __forceinline__ void wait_until(unsigned long long t) { while ((long long)(__builtin_amdgcn_s_memtime() - t) < 0) __builtin_amdgcn_s_sleep(1); }
struct Clk { unsigned ta, ma, cpq, qpc; };
__device__ __forceinline__ int sx24(unsigned v) { return (int)(v << 8) >> 8; }
#define WAIT_FINE ALN \
        "s_memrealtime vcc\n\t" \
        "s_memtime s[100:101]\n\t" \
        "s_brev_b32 %[fi], -2\n\t" \
        "s_waitcnt lgkmcnt(0)\n\t" \
        "s_mov_b32 %[rp], vcc_lo\n\t" \
        "s_lshl_b32 vcc_hi, vcc_lo, 2\n\t" \
        "s_sub_u32 %[x], %[t], vcc_hi\n\t" \
        "s_bfe_i32 %[x], %[x], 0x180000\n\t" \
        "s_min_i32 %[x], %[x], 4000\n\t" \
        "s_max_i32 %[x], %[x], -4000\n\t" \
        "s_add_u32 %[t], vcc_hi, %[x]\n\t" \
        "s_mul_i32 %[x], %[x], %[cq]\n\t" \
        "s_ashr_i32 %[x], %[x], 8\n\t" \
        "s_sub_u32 %[tm], %[t], %[ta]\n\t" \
        "s_bfe_i32 %[tm], %[tm], 0x180000\n\t" \
        "s_min_i32 %[tm], %[tm], 1000000\n\t" \
        "s_max_i32 %[tm], %[tm], -1000000\n\t" \
        "s_mul_i32 %[tm], %[tm], %[cq]\n\t" \
        "s_ashr_i32 %[tm], %[tm], 8\n\t" \
        "s_add_u32 %[tm], %[tm], %[ma]\n\t" \
        "s_sub_u32 %[tm], %[tm], s100\n\t" \
        "s_lshr_b32 vcc_hi, %[cq], 4\n\t" \
        "s_sub_u32 vcc_hi, %[x], vcc_hi\n\t" \
        "s_max_i32 %[tm], %[tm], vcc_hi\n\t" \
        "s_min_i32 %[tm], %[tm], %[x]\n\t" \
        "s_add_u32 %[tm], %[tm], s100\n" \
        "1:\n\t" \
        "s_memrealtime vcc\n\t" \
        "s_memtime s[100:101]\n\t" \
        "s_waitcnt lgkmcnt(0)\n\t" \
        "s_cmp_eq_u32 vcc_lo, %[rp]\n\t" \
        "s_cbranch_scc1 2f\n\t" \
        "s_mov_b32 %[rp], vcc_lo\n\t" \
        "s_lshl_b32 %[x], vcc_lo, 2\n\t" \
        "s_sub_u32 %[x], %[t], %[x]\n\t" \
        "s_bfe_i32 %[x], %[x], 0x180000\n\t" \
        "s_mul_i32 %[x], %[x], %[cq]\n\t" \
        "s_ashr_i32 %[x], %[x], 8\n\t" \
        "s_add_u32 %[x], %[x], s100\n\t" \
        "s_sub_u32 s101, %[x], %[tm]\n\t" \
        "s_cmp_lt_i32 s101, %[fi]\n\t" \
        "s_cselect_b32 %[tm], %[x], %[tm]\n\t" \
        "s_mov_b32 %[fi], 0\n" \
        "2:\n\t" \
        "s_sub_u32 %[x], %[tm], s100\n\t" \
        "s_cmp_gt_i32 %[x], 260\n\t" \
        "s_cbranch_scc1 1b\n\t" \
        "s_cmp_eq_u32 %[fi], 0\n\t" \
        "s_cselect_b32 %[ta], %[t], %[ta]\n\t" \
        "s_cselect_b32 %[ma], %[tm], %[ma]\n" \
        "3:\n\t" \
        "s_memtime s[100:101]\n\t" \
        "s_waitcnt lgkmcnt(0)\n\t" \
        "s_sub_u32 %[x], %[tm], s100\n\t" \
        "s_cmp_gt_i32 %[x], 92\n\t" \
        "s_cbranch_scc1 3b\n\t" \
        "s_max_i32 %[x], %[x], 0\n\t" \
        "s_andn2_b32 %[x], %[x], 3\n\t" \
        "s_sub_u32 %[x], 104, %[x]\n\t" \
        "s_getpc_b64 vcc\n\t" \
        "s_add_u32 vcc_lo, vcc_lo, %[x]\n\t" \
        "s_addc_u32 vcc_hi, vcc_hi, 0\n\t" \
        "s_setpc_b64 vcc\n\t" \
        ".rept 23\n\t" \
        "s_nop 0\n\t" \
        ".endr\n\t"
#define WAIT_FINE_OUT [tm] "=&s"(tm), [rp] "=&s"(rp), [x] "=&s"(x), [fi] "=&s"(fi), [t] "+s"(t), [ta] "+s"(c.ta), [ma] "+s"(c.ma)
__device__ __forceinline__ void wait_fine(unsigned t, Clk& c) {
    unsigned tm, rp, x, fi;
    asm volatile(WAIT_FINE : WAIT_FINE_OUT : [cq] "s"(c.cpq) : "vcc", "scc", "s100", "s101", "memory");
}
__device__ __forceinline__ void wait_fine_bar(unsigned t, Clk& c) {
    unsigned tm, rp, x, fi;
    asm volatile(WAIT_FINE "s_barrier" : WAIT_FINE_OUT : [cq] "s"(c.cpq) : "vcc", "scc", "s100", "s101", "memory");
}
__device__ __forceinline__ void wait_cyc_bar(unsigned tm) {
    unsigned x;
    asm volatile(
        ALN
        "3:\n\t"
        "s_memtime s[100:101]\n\t"
        "s_waitcnt lgkmcnt(0)\n\t"
        "s_sub_u32 %0, %1, s100\n\t"
        "s_cmp_gt_i32 %0, 92\n\t"
        "s_cbranch_scc1 3b\n\t"
        "s_max_i32 %0, %0, 0\n\t"
        "s_andn2_b32 %0, %0, 3\n\t"
        "s_sub_u32 %0, 104, %0\n\t"
        "s_getpc_b64 vcc\n\t"
        "s_add_u32 vcc_lo, vcc_lo, %0\n\t"
        "s_addc_u32 vcc_hi, vcc_hi, 0\n\t"
        "s_setpc_b64 vcc\n\t"
        ".rept 23\n\t"
        "s_nop 0\n\t"
        ".endr\n\t"
        "s_barrier"
        : "=&s"(x) : "s"(tm) : "vcc", "scc", "s100", "s101", "memory");
}
__device__ __forceinline__ unsigned cyc_at(const Clk& c, int d) {
    d = min(d, 20000);
    asm volatile("" : "+s"(d));
    return c.ma + (unsigned)((max(d, -4000) * (int)c.cpq) >> 8);
}
__device__ __forceinline__ void clk_rate(Clk& c, unsigned ta0, unsigned ma0) {
    const int dq = sx24(c.ta - ta0);
    if (dq > 200) {
        const float r = fminf(fmaxf((float)(c.ma - ma0) * __builtin_amdgcn_rcpf((float)dq), 1.f), 8.f);
        asm volatile("s_nop 1\n\tv_readfirstlane_b32 %0, %2\n\tv_readfirstlane_b32 %1, %3\n\ts_nop 3" : "=s"(c.cpq), "=s"(c.qpc)
                     : "v"((unsigned)(256.f * r + 0.5f)), "v"((unsigned)(65536.f * __builtin_amdgcn_rcpf(r))));
    }
}
__device__ __forceinline__ void clk_init(Clk& c) {
    c.ta = 4u * (unsigned)__builtin_amdgcn_s_memrealtime(); c.ma = (unsigned)__builtin_amdgcn_s_memtime();
    for (int i = 0; i < 2; ++i) {
        const unsigned ta0 = c.ta, ma0 = c.ma;
        wait_fine(c.ta + (i ? 360u : 60u), c);
        clk_rate(c, ta0, ma0);
    }
}
__device__ __forceinline__ unsigned sched_lane(int s) { const unsigned l = fresh_lane(); return (l & 31u) * 128u + (l >> 5) * 4u; }
__device__ __forceinline__ int read_sched(__amdgpu_buffer_rsrc_t rwin, int s, unsigned vo, unsigned to, unsigned f, unsigned g0, int& lt, int& xs, unsigned& fc) {
    int d = (int)((f - g0) & 0xFFFFFFu);
    if (__builtin_expect(__any((f >> 24) != (unsigned)s) || s == 0, 0)) {
        for (unsigned spins = 0; __any((f >> 24) != (unsigned)s);) {
            FC(fc);
            if (spin_fail(spins, rwin)) break;
            if (s == 0) __builtin_amdgcn_s_sleep(TSL0); else __builtin_amdgcn_s_sleep(TSLP);
            asm volatile("" ::: "memory");
            if ((f >> 24) != (unsigned)s) f = __builtin_amdgcn_raw_buffer_load_b32(rwin, vo, to, 16);
        }
        const unsigned f0 = __builtin_amdgcn_readfirstlane(f) - 0x100000u;
        d = (int)((f - f0) & 0xFFFFFFu) + (int)((f0 - g0) & 0xFFFFFFu);
    }
    d = max(d, __builtin_amdgcn_update_dpp(0, d, 0xB1, 0xF, 0xF, true));
    d = max(d, __builtin_amdgcn_update_dpp(0, d, 0x4E, 0xF, 0xF, true));
    d = max(d, __builtin_amdgcn_update_dpp(0, d, 0x141, 0xF, 0xF, true));
    d = max(d, __builtin_amdgcn_update_dpp(0, d, 0x140, 0xF, 0xF, true));
    d = max(d, __builtin_amdgcn_update_dpp(0, d, 0x142, 0xA, 0xF, true));
    d = max(__builtin_amdgcn_readlane(d, 16), __builtin_amdgcn_readlane(d, 48));
    const int l0 = lt, x0 = xs;
    d -= 0x400000;
    const bool miss = d > l0 + min(x0 + LTM, LTX);
    if (s >= SAD) { lt = miss ? l0 : min(max(d, l0 - 1 - ((l0 - d) >> LTS)), l0 + LTU); xs = miss ? min(x0 + XSU, XSM) : max(x0 - 1 - (x0 >> XSS), 0); }
    if (s == 1) lt = LT0;
    if (s < SE) xs = max(xs, XE);
    return 0x400000 + (s < SAD ? d + (s ? PENS : PEN0) : miss ? d + (d < DBIG ? PENH : PEN) : l0 + x0) + M1 + M2 + x0;
}
constexpr int QS = 272;
constexpr int TQS = 1152;
constexpr int L_Q = 10240, L_K = L_Q + 2 * TQS, L_S = L_K + 2 * TQS, L_TOK = L_S + 256, L_H = L_TOK + 49152, L_END = L_H + 2 * 40960;
static_assert(C_END <= L_END && L_END == 146176, "LDS");
typedef __attribute__((ext_vector_type(4))) short v4i16_t;
__device__ __forceinline__ u32x2 ldtr(const char* p) { return __builtin_bit_cast(u32x2, __builtin_amdgcn_ds_read_tr16_b64_v4i16((LAS v4i16_t*)p)); }
__device__ __forceinline__ u32x2 ldtr1(const LAS char* p) { return __builtin_bit_cast(u32x2, __builtin_amdgcn_ds_read_tr16_b64_v4i16((LAS v4i16_t*)p)); }
__device__ __forceinline__ u32x4 ldtr2(const LAS char* p0, const LAS char* p1) {
    const u32x2 x = __builtin_bit_cast(u32x2, __builtin_amdgcn_ds_read_tr16_b64_v4i16((LAS v4i16_t*)p0)), y = __builtin_bit_cast(u32x2, __builtin_amdgcn_ds_read_tr16_b64_v4i16((LAS v4i16_t*)p1));
    return (u32x4){x[0], x[1], y[0], y[1]};
}
__device__ __forceinline__ u32x4 ldtr2(const char* p0, const char* p1) { const u32x2 x = ldtr(p0), y = ldtr(p1); return (u32x4){x[0], x[1], y[0], y[1]}; }

__device__ __forceinline__ unsigned poll_lim(int& budget) {
    --budget;
    unsigned lim = budget > 0 ? 0xFFFFFFFEu : 0xFFFFFFFFu;
    asm volatile("" : "+s"(lim));
    return lim;
}
#define GATHER \
        "buffer_load_dwordx4 %[vo], %[rw], %[s0] offen sc1 lds\n\t" \
        "buffer_load_dwordx4 %[vo], %[rw], %[s0] offen offset:1024 sc1 lds\n\t" \
        "buffer_load_dwordx4 %[vo], %[rw], %[s0] offen offset:2048 sc1 lds\n\t" \
        "buffer_load_dwordx4 %[vo], %[rw], %[s0] offen offset:3072 sc1 lds\n\t" \
        "s_movk_i32 m0, 0x1000\n\t" \
        "s_nop 0\n\t" \
        "buffer_load_dwordx4 %[vo], %[rw], %[s4] offen sc1 lds\n\t" \
        "buffer_load_dwordx4 %[vo], %[rw], %[s4] offen offset:1024 sc1 lds\n\t" \
        "buffer_load_dwordx4 %[vo], %[rw], %[s4] offen offset:2048 sc1 lds\n\t" \
        "buffer_load_dwordx4 %[vo], %[rw], %[s4] offen offset:3072 sc1 lds\n\t" \
        "s_movk_i32 m0, 0x2000\n\t" \
        "s_nop 0\n\t" \
        "buffer_load_dwordx4 %[vo], %[rw], %[s1] offen sc1 lds\n\t" \
        "buffer_load_dwordx4 %[vo], %[rw], %[s1] offen offset:1024 sc1 lds"
#define GATHER_R \
        "buffer_load_dwordx4 %[vo], %[rw], %[s1] offen offset:1024 sc1 lds\n\t" \
        "buffer_load_dwordx4 %[vo], %[rw], %[s1] offen sc1 lds\n\t" \
        "s_movk_i32 m0, 0x1000\n\t" \
        "s_nop 0\n\t" \
        "buffer_load_dwordx4 %[vo], %[rw], %[s4] offen offset:3072 sc1 lds\n\t" \
        "buffer_load_dwordx4 %[vo], %[rw], %[s4] offen offset:2048 sc1 lds\n\t" \
        "buffer_load_dwordx4 %[vo], %[rw], %[s4] offen offset:1024 sc1 lds\n\t" \
        "buffer_load_dwordx4 %[vo], %[rw], %[s4] offen sc1 lds\n\t" \
        "s_movk_i32 m0, 0\n\t" \
        "s_nop 0\n\t" \
        "buffer_load_dwordx4 %[vo], %[rw], %[s0] offen offset:3072 sc1 lds\n\t" \
        "buffer_load_dwordx4 %[vo], %[rw], %[s0] offen offset:2048 sc1 lds\n\t" \
        "buffer_load_dwordx4 %[vo], %[rw], %[s0] offen offset:1024 sc1 lds\n\t" \
        "buffer_load_dwordx4 %[vo], %[rw], %[s0] offen sc1 lds"
#define GATHER_IN [vo] "v"(vo), [rw] "s"(rwin), [s0] "s"(so0), [s4] "s"(so0 + 2u * PAIR), [s1] "s"(so0 + 4u * PAIR)
__device__ __forceinline__ void gather_dma(__amdgpu_buffer_rsrc_t rwin, unsigned vo, unsigned so0, bool rev) {
    if (rev) asm volatile("s_movk_i32 m0, 0x2000\n\ts_nop 0\n\t" GATHER_R :: GATHER_IN : "m0", "memory");
    else asm volatile("s_mov_b32 m0, 0\n\ts_nop 0\n\t" GATHER :: GATHER_IN : "m0", "memory");
}
__device__ __forceinline__ void gather_at(unsigned t, Clk& c, __amdgpu_buffer_rsrc_t rwin, unsigned vo, unsigned so0, bool rev) {
    unsigned tm, rp, x, fi;
    if (rev) asm volatile("s_movk_i32 m0, 0x2000\n\t" WAIT_FINE GATHER_R : WAIT_FINE_OUT : [cq] "s"(c.cpq), GATHER_IN : "vcc", "scc", "s100", "s101", "m0", "memory");
    else asm volatile("s_mov_b32 m0, 0\n\t" WAIT_FINE GATHER : WAIT_FINE_OUT : [cq] "s"(c.cpq), GATHER_IN : "vcc", "scc", "s100", "s101", "m0", "memory");
}
__device__ __forceinline__ void dma4k(__amdgpu_buffer_rsrc_t r, unsigned vo, unsigned so, unsigned lds) {
    asm volatile(
        "s_mov_b32 m0, %3\n\t"
        "s_nop 0\n\t"
        "buffer_load_dwordx4 %0, %1, %2 offen sc1 lds\n\t"
        "buffer_load_dwordx4 %0, %1, %2 offen offset:1024 sc1 lds\n\t"
        "buffer_load_dwordx4 %0, %1, %2 offen offset:2048 sc1 lds\n\t"
        "buffer_load_dwordx4 %0, %1, %2 offen offset:3072 sc1 lds"
        :: "v"(vo), "s"(r), "s"(so), "s"(lds) : "m0", "memory");
}
__device__ __forceinline__ int bs2(int c) { return (c & ~3) | ((c & 1) << 1) | ((c >> 1) & 1); }
__device__ __forceinline__ void gather_mem0(char* memL, int tid, int nt = 512) {
    const u16* mem0 = arg(A_MEM0);
    for (int c = tid; c < 640; c += nt) {
        const u32x2* src = (const u32x2*)(mem0 + ((c & 6) + ((c >> 3) & 1)) * D_ + (c >> 4) * 16 + (c & 1) * 4);
        const u32x2 x = src[0], y = src[2];
        *(u32x4*)(memL + c * 16) = (u32x4){(x[0] & 0xFFFFu) | (y[0] << 16), (x[0] >> 16) | (y[0] & 0xFFFF0000u), (x[1] & 0xFFFFu) | (y[1] << 16), (x[1] >> 16) | (y[1] & 0xFFFF0000u)};
    }
}
__device__ __forceinline__ f32x2 hadd(f32x4 c) { return (f32x2){c[0], c[1]} + (f32x2){c[2], c[3]}; }
__device__ __forceinline__ unsigned hpack(f32x4 c) { const f32x2 y = hadd(c); return pack2(y[0], y[1]); }
__device__ __forceinline__ u32x8 cat8(u32x4 lo, u32x4 hi) { return (u32x8){lo[0], lo[1], lo[2], lo[3], hi[0], hi[1], hi[2], hi[3]}; }
__device__ __forceinline__ u32x4 half8(const u32x8& v, int u) { return u ? (u32x4){v[4], v[5], v[6], v[7]} : (u32x4){v[0], v[1], v[2], v[3]}; }
__device__ __forceinline__ void dma1k(__amdgpu_buffer_rsrc_t r, unsigned vo, unsigned so, unsigned lds) {
    asm volatile("s_mov_b32 m0, %3\n\ts_nop 0\n\tbuffer_load_dwordx4 %0, %1, %2 offen sc1 lds" :: "v"(vo), "s"(r), "s"(so), "s"(lds) : "m0", "memory");
}
template <class FM> __device__ __forceinline__ void load_w(u32x8 (&wt)[2][10], const u16* src, unsigned rs, bool perm, unsigned b, unsigned c, int w, FM mid) {
    const __amdgpu_buffer_rsrc_t r = mkrsrc(src, 0x7FFFFFFFu);
    const unsigned l = fresh_lane(), q = l >> 4, lds = (unsigned)(w >> 2) * 65536u, v = w & 3;
    const unsigned vo0 = (perm ? ((q & 1u) << 3) | ((q & 2u) << 1) : q) * rs + ((l & 15u) ^ (q << 1)) * 16u, vo1 = vo0 ^ 128u, a = perm ? rs : 4u * rs;
    const LAS char* p0 = (const LAS char*)(size_t)(lds + q * 2048u + ((l >> 2) & 3u) * 256u + ((2u * v) ^ ((l >> 2) & 7u)) * 32u + (l & 3u) * 8u);
    const LAS char* p1 = (const LAS char*)((size_t)p0 ^ 32u);
    auto issue = [&](int g) {
#pragma unroll
        for (int i = 0; i < 4; ++i)
            asm volatile("s_mov_b32 m0, %3\n\ts_nop 3\n\tbuffer_load_dwordx4 %0, %1, %2 offen lds" :: "v"(i < 2 ? vo0 : vo1), "s"(r), "s"(i * a + (g & 1) * b + (g >> 1) * c), "s"(lds + (g & 3) * 16384u + v * 4096u + i * 1024u) : "m0", "memory");
    };
#pragma unroll
    for (int g = 0; g < 3; ++g) issue(g);
#pragma unroll
    for (int g = 0; g < 10; ++g) {
        asm volatile("s_waitcnt vmcnt(%0)\n\ts_barrier" :: "n"(g < 8 ? 8 : 4 * (9 - g)) : "memory");
        if (g + 3 < 10) issue(g + 3);
        const int o = (g & 3) * 16384;
        wt[0][g] = cat8(ldtr2(p0 + o, p0 + o + 1024), ldtr2(p0 + o + 8192, p0 + o + 9216));
        wt[1][g] = cat8(ldtr2(p1 + o, p1 + o + 1024), ldtr2(p1 + o + 8192, p1 + o + 9216));
        asm volatile("s_waitcnt lgkmcnt(0)" : "+v"(wt[0][g]), "+v"(wt[1][g]) :: "memory");
        if (g == 6) mid();
    }
    BAR0();
}
__device__ __forceinline__ int tile_of(int o) { return 8 * ((o % 10) >> 1) + 2 * (o / 10) + (o & 1); }
__device__ __forceinline__ void wait_tags(__amdgpu_buffer_rsrc_t rwin, int g0, int g1, unsigned n1, u64 val, bool boot) {
    const unsigned l = fresh_lane();
    unsigned o[4];
#pragma unroll
    for (int g = 0; g < 4; ++g) o[g] = g >= g0 && g < g1 && (g + 1 < g1 || l < n1) ? l * 128u + g * 8192u : 0x40000000u;
    for (unsigned spins = 0;;) {
        asm volatile("" ::: "memory");
        u32x2 v[4];
#pragma unroll
        for (int g = 0; g < 4; ++g) v[g] = __builtin_amdgcn_raw_buffer_load_b64(rwin, o[g], W_A1, 16);
#pragma unroll
        for (int g = 0; g < 4; ++g) if (__builtin_bit_cast(u64, v[g]) == val) o[g] = 0x40000000u;
        if (!__any((o[0] & o[1] & o[2] & o[3]) != 0x40000000u)) break;
        if (boot ? ++spins > SPIN_CAP : spin_fail(spins, rwin)) break;
        __builtin_amdgcn_s_sleep(ASLP);
    }
}
template <int N> __device__ __forceinline__ void stage_h(__amdgpu_buffer_rsrc_t rws, unsigned src, unsigned lds, unsigned st) {
    const unsigned l = fresh_lane() * 16u;
#pragma unroll
    for (int i = 0; i < N; ++i) dma1k(rws, l, src + i * st, lds + i * st);
}
template <int N> __device__ __forceinline__ void check_h(__amdgpu_buffer_rsrc_t rws, __amdgpu_buffer_rsrc_t rwin, unsigned src, unsigned lds, unsigned st) {
    const LAS char* p = (const LAS char*)smem + lds + fresh_lane() * 16u;
    for (unsigned spins = 0;;) {
        unsigned m = 0;
#pragma unroll
        for (int i = 0; i < N; ++i) { const u32x4 x = *(const volatile LAS u32x4*)(p + i * st); m = max(max(m, max(x[0], x[1])), max(x[2], x[3])); }
        if (__builtin_expect(!__any(m == ~0u), 1) || spin_fail(spins, rwin)) break;
        stage_h<N>(rws, src, lds, st);
        VMWAIT();
    }
}
__device__ __forceinline__ void wait_h(__amdgpu_buffer_rsrc_t rwin, unsigned fo, int seg, int& hrdy) {
    unsigned lim = 63u;
    if (__builtin_expect(hrdy <= seg, 0)) for (unsigned spins = 0;;) {
        asm volatile("" ::: "memory");
        const u32x4 f = __builtin_amdgcn_raw_buffer_load_b128(rwin, min(fresh_lane(), lim) * 128u, W_H + fo, 16);
        const unsigned long long miss = __ballot((f[0] | f[1] | f[2] | f[3]) != 0u);
        hrdy = miss ? __builtin_ctzll(miss) : (int)lim + 1;
        if (hrdy > seg) break;
        if (spin_fail(spins, rwin)) break;
        lim = (unsigned)min(seg + HLA, 63);
        if (seg < 8) __builtin_amdgcn_s_sleep(HSLP); else __builtin_amdgcn_s_sleep(HSL2);
    }
}
template <int CTRL> __device__ __forceinline__ unsigned dppu(unsigned v) { return (unsigned)__builtin_amdgcn_mov_dpp((int)v, CTRL, 0xF, 0xF, true); }
template <int CTRL> __device__ __forceinline__ u32x4 ror4(u32x4 v) { return (u32x4){dppu<CTRL>(v[0]), dppu<CTRL>(v[1]), dppu<CTRL>(v[2]), dppu<CTRL>(v[3])}; }
template <int N> struct IC { static constexpr int v = N; };
__device__ __forceinline__ float xmax16(float v) {
    auto q = __builtin_amdgcn_permlane16_swap(__float_as_uint(v), __float_as_uint(v), false, false);
    return fmx(__uint_as_float(q[0]), __uint_as_float(q[1]));
}
__device__ __forceinline__ float xsum16(float v) {
    auto q = __builtin_amdgcn_permlane16_swap(__float_as_uint(v), __float_as_uint(v), false, false);
    return __uint_as_float(q[0]) + __uint_as_float(q[1]);
}
__device__ __forceinline__ float addswap16(float a, float b) {
    auto r = __builtin_amdgcn_permlane16_swap(__float_as_uint(a), __float_as_uint(b), false, false);
    return __uint_as_float(r[0]) + __uint_as_float(r[1]);
}
#define LANES() \
    const int lane = (int)fresh_lane(), tid = w * 64 + lane, fr = lane & 15, fq = lane >> 4; \
    const int sidx = (lane & 2) ? 0xEEEE : 0x4444; \
    const int ma0 = fq * 256 + (fr & 1) * 128 + (fr >> 1) * 16; \
    const int prow = 16 * ((fr >> 2) & 1) + 4 * (fr >> 3) + (fr & 3), hoff = fr * 128; \
    (void)tid; (void)sidx; (void)ma0; (void)prow; (void)hoff

__device__ __forceinline__ int seg_after(unsigned long long m, int j) { m = j >= 63 ? 0ull : m >> (j + 1); return m ? j + 1 + __builtin_ctzll(m) : NSEG; }
__device__ __forceinline__ int seg_done(unsigned long long m, int f0, int f1, unsigned long long x = 0) { return min(seg_after(~m, f0), seg_after(m | x, f1)) - 1; }
constexpr unsigned long long MXA = 1ull << 40;
template <int OFF> __device__ __forceinline__ void ldfrag(u32x4& d, const LAS char* p) { asm volatile("ds_read_b128 %0, %1 offset:%2" : "=v"(d) : "v"(p), "n"(OFF) : "memory"); }
template <int U, class FM, class FV> __device__ __forceinline__ void project(const u32x8 (&wt)[2][10], f32x4 (&tc)[2][2], u32x4 (&bb)[8], const LAS char* c0, const LAS char* c1, const LAS char* n0, const LAS char* n1, FM mid, FV vm) {
    constexpr int t = U >> 2, k = U & 3, v = (U + 8) % 40;
    if (U == 32) mid();
    vm(IC<U>());
    const LAS char* p = U < 32 ? (k >> 1 ? c1 : c0) : (k >> 1 ? n1 : n0);
    if (U < 2) asm volatile("s_nop 0\n\ts_waitcnt lgkmcnt(7)\n\tv_mfma_f32_16x16x32_bf16 %0, %3, %2, 0\n\tv_mfma_f32_16x16x32_bf16 %1, %4, %2, 0\n\tds_read_b128 %2, %5 offset:%6"
                            : "=&v"(tc[0][k & 1]), "=&v"(tc[1][k & 1]), "+v"(bb[U & 7]) : "v"(half8(wt[0][t], k >> 1)), "v"(half8(wt[1][t], k >> 1)), "v"(p), "n"((v >> 2) * 4096 + (k & 1) * 2048) : "memory");
    else asm volatile(".rept %7\n\ts_nop 0\n\t.endr\n\ts_waitcnt lgkmcnt(7)\n\tv_mfma_f32_16x16x32_bf16 %0, %3, %2, %0\n\tv_mfma_f32_16x16x32_bf16 %1, %4, %2, %1\n\tds_read_b128 %2, %5 offset:%6"
                      : "+v"(tc[0][k & 1]), "+v"(tc[1][k & 1]), "+v"(bb[U & 7]) : "v"(half8(wt[0][t], k >> 1)), "v"(half8(wt[1][t], k >> 1)), "v"(p), "n"((v >> 2) * 4096 + (k & 1) * 2048), "n"(U < 8 ? 1 : 0) : "memory");
    if constexpr (U < 39) project<U + 1>(wt, tc, bb, c0, c1, n0, n1, mid, vm);
    else asm volatile("s_nop 7" : "+v"(tc[0][0]), "+v"(tc[0][1]), "+v"(tc[1][0]), "+v"(tc[1][1]));
}
__device__ __forceinline__ void project_end(u32x4 (&bb)[8]) {
    asm volatile("s_waitcnt lgkmcnt(0)" : "+v"(bb[0]), "+v"(bb[1]), "+v"(bb[2]), "+v"(bb[3]), "+v"(bb[4]), "+v"(bb[5]), "+v"(bb[6]), "+v"(bb[7]) :: "memory");
}
__device__ __forceinline__ void project0(u32x4 (&bb)[8], const LAS char* c0, const LAS char* c1) {
    ldfrag<0>(bb[0], c0); ldfrag<2048>(bb[1], c0); ldfrag<0>(bb[2], c1); ldfrag<2048>(bb[3], c1);
    ldfrag<4096>(bb[4], c0); ldfrag<6144>(bb[5], c0); ldfrag<4096>(bb[6], c1); ldfrag<6144>(bb[7], c1);
    project_end(bb);
}

__global__ __launch_bounds__(512, 2) B3ATTR void mega_kernel(Args a) {
    char* memL = smem;
    char* tokL = smem + L_TOK;
    char* hL = smem + L_H;
    int w = __builtin_amdgcn_readfirstlane(threadIdx.x >> 6);
    __amdgpu_buffer_rsrc_t rwin = mkrsrc(a.win, W_END);
    char* ws = a.ws;
    __amdgpu_buffer_rsrc_t rws = mkrsrc(ws, (unsigned)OFF_WIN);
    auto fresh_rsrc = [&]() INL { ws = (char*)arg(0); rws = mkrsrc(ws, (unsigned)OFF_WIN); rwin = mkrsrc(arg(1), W_END); };
    const char* pc;
    asm volatile("s_getpc_b64 %0" : "=s"(pc));
    unsigned fs[12] = {};
    const u32x2 tg0 = __builtin_amdgcn_raw_buffer_load_b64(rws, 0u, 0u, 16);
    u64 tag;
    u32x2 rot0;
    {
        const int tid = threadIdx.x;
        if (blockIdx.x < 64) {
            const unsigned j = blockIdx.x;
            if (j == 0 && w == 0) __builtin_amdgcn_raw_buffer_store_b64(__builtin_bit_cast(u32x2, (__builtin_bit_cast(u64, tg0) & ~15ull) + 16ull + (xcc_id() & 7u)), rws, LANE0, 128u, 16);
            const unsigned g0 = (j * 512u + tid) * 16u;
#pragma unroll
            for (int i = 0; i < 3; ++i) st128(SENT, mkrsrc(ws + O_ARM1, O_H + NHF * 40960u - O_ARM1), g0 + i * 524288u, 0, 16);
#pragma unroll
            for (int i = 0; i < 7; ++i) {
                const unsigned g = g0 + i * 524288u;
                st128(SENT, mkrsrc(a.win, W_END + 2 * PAIR), g + (g < W_ARM - W_LAYER ? 0u : W_ZM - (W_ARM - W_LAYER)), 0, 16);
            }
#pragma unroll
            for (int i = 0; i < 3; ++i) st128(SENT, mkrsrc(ws + O_Z, ZTSEG * 2), g0 + i * 524288u, 0, 16);
            static_assert(W_ARM - W_LAYER + W_END + 2 * PAIR - W_ZM <= 7u * 524288u && O_ARM1 + 3u * 524288u >= O_H + NHF * 40960u && ZTSEG * 2 <= 3u * 524288u, "arming");
        } else if (blockIdx.x >= 128) {
            RowTw tw;
            row_tw(tw, tid);
            a1_unit<8, false>(mkrsrc(a.x, S_ * D_ * 2), (blockIdx.x - 128) * 16, rws, rwin, tw, tid, fs);
        } else {
            const unsigned j = blockIdx.x - 64;
            FST(0);
            auto row = [&](unsigned i) { return (j & 7u) + 8u * (i & 15u) + 128u * ((j >> 3) & 3u) + 512u * (((j >> 4) & 2u) + ((i >> 4) & 1u)); };
            u32x4 v[6];
#pragma unroll
            for (int i = 0; i < 6; ++i) {
                const unsigned id = min(tid + 512 * i, 64 * 41 - 1), r = id / 41u;
                v[i] = ((const u32x4*)(r < 32u ? a.wr : a.wi))[row(r) * (DF * 2) / 16u + id - r * 41u];
            }
            u16 n[4];
            if (w == 7) {
                const size_t kn = (size_t)((S_ - row(tid)) & (S_ - 1)) * DF;
                n[0] = a.wr[kn]; n[1] = a.wi[kn]; n[2] = a.wr[kn + 320]; n[3] = a.wi[kn + 320];
            }
            const unsigned wo = WARM ? (w * 8u + (j >> 3)) * 8192u : 0x40000000u;
            const unsigned x0 = __builtin_amdgcn_raw_buffer_load_b32(mkrsrc(pc, CODE_BYTES), (tid & 63) * 128u, wo, 0), x1 = __builtin_amdgcn_raw_buffer_load_b32(mkrsrc(&TWC, sizeof(TwCol)), (tid & 63) * 128u, wo, 0);
#pragma unroll
            for (int i = 0; i < 6; ++i) ((u32x4*)smem)[min(tid + 512 * i, 64 * 41 - 1)] = v[i];
            FST(1);
            BAR();
            FST(2);
            const u16* q0 = (const u16*)smem + (j & 7u);
            u32x4 o[5];
#pragma unroll
            for (int i = 0; i < 5; ++i) {
                const u16* q = q0 + ((tid >> 1) & 7) * 328 + (tid >> 4) * 2 + (tid & 1) + 64 * i;
#pragma unroll
                for (int e = 0; e < 4; ++e) o[i][e] = (unsigned)q[e * 8 * 328] | ((unsigned)q[(32 + e * 8) * 328] << 16);
            }
#pragma unroll
            for (int i = 0; i < 5; ++i) st128(o[i], rws, (tid >> 4) * WST + (tid & 15) * 16u, O_WFT + i * 32u * WST + (j >> 5) * 8192u + (j & 7u) * 1024u + ((j >> 3) & 3u) * 256u, 16);
            if (w == 7) {
                const u16* q = q0 + (tid & 31) * 328;
                const unsigned k = row(tid);
                const c2 h0 = (c2){bf2f(q[0]) + bf2f(n[0]), bf2f(q[32 * 328]) - bf2f(n[1])} * bc(0.25f), h1 = (c2){bf2f(q[320]) + bf2f(n[2]), bf2f(q[32 * 328 + 320]) - bf2f(n[3])} * bc(0.25f);
                st128(__builtin_bit_cast(u32x4, (f32x4){h0.x + h1.x, h0.y + h1.y, h0.x - h1.x, h0.y - h1.y}), rws, (((k >> 8) & 6u) * 256u + (k & 64u) * 4u + (k & 7u) * 32u + ((k >> 4) & 24u) + ((k >> 3) & 7u)) * 16u, O_WFT + NCOL * WST, 16);
            }
            FST(3);
            asm volatile("" :: "v"(x0), "v"(x1));
            FST(4);
        }
        rot0 = __builtin_amdgcn_raw_buffer_load_b64(rws, 0u, 128u, 16);
        VMWAIT();
        FST(8);
        BAR0();
        FST(9);
    }
    tag = (((u64)__builtin_amdgcn_readfirstlane(tg0[1]) << 32) | (__builtin_amdgcn_readfirstlane(tg0[0]) & ~15u)) + 16ull;
    if (w == 0) __builtin_amdgcn_raw_buffer_store_b64(__builtin_bit_cast(u32x2, tag + 8ull), rwin, LANE0, W_A1 + blockIdx.x * 128u, 16);
    if (blockIdx.x >= 128) FSW(0); else if (blockIdx.x >= 64) FSW(6);
    unsigned vx;
    int bid;
    auto set_role = [&](unsigned x0) INL {
        vx = x0;
        const int j = blockIdx.x >> 3, x = vx & 3;
        if (!(vx & 4)) bid = j < 8 ? 8 * x + j : j < 16 ? 24 + 8 * x + j : j < 26 ? 80 + 10 * x + j : j < 30 ? NWRK + 4 * (j - 26) + x : NREC - 22 + 10 * x + j;
        else bid = j < 8 ? 64 + 8 * x + j : j < 16 ? NQT - 8 + 8 * x + j : j < 24 ? NREC - 16 + 10 * x + j : NXT - 24 + 8 * x + j;
    };
    auto get_rot = [&]() INL {
        unsigned r = 0;
        for (unsigned spins = 0; spins < SPIN_CAP; ++spins) {
            const u64 x = __builtin_bit_cast(u64, rot0) - tag;
            r = __builtin_amdgcn_readfirstlane((unsigned)x);
            if (__all(x < 8ull)) break;
            __builtin_amdgcn_s_sleep(2);
            asm volatile("" ::: "memory");
            rot0 = __builtin_amdgcn_raw_buffer_load_b64(rws, 0u, 128u, 16);
        }
        return (r + blockIdx.x + 4u * DIE) & 7u;
    };
    set_role(blockIdx.x < 64 ? (xcc_id() ^ (4u * DIE)) & 7u : get_rot());
    unsigned long long tref = 0;
    int layer = 0;
    unsigned fo = 0;
    unsigned nh = 0, nz = 0, mc = 0, qz = 0, mo = 0, tro = 0;
    auto near_init = [&]() INL {
        unsigned f;
        for (unsigned spins = 0;;) {
            asm volatile("" ::: "memory");
            f = __builtin_amdgcn_raw_buffer_load_b32(rwin, (fresh_lane() & 15u) * 4u, W_NEAR, 16);
            if (!__any(f == ~0u) || spin_fail(spins, rwin)) break;
            __builtin_amdgcn_s_sleep(8);
        }
        const unsigned h = (unsigned)__ballot(f & 1u), q = (unsigned)__ballot(f & 2u) ^ 0xAAAAu;
        auto most = [&](unsigned m) { return (__builtin_popcount(m & 0x0F0Fu) + 3u) >> 3; };
        nh = most(h) * HALF; nz = most(h >> 4) * HALF;
        mc = (vx & 3u) ^ most(q) ^ (STK ? 2u * most((unsigned)__ballot(f & 4u) ^ 0x3333u) : STX);
        qz = most(q >> 4);
        mo = W_MEM + (mc & 1u) * PAIR + nh + (mc >> 1) * 4096u;
        tro = mo - W_MEM + W_T;
    };
    auto zpair = [&](unsigned x, unsigned k) { return (((x >> 1) * 10u + k) * 2u + ((x & 1u) ^ qz)) * PAIR + nz; };
    auto two = [&]() { const unsigned l = fresh_lane(); return l < 4 ? (l >> 1) * PAIR + (l & 1) * 4096u + nh : 0x40000000u; };
    const f32x4 z4 = {0.f, 0.f, 0.f, 0.f};
    int hrdy = 0, budget = (int)SPIN_CAP;
    unsigned fc0 = 0, fc1 = 0, fc2 = 0;
    Clk ck = {0u, 0u, 1600u, 10486u};
    SST(5);
#define xt (bid >= NXT && bid < NWRK)
#define qx (bid >= NQT && bid < NWRK)
#define xq (bid >= NREC && bid < NQT && (bid - NREC) % 10 < 8)
    unsigned tpp = 0;
    int lt = LT1, xs = XS0;
    struct Tk { unsigned vo, tw, sl; const LAS char* sp; };
    auto tk_init = [&]() {
        const unsigned l = fresh_lane();
        Tk k = {l * 16u, two(), (l & 31u) * 128u + 8u - (l >> 5) * 4u, (const LAS char*)(memL + l * 64 + 4 + ((l >> 1) & 7) * 8)};
        PIN2(k.vo, k.tw); PIN2(k.sl, k.sp);
        return k;
    };
    auto sched2 = [&](Tk& k, int s, unsigned f) INL {
        const unsigned g0 = tpp - 0x400000u;
        tpp = g0 + (unsigned)read_sched(rwin, s, k.sl, tro + (unsigned)(s & 3) * 2u * PAIR, f, g0, lt, xs, fc1);
        k.sl &= ~12u;
        RT(6);
    };
    auto sched = [&](Tk& k, int s) INL { sched2(k, s, __builtin_amdgcn_raw_buffer_load_b32(rwin, k.sl, tro + (unsigned)(s & 3) * 2u * PAIR, 16)); };
    unsigned it = 0;
    auto fetch = [&](const Tk& k, int s, bool first, unsigned lim, unsigned dc, unsigned fo, bool tell) INL {
        const unsigned sb = (unsigned)(s & 3) * 6u * PAIR, ta0 = ck.ta, mm0 = ck.ma;
        if (__builtin_expect(s > 0, 1)) {
            if (__builtin_expect(first, 1)) { const unsigned te = cyc_at(ck, sx24(tpp - TKE - ck.ta)); while ((int)((unsigned)__builtin_amdgcn_s_memtime() - te) < 0) __builtin_amdgcn_s_sleep(1); }
            if (__builtin_expect(first, 1)) gather_at(tpp + (tell ? 0 : DGV), ck, rwin, k.vo, mo + sb, VREV && !tell); else { const unsigned c = (mc + 1u + (it & 1u)) & 3u; gather_dma(rwin, k.vo, W_MEM + (c & 1u) * PAIR + (c >> 1) * 4096u + nh + sb, VREV && !tell); }
        }
        __builtin_amdgcn_s_setprio(0);
        RT(1);
        clk_rate(ck, ta0, mm0);
        unsigned tb = ck.ma - dc, tg = (unsigned)s << 24, so = W_T + (unsigned)(s & 3) * 2u * PAIR + fo;
        asm volatile("" : "+s"(tb), "+s"(tg), "+s"(so));
        asm volatile(ALN "s_waitcnt vmcnt(%1) lgkmcnt(0)\n\ts_barrier\n\ts_waitcnt vmcnt(%2)\n\ts_barrier\n\ts_waitcnt vmcnt(0)\n\ts_memtime %0\n\ts_barrier"
                     : "=s"(tref) : "n"(10 - NB1), "n"(10 - NB2) : "memory");
        MT(1);
        if (!tell) { RT(3); return; }
        unsigned x0 = *(const LAS unsigned*)k.sp, x1 = *(const LAS unsigned*)(k.sp + 4096), x2 = *(const LAS unsigned*)(k.sp + 6144);
        __builtin_amdgcn_s_waitcnt(0xC07F);
        asm volatile("" : "+s"(tref) :: "memory");
        const unsigned fw = ((ck.ta + ((((unsigned)tref - tb) * ck.qpc) >> 16)) & 0xFFFFFFu) | tg;
        __builtin_amdgcn_raw_buffer_store_b32(fw, rwin, k.tw, __any(max(max(x0, x1), x2) > lim) ? 0x40000000u : so, 16);
        RT(3);
    };
    auto rout = [&]() { return mkrsrc(layer ? arg(A_OUT) : (const u16*)(ws + OFF_X2), S_ * D_ * 2); };
    auto rec_end = [&](unsigned g) INL {
        if (g) for (unsigned spins = 0;;) {
            asm volatile("" ::: "memory");
            if (__builtin_amdgcn_readfirstlane(__builtin_amdgcn_raw_buffer_load_b32(rws, 0u, g == 3 ? O_FL + (unsigned)(bid & 127) * GST + 4092u : O_FL - g * SEG * D_ * 2u + (unsigned)bid * 128u, 16)) != -1 || spin_fail(spins, rwin)) break;
            __builtin_amdgcn_s_sleep(16);
        }
    };
    auto mytid = [&]() { return w * 64 + (int)fresh_lane(); };
    auto new_layer = [&]() { fo = (unsigned)layer * W_LAYER; asm volatile("" : "+s"(fo), "+s"(w), "+s"(bid), "+s"(nh), "+s"(nz), "+s"(mc), "+s"(qz), "+s"(tag)); hrdy = 0; tpp = 0; lt = LT1; xs = XS0; };
    auto load_qv = [&](u32x8 (&wt)[2][10], int hh, bool v, auto mid) INL {
        const bool o = v && w >= 4;
        load_w(wt, arg(A_WQ + 2 * v + (w >> 2)) + (o ? (hh * 128 + (w & 3) * 32) * D_ : hh * 128 + (w & 3) * 16 * HD), o ? D_ * 2 : HD * 2, !o, o ? 16 * D_ * 2 : 64 * HD * 2, o ? 256 : 128 * HD * 2, w, mid);
    };
    auto hseg = [&](int j) { return O_H + (unsigned)(j - NHF * layer) * 40960u; };
    auto stage2 = [&](int j0, int j1, bool vt) INL {
        if (w == 0) {
            if (j1 >= NHF) wait_h(rwin, fo, j1, hrdy);
            else if (TH) { const unsigned vo[1] = {(fresh_lane() & 3u) * 4u}; wait_told(rwin, vo, W_H + fo + (unsigned)j0 * 128u + 16u, 1); }
            else for (unsigned spins = 0;;) {
                asm volatile("" ::: "memory");
                const unsigned l = fresh_lane();
                if (!__any(__builtin_amdgcn_raw_buffer_load_b32(rws, (l & 31u) * 128u + (l >> 5) * (unsigned)(j1 - j0) * 40960u, hseg(j0) + 36864u, 16) == ~0u) || spin_fail(spins, rwin)) break;
                __builtin_amdgcn_s_sleep(HSLP);
            }
            *(volatile LAS int*)(smem + L_S + 32) = hrdy;
        }
        BAR();
        hrdy = __builtin_amdgcn_readfirstlane(*(volatile LAS int*)(smem + L_S + 32));
        SST(2);
        if (j0 == 1 && vt) __builtin_amdgcn_s_sleep(DH1);
        stage_h<5>(rws, hseg(j0) + w * 1024u, L_H + w * 1024u, 8192u);
        if (!vt) {
            stage_h<5>(rws, hseg(j1) + w * 1024u, L_H + 40960u + w * 1024u, 8192u);
            asm volatile("s_waitcnt vmcnt(5)" ::: "memory");
        } else if (w >= 4 && j1 != j0) {
            stage_h<10>(rws, hseg(j1) + (w - 4) * 1024u, L_H + 40960u + (w - 4) * 1024u, 4096u);
            asm volatile("s_waitcnt vmcnt(10)" ::: "memory");
        } else VMWAIT();
        check_h<5>(rws, rwin, hseg(j0) + w * 1024u, L_H + w * 1024u, 8192u);
        BAR0();
        SST(3);
        if (vt && w >= 4 && j1 != j0) {
            VMWAIT();
            check_h<10>(rws, rwin, hseg(j1) + (w - 4) * 1024u, L_H + 40960u + (w - 4) * 1024u, 4096u);
        }
    };
#define WO(eb, nt) wt[(nt) & 1][((nt) & ~1) + (eb)]
    unsigned zb = W_ZM;
    auto probe = [&]() INL {
        {
            unsigned* best = (unsigned*)smem;
            const int tid = mytid();
            const __amdgpu_buffer_rsrc_t rw = mkrsrc(a.win + (bid & 4 ? W_END : W_PROBE), 2 * PAIR);
            const unsigned base = (unsigned)(bid & 8) * 64u + w * 1024u;
            if (tid < 8) best[tid] = 100000u;
            if (w == 0) wait_tags(rwin, 0, 1, 64u, tag + 8ull, true);
            BAR();
            unsigned mn[4] = {~0u, ~0u, ~0u, ~0u};
            for (int r = 0; r < NPR; ++r) {
#pragma unroll
                for (int i = 0; i < 4; ++i) {
                    const unsigned long long t1 = __builtin_amdgcn_s_memtime();
                    asm volatile("s_waitcnt lgkmcnt(0)" ::: "memory");
                    unsigned v = __builtin_amdgcn_raw_buffer_load_b32(rw, base + r * 128u + i * HALF, 0, 16);
                    asm volatile("s_waitcnt vmcnt(0)" : "+v"(v) :: "memory");
                    const unsigned long long t2 = __builtin_amdgcn_s_memtime();
                    mn[i] = min(mn[i], (unsigned)(t2 - t1));
                }
            }
            if ((tid & 63) == 0) {
#pragma unroll
                for (int i = 0; i < 4; ++i) atomicMin(&best[i + (w & 4)], mn[i]);
            }
            BAR();
            const unsigned h = min(min(best[1], best[3]), min(best[5], best[7])) < min(min(best[0], best[2]), min(best[4], best[6])), q = best[h + 2] + best[h + 6] < best[h] + best[h + 4], k = best[h + 4] + best[h + 6] < best[h] + best[h + 2];
            if (w == 0) __builtin_amdgcn_raw_buffer_store_b32(h + 2u * q + 4u * k, rwin, LANE0, W_NEAR + (unsigned)(bid & 15) * 4u, 16);
            BAR();
        }
    };
    auto vo_role = [&](const u32x8 (&wt)[2][10]) INL {
        const int hh = bid & 31;
        auto steps = [&](auto cc) INL {
            constexpr int C = decltype(cc)::v & 1;
            constexpr bool TK = decltype(cc)::v == 3;
            LANES();
            Tk tk = {0u, 0u, 0x40000000u, nullptr};
            if (TK) { tk = tk_init(); __builtin_amdgcn_s_setprio(TKP); }
            const unsigned zi = (unsigned)(hh * 128 + fr * 8) * 2u;
            unsigned zo0 = zpair(w & 3, fq) + zi, zo1 = zo0 + 8 * PAIR, zo2 = zpair(w & 3, 8 + (fq >> 1)) + zi + (fq & 1) * 8;
            const LAS char* va = (const LAS char*)(smem + L_Q + (fr & 1) * 544 + ((fr >> 1) & 1) * 1152 + (16 * fq + 2 * (fr >> 2)) * 4);
            LAS char* vw = (LAS char*)(smem + L_Q + (fq & 1) * 544 + (fq >> 1) * 1152 + (16 * w + bs2(fr)) * 4);
            const LAS char* mp0 = (const LAS char*)(memL + ma0);
            PIN2(zo0, zo1); PIN1(zo2);
            if (C == 0) { PIN2(mp0, vw); } else PIN1(va);
            u32x4 za = SENT, zc = SENT;
            u32x2 ze = {0u, 0u};
            auto put_z2 = [&](int s) {
                const unsigned z2b = s < NSEG ? O_Z2 + (unsigned)s * (NOWN * 8192u) + (unsigned)(10 * (w & 3) * 8192 + hh * 256) : 0x40000000u, l2 = fresh_lane();
                st128(za, rws, (l2 >> 4) * 8192u + (l2 & 15u) * 16u, z2b, 16);
                st128(zc, rws, (l2 >> 4) * 8192u + (l2 & 15u) * 16u, z2b + 4u * 8192u, 16);
                __builtin_amdgcn_raw_buffer_store_b64(ze, rws, (l2 >> 5) * 8192u + (l2 & 15u) * 16u + ((l2 >> 4) & 1u) * 8u, z2b + 8u * 8192u, 16);
            };
            for (int s = 0; s < NSEG; ++s) {
                RT(0);
                bool redo = false;
                unsigned sf = 0;
                do {
                    ++it;
                    if (C == 0) {
                        const unsigned lim = s > 0 ? poll_lim(budget) : ~0u;
                        asm volatile(
                            "v_mov_b64 v[200:201], 0\n\t"
                            "v_mov_b64 v[202:203], 0\n\t"
                            "v_mov_b64 v[204:205], 0\n\t"
                            "v_mov_b64 v[206:207], 0\n\t"
                            "v_mov_b32 v208, 0\n\t"
                            "s_setprio 2\n\t"
                            ALN
                            "s_waitcnt lgkmcnt(0)\n\t"
                            "s_barrier\n\t"
                            MTA(0)
                            "ds_read_b128 v[212:215], %[mp] offset:" VOFF0 "\n\t"
                            "ds_read_b128 v[216:219], %[mp] offset:" VOFF1 "\n\t"
                            "ds_read_b128 v[220:223], %[mp] offset:" VOFF2 "\n\t"
                            "s_barrier\n\t"
                            "ds_read_b128 v[224:227], %[mp] offset:" VOFF3 "\n\t"
                            "ds_read_b128 v[228:231], %[mp] offset:" VOFF4 "\n\t"
                            "ds_read_b128 v[232:235], %[mp] offset:" VOFF5 "\n\t"
                            "s_waitcnt lgkmcnt(5)\n\t"
                            "v_smfmac_f32_16x16x64_bf16 v[200:203], v[212:215], %[w00], %[ix]\n\t"
                            "s_waitcnt lgkmcnt(4)\n\t"
                            "v_smfmac_f32_16x16x64_bf16 v[200:203], v[216:219], %[w01], %[ix]\n\t"
                            "s_barrier\n\t"
                            MTA(1)
                            "ds_read_b128 v[236:239], %[mp] offset:" VOFF6 "\n\t"
                            "s_waitcnt lgkmcnt(4)\n\t"
                            "v_smfmac_f32_16x16x64_bf16 v[200:203], v[220:223], %[w02], %[ix]\n\t"
                            "ds_read_b128 v[240:243], %[mp] offset:" VOFF7 "\n\t"
                            "s_waitcnt lgkmcnt(4)\n\t"
                            "v_smfmac_f32_16x16x64_bf16 v[200:203], v[224:227], %[w03], %[ix]\n\t"
                            "ds_read_b128 v[244:247], %[mp] offset:" VOFF8 "\n\t"
                            "s_waitcnt lgkmcnt(4)\n\t"
                            "v_smfmac_f32_16x16x64_bf16 v[200:203], v[228:231], %[w04], %[ix]\n\t"
                            "ds_read_b128 v[248:251], %[mp] offset:" VOFF9 "\n\t"
                            "s_waitcnt lgkmcnt(4)\n\t"
                            "v_smfmac_f32_16x16x64_bf16 v[200:203], v[232:235], %[w05], %[ix]\n\t"
                            "s_waitcnt lgkmcnt(3)\n\t"
                            "v_smfmac_f32_16x16x64_bf16 v[200:203], v[236:239], %[w06], %[ix]\n\t"
                            "s_waitcnt lgkmcnt(2)\n\t"
                            "v_smfmac_f32_16x16x64_bf16 v[200:203], v[240:243], %[w07], %[ix]\n\t"
                            "s_waitcnt lgkmcnt(1)\n\t"
                            "v_smfmac_f32_16x16x64_bf16 v[200:203], v[244:247], %[w08], %[ix]\n\t"
                            "s_waitcnt lgkmcnt(0)\n\t"
                            "v_smfmac_f32_16x16x64_bf16 v[200:203], v[248:251], %[w09], %[ix]\n\t"
                            "s_nop 2\n\t"
                            "v_smfmac_f32_16x16x64_bf16 v[204:207], v[212:215], %[w10], %[ix]\n\t"
                            "s_nop 2\n\t"
                            "v_smfmac_f32_16x16x64_bf16 v[204:207], v[216:219], %[w11], %[ix]\n\t"
                            "v_add_f32 v200, v200, v202\n\t"
                            "v_add_f32 v201, v201, v203\n\t"
                            "v_cvt_pk_bf16_f32 v200, v200, v201\n\t"
                            "ds_write_b32 %[vw], v200\n\t"
                            MTA(2)
                            "v_smfmac_f32_16x16x64_bf16 v[204:207], v[220:223], %[w12], %[ix]\n\t"
                            "v_max3_u32 v208, v208, v212, v213\n\t"
                            "v_max3_u32 v208, v208, v214, v215\n\t"
                            "v_smfmac_f32_16x16x64_bf16 v[204:207], v[224:227], %[w13], %[ix]\n\t"
                            "v_max3_u32 v208, v208, v216, v217\n\t"
                            "v_max3_u32 v208, v208, v218, v219\n\t"
                            "v_smfmac_f32_16x16x64_bf16 v[204:207], v[228:231], %[w14], %[ix]\n\t"
                            "v_max3_u32 v208, v208, v220, v221\n\t"
                            "v_max3_u32 v208, v208, v222, v223\n\t"
                            "v_smfmac_f32_16x16x64_bf16 v[204:207], v[232:235], %[w15], %[ix]\n\t"
                            "v_max3_u32 v208, v208, v224, v225\n\t"
                            "v_max3_u32 v208, v208, v226, v227\n\t"
                            "v_smfmac_f32_16x16x64_bf16 v[204:207], v[236:239], %[w16], %[ix]\n\t"
                            "v_max3_u32 v208, v208, v228, v229\n\t"
                            "v_max3_u32 v208, v208, v230, v231\n\t"
                            "v_smfmac_f32_16x16x64_bf16 v[204:207], v[240:243], %[w17], %[ix]\n\t"
                            "s_waitcnt lgkmcnt(0)\n\t"
                            "s_barrier\n\t"
                            MTA(5)
                            "v_smfmac_f32_16x16x64_bf16 v[204:207], v[244:247], %[w18], %[ix]\n\t"
                            "v_max3_u32 v208, v208, v232, v233\n\t"
                            "v_max3_u32 v208, v208, v234, v235\n\t"
                            "v_smfmac_f32_16x16x64_bf16 v[204:207], v[248:251], %[w19], %[ix]\n\t"
                            "v_max3_u32 v208, v208, v236, v237\n\t"
                            "v_max3_u32 v208, v208, v238, v239\n\t"
                            "v_max3_u32 v208, v208, v240, v241\n\t"
                            "v_max3_u32 v208, v208, v242, v243\n\t"
                            "v_max3_u32 v208, v208, v244, v245\n\t"
                            "v_max3_u32 v208, v208, v246, v247\n\t"
                            "v_max3_u32 v208, v208, v248, v249\n\t"
                            "v_max3_u32 v208, v208, v250, v251\n\t"
                            "v_cmp_lt_u32 vcc, %[lim], v208\n\t"
                            "v_add_f32 v204, v204, v206\n\t"
                            "v_add_f32 v205, v205, v207\n\t"
                            "v_cvt_pk_bf16_f32 v204, v204, v205\n\t"
                            "ds_write_b32 %[vw], v204 offset:256\n\t"
                            MTA(3)
                            "s_cbranch_vccz 1f\n\t"
                            "v_mov_b32 v208, %[it]\n\t"
                            "v_mov_b32 v209, 0\n\t"
                            "ds_write_b32 v209, v208 offset:%[ls]\n\t"
                            "1:\n\t"
                            "s_waitcnt vmcnt(0) lgkmcnt(0)\n\t"
                            "s_barrier\n\t"
                            "s_setprio 0\n\t"
                            :: [mp] "v"(mp0), [vw] "v"(vw), [ix] "v"(sidx), [lim] "s"(lim), [it] "s"(it), [ls] "n"(L_S),
                              [w00] "v"(wt[0][VREV ? 9 - 0 : 0]), [w01] "v"(wt[0][VREV ? 9 - 1 : 1]), [w02] "v"(wt[0][VREV ? 9 - 2 : 2]), [w03] "v"(wt[0][VREV ? 9 - 3 : 3]), [w04] "v"(wt[0][VREV ? 9 - 4 : 4]), [w05] "v"(wt[0][VREV ? 9 - 5 : 5]), [w06] "v"(wt[0][VREV ? 9 - 6 : 6]), [w07] "v"(wt[0][VREV ? 9 - 7 : 7]), [w08] "v"(wt[0][VREV ? 9 - 8 : 8]), [w09] "v"(wt[0][VREV ? 9 - 9 : 9]), [w10] "v"(wt[1][VREV ? 9 - 0 : 0]), [w11] "v"(wt[1][VREV ? 9 - 1 : 1]), [w12] "v"(wt[1][VREV ? 9 - 2 : 2]), [w13] "v"(wt[1][VREV ? 9 - 3 : 3]), [w14] "v"(wt[1][VREV ? 9 - 4 : 4]), [w15] "v"(wt[1][VREV ? 9 - 5 : 5]), [w16] "v"(wt[1][VREV ? 9 - 6 : 6]), [w17] "v"(wt[1][VREV ? 9 - 7 : 7]), [w18] "v"(wt[1][VREV ? 9 - 8 : 8]), [w19] "v"(wt[1][VREV ? 9 - 9 : 9])
                            : "v200", "v201", "v202", "v203", "v204", "v205", "v206", "v207", "v208", "v209", "v212", "v213", "v214", "v215", "v216", "v217", "v218", "v219", "v220", "v221", "v222", "v223", "v224", "v225", "v226", "v227", "v228", "v229", "v230", "v231", "v232", "v233", "v234", "v235", "v236", "v237", "v238", "v239", "v240", "v241", "v242", "v243", "v244", "v245", "v246", "v247", "v248", "v249", "v250", "v251", "vcc", "memory");
                        MT(4);
                        redo = __builtin_amdgcn_readfirstlane(*(volatile LAS unsigned*)(smem + L_S)) == it;
                        if (redo) FC(fc0);
                    } else {
                        f32x16 ca, cb;
                        f32x8 cc;
                        asm volatile("v_mov_b64 v[212:213], 0\n\tv_mov_b64 v[214:215], 0\n\tv_mov_b64 v[216:217], 0\n\tv_mov_b64 v[218:219], 0\n\tv_mov_b64 v[220:221], 0\n\tv_mov_b64 v[222:223], 0\n\tv_mov_b64 v[224:225], 0\n\tv_mov_b64 v[226:227], 0\n\tv_mov_b64 v[228:229], 0\n\tv_mov_b64 v[230:231], 0\n\tv_mov_b64 v[232:233], 0\n\tv_mov_b64 v[234:235], 0\n\tv_mov_b64 v[236:237], 0\n\tv_mov_b64 v[238:239], 0\n\tv_mov_b64 v[240:241], 0\n\tv_mov_b64 v[242:243], 0\n\tv_mov_b64 v[244:245], 0\n\tv_mov_b64 v[246:247], 0\n\tv_mov_b64 v[248:249], 0\n\tv_mov_b64 v[250:251], 0\n\t" : "={v[212:227]}"(ca), "={v[228:243]}"(cb), "={v[244:251]}"(cc));
                        unsigned sr = tro + (unsigned)(s & 3) * 2u * PAIR;
                        asm volatile("" : "+s"(sr));
                        if (TK) fetch(tk, s, !redo, s > 0 ? poll_lim(budget) : ~0u, 0u, 0u, false);
                        else asm volatile(ALN "s_waitcnt lgkmcnt(0)\n\ts_barrier\n\ts_barrier\n\ts_barrier" ::: "memory");
                        unsigned vd, vv, zs = zb;
                        u32x4 af1;
                        asm volatile(
                            "s_setprio 1\n\t"
                            "s_waitcnt lgkmcnt(0)\n\t"
                            "s_barrier\n\t"
                            "ds_read_b64_tr_b16 v[192:193], %[va]\n\t"
                            "ds_read_b64_tr_b16 v[194:195], %[va] offset:32\n\t"
                            "s_waitcnt lgkmcnt(0)\n\t"
                            "v_smfmac_f32_16x16x64_bf16 v[212:215], v[192:195], %[w0], %[ix]\n\t"
                            "v_smfmac_f32_16x16x64_bf16 v[220:223], v[192:195], %[w2], %[ix]\n\t"
                            "v_smfmac_f32_16x16x64_bf16 v[216:219], v[192:195], %[w1], %[ix]\n\t"
                            "v_smfmac_f32_16x16x64_bf16 v[224:227], v[192:195], %[w3], %[ix]\n\t"
                            "s_barrier\n\t"
                            MTA(4)
                            "ds_read_b64_tr_b16 v[196:197], %[va] offset:256\n\t"
                            "v_smfmac_f32_16x16x64_bf16 v[228:231], v[192:195], %[w4], %[ix]\n\t"
                            "ds_read_b64_tr_b16 v[198:199], %[va] offset:288\n\t"
                            "v_smfmac_f32_16x16x64_bf16 v[236:239], v[192:195], %[w6], %[ix]\n\t"
                            "ds_read_b32 v210, %[vz] offset:%[ls]\n\t"
                            "v_smfmac_f32_16x16x64_bf16 v[232:235], v[192:195], %[w5], %[ix]\n\t"
                            "v_smfmac_f32_16x16x64_bf16 v[240:243], v[192:195], %[w7], %[ix]\n\t"
                            "v_smfmac_f32_16x16x64_bf16 v[244:247], v[192:195], %[w8], %[ix]\n\t"
                            "v_smfmac_f32_16x16x64_bf16 v[248:251], v[192:195], %[w9], %[ix]\n\t"
                            "buffer_load_dword %[sf], %[sl], %[rw], %[sr] offen sc1\n\t"
                            : [sf] "=&v"(sf), "+{v[212:227]}"(ca), "+{v[228:243]}"(cb), "+{v[244:251]}"(cc), "=&{v[196:199]}"(af1), "=&{v210}"(vv)
                            : [va] "v"(va), [vz] "v"(0u), [ls] "n"(L_S), [ix] "v"(sidx), [sl] "v"(tk.sl), [sr] "s"(sr), [rw] "s"(rwin),
                              [w0] "v"(WO(0, 0)), [w1] "v"(WO(0, 1)), [w2] "v"(WO(0, 2)), [w3] "v"(WO(0, 3)), [w4] "v"(WO(0, 4)), [w5] "v"(WO(0, 5)), [w6] "v"(WO(0, 6)), [w7] "v"(WO(0, 7)), [w8] "v"(WO(0, 8)), [w9] "v"(WO(0, 9))
                            : "v192", "v193", "v194", "v195", "memory");
                        asm volatile(
                            "s_waitcnt lgkmcnt(0)\n\t"
                            "v_smfmac_f32_16x16x64_bf16 v[212:215], v[196:199], %[w0], %[ix]\n\t"
                            "v_readfirstlane_b32 %[vd], v210\n\t"
                            "v_smfmac_f32_16x16x64_bf16 v[220:223], v[196:199], %[w2], %[ix]\n\t"
                            "s_cmp_eq_u32 %[vd], %[it]\n\t"
                            "s_cselect_b32 %[zs], 2.0, %[zs]\n\t"
                            "v_smfmac_f32_16x16x64_bf16 v[216:219], v[196:199], %[w1], %[ix]\n\t"
                            "s_nop 1\n\t"
                            "v_smfmac_f32_16x16x64_bf16 v[224:227], v[196:199], %[w3], %[ix]\n\t"
                            "v_add_f32 v212, v212, v214\n\t"
                            "v_smfmac_f32_16x16x64_bf16 v[228:231], v[196:199], %[w4], %[ix]\n\t"
                            "v_add_f32 v213, v213, v215\n\t"
                            "v_smfmac_f32_16x16x64_bf16 v[236:239], v[196:199], %[w6], %[ix]\n\t"
                            "v_add_f32 v220, v220, v222\n\t"
                            "v_smfmac_f32_16x16x64_bf16 v[232:235], v[196:199], %[w5], %[ix]\n\t"
                            "v_add_f32 v221, v221, v223\n\t"
                            "v_smfmac_f32_16x16x64_bf16 v[240:243], v[196:199], %[w7], %[ix]\n\t"
                            "v_cvt_pk_bf16_f32 v200, v212, v213\n\t"
                            "v_smfmac_f32_16x16x64_bf16 v[244:247], v[196:199], %[w8], %[ix]\n\t"
                            "v_cvt_pk_bf16_f32 v201, v220, v221\n\t"
                            "v_smfmac_f32_16x16x64_bf16 v[248:251], v[196:199], %[w9], %[ix]\n\t"
                            "v_add_f32 v216, v216, v218\n\t"
                            "v_add_f32 v217, v217, v219\n\t"
                            "v_add_f32 v224, v224, v226\n\t"
                            "v_add_f32 v225, v225, v227\n\t"
                            "v_cvt_pk_bf16_f32 v202, v216, v217\n\t"
                            "v_cvt_pk_bf16_f32 v203, v224, v225\n\t"
                            "v_permlane32_swap_b32 v200, v201\n\t"
                            "v_pk_add_f32 v[228:229], v[228:229], v[230:231]\n\t"
                            "v_permlane32_swap_b32 v202, v203\n\t"
                            "v_min_u32 v201, -2, v201\n\t"
                            "v_pk_add_f32 v[236:237], v[236:237], v[238:239]\n\t"
                            "v_min_u32 v203, -2, v203\n\t"
                            "v_permlane16_swap_b32 v200, v202\n\t"
                            "v_pk_add_f32 v[232:233], v[232:233], v[234:235]\n\t"
                            "v_permlane16_swap_b32 v201, v203\n\t"
                            "v_pk_add_f32 v[240:241], v[240:241], v[242:243]\n\t"
                            MTA(5)
                            "buffer_store_dwordx4 v[200:203], %[z0], %[rw], %[zs] offen sc1\n\t"
                            "v_cvt_pk_bf16_f32 v204, v228, v229\n\t"
                            "v_cvt_pk_bf16_f32 v205, v236, v237\n\t"
                            "v_cvt_pk_bf16_f32 v206, v232, v233\n\t"
                            "v_cvt_pk_bf16_f32 v207, v240, v241\n\t"
                            "v_permlane32_swap_b32 v204, v205\n\t"
                            "v_pk_add_f32 v[244:245], v[244:245], v[246:247]\n\t"
                            "v_permlane32_swap_b32 v206, v207\n\t"
                            "v_min_u32 v205, -2, v205\n\t"
                            "v_pk_add_f32 v[248:249], v[248:249], v[250:251]\n\t"
                            "v_min_u32 v207, -2, v207\n\t"
                            "v_permlane16_swap_b32 v204, v206\n\t"
                            "v_cvt_pk_bf16_f32 v208, v244, v245\n\t"
                            "v_permlane16_swap_b32 v205, v207\n\t"
                            "v_cvt_pk_bf16_f32 v209, v248, v249\n\t"
                            "buffer_store_dwordx4 v[204:207], %[z1], %[rw], %[zs] offen sc1\n\t"
                            "s_nop 0\n\t"
                            "v_permlane32_swap_b32 v208, v209\n\t"
                            "v_min_u32 v209, -2, v209\n\t"
                            "buffer_store_dwordx2 v[208:209], %[z2], %[rw], %[zs] offen sc1\n\t"
                            : [vd] "=&s"(vd), [zs] "+s"(zs), "=&{v[200:203]}"(za), "=&{v[204:207]}"(zc), "=&{v[208:209]}"(ze), "+{v[212:227]}"(ca), "+{v[228:243]}"(cb), "+{v[244:251]}"(cc), "+v"(sf)
                            : [ix] "v"(sidx), [it] "s"(it), [rw] "s"(rwin), [z0] "v"(zo0), [z1] "v"(zo1), [z2] "v"(zo2), "{v[196:199]}"(af1), "{v210}"(vv),
                              [w0] "v"(WO(1, 0)), [w1] "v"(WO(1, 1)), [w2] "v"(WO(1, 2)), [w3] "v"(WO(1, 3)), [w4] "v"(WO(1, 4)), [w5] "v"(WO(1, 5)), [w6] "v"(WO(1, 6)), [w7] "v"(WO(1, 7)), [w8] "v"(WO(1, 8)), [w9] "v"(WO(1, 9))
                            : "scc", "memory");
                        redo = vd == it;
                        RT(4);
                        MT(6);
                        tref = __builtin_amdgcn_s_memtime();
                        __builtin_amdgcn_s_setprio(TKP);
                        asm volatile("s_waitcnt vmcnt(3)" : "+v"(sf) :: "memory");
                    }
                } while (__builtin_expect(redo, 0));
                if (s < 8) SST(8 + s);
                if (C == 0) {
                    if (s > 0 && s + 1 < NSEG) {
                        __builtin_amdgcn_s_sleep(DZR);
                        const unsigned zq = zb == W_ZM ? W_ZM + 2u * NOWN * PAIR : zb - NOWN * PAIR;
                        unsigned s1;
                        asm volatile("v_mov_b32 %0, -1" : "=v"(s1));
                        st128(((u32x4){s1, s1, s1, s1}), rwin, zo0, zq, 16);
                        st128(((u32x4){s1, s1, s1, s1}), rwin, zo1, zq, 16);
                        __builtin_amdgcn_raw_buffer_store_b64((u32x2){s1, s1}, rwin, zo2, zq, 16);
                    }
                } else {
                    if (s > TAILS) __builtin_amdgcn_raw_buffer_store_b32((unsigned)s - 1u, rwin, 0u, W_FZ2 + fo + (unsigned)(hh * 128 + (w & 3) * 4), 16);
                    if (TK && __builtin_expect(s == 0, 0)) { wait_h(rwin, fo, 5, hrdy); clk_init(ck); }
                    if (TK && s + 1 < NSEG) sched2(tk, s, sf);
                    if (!TK) wait_until(tref + DZ2);
                    if (s + 2 < NSEG) put_z2(s);
                }
                if (budget <= 0) set_tmo(rwin);
                zb = zb == W_ZM + 2u * NOWN * PAIR ? W_ZM : zb + NOWN * PAIR;
                RT(5);
                RTS();
                MT(7);
                MTS();
            }
        };
        const int tid = mytid();
        gather_mem0(memL, tid);
        if (tid < 8) *(unsigned*)(smem + L_S + tid * 4) = 0u;
        if (w < 4) steps(IC<0>()); else if (w < 7) steps(IC<1>()); else steps(IC<3>());
    };
    auto vt_role = [&](const u32x8 (&wt)[2][10], int hh, unsigned long long mv) INL {
        constexpr bool vt = true;
        const int jl = 63 - __builtin_clzll(mv);
        auto nextv = [&](int j) { return min(seg_after(mv, j), jl); };
        const int jv1 = nextv(-1), jv2 = nextv(jv1);
        if (w == 0) *(unsigned*)(smem + L_S + (fresh_lane() & 7u) * 4) = 0u;
        stage2(jv1, jv2, true);
        {
            LANES();
            const int ni = __builtin_popcountll(mv) + 1;
            if (w < 4) {
                const LAS char* c0 = (const LAS char*)(hL + hoff + ((fq ^ (fr & 7)) << 4));
                const LAS char* c1 = (const LAS char*)(hL + hoff + (((4 + fq) ^ (fr & 7)) << 4));
                const LAS char* n0 = c0 + 40960; const LAS char* n1 = c1 + 40960;
                u32x4 bb[8];
                project0(bb, c0, c1);
                for (int i = 1; i < ni; ++i) {
                    if (vt) MT(0);
                    f32x4 tc[2][2];
                    project<0>(wt, tc, bb, c0, c1, n0, n1,
                        [&]() {
                            MT(1);
                            if (i == 1) SST(16);
                            asm volatile("s_waitcnt vmcnt(0) lgkmcnt(0)\n\ts_barrier" ::: "memory");
                            if (i == 1) SST(17);
                            MT(2);
                        }, [](auto) {});
                    { const LAS char* x = c0; c0 = n0; n0 = x; x = c1; c1 = n1; n1 = x; }
                    char* tb = tokL + (i & 1) * 32 * QS + (16 * w + 4 * fq) * 2;
#pragma unroll
                    for (int g = 0; g < 2; ++g)
#pragma unroll
                        for (int tt = 0; tt < 2; ++tt) *(u32x2*)(tb + (prow + 8 * tt) * QS + g * 128) = (u32x2){pack2(tc[g][tt][0], tc[g][tt][1]), pack2(tc[g][tt][2], tc[g][tt][3])};
                    asm volatile("" ::: "memory");
                    *(volatile LAS int*)(smem + L_S + 16 + w * 4) = i;
                    if (i == 1) SST(18);
                    if (__builtin_expect(i == 1, 0)) __builtin_amdgcn_s_sleep(VSL);
                    if (vt) { MT(3); const int s = i & 63; MTRT(); MTS(); }
                }
                project_end(bb);
            } else {
                __builtin_amdgcn_s_setprio(OPR);
                const unsigned hv = lane * 16u, fz_ = W_FZ + fo + fz_at(hh, w - 4) + (jv1 ? 4096u : 0u);
                unsigned fp = ~0u;
                unsigned fzp = 0x40000000u;
                int jo = jv1, jh = jv2;
                for (int i = 1; i < ni; ++i, jo = nextv(jo)) {
                    const int b = i + (i >= ZLAG);
                    if (__builtin_expect(i == 1 || i == ZLAG, 0)) {
                        VMWAIT();
                        if (i == ZLAG) {
                            jh = nextv(jh);
                            wait_h(rwin, fo, jh, hrdy);
                            BAR0();
                            for (int t = 0; t < 10; ++t) dma1k(rws, hv, hseg(jh) + (unsigned)((w - 4) * 1024 + t * 4096), (unsigned)(L_H + ((i + 1) & 1) * 40960 + (w - 4) * 1024 + t * 4096));
                            VMWAIT();
                        }
                    }
                    jh = nextv(jh);
                    if (b < ni) BAR0();
                    if (__builtin_expect(jo + layer == 0 && w == 7, 0)) near_init();
                    wait_h(rwin, fo, jh, hrdy);
                    if (i == 1) SST(19);
                    if (vt) MT(1);
                    for (;;) {
                        const u32x4 f = *(volatile LAS u32x4*)(smem + L_S + 16);
                        if (__builtin_amdgcn_readfirstlane(min(min(f[0], f[1]), min(f[2], f[3]))) >= (unsigned)i) break;
                        __builtin_amdgcn_s_sleep(1);
                    }
                    asm volatile("" ::: "memory");
                    if (i == 1) SST(20);
                    if (__builtin_expect(jo == 0 && w == 7, 0)) __builtin_amdgcn_raw_buffer_store_b32((4u * (unsigned)__builtin_amdgcn_s_memrealtime() + (layer ? DVT : DVT0)) & 0xFFFFFFu, rwin, two(), W_T + (unsigned)hh * 128u + 4u, 16);
                    if (vt) MT(0);
                    u32x4 vf[2][4];
#pragma unroll
                    for (int t = 0; t < 2; ++t)
#pragma unroll
                        for (int ks = 0; ks < 4; ++ks) vf[t][ks] = *(const u32x4*)(tokL + (i & 1) * 32 * QS + (16 * t + fr) * QS + ks * 64 + fq * 16);
                    const unsigned ztb = O_Z + (unsigned)((jo - layer) * (int)ZTSEG) * 2u + (unsigned)(10 * (w - 4) * H_ + hh) * 1024u;
                    f32x4 c[2][2];
                    u32x4 r;
                    unsigned hs = hseg(jh) + (unsigned)((w - 4) * 1024), hd = (unsigned)(L_H + ((b + 1) & 1) * 40960 + (w - 4) * 1024);
                    asm volatile("" : "+s"(hs), "+s"(hd));
#pragma unroll
                    for (int nt = 0; nt < 11; ++nt) {
                        auto& cc = c[nt & 1]; auto& cp = c[(nt + 1) & 1];
#pragma unroll
                        for (int ks = 0; ks < 4; ++ks) {
                            if (nt < 10) { cc[0] = mfma32(vf[0][ks], half8(WO(ks >> 1, nt < 10 ? nt : 0), ks & 1), ks ? cc[0] : z4); PIN1(cc[0]); }
                            if (nt > 0 && ks >= 1 && ks < 3) { r[2 * ks - 2] = pack2(cp[ks - 1][0], cp[ks - 1][1]); PIN1(r); }
                            if (nt < 10) { cc[1] = mfma32(vf[1][ks], half8(WO(ks >> 1, nt < 10 ? nt : 0), ks & 1), ks ? cc[1] : z4); PIN1(cc[1]); }
                            if (nt > 0 && ks >= 1 && ks < 3) { r[2 * ks - 1] = nosent1(pack2(cp[ks - 1][2], cp[ks - 1][3])); PIN1(r); }
                            if (nt > 0 && ks == 2) st128(r, rws, lane * 16u, ztb + (unsigned)((nt - 1) * H_) * 1024u, 16);
                            if (nt < 5 && !(ks & 1)) dma1k(rws, hv, hs + (2 * nt + (ks >> 1)) * 4096u, hd + (2 * nt + (ks >> 1)) * 4096u);
                            if (nt == 5 && ks == 0) {
                                asm volatile("s_waitcnt vmcnt(14)" ::: "memory");
                                __builtin_amdgcn_raw_buffer_store_b32(fp, rwin, 0u, fzp, 16);
                            }
                            SB();
                        }
                    }
                    if (vt) MT(2);
                    if (i == 1) SST(21);
                    fp = (unsigned)jo;
                    fzp = fz_;
                    if (__builtin_expect(jo <= ZEARLY || i + 1 == ni, 0)) {
                        VMWAIT();
                        __builtin_amdgcn_raw_buffer_store_b32(fp, rwin, 0u, fz_, 16);
                    } else asm volatile("s_waitcnt vmcnt(7)" ::: "memory");
                    if (jo < 8) SST(8 + jo);
                    if (vt && (!BSPARSE || jo % 6 == 2)) { const int s = jo; RT(0); RTS(); }
                    if (vt) { MT(3); const int s = i & 63; MTRT(); MTS(); }
                }
            }
            __builtin_amdgcn_s_setprio(0);
            VMWAIT();
            BAR();
        }
    };
    auto qt_role = [&](const u32x8 (&wt)[2][10], int hh, auto qq) INL {
        constexpr bool qt = decltype(qq)::v;
        const unsigned long long mq = bid < NQT ? MXQ : ~MXQ;
        const int jl = 63 - __builtin_clzll(mq);
        auto nextq = [&](int j) { return min(seg_after(mq, j), jl); };
        const int jq0 = qt ? nextq(0) : 0, jq1 = qt ? nextq(jq0) : 0;
        if (qt && jq0 + layer == 1 && w == 7) near_init();
        stage2(jq0, jq1, !qt);
        if (qt && jq0 == 1 && w == 7) __builtin_amdgcn_raw_buffer_store_b32((4u * (unsigned)__builtin_amdgcn_s_memrealtime() + (layer ? DQA : DQA0)) & 0xFFFFFFu, rwin, two(), W_T + (unsigned)hh * 128u + 8u, 16);
        auto check2 = [&]() INL { VMWAIT(); check_h<5>(rws, rwin, hseg(jq1) + w * 1024u, L_H + 40960u + w * 1024u, 8192u); };
        {
            LANES();
            const unsigned to0 = (unsigned)((w ^ 4) >> 2) * 8192u + (unsigned)((w & 3) * 64 + (lane ^ ((fq & 1) * (w < 4 ? 8 : 5)))) * 16u, to1 = to0 + 4096u, hv = lane * 16u;
            const LAS char* c0 = (const LAS char*)(hL + hoff + ((fq ^ (fr & 7)) << 4));
            const LAS char* c1 = (const LAS char*)(hL + hoff + (((4 + fq) ^ (fr & 7)) << 4));
            u32x4 bb[8], g0 = SENT, g1 = SENT;
            f32x4 tc[2][2];
            auto pack = [&]() {
                g0 = (u32x4){pack2(tc[0][0][0], tc[0][0][1]), pack2(tc[0][0][2], tc[0][0][3]), pack2(tc[1][0][0], tc[1][0][1]), pack2(tc[1][0][2], tc[1][0][3])};
                g1 = (u32x4){pack2(tc[0][1][0], tc[0][1][1]), pack2(tc[0][1][2], tc[0][1][3]), pack2(tc[1][1][0], tc[1][1][1]), pack2(tc[1][1][2], tc[1][1][3])};
            };
            const LAS char* n0 = c0 + 40960; const LAS char* n1 = c1 + 40960;
            if (!qt) {
                project0(bb, c0, c1);
                project<0>(wt, tc, bb, c0, c1, c0, c1, []() {}, [](auto) {});
                pack();
                *(u32x4*)(tokL + to0) = g0; *(u32x4*)(tokL + to1) = g1;
                project_end(bb);
            } else {
                project0(bb, c0, c1);
                unsigned hs = hseg(jq1) + (unsigned)(w * 1024), hd = (unsigned)(L_H + 40960 + w * 1024), fv = lane == 0 ? 0u : 0x40000000u, fj = 0;
                const unsigned fq_ = W_FQ + fo + (unsigned)(hh * 128 + (jq0 > 1 ? 4096 : 0) + w * 4);
                for (int j = jq0, jn = jq1;; j = jn, jn = nextq(jn)) {
                    MT(0);
                    project<0>(wt, tc, bb, c0, c1, n0, n1,
                        [&]() {
                            if (__builtin_expect(j == jq0, 0)) check2();
                            wait_h(rwin, fo, nextq(jn), hrdy);
                            hs = hseg(nextq(jn)) + (unsigned)(w * 1024); hd = (unsigned)(2 * L_H + 40960 + 2 * w * 1024) - hd;
                            asm volatile("" : "+s"(hs), "+s"(hd));
                            MT(1);
                            VMWAIT();
                            BAR();
                            MT(2);
                        },
                        [&](auto uu) {
                            constexpr int u = decltype(uu)::v;
                            if (u == 34 || u == 38 || u == 2 || u == 6 || u == 10) { constexpr int t = ((u + 6) % 40) / 4; dma1k(rws, hv, hs + t * 8192u, hd + t * 8192u); }
                            if (u == UFQ) { asm volatile("s_waitcnt vmcnt(3)" ::: "memory"); __builtin_amdgcn_raw_buffer_store_b32(fj, rwin, fv, fq_, 16); asm volatile("" ::: "memory"); }
                        });
                    pack();
                    const unsigned qs = O_QK + (unsigned)(j * H_ + hh) * 16384u;
                    st128(g0, rws, to0, qs, 16);
                    st128(g1, rws, to1, qs, 16);
                    { const LAS char* x = c0; c0 = n0; n0 = x; x = c1; c1 = n1; n1 = x; }
                    fj = (unsigned)j;
                    PIN1(fj);
                    if (__builtin_expect(j <= QEARLY || j == jl, 0)) {
                        VMWAIT();
                        __builtin_amdgcn_raw_buffer_store_b32(fj, rwin, fv, fq_, 16);
                        if (j == jl) break;
                    }
                    MT(3);
                    if (j < 8) SST(8 + j);
                    if (!BSPARSE || j % 6 == 2) { const int s = j; RT(0); RTS(); MTRT(); MTS(); }
                }
                project_end(bb);
                VMWAIT();
                BAR0();
            }
        }
        SST(4);
    };
    auto qk_role = [&](const u32x8 (&wt)[2][10]) INL {
        const int hh = bid;
        int qrdy = 0;
        auto steps = [&](auto cc) INL {
        constexpr int C = decltype(cc)::v >> 1, R = decltype(cc)::v & 1;
        LANES();
        const LAS char* ka = (const LAS char*)(smem + L_K + (fr & 1) * TQS + (4 * fq + (fr >> 2)) * 8);
        const LAS char* qa = (const LAS char*)(smem + L_Q + (fr & 1) * 544 + ((fr >> 1) & 1) * 1152 + (16 * fq + 2 * (fr >> 2)) * 4);
        LAS char* qw = (LAS char*)(C < 2 ? smem + L_Q + (fq & 1) * 544 + (fq >> 1) * 1152 + (32 * w + 8 * (fr >> 2) + bs2(fr & 3)) * 4
                                         : smem + L_K + (fq >> 1) * TQS + (32 * (w & 3) + fr) * 8 + (fq & 1) * 4);
        const LAS char* mp0 = (const LAS char*)(memL + ma0);
        const int z = 16 * (fr >> 3) + 4 * (fr & 3) + 2 * ((fr >> 2) & 1);
        const int ko = ((z >> 4) * 256 + fq * 16 + ((z & 15) ^ ((fq & 1) * 5))) * 16;
        unsigned pa0 = 2u * (hh >> 3) * PAIR + (unsigned)(C * 4096 + (hh & 7) * 512 + R * 256 + lane * 4);
        unsigned pa2 = ((8u + (fr >> 3)) * PAIR + C * 4096u + (unsigned)((hh >> 1) * 256 + R * 128 + (hh & 1) * 64 + fq * 16 + (fr & 6) * 2)) | (fr & 1) << 30;
        PIN2(ka, qw); PIN1(mp0);
        constexpr bool TK = C == 2 && R == 1;
        Tk tk = {0u, 0u, 0x40000000u, nullptr};
        if (TK) { tk = tk_init(); __builtin_amdgcn_s_setprio(TKP); }
        if (C < 2) { PIN2(qa, pa0); PIN1(pa2); }
        int qf_ = 0;
        int tbo = 0;
        for (int s = 0; s < NSEG; ++s) {
            RT(0);
            const char* tb = tokL + tbo;
            unsigned pb = __builtin_amdgcn_readfirstlane(s + 1 < NSEG ? p_slot(s) + nh : 0x40000000u);
            asm volatile("" : "+s"(pb));
            bool redo = false;
            unsigned sf = 0;
            if (s <= 1) BAR();
            u32x8 kx[2][2];
            if (C < 2) {
#pragma unroll
                for (int u = 0; u < 4; ++u) kx[u >> 1][u & 1] = cat8(*(const u32x4*)(tb + (ko ^ ((u >> 1) * 16)) + (u & 1) * 2048), *(const u32x4*)(tb + (ko ^ ((u >> 1) * 16)) + (u & 1) * 2048 + 1024));
                PIN2(kx[0][0], kx[0][1]); PIN2(kx[1][0], kx[1][1]);
            }
            do {
                ++it;
                f32x4 a0 = zero4(), a1 = zero4();
                PIN2(a0, a1);
                u32x4 m[10];
                if (C < 2) {
                    __builtin_amdgcn_s_setprio(2);
                    asm volatile(ALN ::: "memory");
                    BAR();
                    MT(0);
#pragma unroll
                    for (int t = 0; t < NB1; ++t) { m[t] = *(const LAS u32x4*)(mp0 + t * 1024); SB(); }
                    BAR0();
#pragma unroll
                    for (int t = 0; t < 10; ++t) {
                        if (t == QB2) { BAR0(); MT(1); }
                        if (t + NB1 < 10) m[t + NB1] = *(const LAS u32x4*)(mp0 + (t + NB1) * 1024);
                        SB();
                        a0 = smfmac(m[t], wt[0][t], a0, sidx); a1 = smfmac(m[t], wt[1][t], a1, sidx);
                        PIN2(a0, a1);
                    }
                } else {
                    unsigned mx = 0;
                    asm volatile("" : "+{v[200:203]}"(a0), "+{v[204:207]}"(a1), "+{v208}"(mx));
                    unsigned lim = s > 0 ? poll_lim(budget) : ~0u, sr = tro + (unsigned)(s & 3) * 2u * PAIR;
                    asm volatile("" : "+s"(lim), "+s"(sr));
                    if (TK) fetch(tk, s, !redo, lim, DQC, (unsigned)hh * 128u, true);
                    else asm volatile(ALN "s_waitcnt lgkmcnt(0)\n\ts_barrier\n\ts_barrier\n\ts_barrier" ::: "memory");
                    asm volatile(
                        "ds_read_b128 v[212:215], %[mp] offset:0\n\t"
                        "ds_read_b128 v[216:219], %[mp] offset:1024\n\t"
                        "ds_read_b128 v[220:223], %[mp] offset:2048\n\t"
                        "ds_read_b128 v[224:227], %[mp] offset:3072\n\t"
                        "ds_read_b128 v[228:231], %[mp] offset:4096\n\t"
                        "ds_read_b128 v[232:235], %[mp] offset:5120\n\t"
                        "ds_read_b128 v[236:239], %[mp] offset:6144\n\t"
                        "ds_read_b128 v[240:243], %[mp] offset:7168\n\t"
                        "ds_read_b128 v[244:247], %[mp] offset:8192\n\t"
                        "ds_read_b128 v[248:251], %[mp] offset:9216\n\t"
                        MTA(4)
                        "s_waitcnt lgkmcnt(9)\n\t"
                        "v_smfmac_f32_16x16x64_bf16 v[200:203], v[212:215], %[w00], %[ix]\n\t"
                        "v_max3_u32 v208, v208, v212, v213\n\t"
                        "v_smfmac_f32_16x16x64_bf16 v[204:207], v[212:215], %[w10], %[ix]\n\t"
                        "s_waitcnt lgkmcnt(8)\n\t"
                        "v_max3_u32 v208, v208, v214, v215\n\t"
                        "v_smfmac_f32_16x16x64_bf16 v[200:203], v[216:219], %[w01], %[ix]\n\t"
                        "v_max3_u32 v208, v208, v216, v217\n\t"
                        "v_smfmac_f32_16x16x64_bf16 v[204:207], v[216:219], %[w11], %[ix]\n\t"
                        "s_waitcnt lgkmcnt(7)\n\t"
                        "v_max3_u32 v208, v208, v218, v219\n\t"
                        "v_smfmac_f32_16x16x64_bf16 v[200:203], v[220:223], %[w02], %[ix]\n\t"
                        "v_max3_u32 v208, v208, v220, v221\n\t"
                        "v_smfmac_f32_16x16x64_bf16 v[204:207], v[220:223], %[w12], %[ix]\n\t"
                        "s_waitcnt lgkmcnt(6)\n\t"
                        "v_max3_u32 v208, v208, v222, v223\n\t"
                        "v_smfmac_f32_16x16x64_bf16 v[200:203], v[224:227], %[w03], %[ix]\n\t"
                        "v_max3_u32 v208, v208, v224, v225\n\t"
                        "v_smfmac_f32_16x16x64_bf16 v[204:207], v[224:227], %[w13], %[ix]\n\t"
                        "s_waitcnt lgkmcnt(0)\n\t"
                        "v_max3_u32 v208, v208, v226, v227\n\t"
                        "v_smfmac_f32_16x16x64_bf16 v[200:203], v[228:231], %[w04], %[ix]\n\t"
                        "v_max3_u32 v208, v208, v228, v229\n\t"
                        "v_smfmac_f32_16x16x64_bf16 v[204:207], v[228:231], %[w14], %[ix]\n\t"
                        "v_max3_u32 v208, v208, v230, v231\n\t"
                        "v_smfmac_f32_16x16x64_bf16 v[200:203], v[232:235], %[w05], %[ix]\n\t"
                        "v_max3_u32 v208, v208, v232, v233\n\t"
                        "v_smfmac_f32_16x16x64_bf16 v[204:207], v[232:235], %[w15], %[ix]\n\t"
                        "v_max3_u32 v208, v208, v234, v235\n\t"
                        "v_smfmac_f32_16x16x64_bf16 v[200:203], v[236:239], %[w06], %[ix]\n\t"
                        "v_max3_u32 v208, v208, v236, v237\n\t"
                        "v_smfmac_f32_16x16x64_bf16 v[204:207], v[236:239], %[w16], %[ix]\n\t"
                        "v_max3_u32 v208, v208, v238, v239\n\t"
                        "v_smfmac_f32_16x16x64_bf16 v[200:203], v[240:243], %[w07], %[ix]\n\t"
                        "v_max3_u32 v208, v208, v240, v241\n\t"
                        "v_smfmac_f32_16x16x64_bf16 v[204:207], v[240:243], %[w17], %[ix]\n\t"
                        "v_max3_u32 v208, v208, v242, v243\n\t"
                        "v_smfmac_f32_16x16x64_bf16 v[200:203], v[244:247], %[w08], %[ix]\n\t"
                        "v_max3_u32 v208, v208, v244, v245\n\t"
                        "v_smfmac_f32_16x16x64_bf16 v[204:207], v[244:247], %[w18], %[ix]\n\t"
                        "v_max3_u32 v208, v208, v246, v247\n\t"
                        "v_smfmac_f32_16x16x64_bf16 v[200:203], v[248:251], %[w09], %[ix]\n\t"
                        "v_max3_u32 v208, v208, v248, v249\n\t"
                        "v_smfmac_f32_16x16x64_bf16 v[204:207], v[248:251], %[w19], %[ix]\n\t"
                        "v_max3_u32 v208, v208, v250, v251\n\t"
                        "buffer_load_dword %[sf], %[sl], %[rw], %[sr] offen sc1\n\t"
                        "v_cmp_lt_u32 vcc, %[lim], v208\n\t"
                        "s_nop 3\n\t"
                        "v_pk_add_f32 v[200:201], v[200:201], v[202:203]\n\t"
                        "v_pk_add_f32 v[204:205], v[204:205], v[206:207]\n\t"
                        "v_cvt_pk_bf16_f32 v200, v200, v201\n\t"
                        "v_cvt_pk_bf16_f32 v204, v204, v205\n\t"
                        "ds_write2_b32 %[qw], v200, v204 offset1:32\n\t"
                        MTA(2)
                        "s_cbranch_vccz 1f\n\t"
                        "v_mov_b32 v208, %[it]\n\t"
                        "v_mov_b32 v209, 0\n\t"
                        "ds_write_b32 v209, v208 offset:%[ls]\n\t"
                        "1:\n\t"
                        "s_waitcnt vmcnt(%[nv]) lgkmcnt(0)\n\t"
                        "s_barrier\n\t"
                        "s_waitcnt vmcnt(0)\n\t"
                        : [sf] "=&v"(sf), "+v"(qf_), "+{v[200:203]}"(a0), "+{v[204:207]}"(a1), "+{v208}"(mx)
                        : [mp] "v"(mp0), [qw] "v"(qw), [ix] "v"(sidx), [lim] "s"(lim), [it] "s"(it), [ls] "n"(L_S), [sl] "v"(tk.sl), [sr] "s"(sr), [rw] "s"(rwin), [nv] "n"(TK ? 2 : 1),
                          [w00] "v"(wt[0][0]), [w01] "v"(wt[0][1]), [w02] "v"(wt[0][2]), [w03] "v"(wt[0][3]), [w04] "v"(wt[0][4]), [w05] "v"(wt[0][5]), [w06] "v"(wt[0][6]), [w07] "v"(wt[0][7]), [w08] "v"(wt[0][8]), [w09] "v"(wt[0][9]), [w10] "v"(wt[1][0]), [w11] "v"(wt[1][1]), [w12] "v"(wt[1][2]), [w13] "v"(wt[1][3]), [w14] "v"(wt[1][4]), [w15] "v"(wt[1][5]), [w16] "v"(wt[1][6]), [w17] "v"(wt[1][7]), [w18] "v"(wt[1][8]), [w19] "v"(wt[1][9])
                        : "v209", "v212", "v213", "v214", "v215", "v216", "v217", "v218", "v219", "v220", "v221", "v222", "v223", "v224", "v225", "v226", "v227", "v228", "v229", "v230", "v231", "v232", "v233", "v234", "v235", "v236", "v237", "v238", "v239", "v240", "v241", "v242", "v243", "v244", "v245", "v246", "v247", "v248", "v249", "v250", "v251", "vcc", "memory");
                }
                if (C < 2) {
                    *(LAS unsigned*)qw = hpack(a0); *(LAS unsigned*)(qw + 16) = hpack(a1);
                    SB();
                    MT(2);
                    unsigned vd;
#define SC0 "v[212:215]"
#define SC1 "v[216:219]"
#define SC2 "v[220:223]"
#define DPP(op, d, ctl) op " " d ", " d ", " d " " ctl " row_mask:0xf bank_mask:0xf\n\t"
#define TAIL(A0, A2, B0, B2, C0, C2) \
                        "v_mov_b64 v[212:213], 0\n\tv_mov_b64 v[214:215], 0\n\tv_mov_b64 v[216:217], 0\n\tv_mov_b64 v[218:219], 0\n\tv_mov_b64 v[220:221], 0\n\tv_mov_b64 v[222:223], 0\n\t" \
                        "s_setprio 3\n\t" \
                        "s_waitcnt lgkmcnt(0)\n\t" \
                        "s_barrier\n\t" MTA(3) \
                        "ds_read_b64_tr_b16 v[224:225], %[qa]\n\t" \
                        "ds_read_b64_tr_b16 v[226:227], %[qa] offset:32\n\t" \
                        "ds_read_b64_tr_b16 v[228:229], %[qa] offset:256\n\t" \
                        "ds_read_b64_tr_b16 v[230:231], %[qa] offset:288\n\t" \
                        "ds_read_b64_tr_b16 v[232:233], %[ka]\n\t" \
                        "ds_read_b64_tr_b16 v[234:235], %[ka] offset:128\n\t" \
                        "ds_read_b64_tr_b16 v[236:237], %[ka] offset:256\n\t" \
                        "ds_read_b64_tr_b16 v[238:239], %[ka] offset:384\n\t" \
                        "ds_read_b64_tr_b16 v[240:241], %[ka] offset:512\n\t" \
                        "ds_read_b64_tr_b16 v[242:243], %[ka] offset:640\n\t" \
                        "s_waitcnt lgkmcnt(8)\n\t" \
                        "v_smfmac_f32_16x16x64_bf16 " SC0 ", v[224:227], %[k00], %[ix]\n\t" \
                        "ds_read_b64_tr_b16 v[244:245], %[ka] offset:768\n\t" \
                        "v_smfmac_f32_16x16x64_bf16 " SC1 ", v[224:227], %[k10], %[ix]\n\t" \
                        "ds_read_b64_tr_b16 v[246:247], %[ka] offset:896\n\t" \
                        "s_waitcnt lgkmcnt(8)\n\t" \
                        "v_smfmac_f32_16x16x64_bf16 " SC0 ", v[228:231], %[k01], %[ix]\n\t" \
                        "ds_read_b32 v248, %[vz] offset:%[ls]\n\t" \
                        "v_smfmac_f32_16x16x64_bf16 " SC1 ", v[228:231], %[k11], %[ix]\n\t" \
                        "s_waitcnt lgkmcnt(5)\n\t" \
                        "v_smfmac_f32_16x16x64_bf16 " SC2 ", v[224:227], v[232:239], %[ix]\n\t" \
                        "s_waitcnt lgkmcnt(1)\n\t" \
                        "v_smfmac_f32_16x16x64_bf16 " SC2 ", v[228:231], v[240:247], %[ix]\n\t" \
                        "s_waitcnt lgkmcnt(0)\n\t" \
                        MTA(4) "v_readfirstlane_b32 %[vd], v248\n\t" \
                        "v_add_f32 v232, " A0 ", " A2 "\n\t" \
                        "s_nop 0\n\t" \
                        "v_add_f32 v233, " B0 ", " B2 "\n\t" \
                        "v_max_f32 v235, v232, v233\n\t" \
                        "s_cmp_eq_u32 %[vd], %[it]\n\t" \
                        "s_cselect_b32 %[p0], 2.0, %[p0]\n\t" \
                        DPP("v_max_f32_dpp", "v235", "row_ror:8") \
                        "v_add_f32 v234, " C0 ", " C2 "\n\t" \
                        "v_max_f32 v235, v235, v234\n\t" \
                        "s_add_u32 %[p1], %[p0], 0x4000\n\t" \
                        "s_nop 0\n\t" DPP("v_max_f32_dpp", "v235", "row_ror:4") \
                        "s_nop 1\n\t" DPP("v_max_f32_dpp", "v235", "row_ror:2") \
                        "s_nop 1\n\t" DPP("v_max_f32_dpp", "v235", "row_ror:1") \
                        "v_mul_f32 v235, 0xbe0293ee, v235\n\t" \
                        "v_fmamk_f32 v232, v232, 0x3e0293ee, v235\n\t" \
                        "v_fmamk_f32 v233, v233, 0x3e0293ee, v235\n\t" \
                        "v_exp_f32 v232, v232\n\t" \
                        "v_exp_f32 v233, v233\n\t" \
                        "v_fmamk_f32 v234, v234, 0x3e0293ee, v235\n\t" \
                        "v_add_f32 v235, v232, v233\n\t" \
                        "v_exp_f32 v234, v234\n\t" \
                        "s_nop 0\n\t" DPP("v_add_f32_dpp", "v235", "row_ror:8") \
                        "v_add_f32 v235, v235, v234\n\t" \
                        "v_mov_b32_dpp v236, v234 quad_perm:[1,1,3,3] row_mask:0xf bank_mask:0xf\n\t" \
                        "s_nop 0\n\t" DPP("v_add_f32_dpp", "v235", "row_ror:4") \
                        "s_nop 1\n\t" DPP("v_add_f32_dpp", "v235", "row_ror:2") \
                        "s_nop 1\n\t" DPP("v_add_f32_dpp", "v235", "row_ror:1") \
                        "v_rcp_f32 v235, v235\n\t" \
                        "s_nop 0\n\t" \
                        "v_mul_f32_e64 v232, v232, v235 clamp\n\t" \
                        "v_mul_f32_e64 v233, v233, v235 clamp\n\t" \
                        "v_cvt_pk_bf16_f32 v232, v232, v233\n\t" \
                        MTA(5) "buffer_store_dword v232, %[pa], %[rw], %[p0] offen sc1\n\t" \
                        "v_mul_f32_e64 v234, v234, v235 clamp\n\t" \
                        "v_mul_f32_e64 v236, v236, v235 clamp\n\t" \
                        "buffer_store_dword v232, %[pa], %[rw], %[p1] offen sc1\n\t" \
                        "v_cvt_pk_bf16_f32 v234, v234, v236\n\t" \
                        "buffer_store_dword v234, %[pm], %[rw], %[p0] offen sc1\n\t" \
                        "s_setprio 0"
#define TAILIO : [vd] "=&s"(vd), [p0] "+s"(ps), [p1] "=&s"(ps1) \
                        : [qa] "v"(qa), [ka] "v"(ka), [vz] "v"(0u), [ls] "n"(L_S), [ix] "v"(sidx), [it] "s"(it), [pa] "v"(pa0), [pm] "v"(pa2), [rw] "s"(rwin), \
                          [k00] "v"(kx[0][0]), [k01] "v"(kx[0][1]), [k10] "v"(kx[1][0]), [k11] "v"(kx[1][1]) \
                        : "v212", "v213", "v214", "v215", "v216", "v217", "v218", "v219", "v220", "v221", "v222", "v223", "v224", "v225", "v226", "v227", "v228", "v229", "v230", "v231", "v232", \
                          "v233", "v234", "v235", "v236", "v237", "v238", "v239", "v240", "v241", "v242", "v243", "v244", "v245", "v246", "v247", "v248", "scc", "memory"
                    unsigned ps = pb, ps1;
                    if (R == 0) asm volatile(TAIL("v212", "v214", "v216", "v218", "v220", "v222") TAILIO);
                    else asm volatile(TAIL("v213", "v215", "v217", "v219", "v221", "v223") TAILIO);
                    redo = vd == it;
                    RT(4);
                    MT(6);
                } else {
                    MT(3);
                    if (TK) { __builtin_amdgcn_s_setprio(TKP); if (TKS) __builtin_amdgcn_s_sleep(TKS); }
                    redo = __builtin_amdgcn_readfirstlane(*(volatile LAS unsigned*)(smem + L_S)) == it;
                    if (redo) FC(fc0);
                }
            } while (__builtin_expect(redo, 0));
            if (s < 8) SST(8 + s);
            if (s >= 56) SST(s - 40);
            if (C + R == 1) {
                if (s + layer > 0) {
                    __builtin_amdgcn_s_sleep(DDMA);
                    unsigned l2 = fresh_lane(), s1;
                    asm volatile("v_mov_b32 %0, -1" : "=v"(s1));
                    const unsigned base = p_slot(s - 1) + nh;
                    asm volatile("s_waitcnt vmcnt(3)" ::: "memory");
                    st128(((u32x4){s1, s1, s1, s1}), rwin, (l2 >> 5) * PAIR + (l2 & 31u) * 16u, base + (unsigned)(2 * (hh >> 3)) * PAIR + (unsigned)(R * 4096 + (hh & 7) * 512), 16);
                    if (R) __builtin_amdgcn_raw_buffer_store_b64((u32x2){s1, s1}, rwin, (l2 >> 5) * PAIR + (l2 & 16u) * 256u + (l2 & 8u) * 16u + (l2 & 7u) * 8u, base + 8u * PAIR + (unsigned)((hh >> 1) * 256 + (hh & 1) * 64), 16);
                    else st128(((u32x4){s1, s1, s1, s1}), rwin, two(), W_T + (unsigned)((s + 2) & 3) * 2u * PAIR + (unsigned)hh * 128u, 16);
                }
            } else if (TK) {
                if (s + 1 < NSEG) sched2(tk, s, sf);
            } else if (C == 2 && w == 4) {
                if (s + 2 < NSEG) {
                    auto done = [&](int f) INL {
                        f = min(f, __builtin_amdgcn_update_dpp(f, f, 0xB1, 0xF, 0xF, false)); f = min(f, __builtin_amdgcn_update_dpp(f, f, 0x4E, 0xF, 0xF, false));
                        f = min(f, __builtin_amdgcn_update_dpp(f, f, 0x141, 0xF, 0xF, false));
                        return seg_done(MXQ, __builtin_amdgcn_readlane(f, 0), __builtin_amdgcn_readlane(f, 8));
                    };
                    qrdy = max(qrdy, done(qf_));
                    const int sq = s + 2;
                    if (__builtin_expect(s == 0, 0)) __builtin_amdgcn_s_sleep(DQ0);
                    if (__builtin_expect(qrdy < sq, 0)) for (unsigned spins = 0;;) {
                        asm volatile("" ::: "memory");
                        const int f = (int)__builtin_amdgcn_raw_buffer_load_b32(rwin, (unsigned)((lane & 8) * 512 + (lane & 7) * 4), W_FQ + fo + (unsigned)hh * 128u, 16);
                        qrdy = done(f);
                        if (qrdy >= sq) break;
                        FC(fc2);
                        if (spin_fail(spins, rwin)) break;
                        __builtin_amdgcn_s_sleep(QSL);
                    }
                    const unsigned so = O_QK + (unsigned)(((s + 2) * H_ + hh) * 16384), lo = (unsigned)(L_TOK + (tbo ? tbo - 16384 : 32768)), l2 = fresh_lane();
                    if (__builtin_expect(s == 0, 0)) {
                        for (int i = 0; i < 4; ++i) dma4k(rws, l2 * 16u, so - H_ * 16384u + i * 4096u, lo - 16384u + i * 4096u);
                    }
#pragma unroll
                    for (int i = 0; i < 4; ++i) dma4k(rws, l2 * 16u, so + i * 4096u, lo + i * 4096u);
                    if (__builtin_expect(s == 0, 0)) asm volatile("s_waitcnt vmcnt(16)" ::: "memory");
                    if (qrdy < min(s + 5, NSEG - 1))
                        asm volatile("buffer_load_dword %0, %1, %2, %3 offen sc1" : "+v"(qf_) : "v"((l2 & 8u) * 512u + (l2 & 7u) * 4u), "s"(rwin), "s"(W_FQ + fo + (unsigned)hh * 128u) : "memory");
                }
            } else {
                asm volatile("s_waitcnt vmcnt(3)" ::: "memory");
                if (s > TAILS) __builtin_amdgcn_raw_buffer_store_b32((unsigned)s - 1u, rwin, 0u, W_FPT + fo + (unsigned)(hh * 128 + (w & 3) * 4), 16);
                if (C == 2 && s + 1 < NSEG) __builtin_amdgcn_s_sleep(DPT);
                const float scale = 0.08838834764831845f, l2e = 1.44269504088896340736f * (NORND ? scale : 1.f);
                const int tq = (w >> 1) & 1, qh = w & 1;
                f32x4 sc[3] = {z4, z4, z4};
                u32x4 kt[2][4], qf[4], kf[4];
                const unsigned l2 = fresh_lane(), kto = ((l2 & 8u) * 512u + (l2 & 51u) * 16u + (l2 & 4u) * 32u) ^ ((l2 & 16u) * 5u), qto = ((l2 & 55u) * 16u + qh * 128u) ^ ((l2 & 16u) * 8u);
#pragma unroll
                for (int ks = 0; ks < 4; ++ks) {
                    qf[ks] = *(const u32x4*)(tb + 8192 + (tq * 4 + ks) * 1024 + qto);
                    kt[0][ks] = *(const u32x4*)(tb + ks * 1024 + kto);
                    kt[1][ks] = *(const u32x4*)(tb + ks * 1024 + (kto ^ 64u));
                    SB();
                }
#pragma unroll
                for (int ks = 0; ks < 4; ++ks) {
                    sc[0] = mfma32(kt[0][ks], qf[ks], sc[0]); PIN1(sc[0]);
                    kf[ks] = ldtr2(ka + ks * 256, ka + ks * 256 + 128);
                    SB();
                    sc[1] = mfma32(kt[1][ks], qf[ks], sc[1]); PIN1(sc[1]);
                }
#pragma unroll
                for (int ks = 0; ks < 4; ++ks) sc[2] = mfma32(kf[ks], qf[ks], sc[2]);
                const bool hi = (l2 & 8u) != 0u;
                float v[6];
#pragma unroll
                for (int r = 0; r < 4; ++r) v[r] = hi ? sc[1][r] : sc[0][r];
                v[4] = hi ? sc[2][2] : sc[2][0]; v[5] = hi ? sc[2][3] : sc[2][1];
#pragma unroll
                for (int r = 0; r < 6; r += 2) {
                    if (NORND) break;
                    const unsigned b0 = pack2(v[r], v[r + 1]);
                    const unsigned b1 = pack2(lo2f(b0) * scale, hi2f(b0) * scale);
                    v[r] = lo2f(b1); v[r + 1] = hi2f(b1);
                }
                const int lim = 8 * (w & 3) + (int)(l2 & 7u) - 8 * (int)(l2 >> 4) - (int)((l2 & 8u) >> 1);
#pragma unroll
                for (int r = 0; r < 4; ++r) if (r > lim) v[r] = NORND ? -1000000000.0f / scale : bfr(-1000000000.0f);
                float mx = fmx(fmx(fmx(fmx(fmx(v[0], v[1]), v[2]), v[3]), v[4]), v[5]);
                mx = xmax3216(fmx(mx, ror8f(mx)));
                const float mb = -mx * l2e;
#pragma unroll
                for (int r = 0; r < 6; ++r) v[r] = __builtin_amdgcn_exp2f(fmaf(v[r], l2e, mb));
                float sum = fmaf(v[4] + v[5], 0.5f, (v[0] + v[1]) + (v[2] + v[3]));
                sum = xsum3216(sum + ror8f(sum));
                const float inv = __builtin_amdgcn_rcpf(sum);
                const unsigned p0 = pack2(v[0] * inv, v[1] * inv), p1 = nosent1(pack2(v[2] * inv, v[3] * inv)), p2 = nosent1(pack2(v[4] * inv, v[5] * inv));
                const unsigned base = (s + 2 < NSEG ? O_PT : W_PL + fo - (NSEG - 2) * PTSEG * 2u) + (unsigned)(s * PTSEG) * 2u;
                const __amdgpu_buffer_rsrc_t rp = s + 2 < NSEG ? rws : rwin;
                const unsigned oob = (l2 & 8u) << 27;
                st128(((u32x4){p0, p1, dppu<0x128>(p0), dppu<0x128>(p1)}), rp, (l2 * 16u) | oob, base + (unsigned)((hh * 2 + tq) * 1024 + qh * 128), 16);
                __builtin_amdgcn_raw_buffer_store_b64((u32x2){p2, dppu<0x128>(p2)}, rp, ((l2 & 7u) * 16u + (l2 >> 4) * 8u) | oob | (l2 & 32u) << 25, base + 65536u + (unsigned)((tq * 32 + hh) * 256 + qh * 128), 16);
            }
            tbo = tbo == 32768 ? 0 : tbo + 16384;
            if (C == 2 && __builtin_expect(budget <= 0, 0)) set_tmo(rwin);
            RT(5);
            RTS();
            MT(7);
            MTS();
        }
        };
        if (w) gather_mem0(memL, mytid() - 64, 448);
        else *(unsigned*)(smem + L_S) = 0u;
        if (layer == 0) near_init();
        qt_role(wt, hh, IC<0>());
        if (w == 7) { ck.ta = 4u * (unsigned)__builtin_amdgcn_s_memrealtime() + 8u; ck.ma = (unsigned)__builtin_amdgcn_s_memtime(); }
        if (w == 0) steps(IC<0>()); else if (w == 1) steps(IC<1>()); else if (w == 2) steps(IC<2>()); else if (w == 3) steps(IC<3>()); else if (w < 7) steps(IC<4>()); else steps(IC<5>());
    };
    auto own_role = [&]() INL {
        const unsigned long long tstart = __builtin_amdgcn_s_memtime();
        if (layer == 0) near_init();
        LANES();
        const int o = bid - 96, hg = (w + ROT * o) & 3, ot = tile_of(o);
        const unsigned ztoff = (unsigned)((o * H_ + 8 * hg) * 64 + lane) * 16u;
        const unsigned zoff = zpair(o / 10, o % 10) + (unsigned)(lane * 16);
        const unsigned poff = (2u * hg + (mc & 1u)) * PAIR + nh + (mc >> 1) * 4096u + (unsigned)((fq >> 1) * 512 + (fr >> 2) * 128 + (fr & 1) * 64 + (fq & 1) * 32 + (fr & 2) * 8);
        const unsigned pmoff = (8u + (mc & 1u)) * PAIR + nh + (mc >> 1) * 4096u + (unsigned)(fq * 256 + (fr >> 3) * 128 + (fr & 2) * 32 + (fr & 4) * 8 + (fr & 1) * 16);
        const unsigned moff = (unsigned)(ot >> 4) * 2u * PAIR + nh + (unsigned)((ot & 15) * 256 + lane * 4);
        constexpr unsigned mc2 = 4096u;
        char* op = smem + 8192;
        if (w == 4 && lane < 2) *(volatile LAS int*)(smem + 4096 + lane * 4) = -1;
        BAR();
        SST(2);
        if (w == 7) clk_init(ck);
        wait_until(tstart + DO0);
        if (w < 4) {
            u32x8 zf[4];
            int zr = -1;
            unsigned zb = W_ZM + (unsigned)layer * (NOWN * PAIR), zso = O_Z - (unsigned)layer * (unsigned)(ZTSEG * 2);
            const unsigned ra = (unsigned)((ot >> 4) * 2 + (fq & 1)) * PAIR + nh + (unsigned)((fq >> 1) * 4096 + (ot & 15) * 256 + fr * 16);
            int np = 0;
            auto piece = [&]() INL {
                asm volatile("s_mov_b32 m0, %3\n\ts_nop 3\n\tbuffer_load_dwordx4 %0, %1, %2 offen sc1 lds\n\tbuffer_load_dwordx4 %0, %1, %2 offen offset:1024 sc1 lds"
                             :: "v"(ztoff), "s"(rws), "s"(O_Z + (unsigned)((NSEG - 1 - layer) * (int)ZTSEG) * 2u + np * 2048u), "s"(32768 + hg * 8192 + np * 2048) : "m0", "memory");
                ++np;
            };
            for (int s = 0; s + 1 < NSEG; ++s) {
                const unsigned pb = p_slot(s);
                const int sz = s;
                while (__builtin_expect(zr < sz, 0)) {
                    __builtin_amdgcn_s_sleep(1);
                    zr = __builtin_amdgcn_readfirstlane(*(volatile LAS int*)(smem + 4096));
                }
                if (s == 0) SST(1);
                asm volatile(
                    "1:\n\t"
                    "buffer_load_dwordx4 v[160:163], %[zt], %[rs], %[so] offen sc1\n\t"
                    "buffer_load_dwordx4 v[164:167], %[zt], %[rs], %[so] offen offset:1024 sc1\n\t"
                    "buffer_load_dwordx4 v[168:171], %[zt], %[rs], %[so] offen offset:2048 sc1\n\t"
                    "buffer_load_dwordx4 v[172:175], %[zt], %[rs], %[so] offen offset:3072 sc1\n\t"
                    "buffer_load_dwordx4 v[176:179], %[zt], %[rs], %[s4] offen sc1\n\t"
                    "buffer_load_dwordx4 v[180:183], %[zt], %[rs], %[s4] offen offset:1024 sc1\n\t"
                    "buffer_load_dwordx4 v[184:187], %[zt], %[rs], %[s4] offen offset:2048 sc1\n\t"
                    "buffer_load_dwordx4 v[188:191], %[zt], %[rs], %[s4] offen offset:3072 sc1\n\t"
                    "s_cmp_lg_u32 %[s], 0\n\t"
                    "s_cbranch_scc1 2f\n\t"
                    "s_waitcnt vmcnt(0)\n\t"
                    "v_max3_u32 v100, v161, v163, v165\n\t"
                    "v_max3_u32 v100, v100, v167, v169\n\t"
                    "v_max3_u32 v100, v100, v171, v173\n\t"
                    "v_max3_u32 v100, v100, v175, v177\n\t"
                    "v_max3_u32 v100, v100, v179, v181\n\t"
                    "v_max3_u32 v100, v100, v183, v185\n\t"
                    "v_max3_u32 v100, v100, v187, v189\n\t"
                    "v_max_u32 v100, v100, v191\n\t"
                    "v_cmp_eq_u32 vcc, -1, v100\n\t"
                    "s_sub_i32 %[tr], %[tr], 1\n\t"
                    "s_cbranch_vccz 2f\n\t"
                    "s_cmp_gt_i32 %[tr], 0\n\t"
                    "s_cbranch_scc1 1b\n\t"
                    "2:"
                    : "=&{v[160:167]}"(zf[0]), "=&{v[168:175]}"(zf[1]), "=&{v[176:183]}"(zf[2]), "=&{v[184:191]}"(zf[3]), [tr] "+s"(budget)
                    : [zt] "v"(ztoff), [rs] "s"(rws), [so] "s"(zso), [s4] "s"(zso + 4096u), [s] "s"(s) : "v100", "vcc", "scc", "memory");
                if (s == 0) SST(3);
                zso += (unsigned)(ZTSEG * 2);
                if (zr == NSEG - 1 && np < 4) piece();
                if (__builtin_expect(budget <= 0, 0)) set_tmo(rwin);
                RT(6);
                const unsigned spa = (unsigned)((s & 1) * 2048 + (w < 3 ? w * 512 : 0) + lane * 8);
#define OST(r)
                unsigned long long t0_, t1_, t2_, t3_, t4_ = 0, bad;
                int tries = budget;
                const unsigned sw = (unsigned)(((s & 3) + 1) & 4) * 1024u;
                unsigned po = poff ^ sw, pm = pmoff ^ sw, x0 = 4096u, x1 = 4096u;
                const unsigned mb = W_MEM + (unsigned)((s + 1) & 3) * 6u * PAIR;
                if (w < 3) asm volatile(
                    ALN
                    "s_barrier\n\t"
                    OST("%[t0]")
                    "v_mov_b64 v[96:97], 0\n\t"
                    "v_mov_b64 v[98:99], 0\n\t"
                    "v_mov_b32 v100, 0\n\t"
                    "s_barrier\n\t"
                    "1:\n\t"
                    "buffer_load_dwordx4 v[208:211], %[po], %[rw], %[pb] offen sc1\n\t"
                    "buffer_load_dwordx4 v[212:215], %[po], %[rw], %[pb] offen offset:1024 sc1\n\t"
                    "buffer_load_dwordx4 v[216:219], %[po], %[rw], %[pb] offen offset:2048 sc1\n\t"
                    "buffer_load_dwordx4 v[220:223], %[po], %[rw], %[pb] offen offset:3072 sc1\n\t"
                    OST("%[t1]")
                    "s_waitcnt vmcnt(3)\n\t"
                    OST("%[t3]")
                    "v_smfmac_f32_16x16x64_bf16 v[96:99], v[208:211], v[160:167], %[ix]\n\t"
                    "v_max3_u32 v100, v100, v208, v209\n\t"
                    "v_max3_u32 v100, v100, v210, v211\n\t"
                    "s_waitcnt vmcnt(2)\n\t"
                    "v_smfmac_f32_16x16x64_bf16 v[96:99], v[212:215], v[168:175], %[ix]\n\t"
                    "v_max3_u32 v100, v100, v212, v213\n\t"
                    "v_max3_u32 v100, v100, v214, v215\n\t"
                    "s_waitcnt vmcnt(1)\n\t"
                    "v_smfmac_f32_16x16x64_bf16 v[96:99], v[216:219], v[176:183], %[ix]\n\t"
                    "v_max3_u32 v100, v100, v216, v217\n\t"
                    "v_max3_u32 v100, v100, v218, v219\n\t"
                    "s_waitcnt vmcnt(0)\n\t"
                    "v_smfmac_f32_16x16x64_bf16 v[96:99], v[220:223], v[184:191], %[ix]\n\t"
                    "v_max3_u32 v100, v100, v220, v221\n\t"
                    "v_max3_u32 v100, v100, v222, v223\n\t"
                    "v_cmp_eq_u32 vcc, -1, v100\n\t"
                    OST("%[t2]")
                    "s_nop 4\n\t"
                    "v_pk_add_f32 v[96:97], v[96:97], v[98:99]\n\t"
                    "ds_write_b64 %[sp], v[96:97]\n\t"
                    "s_cbranch_vccz 2f\n\t"
                    "s_sub_i32 %[tr], %[tr], 1\n\t"
                    "s_cmp_lt_i32 %[tr], 1\n\t"
                    "s_cbranch_scc1 2f\n\t"
                    "v_xor_b32 %[po], %[x0], %[po]\n\t"
                    "s_xor_b32 %[x0], %[x0], 0x5000\n\t"
                    "s_waitcnt lgkmcnt(0)\n\t"
                    "v_mov_b64 v[96:97], 0\n\t"
                    "v_mov_b64 v[98:99], 0\n\t"
                    "v_mov_b32 v100, 0\n\t"
                    "s_branch 1b\n\t"
                    "2:\n\t"
                    "s_waitcnt lgkmcnt(0)\n\t"
                    "s_barrier\n\t"
                    "s_sleep 3\n\t"
                    : [tr] "+s"(tries), [po] "+v"(po), [x0] "+s"(x0), [t0] "=&s"(t0_), [t1] "=&s"(t1_), [t2] "=&s"(t2_), [t3] "=&s"(t3_)
                    : [sp] "v"(spa), [ix] "v"(sidx), [rw] "s"(rwin), [pb] "s"(pb), "{v[160:167]}"(zf[0]), "{v[168:175]}"(zf[1]), "{v[176:183]}"(zf[2]), "{v[184:191]}"(zf[3])
                    : "v96", "v97", "v98", "v99", "v100", "v208", "v209", "v210", "v211", "v212", "v213", "v214", "v215", "v216", "v217", "v218", "v219", "v220", "v221", "v222", "v223", "vcc", "scc", "memory");
                else asm volatile(
                    ALN
                    "s_barrier\n\t"
                    "buffer_load_dwordx4 v[128:131], %[zo], %[rw], %[zb] offen sc1\n\t"
                    "buffer_load_dwordx4 v[132:135], %[zo], %[rw], %[zb] offen offset:1024 sc1\n\t"
                    "buffer_load_dwordx4 v[136:139], %[zo], %[rw], %[zb] offen offset:2048 sc1\n\t"
                    "buffer_load_dwordx4 v[140:143], %[zo], %[rw], %[zb] offen offset:3072 sc1\n\t"
                    "buffer_load_dwordx4 v[144:147], %[zo], %[rw], %[z4] offen sc1\n\t"
                    "buffer_load_dwordx4 v[148:151], %[zo], %[rw], %[z4] offen offset:1024 sc1\n\t"
                    "buffer_load_dwordx4 v[152:155], %[zo], %[rw], %[z4] offen offset:2048 sc1\n\t"
                    "buffer_load_dwordx4 v[156:159], %[zo], %[rw], %[z4] offen offset:3072 sc1\n\t"
                    OST("%[t0]")
                    "v_mov_b64 v[96:97], 0\n\t"
                    "v_mov_b64 v[98:99], 0\n\t"
                    "v_mov_b32 v100, 0\n\t"
                    "s_barrier\n\t"
                    "s_sleep %[fd]\n\t"
                    "buffer_load_dwordx4 v[208:211], %[po], %[rw], %[pb] offen sc1\n\t"
                    "buffer_load_dwordx4 v[212:215], %[po], %[rw], %[pb] offen offset:1024 sc1\n\t"
                    "buffer_load_dwordx4 v[216:219], %[po], %[rw], %[pb] offen offset:2048 sc1\n\t"
                    "buffer_load_dwordx4 v[220:223], %[po], %[rw], %[pb] offen offset:3072 sc1\n\t"
                    "buffer_load_dwordx4 v[224:227], %[pm], %[rw], %[pb] offen sc1\n\t"
                    "buffer_load_dwordx4 v[228:231], %[pm], %[rw], %[pb] offen offset:1024 sc1\n\t"
                    "buffer_load_dwordx4 v[232:235], %[pm], %[rw], %[pb] offen offset:2048 sc1\n\t"
                    "buffer_load_dwordx4 v[236:239], %[pm], %[rw], %[pb] offen offset:3072 sc1\n\t"
                    OST("%[t1]")
                    "s_waitcnt vmcnt(8)\n\t"
                    "v_max3_u32 v101, v129, v131, v133\n\t"
                    "v_max3_u32 v101, v101, v135, v137\n\t"
                    "v_max3_u32 v101, v101, v139, v141\n\t"
                    "v_max3_u32 v101, v101, v143, v145\n\t"
                    "v_max3_u32 v101, v101, v147, v149\n\t"
                    "v_max3_u32 v101, v101, v151, v153\n\t"
                    "v_max3_u32 v101, v101, v155, v157\n\t"
                    "v_max_u32 v101, v101, v159\n\t"
                    "v_cmp_eq_u32 vcc, -1, v101\n\t"
                    "s_cbranch_vccz 3f\n\t"
                    "buffer_load_dwordx4 v[128:131], %[zo], %[rw], %[zb] offen sc1\n\t"
                    "buffer_load_dwordx4 v[132:135], %[zo], %[rw], %[zb] offen offset:1024 sc1\n\t"
                    "buffer_load_dwordx4 v[136:139], %[zo], %[rw], %[zb] offen offset:2048 sc1\n\t"
                    "buffer_load_dwordx4 v[140:143], %[zo], %[rw], %[zb] offen offset:3072 sc1\n\t"
                    "buffer_load_dwordx4 v[144:147], %[zo], %[rw], %[z4] offen sc1\n\t"
                    "buffer_load_dwordx4 v[148:151], %[zo], %[rw], %[z4] offen offset:1024 sc1\n\t"
                    "buffer_load_dwordx4 v[152:155], %[zo], %[rw], %[z4] offen offset:2048 sc1\n\t"
                    "buffer_load_dwordx4 v[156:159], %[zo], %[rw], %[z4] offen offset:3072 sc1\n\t"
                    "s_waitcnt vmcnt(0)\n\t"
                    "v_max3_u32 v101, v129, v131, v133\n\t"
                    "v_max3_u32 v101, v101, v135, v137\n\t"
                    "v_max3_u32 v101, v101, v139, v141\n\t"
                    "v_max3_u32 v101, v101, v143, v145\n\t"
                    "v_max3_u32 v101, v101, v147, v149\n\t"
                    "v_max3_u32 v101, v101, v151, v153\n\t"
                    "v_max3_u32 v101, v101, v155, v157\n\t"
                    "v_max_u32 v101, v101, v159\n\t"
                    "v_mov_b32 v100, v101\n\t"
                    "3:\n\t"
                    "v_permlane16_swap_b32 v128, v129\n\t"
                    "v_permlane16_swap_b32 v130, v131\n\t"
                    "v_permlane16_swap_b32 v132, v133\n\t"
                    "v_permlane16_swap_b32 v134, v135\n\t"
                    "v_permlane16_swap_b32 v136, v137\n\t"
                    "v_permlane16_swap_b32 v138, v139\n\t"
                    "v_permlane16_swap_b32 v140, v141\n\t"
                    "v_permlane16_swap_b32 v142, v143\n\t"
                    "v_permlane16_swap_b32 v144, v145\n\t"
                    "v_permlane16_swap_b32 v146, v147\n\t"
                    "v_permlane16_swap_b32 v148, v149\n\t"
                    "v_permlane16_swap_b32 v150, v151\n\t"
                    "v_permlane16_swap_b32 v152, v153\n\t"
                    "v_permlane16_swap_b32 v154, v155\n\t"
                    "v_permlane16_swap_b32 v156, v157\n\t"
                    "v_permlane16_swap_b32 v158, v159\n\t"
                    "s_mov_b32 vcc_lo, 0x00ff00ff\n\t"
                    "s_mov_b32 vcc_hi, 0x00ff00ff\n\t"
                    "s_waitcnt vmcnt(7)\n\t"
                    OST("%[t3]")
                    "v_smfmac_f32_16x16x64_bf16 v[96:99], v[208:211], v[160:167], %[ix]\n\t"
                    "v_max3_u32 v100, v100, v208, v209\n\t"
                    "v_max3_u32 v100, v100, v210, v211\n\t"
                    "s_waitcnt vmcnt(6)\n\t"
                    "v_smfmac_f32_16x16x64_bf16 v[96:99], v[212:215], v[168:175], %[ix]\n\t"
                    "v_max3_u32 v100, v100, v212, v213\n\t"
                    "v_max3_u32 v100, v100, v214, v215\n\t"
                    "s_waitcnt vmcnt(5)\n\t"
                    "v_smfmac_f32_16x16x64_bf16 v[96:99], v[216:219], v[176:183], %[ix]\n\t"
                    "v_max3_u32 v100, v100, v216, v217\n\t"
                    "v_max3_u32 v100, v100, v218, v219\n\t"
                    "s_waitcnt vmcnt(4)\n\t"
                    "v_smfmac_f32_16x16x64_bf16 v[96:99], v[220:223], v[184:191], %[ix]\n\t"
                    "v_max3_u32 v100, v100, v220, v221\n\t"
                    "v_max3_u32 v100, v100, v222, v223\n\t"
                    "s_waitcnt vmcnt(3)\n\t"
                    "v_smfmac_f32_16x16x64_bf16 v[96:99], v[224:227], v[128:135], %[ix]\n\t"
                    "v_max3_u32 v100, v100, v224, v225\n\t"
                    "v_max3_u32 v100, v100, v226, v227\n\t"
                    "s_waitcnt vmcnt(2)\n\t"
                    "v_smfmac_f32_16x16x64_bf16 v[96:99], v[228:231], v[136:143], %[ix]\n\t"
                    "s_barrier\n\t"
                    "ds_read_b64 v[102:103], %[sp]\n\t"
                    "ds_read_b64 v[104:105], %[sp] offset:512\n\t"
                    "ds_read_b64 v[106:107], %[sp] offset:1024\n\t"
                    "v_max3_u32 v100, v100, v228, v229\n\t"
                    "v_max3_u32 v100, v100, v230, v231\n\t"
                    "s_waitcnt vmcnt(1)\n\t"
                    "v_smfmac_f32_16x16x64_bf16 v[96:99], v[232:235], v[144:151], %[ix]\n\t"
                    "v_max3_u32 v100, v100, v232, v233\n\t"
                    "v_max3_u32 v100, v100, v234, v235\n\t"
                    "s_waitcnt vmcnt(0)\n\t"
                    "v_smfmac_f32_16x16x64_bf16 v[96:99], v[236:239], v[152:159], %[ix]\n\t"
                    "v_max3_u32 v100, v100, v236, v237\n\t"
                    "v_max3_u32 v100, v100, v238, v239\n\t"
                    OST("%[t2]")
                    "v_cmp_eq_u32 %[bad], -1, v100\n\t"
                    "s_waitcnt lgkmcnt(0)\n\t"
                    "v_add_f32 v102, v102, v104\n\t"
                    "v_add_f32 v103, v103, v105\n\t"
                    "v_add_f32 v102, v102, v106\n\t"
                    "v_add_f32 v103, v103, v107\n\t"
                    "v_add_f32 v96, v96, v102\n\t"
                    "v_add_f32 v97, v97, v103\n\t"
                    "v_add_f32 v96, v96, v98\n\t"
                    "v_add_f32 v97, v97, v99\n\t"
                    "s_cmp_lg_u64 %[bad], 0\n\t"
                    "s_cbranch_scc1 4f\n\t"
                    "2:\n\t"
                    "v_cndmask_b32_dpp v98, v97, v96, vcc row_ror:8 row_mask:0xf bank_mask:0xf\n\t"
                    "s_not_b64 vcc, vcc\n\t"
                    "v_cndmask_b32_dpp v99, v96, v97, vcc row_ror:8 row_mask:0xf bank_mask:0xf\n\t"
                    "v_cvt_pk_bf16_f32 v98, v98, v99\n\t"
                    "v_min_u32 v98, -2, v98\n\t"
                    "buffer_store_dword v98, %[mo], %[rw], %[m0] offen sc1\n\t"
                    "buffer_store_dword v98, %[mo], %[rw], %[m1] offen sc1\n\t"
                    "buffer_store_dword v98, %[mo], %[rw], %[m2] offen sc1\n\t"
                    "buffer_store_dword v98, %[mo], %[rw], %[m3] offen sc1\n\t"
                    OST("%[t4]")
                    "s_branch 5f\n\t"
                    "4:\n\t"
                    "s_sub_i32 %[tr], %[tr], 1\n\t"
                    "s_cmp_lt_i32 %[tr], 1\n\t"
                    "s_cbranch_scc1 2b\n\t"
                    "v_xor_b32 %[po], %[x0], %[po]\n\t"
                    "s_xor_b32 %[x0], %[x0], 0x5000\n\t"
                    "v_xor_b32 %[pm], %[x1], %[pm]\n\t"
                    "s_xor_b32 %[x1], %[x1], 0x5000\n\t"
                    "buffer_load_dwordx4 v[128:131], %[zo], %[rw], %[zb] offen sc1\n\t"
                    "buffer_load_dwordx4 v[132:135], %[zo], %[rw], %[zb] offen offset:1024 sc1\n\t"
                    "buffer_load_dwordx4 v[136:139], %[zo], %[rw], %[zb] offen offset:2048 sc1\n\t"
                    "buffer_load_dwordx4 v[140:143], %[zo], %[rw], %[zb] offen offset:3072 sc1\n\t"
                    "buffer_load_dwordx4 v[144:147], %[zo], %[rw], %[z4] offen sc1\n\t"
                    "buffer_load_dwordx4 v[148:151], %[zo], %[rw], %[z4] offen offset:1024 sc1\n\t"
                    "buffer_load_dwordx4 v[152:155], %[zo], %[rw], %[z4] offen offset:2048 sc1\n\t"
                    "buffer_load_dwordx4 v[156:159], %[zo], %[rw], %[z4] offen offset:3072 sc1\n\t"
                    "buffer_load_dwordx4 v[208:211], %[po], %[rw], %[pb] offen sc1\n\t"
                    "buffer_load_dwordx4 v[212:215], %[po], %[rw], %[pb] offen offset:1024 sc1\n\t"
                    "buffer_load_dwordx4 v[216:219], %[po], %[rw], %[pb] offen offset:2048 sc1\n\t"
                    "buffer_load_dwordx4 v[220:223], %[po], %[rw], %[pb] offen offset:3072 sc1\n\t"
                    "buffer_load_dwordx4 v[224:227], %[pm], %[rw], %[pb] offen sc1\n\t"
                    "buffer_load_dwordx4 v[228:231], %[pm], %[rw], %[pb] offen offset:1024 sc1\n\t"
                    "buffer_load_dwordx4 v[232:235], %[pm], %[rw], %[pb] offen offset:2048 sc1\n\t"
                    "buffer_load_dwordx4 v[236:239], %[pm], %[rw], %[pb] offen offset:3072 sc1\n\t"
                    "v_mov_b64 v[96:97], 0\n\t"
                    "v_mov_b64 v[98:99], 0\n\t"
                    "v_mov_b32 v100, 0\n\t"
                    "s_waitcnt vmcnt(0)\n\t"
                    "v_max3_u32 v101, v129, v131, v133\n\t"
                    "v_max3_u32 v101, v101, v135, v137\n\t"
                    "v_max3_u32 v101, v101, v139, v141\n\t"
                    "v_max3_u32 v101, v101, v143, v145\n\t"
                    "v_max3_u32 v101, v101, v147, v149\n\t"
                    "v_max3_u32 v101, v101, v151, v153\n\t"
                    "v_max3_u32 v101, v101, v155, v157\n\t"
                    "v_max_u32 v101, v101, v159\n\t"
                    "v_mov_b32 v100, v101\n\t"
                    "v_permlane16_swap_b32 v128, v129\n\t"
                    "v_permlane16_swap_b32 v130, v131\n\t"
                    "v_permlane16_swap_b32 v132, v133\n\t"
                    "v_permlane16_swap_b32 v134, v135\n\t"
                    "v_permlane16_swap_b32 v136, v137\n\t"
                    "v_permlane16_swap_b32 v138, v139\n\t"
                    "v_permlane16_swap_b32 v140, v141\n\t"
                    "v_permlane16_swap_b32 v142, v143\n\t"
                    "v_permlane16_swap_b32 v144, v145\n\t"
                    "v_permlane16_swap_b32 v146, v147\n\t"
                    "v_permlane16_swap_b32 v148, v149\n\t"
                    "v_permlane16_swap_b32 v150, v151\n\t"
                    "v_permlane16_swap_b32 v152, v153\n\t"
                    "v_permlane16_swap_b32 v154, v155\n\t"
                    "v_permlane16_swap_b32 v156, v157\n\t"
                    "v_permlane16_swap_b32 v158, v159\n\t"
                    "s_nop 1\n\t"
                    "v_smfmac_f32_16x16x64_bf16 v[96:99], v[208:211], v[160:167], %[ix]\n\t"
                    "v_max3_u32 v100, v100, v208, v209\n\t"
                    "v_max3_u32 v100, v100, v210, v211\n\t"
                    "v_smfmac_f32_16x16x64_bf16 v[96:99], v[212:215], v[168:175], %[ix]\n\t"
                    "v_max3_u32 v100, v100, v212, v213\n\t"
                    "v_max3_u32 v100, v100, v214, v215\n\t"
                    "v_smfmac_f32_16x16x64_bf16 v[96:99], v[216:219], v[176:183], %[ix]\n\t"
                    "v_max3_u32 v100, v100, v216, v217\n\t"
                    "v_max3_u32 v100, v100, v218, v219\n\t"
                    "v_smfmac_f32_16x16x64_bf16 v[96:99], v[220:223], v[184:191], %[ix]\n\t"
                    "v_max3_u32 v100, v100, v220, v221\n\t"
                    "v_max3_u32 v100, v100, v222, v223\n\t"
                    "v_smfmac_f32_16x16x64_bf16 v[96:99], v[224:227], v[128:135], %[ix]\n\t"
                    "v_max3_u32 v100, v100, v224, v225\n\t"
                    "v_max3_u32 v100, v100, v226, v227\n\t"
                    "v_smfmac_f32_16x16x64_bf16 v[96:99], v[228:231], v[136:143], %[ix]\n\t"
                    "v_max3_u32 v100, v100, v228, v229\n\t"
                    "v_max3_u32 v100, v100, v230, v231\n\t"
                    "v_smfmac_f32_16x16x64_bf16 v[96:99], v[232:235], v[144:151], %[ix]\n\t"
                    "v_max3_u32 v100, v100, v232, v233\n\t"
                    "v_max3_u32 v100, v100, v234, v235\n\t"
                    "v_smfmac_f32_16x16x64_bf16 v[96:99], v[236:239], v[152:159], %[ix]\n\t"
                    "v_max3_u32 v100, v100, v236, v237\n\t"
                    "v_max3_u32 v100, v100, v238, v239\n\t"
                    "v_cmp_eq_u32 %[bad], -1, v100\n\t"
                    "s_nop 5\n\t"
                    "v_add_f32 v96, v96, v102\n\t"
                    "v_add_f32 v97, v97, v103\n\t"
                    "v_add_f32 v96, v96, v98\n\t"
                    "v_add_f32 v97, v97, v99\n\t"
                    "s_cmp_lg_u64 %[bad], 0\n\t"
                    "s_cbranch_scc1 4b\n\t"
                    "s_branch 2b\n\t"
                    "5:\n\t"
                    : [tr] "+s"(tries), [po] "+v"(po), [pm] "+v"(pm), [x0] "+s"(x0), [x1] "+s"(x1), [bad] "=&s"(bad), [t0] "=&s"(t0_), [t1] "=&s"(t1_), [t2] "=&s"(t2_), [t3] "=&s"(t3_), [t4] "=&s"(t4_)
                    : [zo] "v"(zoff), [sp] "v"(spa), [ix] "v"(sidx), [rw] "s"(rwin), [zb] "s"(zb), [z4] "s"(zb + 4096u), [pb] "s"(pb), [mo] "v"(moff), [fd] "n"(FDL),
                      [m0] "s"(mb), [m1] "s"(mb + PAIR), [m2] "s"(mb + mc2), [m3] "s"(mb + mc2 + PAIR), "{v[160:167]}"(zf[0]), "{v[168:175]}"(zf[1]), "{v[176:183]}"(zf[2]), "{v[184:191]}"(zf[3])
                    : "v96", "v97", "v98", "v99", "v100", "v101", "v102", "v103", "v104", "v105", "v106", "v107", "v128", "v129", "v130", "v131", "v132", "v133", "v134", "v135", "v136", "v137", "v138", "v139", "v140", "v141", "v142", "v143", "v144", "v145", "v146", "v147", "v148", "v149", "v150", "v151", "v152", "v153", "v154", "v155", "v156", "v157", "v158", "v159", "v208", "v209", "v210", "v211", "v212", "v213", "v214", "v215", "v216", "v217", "v218", "v219", "v220", "v221", "v222", "v223", "v224", "v225", "v226", "v227", "v228", "v229", "v230", "v231", "v232", "v233", "v234", "v235", "v236", "v237", "v238", "v239", "vcc", "scc", "memory");
                budget = tries;
                if (w == 3) *(volatile LAS int*)(smem + 4100) = s;
                __builtin_amdgcn_s_sleep(DPS - 4);
                if (w == 3) {
                    st128(SENT, rwin, ra, W_MEM + (unsigned)(s & 3) * 6u * PAIR, 16);
                    if (lane == 0 && s > 1) __builtin_amdgcn_raw_buffer_store_b32((unsigned)s - 2u, rwin, 0u, W_GO + fo + (unsigned)o * 128u, 16);
                }
                for (;;) {
                    const u32x2 x = *(volatile LAS u32x2*)(smem + 4096);
                    zr = __builtin_amdgcn_readfirstlane(x[0]);
                    if (__builtin_amdgcn_readfirstlane(x[1]) == s) break;
                    __builtin_amdgcn_s_sleep(2);
                }
                zb = zb == W_ZM + 2u * NOWN * PAIR ? W_ZM : zb + NOWN * PAIR;
            }
            const unsigned zq = zb == W_ZM ? W_ZM + 2u * NOWN * PAIR : zb - NOWN * PAIR;
            while (np < 4) {
                while (zr < NSEG - 1) { __builtin_amdgcn_s_sleep(1); zr = __builtin_amdgcn_readfirstlane(*(volatile LAS int*)(smem + 4096)); }
                piece();
            }
            __builtin_amdgcn_s_sleep(DTK);
            for (int e = 0; e < 2; ++e) {
                f32x4 c[2];
                SST(16 + 3 * e);
                for (unsigned spins = 0;;) {
                    asm volatile("" ::: "memory");
                    SST(17 + 3 * e);
                    u32x4 pt[16], ptm[2][2], zm[2];
                    const unsigned pl = W_PL + fo + (unsigned)e * (PTSEG * 2u);
#pragma unroll
                    for (int q = 0; q < 2; ++q) zm[q] = __builtin_amdgcn_raw_buffer_load_b128(rwin, zoff + hg * 2048u + q * 1024u, e ? zb : zq, 16);
#pragma unroll
                    for (int q = 0; q < 16; ++q) pt[q] = __builtin_amdgcn_raw_buffer_load_b128(rwin, ((unsigned)(16 * hg) * 1024u + lane * 16u) | (q & 1 ? 0u : (lane & 32u) << 25), pl + q * 1024u, 16);
#pragma unroll
                    for (int q = 0; q < 4; ++q) ptm[q >> 1][q & 1] = __builtin_amdgcn_raw_buffer_load_b128(rwin, (unsigned)(hg * 2048 + lane * 16), pl + 65536u + (q >> 1) * 8192u + (q & 1) * 1024u, 16);
                    unsigned m = max(smax(zm[0]), smax(zm[1]));
#pragma unroll
                    for (int t = 0; t < 2; ++t) {
                        c[t] = z4;
#pragma unroll
                        for (int q = 0; q < 8; ++q) { c[t] = mfma32(half8(zf[q >> 1], q & 1), pt[2 * q + t], c[t]); m = max(m, smax(pt[2 * q + t])); }
#pragma unroll
                        for (int q = 0; q < 2; ++q) { c[t] = mfma32((u32x4){zm[q][0], zm[q][2], zm[q][1], zm[q][3]}, ptm[t][q], c[t]); m = max(m, smax(ptm[t][q])); }
                    }
                    if (!__any(m == 0xFFFFFFFFu)) break;
                    if (spin_fail(spins, rwin)) break;
                }
                SST(18 + 3 * e);
#pragma unroll
                for (int t = 0; t < 2; ++t) *(f32x4*)(op + e * 8192 + hg * 2048 + (16 * t + fr) * 64 + fq * 16) = c[t];
                BAR();
                VMWAIT();
#pragma unroll
                for (int q = 0; q < 4; ++q) zf[q] = cat8(*(const u32x4*)(smem + 32768 + hg * 8192 + q * 2048 + lane * 16), *(const u32x4*)(smem + 33792 + hg * 8192 + q * 2048 + lane * 16));
                if (e == 0) BAR0();
            }
            if (layer == 0) {
#pragma unroll
                for (int q = 0; q < 4; ++q) st128(SENT, rwin, zoff + hg * 2048u + (q & 1) * 1024u, q < 2 ? zb : zq, 16);
                if (w == 3) st128(SENT, rwin, ra, W_MEM + 3u * 6u * PAIR, 16);
            }
        } else if (w == 4) {
            const unsigned fl = fz_at(lane & 31, o / 10) + (lane & 32) * 128u;
            int zk = -1;
            auto note = [&](int f) INL {
                f = min(f, __builtin_amdgcn_update_dpp(f, f, 0xB1, 0xF, 0xF, false)); f = min(f, __builtin_amdgcn_update_dpp(f, f, 0x4E, 0xF, 0xF, false));
                f = min(f, __builtin_amdgcn_update_dpp(f, f, 0x141, 0xF, 0xF, false)); f = min(f, __builtin_amdgcn_update_dpp(f, f, 0x140, 0xF, 0xF, false));
                zk = seg_done(MXV, min(__builtin_amdgcn_readlane(f, 0), __builtin_amdgcn_readlane(f, 16)), min(__builtin_amdgcn_readlane(f, 32), __builtin_amdgcn_readlane(f, 48)), MXA);
                *(volatile LAS int*)(smem + 4096) = zk;
            };
            for (int s = 0; s < NSEG; ++s) {
                for (unsigned spins = 0; __builtin_expect(zk < s && s > 0, 0);) {
                    if (spins == 0) __builtin_amdgcn_s_sleep(DW4);
                    asm volatile("" ::: "memory");
                    note((int)__builtin_amdgcn_raw_buffer_load_b32(rwin, fl, W_FZ + fo, 16));
                    FC(fc2);
                    if (spin_fail(spins, rwin)) note(NSEG);
                    if (zk < s) __builtin_amdgcn_s_sleep(ZPS);
                }
                if (LKE && s > 0 && zk < (s + 8 >= NSEG ? NSEG - 1 : s + ZLA)) { __builtin_amdgcn_s_sleep(LKE); note((int)__builtin_amdgcn_raw_buffer_load_b32(rwin, fl, W_FZ + fo, 16)); }
                asm volatile("s_waitcnt lgkmcnt(0)\n\ts_barrier\n\ts_barrier" ::: "memory");
                int f = 0;
                const bool look = !LKE && zk < (s + 8 >= NSEG ? NSEG - 1 : s + ZLA);
                if (look) { __builtin_amdgcn_s_sleep(6); f = (int)__builtin_amdgcn_raw_buffer_load_b32(rwin, fl, W_FZ + fo, 16); }
                SB();
                BAR0();
                if (look) note(f);
            }
        } else if (w < 7) {
            const unsigned ooff = (unsigned)((S_ - (7 - w) * SEG + (lane >> 1)) * D_ + ot * 16 + (lane & 1) * 8) * 2u;
            const __amdgpu_buffer_rsrc_t ro = rout();
            for (int s = 0; s < 3 * NSEG - 3 + (w - 5); ++s) BAR0();
            if (w == 5) __builtin_amdgcn_s_sleep(DPS);
            const u32x4 xr = __builtin_amdgcn_raw_buffer_load_b128(rws, ooff, O_X1, 16);
            BAR0();
            BAR0();
            const char* src = op + (w - 5) * 8192 + lane * 32;
            f32x4 x = *(const f32x4*)src, y = *(const f32x4*)(src + 16);
#pragma unroll
            for (int ww = 1; ww < 4; ++ww) { x += *(const f32x4*)(src + ww * 2048); y += *(const f32x4*)(src + ww * 2048 + 16); }
            const u32x4 r = {pack2(lo2f(xr[0]) + bfr(x[0]), hi2f(xr[0]) + bfr(x[1])), pack2(lo2f(xr[1]) + bfr(x[2]), hi2f(xr[1]) + bfr(x[3])),
                             pack2(lo2f(xr[2]) + bfr(y[0]), hi2f(xr[2]) + bfr(y[1])), pack2(lo2f(xr[3]) + bfr(y[2]), hi2f(xr[3]) + bfr(y[3]))};
            st128(nosent4(r), ro, ooff, 0u, 16);
            RST(19 + w, layer, w, w & 3);
            if (w == 5) BAR0();
        } else {
            Tk tk = tk_init();
            __builtin_amdgcn_s_setprio(3);
            for (int s = 0; s < NSEG; ++s) {
                const unsigned ta0 = ck.ta, mm0 = ck.ma;
                unsigned tp = tpp + lt + xs + M1;
                if (s < SAD) {
                    if (s) wait_fine(tpp + CF, ck);
                    tp = M2 + xs + (s ? 0 : PEN1);
                    sched(tk, s);
                    tp = tpp - tp;
                    if (s == 0) { wait_fine(tp - DZ0, ck); *(volatile LAS int*)(smem + 4096) = 0; }
                }
                wait_fine_bar(tp - DZP, ck);
                wait_cyc_bar(cyc_at(ck, sx24(tp + (s + 1 < NSEG ? 0 : ML) - ck.ta)));
                clk_rate(ck, ta0, mm0);
                asm volatile("s_barrier ; 7" ::: "memory");
                if (s >= SAD && s + 1 < NSEG) sched(tk, s);
                RTS();
            }
        }
        __builtin_amdgcn_s_setprio(0);
        SST(23);
    };
    static_assert(NSEG % 3 == 1, "ring");
    auto a1_units = [&](auto late, int& u, int u1) INL {
        constexpr bool LATE = decltype(late)::v;
        int tid = mytid();
        RowTw tw;
        if (LATE) row_tw(tw, tid);
        for (; u < u1; u += !LATE ? 24 : u >= 232 && u < 240 ? 8 : 256) {
            if (!LATE) tid = mytid();
            if (w == 4) {
                for (unsigned spins = 0;;) {
                    asm volatile("" ::: "memory");
                    if (LATE) {
                        const int go = __builtin_amdgcn_readfirstlane(__builtin_amdgcn_raw_buffer_load_b32(rwin, 0u, W_GO + (unsigned)(u & 31) * 128u, 16)) - min((u >> 2) - 2, NSEG - 4);
                        if (go >= 0) break;
                        if (go < -3) __builtin_amdgcn_s_sleep(127);
                    } else if (!__any(__builtin_amdgcn_raw_buffer_load_b32(rwin, min(fresh_lane(), (unsigned)NOWN - 1u) * 4u, W_OUT + (unsigned)(u >> 2) * 256u, 16) != 0u)) break;
                    if (spin_fail(spins, rwin)) break;
                    if (u < 200) __builtin_amdgcn_s_sleep(WSLP); else __builtin_amdgcn_s_sleep(16);
                }
            }
            BAR();
            SST(24);
            a1_unit<4, LATE>(mkrsrc(ws + OFF_X2, S_ * D_ * 2), u * 8, rws, rwin, tw, tid, fs);
            if (!LATE) VMWAIT();
            FST(8);
            BAR0();
            FST(9);
            if (!LATE && w == 0) __builtin_amdgcn_raw_buffer_store_b64(__builtin_bit_cast(u32x2, tag + 9ull), rwin, LANE0, W_A1 + (unsigned)u * 128u, 16);
            SST(25);
            FSW(7);
            if (!LATE && u < 176) __builtin_amdgcn_s_sleep(A1S);
        }
    };
    auto out_role = [&](auto nt, int t0, int s0, auto between) INL {
        constexpr int NT = decltype(nt)::v, W0 = 8 - NT;
        const int o = s0 * 10 + (t0 >> 2), send = NSEG - 2;
        int rdy = -1, go = -1, zrdy = -1;
        for (int i = 0; i < s0; ++i) __builtin_amdgcn_s_sleep(OST0);
        for (int s = s0; s < send; s += 4) {
        LANES();
        const unsigned ztoff = (unsigned)((t0 * H_ + 4 * w) * 64 + lane) * 16u;
        const unsigned ptoff = (unsigned)(8 * w * 64 + lane) * 16u, ptmoff = 65536u + (unsigned)(4 * w * 16 + lane) * 16u;
        const unsigned ptoff0 = ptoff | (lane & 32u) << 25;
        const unsigned ooff = (unsigned)((lane >> 1) * D_ + tile_of(t0 + w - W0) * 16 + (lane & 1) * 8) * 2u;
        const unsigned fg = fo + (unsigned)((4 * w + (lane & 3)) * 128), gw = (unsigned)(t0 / 10) * 4u;
        const unsigned tfoff = fg + (lane < 16 ? W_FPT + (lane >> 2) * 4u : W_FZ2 + gw), zfoff = fo + W_FZ + fz_at(4 * w + (lane & 3), gw / 4u) + (lane & 32u) * 128u;
        auto wait_rdy = [&](int s) INL {
            for (unsigned spins = 0; rdy < s;) {
                asm volatile("" ::: "memory");
                if (s < TAILS || go < s - 2) {
                    go = (int)__builtin_amdgcn_readfirstlane(__builtin_amdgcn_raw_buffer_load_b32(rwin, 0u, W_GO + fo + (unsigned)o * 128u, 16));
                    rdy = max(rdy, go);
                    if (rdy < s) { if (s < TAILS || go < s - 3) __builtin_amdgcn_s_sleep(OSLP); else __builtin_amdgcn_s_sleep(OSLP3); }
                } else {
                    if (!__any((int)__builtin_amdgcn_raw_buffer_load_b32(rwin, tfoff, 0u, 16) < s)) rdy = s;
                    else __builtin_amdgcn_s_sleep(OSLP2);
                }
                if (rdy < s && spin_fail(spins, rwin)) break;
            }
        };
        const __amdgpu_buffer_rsrc_t ro = rout();
        u32x4 zf[NT][4];
        auto fetch_z = [&](int s) INL {
            for (unsigned spins = 0; zrdy < s;) {
                asm volatile("" ::: "memory");
                int f = (int)__builtin_amdgcn_raw_buffer_load_b32(rwin, zfoff, 0u, 16);
                f = min(f, __builtin_amdgcn_update_dpp(f, f, 0xB1, 0xF, 0xF, false)); f = min(f, __builtin_amdgcn_update_dpp(f, f, 0x4E, 0xF, 0xF, false));
                f = min(f, __builtin_amdgcn_update_dpp(f, f, 0x141, 0xF, 0xF, false));
                zrdy = seg_done(MXV, __builtin_amdgcn_readlane(f, 0), __builtin_amdgcn_readlane(f, 32));
                if (zrdy >= s || spin_fail(spins, rwin)) break;
                __builtin_amdgcn_s_sleep(OSLP);
            }
#pragma unroll
            for (int q = 0; q < 4 * NT; ++q) zf[q >> 2][q & 3] = __builtin_amdgcn_raw_buffer_load_b128(rws, ztoff + (q & 3) * 1024u, O_Z + (unsigned)((s - layer) * (int)ZTSEG) * 2u + (q >> 2) * (H_ * 1024u), 16);
        };
            u32x4 xr, zx[NT], pt[8], ptm[2];
            RT(0);
            fetch_z(s);
            if (NT > 1 && w >= W0 && layer == 0 && s >= 4 && s < 60) {
                asm volatile("s_waitcnt vmcnt(%0)" :: "n"(4 * NT) : "memory");
                __builtin_amdgcn_raw_buffer_store_b32(0u, rwin, LANE0, W_OUT + (unsigned)((s - 4) * 64 + t0 + w - W0) * 4u, 16);
            }
            if (w >= W0) xr = __builtin_amdgcn_raw_buffer_load_b128(rws, ooff, O_X1 + (unsigned)(s * SEG * D_ * 2), 16);
            if (s == 61) SST(27);
            wait_rdy(s);
            if (s == 61) SST(28);
            RT(1);
            const unsigned ptb = O_PT + (unsigned)(s * PTSEG) * 2u;
#pragma unroll
            for (int q = 0; q < 8; ++q) pt[q] = __builtin_amdgcn_raw_buffer_load_b128(rws, (q & 1 ? ptoff : ptoff0) + q * 1024u, ptb, 16);
#pragma unroll
            for (int q = 0; q < 2; ++q) ptm[q] = __builtin_amdgcn_raw_buffer_load_b128(rws, ptmoff + q * 8192u, ptb, 16);
#pragma unroll
            for (int q = 0; q < NT; ++q) zx[q] = __builtin_amdgcn_raw_buffer_load_b128(rws, tid * 16u, O_Z2 + (unsigned)(s * NOWN + t0 + q) * 8192u, 16);
            BAR0();
            f32x4 c[2 * NT];
#pragma unroll
            for (int q = 0; q < 4; ++q) {
#pragma unroll
                for (int u = 0; u < 2 * NT; ++u) c[u] = mfma32(zf[u >> 1][q], pt[2 * q + (u & 1)], q ? c[u] : z4);
                SB();
            }
#pragma unroll
            for (int u = 0; u < 2 * NT; ++u) {
                const int tq = u & 1, t = u >> 1;
                c[u] = mfma32((u32x4){zx[t][0], zx[t][2], zx[t][1], zx[t][3]}, ptm[tq], c[u]);
                *(f32x4*)(smem + w * (NT * 2048) + t * 2048 + (16 * tq + fr) * 64 + ((fq ^ ((fr >> 1) & 3)) << 4)) = c[u];
            }
            BAR();
            if (s == 61) SST(30);
            if (w >= W0) {
                const char* p0 = smem + (w - W0) * 2048 + (lane >> 1) * 64 + (((2 * (lane & 1)) ^ ((lane >> 2) & 3)) << 4);
                const char* p1 = smem + (w - W0) * 2048 + (lane >> 1) * 64 + (((2 * (lane & 1) + 1) ^ ((lane >> 2) & 3)) << 4);
                f32x4 x = *(const f32x4*)p0, y = *(const f32x4*)p1;
#pragma unroll
                for (int ww = 1; ww < 8; ++ww) { x += *(const f32x4*)(p0 + ww * (NT * 2048)); y += *(const f32x4*)(p1 + ww * (NT * 2048)); }
                const u32x4 r = {pack2(lo2f(xr[0]) + bfr(x[0]), hi2f(xr[0]) + bfr(x[1])), pack2(lo2f(xr[1]) + bfr(x[2]), hi2f(xr[1]) + bfr(x[3])),
                                 pack2(lo2f(xr[2]) + bfr(y[0]), hi2f(xr[2]) + bfr(y[1])), pack2(lo2f(xr[3]) + bfr(y[2]), hi2f(xr[3]) + bfr(y[3]))};
                if (layer) st128(r, ro, ooff, (unsigned)(s * SEG * D_ * 2), OAUX);
                else {
                    st128(nosent4(r), ro, ooff, (unsigned)(s * SEG * D_ * 2), 16);
                    if (NT == 1 && s < 56) { VMWAIT(); __builtin_amdgcn_raw_buffer_store_b32(0u, rwin, LANE0, W_OUT + (unsigned)(s * 64 + t0) * 4u, 16); }
                }
            } else __builtin_amdgcn_s_sleep(OSL4);
            if (s < 8 && NT > 1) SST(8 + s);
            if (s >= 56) SST(s - 40);
            RT(2);
            RTS();
            between(s);
        }
    };
    u32x8 wt[2][10];
#define PINW() asm volatile("" : "+{v[0:7]}"(wt[0][0]), "+{v[8:15]}"(wt[1][0]), "+{v[16:23]}"(wt[0][1]), "+{v[24:31]}"(wt[1][1]), "+{v[32:39]}"(wt[0][2]), "+{v[40:47]}"(wt[1][2]), "+{v[48:55]}"(wt[0][3]), "+{v[56:63]}"(wt[1][3]), "+{v[64:71]}"(wt[0][4]), "+{v[72:79]}"(wt[1][4]), "+{v[80:87]}"(wt[0][5]), "+{v[88:95]}"(wt[1][5]), "+{v[96:103]}"(wt[0][6]), "+{v[104:111]}"(wt[1][6]), "+{v[112:119]}"(wt[0][7]), "+{v[120:127]}"(wt[1][7]), "+{v[128:135]}"(wt[0][8]), "+{v[136:143]}"(wt[1][8]), "+{v[144:151]}"(wt[0][9]), "+{v[152:159]}"(wt[1][9]))
#define KILLW() asm volatile("" : "={v[0:7]}"(wt[0][0]), "={v[8:15]}"(wt[1][0]), "={v[16:23]}"(wt[0][1]), "={v[24:31]}"(wt[1][1]), "={v[32:39]}"(wt[0][2]), "={v[40:47]}"(wt[1][2]), "={v[48:55]}"(wt[0][3]), "={v[56:63]}"(wt[1][3]), "={v[64:71]}"(wt[0][4]), "={v[72:79]}"(wt[1][4]), "={v[80:87]}"(wt[0][5]), "={v[88:95]}"(wt[1][5]), "={v[96:103]}"(wt[0][6]), "={v[104:111]}"(wt[1][6]), "={v[112:119]}"(wt[0][7]), "={v[120:127]}"(wt[1][7]), "={v[128:135]}"(wt[0][8]), "={v[136:143]}"(wt[1][8]), "={v[144:151]}"(wt[0][9]), "={v[152:159]}"(wt[1][9]))
    if (blockIdx.x < 64) {
        for (;;) {
            load_qv(wt, bid & 31, bid >= 32, [&]() INL {
                rot0 = __builtin_amdgcn_raw_buffer_load_b64(rws, 0u, 128u, 16);
                if (w == 0) wait_tags(rwin, 0, 1, 64u, tag + 8ull, true);
                if (w == 7) clk_init(ck);
                if (w == 1) { const unsigned x = __builtin_amdgcn_raw_buffer_load_b32(mkrsrc(arg(A_MEM0), NM * D_ * 2), fresh_lane() * 160u, 0, 0); asm volatile("" :: "v"(x)); }
            });
            const unsigned x = get_rot();
            if (__builtin_expect(x == vx, 1)) break;
            set_role(x);
        }
        SST(1);
        PINW();
    } else {
        if (bid >= NWRK) probe();
        KILLW();
    }
    for (; layer < 2; ++layer) {
        new_layer();
        PINW();
        BAR();
        if (qx || (layer ? bid < 96 : (bid >= 32 && bid < 64) || (bid >= 104 && bid < NREC) || xq)) {
            const int ci = qx ? bid - 80 : bid < 96 ? bid : bid < NREC ? bid - 104 : 64 + (bid - NREC) / 10 * 8 + (bid - NREC) % 10;
            SST(26);
            a2_unit(ci >= 100 ? ci - 100 : ci + 60, layer, rws, rwin, mytid(), [&](int k) INL {
                if (k == 2) SST(28);
                else if (w == 0) {
                    if (layer && bid >= 64) for (unsigned spins = 0;;) {
                        asm volatile("" ::: "memory");
                        if (__all(__builtin_bit_cast(u64, __builtin_amdgcn_raw_buffer_load_b64(rwin, 0u, W_A1 + (unsigned)(200 + (bid & 15)) * 128u, 16)) == tag + 9ull) || spin_fail(spins, rwin)) break;
                        __builtin_amdgcn_s_sleep(100);
                    }
                    if (layer && bid >= 64) rec_end(GATE);
                    wait_tags(rwin, 0, 4, layer ? 32u : 64u, tag + 8ull + layer, !layer);
                    SST(27);
                }
            }, fs);
            SST(29);
            VMWAIT();
            FST(8);
            BAR0();
            FST(9);
            if (w == 0) {
                __builtin_amdgcn_raw_buffer_store_b32(0u, rwin, LANE0, W_COL + fo + (unsigned)ci * 128u, 16);
                if (bid == NQT && layer == 1) __builtin_amdgcn_raw_buffer_store_b64(__builtin_bit_cast(u32x2, tag), rws, LANE0, 0u, 16);
            }
            SST(30);
            FSW(1 + layer);
        }
#define NEXTB() do { PINW(); asm volatile("" : "+s"(bid), "+s"(w)); } while (0)
        NEXTB();
        if (bid < 32) { fresh_rsrc(); qk_role(wt); PINW(); }
        NEXTB();
        const int hh = bid < 96 ? bid & 31 : bid < NQT ? (bid - NREC) / 10 * 8 + (bid - NREC) % 10 : (bid - NQT) & 31;
        if (((qx || (bid >= 32 && bid < 64)) && layer == 0) || xq) { if (layer) { if (w == 0) rec_end(GATX); BAR0(); } load_qv(wt, hh, xt || bid < 64, []() {}); SST(1); PINW(); }
        NEXTB();
        if (bid >= 32 && bid < 64) { if (layer == 0) near_init(); fresh_rsrc(); vo_role(wt); PINW(); }
        NEXTB();
        if ((bid >= 64 && bid < 96) || xt) {
            fresh_rsrc();
            vt_role(wt, hh, xt ? MXV : ~MXV);
            if (xt) {
                out_role(IC<1>(), 5 * (hh & 7) + (hh & 1 ? 0 : 4), hh >> 3, [&](int s) INL {
                    if (s < 28) {
                        if (layer == 0) for (unsigned i = (s >> 2) * 2; i < (s >> 2) * 2 + 2; ++i)
                            st128(SENT, i < 7 ? mkrsrc(ws + O_ARM, O_ARM1 - O_ARM) : i < 12 ? mkrsrc(ws + O_Z - ZTSEG * 2, ZTSEG * 2) : mkrsrc(arg(1) + (W_ARM - W_LAYER) / 2, W_LAYER),
                                  (unsigned)(hh * 512 + mytid()) * 16u, (i < 7 ? i : i < 12 ? i - 7 : i - 12) * 262144u, 16);
                    } else if (s < 32) {
                        VMWAIT();
                        BAR0();
                        if (w >= 4) __builtin_amdgcn_raw_buffer_store_b32(NSEG - 1, rwin, LANE0, W_FZ + fo + fz_at(hh, w - 4) + 4096u, 16);
                    }
                });
                static_assert(O_ARM + 7u * 262144u >= O_ARM1 && ZTSEG * 2 <= 5u * 262144u && W_LAYER <= 262144u && !(MXV >> 40), "arming");
            }
            PINW();
        }
        NEXTB();
        if ((qx && !xt) || xq) { fresh_rsrc(); qt_role(wt, hh, IC<1>()); PINW(); }
        NEXTB();
        if (bid >= 96 && !qx) {
            const int fi = xq ? 256 : bid >= NWRK ? 255 - bid : bid >= NREC ? 8 + (bid - NREC) / 10 * 2 + (bid - NREC) % 10 : bid - 72, tid = mytid();
            RowTw tw;
            row_tw(tw, tid);
            if (layer == 0 && fi >= 16 && fi < 32) { if (w == 0) wait_tags(rwin, 0, 1, 64u, tag + 8ull, true); BAR0(); }
            if (layer && fi < 64) {
                for (unsigned spins = 0;;) {
                    asm volatile("" ::: "memory");
                    if (__all(__builtin_bit_cast(u64, __builtin_amdgcn_raw_buffer_load_b64(rwin, 0u, W_A1 + (unsigned)fi * 128u, 16)) == tag + 9ull) || spin_fail(spins, rwin)) break;
                    __builtin_amdgcn_s_sleep(8);
                }
            }
            bool cols = false;
            for (int u = fi; u < 256; u += 24) {
                a3_unit(mkrsrc(layer ? (const u16*)(ws + OFF_X2) : arg(A_X), S_ * D_ * 2), u * 8, layer, rws, rwin, tw, tid, [&]() INL {
                    if (cols) return;
                    cols = true;
                    if (w == 7) {
                        const unsigned l = fresh_lane();
                        unsigned o[3] = {l * 128u, l * 128u + 8192u, l < 32u ? l * 128u + 16384u : 0x40000000u};
                        for (unsigned spins = 0;;) {
                            asm volatile("" ::: "memory");
                            unsigned v[3];
#pragma unroll
                            for (int g = 0; g < 3; ++g) v[g] = __builtin_amdgcn_raw_buffer_load_b32(rwin, o[g], W_COL + fo, 16);
#pragma unroll
                            for (int g = 0; g < 3; ++g) if (v[g] == 0u) o[g] = 0x40000000u;
                            if (!__any((v[0] | v[1] | v[2]) != 0u) || spin_fail(spins, rwin)) break;
                            __builtin_amdgcn_s_sleep(CSLP);
                        }
                    }
                    BAR0();
                    SST(6);
                }, fs);
                VMWAIT();
                FST(8);
                BAR0();
                FST(9);
                if (w == 0) __builtin_amdgcn_raw_buffer_store_b32(0u, rwin, LANE0, W_H + fo + (unsigned)((u >> 2) * 128 + (u & 3) * 4), 16);
                if (u < 64) SST(7);
                FSW(u < 64 ? 3 + layer : 5);
                if (fi >= 24) break;
                if (u < 64) u += 40;
            }
            if (bid < NREC) { fresh_rsrc(); own_role(); }
            else if (xq) out_role(IC<4>(), 5 * (hh & 7) + (hh & 1), hh >> 3, [](int) {});
            else if (layer == 0) { int u = fi; a1_units(IC<0>(), u, 224); u = bid < NQT ? 192 + (bid - NREC) / 10 + 4 * ((bid - NREC) % 10) : bid < 248 ? bid - 8 : bid; a1_units(IC<1>(), u, 256); }
            KILLW();
        }
        NEXTB();
        SST(31);
    }
}

constexpr int L_DBG = 0;
extern "C" size_t dbg_offset(int i) {
    const size_t o[] = {OFF_WIN, W_TMO, W_NEAR, OFF_F, OFF_X1, OFF_X2, OFF_H, OFF_WFT};
    return o[i];
}

extern "C" void kernel_launch(void* const* d_in, const int* in_sizes, int n_in, void* d_out, int out_size, void* d_ws, size_t ws_size,
                              hipStream_t stream) {
    char* ws = (char*)d_ws;
    hipFuncSetAttribute((const void*)mega_kernel, hipFuncAttributeMaxDynamicSharedMemorySize, L_END + L_DBG);
    char* win = (char*)(((uintptr_t)(ws + OFF_WIN) + 0x1FFFFFu) & ~(uintptr_t)0x1FFFFFu);
    Args a{ws, win, (const u16*)d_in[0], (const u16*)d_in[1], (const u16*)d_in[2], (const u16*)d_in[4], (const u16*)d_in[5], (const u16*)d_in[6], (const u16*)d_in[7], (const u16*)d_in[8], (u16*)d_out};
    mega_kernel<<<256, 512, L_END + L_DBG, stream>>>(a);
}
```

```cpp
#define TLBW 18
#include <hip/hip_runtime.h>
#include <hip/hip_bf16.h>
#include <stdint.h>

typedef unsigned short u16;
typedef unsigned long long u64;
typedef __attribute__((ext_vector_type(8))) __bf16 bf16x8;
typedef __attribute__((ext_vector_type(4))) short s16x4;
typedef __attribute__((ext_vector_type(4))) float f32x4;
typedef __attribute__((ext_vector_type(8))) float f32x8;
typedef __attribute__((ext_vector_type(16))) float f32x16;
typedef __attribute__((ext_vector_type(4))) unsigned u32x4;
typedef __attribute__((ext_vector_type(2))) unsigned u32x2;
typedef __attribute__((ext_vector_type(8))) unsigned u32x8;
typedef __attribute__((ext_vector_type(16))) __bf16 bf16x16;
typedef __attribute__((ext_vector_type(2))) unsigned short u16x2;
#define LAS __attribute__((address_space(3)))
#define INL __attribute__((always_inline))

constexpr int S_ = 2048, D_ = 640, DF = 321, H_ = 32, DH = 128, HD = 4096, NM = 8, SEG = 32, NSEG = 64;
constexpr int NOWN = 40;
constexpr int NCOL = 160;
constexpr size_t ZTSEG = (size_t)NOWN * H_ * 512;
constexpr unsigned PAIR = 16384, HALF = 8192;
constexpr unsigned W_MEM = 0, W_T = W_MEM + 24 * PAIR, W_P = 33 * PAIR, W_PROBE = 74 * PAIR, W_GO = W_PROBE + 2 * PAIR, W_FZ = W_GO + 5120, W_FPT = W_FZ + 8192, W_FZ2 = W_FPT + 4096,
                   W_FQ = W_FZ2 + 4096, W_H = W_FQ + 8192, W_COL = W_H + HALF, W_PL = W_COL + 20480, W_LAYER = W_PL + 2 * 81920 - W_GO, W_OUT = 32 * PAIR, W_MISC = W_OUT + 14336,
                   W_ARM = W_GO + 2 * W_LAYER, W_A1 = 106 * PAIR, W_ZM = 2u * 1024 * 1024, W_END = W_ZM + 120 * PAIR;
static_assert(W_ARM <= W_A1 && W_ARM % 1024 == 0, "window");
__device__ __forceinline__ unsigned p_slot(int s) { return W_P + (unsigned)((s & 3) * 10 + (((s & 3) + 1) >> 2)) * PAIR; }
constexpr unsigned W_TMO = W_MISC, W_NEAR = W_MISC + 128;
constexpr int PTSEG = 32768 + 8192;

#define FPAD 256
constexpr unsigned FST_ = S_ * 16 + FPAD, GST = 4096 + FPAD, WST = S_ * 8 + FPAD;
constexpr size_t al(size_t x) { return (x + 255) & ~(size_t)255; }
constexpr size_t OFF_CTL  = 0;
constexpr size_t OFF_WFT  = 4096 + 4 * 1024 * 1024;
constexpr size_t OFF_F    = OFF_WFT + al((size_t)(NCOL + 2) * WST);
constexpr size_t OFF_X1   = OFF_F + al((size_t)NCOL * FST_);
constexpr size_t OFF_X2   = OFF_X1 + al((size_t)S_ * D_ * 2);
constexpr size_t OFF_FL   = OFF_X2 + al((size_t)S_ * D_ * 2);
constexpr int NHF = 5;
constexpr size_t OFF_H    = OFF_FL + 3 * (size_t)NCOL * GST + NHF * 40960;
constexpr size_t OFF_Z    = OFF_H + al((size_t)S_ * D_ * 2) + ZTSEG * 2;
constexpr size_t OFF_Z2   = OFF_Z + al(ZTSEG * NSEG * 2);
constexpr size_t OFF_PT   = OFF_Z2 + al((size_t)NSEG * NOWN * 8192);
constexpr size_t OFF_QK   = OFF_PT + al((size_t)NSEG * PTSEG * 2);
constexpr size_t OFF_WIN  = OFF_QK + (size_t)NSEG * H_ * 16384;
constexpr size_t OFF_END  = OFF_WIN + (size_t)6 * 1024 * 1024;
static_assert(OFF_END <= (size_t)256 * 1024 * 1024, "workspace");
constexpr unsigned O_FL = (unsigned)OFF_FL, O_ARM = O_FL - 256 * D_ * 2, O_ARM1 = O_FL + 2 * NCOL * GST, O_WFT = (unsigned)OFF_WFT, O_F = (unsigned)OFF_F, O_H = (unsigned)OFF_H, O_X1 = (unsigned)OFF_X1, O_Z = (unsigned)OFF_Z, O_Z2 = (unsigned)OFF_Z2, O_PT = (unsigned)OFF_PT, O_QK = (unsigned)OFF_QK;

constexpr double PI_ = 3.14159265358979323846;
constexpr double tay(double x, int s) {
    double t = s ? x : 1.0, r = t;
    for (int i = s + 1; i < 18; i += 2) { t *= -x * x / (double)(i * (i + 1)); r += t; }
    return r;
}
struct Cs { float c, s; };
constexpr Cs cis(int m, int n) {
    m %= n;
    const int q = (m * 8 / n + 1) / 2;
    const double x = 2.0 * PI_ * (double)(4 * m - q * n) / (double)(4 * n), c = tay(x, 0), s = tay(x, 1);
    return (q & 3) == 0 ? Cs{(float)c, (float)-s} : (q & 3) == 1 ? Cs{(float)-s, (float)-c} : (q & 3) == 2 ? Cs{(float)-c, (float)s} : Cs{(float)s, (float)c};
}
template <int NI> struct alignas(16) Tw7 { Cs v[4][NI][2]; };
template <int NI, int N, int KIND> constexpr Tw7<NI> mk_tw() {
    Tw7<NI> r{};
    for (int i = 0; i < NI; ++i)
        for (int q = 1; q <= 8; ++q) r.v[(q - 1) >> 1][i][(q - 1) & 1] = cis((KIND == 0 ? i : KIND == 1 ? 8 * (i >> 3) : (i >> 3) + 10 * (i & 7)) * q, N);
    return r;
}
struct TwCol { Tw7<256> a; Tw7<32> b; Tw7<4> c; };
static __device__ const TwCol TWC = {mk_tw<256, 2048, 0>(), mk_tw<32, 256, 0>(), mk_tw<4, 32, 0>()};
static __device__ const Tw7<80> TWR2 = mk_tw<80, 640, 1>();
static __device__ const Tw7<80> TWR3 = mk_tw<80, 640, 2>();

#define st128(v, r, vo, so, aux) do { u32x4 d_ = (v); asm volatile("" : "+v"(d_)); __builtin_amdgcn_raw_buffer_store_b128(d_, r, vo, so, aux); asm volatile("s_nop 1" :: "v"(d_)); } while (0)
__device__ __forceinline__ float bf2f(u16 v) { return __uint_as_float((unsigned)v << 16); }
__device__ __forceinline__ u16 f2bf(float f) { __bf16 b = (__bf16)f; return __builtin_bit_cast(u16, b); }
__device__ __forceinline__ float bfr(float f) { return (float)(__bf16)f; }
typedef __attribute__((ext_vector_type(2))) float f32x2;
typedef __attribute__((ext_vector_type(2))) __bf16 bf16x2;
__device__ __forceinline__ unsigned pack2(float a, float b) { return __builtin_bit_cast(unsigned, __builtin_convertvector((f32x2){a, b}, bf16x2)); }
__device__ __forceinline__ float lo2f(unsigned v) { return __uint_as_float(v << 16); }
__device__ __forceinline__ float hi2f(unsigned v) { return __uint_as_float(v & 0xFFFF0000u); }
__device__ __forceinline__ float fmx(float a, float b) { return __builtin_elementwise_maximum(a, b); }
__device__ __forceinline__ f32x4 mfma32(u32x4 a, u32x4 b, f32x4 c) {
    return __builtin_amdgcn_mfma_f32_16x16x32_bf16(__builtin_bit_cast(bf16x8, a), __builtin_bit_cast(bf16x8, b), c, 0, 0, 0);
}
__device__ __forceinline__ f32x4 mfma16(u32x2 a, u32x2 b, f32x4 c) {
    return __builtin_amdgcn_mfma_f32_16x16x16bf16_1k(__builtin_bit_cast(s16x4, a), __builtin_bit_cast(s16x4, b), c, 0, 0, 0);
}
__device__ __forceinline__ f32x4 smfmac(u32x4 a, u32x8 b, f32x4 c, int idx) {
    return __builtin_amdgcn_smfmac_f32_16x16x64_bf16(__builtin_bit_cast(bf16x8, a), __builtin_bit_cast(bf16x16, b), c, idx, 0, 0);
}
__device__ __forceinline__ int pi16(int k) { return ((k & 3) << 2) | ((k >> 1) & 2) | (k >> 3); }
__device__ __forceinline__ float addswap32(float a, float b) {
    auto r = __builtin_amdgcn_permlane32_swap(__float_as_uint(a), __float_as_uint(b), false, false);
    return __uint_as_float(r[0]) + __uint_as_float(r[1]);
}
__device__ __forceinline__ float xmax3216(float v) {
    auto r = __builtin_amdgcn_permlane32_swap(__float_as_uint(v), __float_as_uint(v), false, false);
    v = fmx(__uint_as_float(r[0]), __uint_as_float(r[1]));
    auto q = __builtin_amdgcn_permlane16_swap(__float_as_uint(v), __float_as_uint(v), false, false);
    return fmx(__uint_as_float(q[0]), __uint_as_float(q[1]));
}
__device__ __forceinline__ float xsum3216(float v) {
    auto r = __builtin_amdgcn_permlane32_swap(__float_as_uint(v), __float_as_uint(v), false, false);
    v = __uint_as_float(r[0]) + __uint_as_float(r[1]);
    auto q = __builtin_amdgcn_permlane16_swap(__float_as_uint(v), __float_as_uint(v), false, false);
    return __uint_as_float(q[0]) + __uint_as_float(q[1]);
}
template <int CTRL> __device__ __forceinline__ float dppf(float v) { return __uint_as_float((unsigned)__builtin_amdgcn_update_dpp(0, (int)__float_as_uint(v), CTRL, 0xF, 0xF, false)); }
__device__ __forceinline__ float wsum(float v) {
    v += dppf<0xB1>(v); v += dppf<0x4E>(v); v += dppf<0x141>(v); v += dppf<0x140>(v);
    return xsum3216(v);
}
__device__ __forceinline__ unsigned ror8(unsigned v) { return (unsigned)__builtin_amdgcn_update_dpp(0, (int)v, 0x128, 0xF, 0xF, false); }
__device__ __forceinline__ float ror8f(float v) { return __uint_as_float(ror8(__float_as_uint(v))); }
__device__ __forceinline__ unsigned nosent(unsigned v) {
    u16x2 lim = {0xFFFE, 0xFFFE};
    return __builtin_bit_cast(unsigned, __builtin_elementwise_min(__builtin_bit_cast(u16x2, v), lim));
}
__device__ __forceinline__ unsigned pkmax(unsigned a, unsigned b) {
    return __builtin_bit_cast(unsigned, __builtin_elementwise_max(__builtin_bit_cast(u16x2, a), __builtin_bit_cast(u16x2, b)));
}
__device__ __forceinline__ bool has_sent(u32x4 v) {
    unsigned m = pkmax(pkmax(v.x, v.y), pkmax(v.z, v.w));
    return (m & 0xFFFFu) == 0xFFFFu || (m >> 16) == 0xFFFFu;
}

__device__ __forceinline__ unsigned nosent1(unsigned v) { return min(v, 0xFFFFFFFEu); }
__device__ __forceinline__ unsigned smax(u32x4 v) { return max(v[1], v[3]); }
__device__ __forceinline__ u32x4 sent4() { unsigned x; asm volatile("v_mov_b32 %0, -1" : "=v"(x)); return (u32x4){x, x, x, x}; }
#define SENT sent4()
__device__ __forceinline__ f32x4 zero4() { f32x2 x, y; asm volatile("v_mov_b64 %0, 0\n\tv_mov_b64 %1, 0" : "=v"(x), "=v"(y)); return (f32x4){x[0], x[1], y[0], y[1]}; }
__device__ __forceinline__ u32x4 nosent4(u32x4 v) { return (u32x4){nosent1(v[0]), nosent1(v[1]), nosent1(v[2]), nosent1(v[3])}; }
#define SPIN_CAP 400000u
__device__ __forceinline__ void set_tmo(__amdgpu_buffer_rsrc_t rwin) { __builtin_amdgcn_raw_buffer_store_b32(0u, rwin, 0u, W_TMO, 16); }
__device__ __forceinline__ bool spin_fail(unsigned& spins, __amdgpu_buffer_rsrc_t rwin) {
    if (++spins > SPIN_CAP) { set_tmo(rwin); return true; }
    return (spins & 1023u) == 0u && __builtin_amdgcn_readfirstlane(__builtin_amdgcn_raw_buffer_load_b32(rwin, 0u, W_TMO, 16)) == 0u;
}
#define GATE 2
#define GATX 2
__device__ __forceinline__ void tell_at(__amdgpu_buffer_rsrc_t rwin, unsigned so, unsigned d) {
    __builtin_amdgcn_raw_buffer_store_b32(((unsigned)__builtin_amdgcn_s_memrealtime() + d) & 0xFFFFFFu, rwin, 0u, so, 16);
}
template <int N> __device__ __forceinline__ void wait_told(__amdgpu_buffer_rsrc_t rwin, const unsigned (&vo)[N], unsigned so, int slp) {
    unsigned v[N];
#pragma unroll
    for (int g = 0; g < N; ++g) v[g] = ~0u;
    for (unsigned spins = 0;;) {
        asm volatile("" ::: "memory");
        unsigned x[N], m = 0;
#pragma unroll
        for (int g = 0; g < N; ++g) x[g] = __builtin_amdgcn_raw_buffer_load_b32(rwin, v[g] == ~0u ? vo[g] : 0x40000000u, so, 16);
#pragma unroll
        for (int g = 0; g < N; ++g) { if (v[g] == ~0u) v[g] = x[g]; m = max(m, v[g]); }
        if (!__any(m == ~0u) || spin_fail(spins, rwin)) break;
        if (slp) __builtin_amdgcn_s_sleep(8); else __builtin_amdgcn_s_sleep(3);
    }
    for (int i = 0; i < 400; ++i) {
        const unsigned t = (unsigned)__builtin_amdgcn_s_memrealtime();
        bool e = false;
#pragma unroll
        for (int g = 0; g < N; ++g) e |= ((v[g] - t) << 8) - 1u < 0x7FFFFFFFu;
        if (!__any(e)) break;
    }
}
#define TG 250
#define TH 155
#define TH0 185
#define DVT0 950
#define A2P 0
#define TWG 1
#define TRK 4
#define PS1 3
#define PS2 3
#define PS3 2

typedef f32x2 c2;
__device__ __forceinline__ c2 swp(c2 a) { return __builtin_shufflevector(a, a, 1, 0); }
__device__ __forceinline__ c2 fm(c2 a, c2 b, c2 c) { return __builtin_elementwise_fma(a, b, c); }
__device__ __forceinline__ c2 bc(float x) { return (c2){x, x}; }
__device__ __forceinline__ c2 sgn() { c2 s = {-1.f, 1.f}; asm("" : "+s"(s)); return s; }
template <int SG> __device__ __forceinline__ c2 addi(c2 a, c2 b) { return fm(swp(b), SG > 0 ? sgn() : swp(sgn()), a); }
template <int SG> __device__ __forceinline__ c2 cfin(c2 a, c2 w, c2 t) {
    c2 d;
    if (SG < 0) asm("v_pk_fma_f32 %0, %1, %2, %3 op_sel:[1,1,0] op_sel_hi:[0,1,1] neg_lo:[1,0,0]" : "=v"(d) : "v"(a), "v"(w), "v"(t));
    else asm("v_pk_fma_f32 %0, %1, %2, %3 op_sel:[1,1,0] op_sel_hi:[0,1,1] neg_hi:[1,0,0]" : "=v"(d) : "v"(a), "v"(w), "v"(t));
    return d;
}
struct Tw { f32x4 v[4]; };
template <int NI> __device__ __forceinline__ void ldtw(Tw& t, const Tw7<NI>& tab, int i) {
#pragma unroll
    for (int j = 0; j < 4; ++j) t.v[j] = *(const f32x4*)tab.v[j][i];
}
template <int NI> __device__ __forceinline__ void ldtw(Tw& t, const LAS f32x4* p) {
#pragma unroll
    for (int j = 0; j < 4; ++j) t.v[j] = p[j * NI];
}
template <int SG> __device__ __forceinline__ void twid(c2 (&u)[8], const Tw& t) {
    c2 w[8], m[8];
#pragma unroll
    for (int r = 1; r < 8; ++r) {
        const f32x4 x = t.v[(r - 1) >> 1];
        w[r] = r & 1 ? (c2){x[0], x[1]} : (c2){x[2], x[3]};
        m[r] = u[r] * bc(w[r].x);
    }
    if (TWG) asm("" : "+v"(m[1]), "+v"(m[2]), "+v"(m[3]), "+v"(m[4]), "+v"(m[5]), "+v"(m[6]), "+v"(m[7]));
#pragma unroll
    for (int r = 1; r < 8; ++r) u[r] = cfin<SG>(u[r], w[r], m[r]);
}
template <int SG> __device__ __forceinline__ void dft4(c2& a, c2& b, c2& c, c2& d) {
    const c2 t0 = a + c, t1 = a - c, t2 = b + d, t3 = b - d;
    a = t0 + t2; c = t0 - t2; b = addi<SG>(t1, t3); d = addi<-SG>(t1, t3);
}
template <int SG> __device__ __forceinline__ void dft8(c2 (&u)[8]) {
    c2 e0 = u[0], e1 = u[2], e2 = u[4], e3 = u[6], o0 = u[1], o1 = u[3], o2 = u[5], o3 = u[7];
    dft4<SG>(e0, e1, e2, e3); dft4<SG>(o0, o1, o2, o3);
    const c2 r = bc(0.70710678118654752440f);
    o1 = addi<SG>(o1, o1); o3 = addi<-SG>(o3, o3);
    u[0] = e0 + o0; u[4] = e0 - o0;
    u[1] = fm(o1, r, e1); u[5] = fm(o1, -r, e1);
    u[2] = addi<SG>(e2, o2); u[6] = addi<-SG>(e2, o2);
    u[3] = fm(o3, -r, e3); u[7] = fm(o3, r, e3);
}
template <int SG> __device__ __forceinline__ void dft5(c2& x0, c2& x1, c2& x2, c2& x3, c2& x4) {
    const c2 c1 = bc(0.30901699437494742410f), c2_ = bc(-0.80901699437494742410f), s1 = bc(0.95105651629515357212f), s2 = bc(0.58778525229247312917f);
    const c2 t1 = x1 + x4, t2 = x2 + x3, t3 = x1 - x4, t4 = x2 - x3;
    const c2 a1 = fm(c2_, t2, fm(c1, t1, x0)), a2 = fm(c1, t2, fm(c2_, t1, x0));
    const c2 b1 = fm(s2, t4, s1 * t3), b2 = fm(-s1, t4, s2 * t3);
    x0 = x0 + t1 + t2;
    x1 = addi<SG>(a1, b1); x4 = addi<-SG>(a1, b1); x2 = addi<SG>(a2, b2); x3 = addi<-SG>(a2, b2);
}
template <int SG> __device__ __forceinline__ void dft10(c2 (&u)[10]) {
    c2 e0 = u[0], e1 = u[2], e2 = u[4], e3 = u[6], e4 = u[8], o0 = u[5], o1 = u[7], o2 = u[9], o3 = u[1], o4 = u[3];
    dft5<SG>(e0, e1, e2, e3, e4); dft5<SG>(o0, o1, o2, o3, o4);
    u[0] = e0 + o0; u[5] = e0 - o0; u[6] = e1 + o1; u[1] = e1 - o1; u[2] = e2 + o2; u[7] = e2 - o2; u[8] = e3 + o3; u[3] = e3 - o3; u[4] = e4 + o4; u[9] = e4 - o4;
}
__device__ __forceinline__ void swap32(c2& v) { auto r = __builtin_amdgcn_permlane32_swap(__float_as_uint(v.x), __float_as_uint(v.y), false, false); v = (c2){__uint_as_float(r[0]), __uint_as_float(r[1])}; }
template <int B> __device__ __forceinline__ void swapl(c2& a, c2& b) {
#pragma unroll
    for (int i = 0; i < 2; ++i) {
        auto r = B == 5 ? __builtin_amdgcn_permlane32_swap(__float_as_uint(a[i]), __float_as_uint(b[i]), false, false) : __builtin_amdgcn_permlane16_swap(__float_as_uint(a[i]), __float_as_uint(b[i]), false, false);
        a[i] = __uint_as_float(r[0]); b[i] = __uint_as_float(r[1]);
    }
}
__device__ __forceinline__ c2 unpk(unsigned v) { return (c2){lo2f(v), hi2f(v)}; }
__device__ __forceinline__ float hsum32(c2 s) {
    float v = s.x + s.y;
    v += dppf<0xB1>(v); v += dppf<0x4E>(v); v += dppf<0x141>(v); v += dppf<0x140>(v);
    auto q = __builtin_amdgcn_permlane16_swap(__float_as_uint(v), __float_as_uint(v), false, false);
    return __uint_as_float(q[0]) + __uint_as_float(q[1]);
}
__device__ __forceinline__ void ln_row(const c2 (&v)[10], unsigned (&o)[10]) {
    const c2 s = ((v[0] + v[1]) + (v[2] + v[3])) + ((v[4] + v[5]) + (v[6] + v[7])) + (v[8] + v[9]);
    const float mean = hsum32(s) * (1.f / 640.f);
    c2 q0 = bc(0.f), q1 = bc(0.f);
#pragma unroll
    for (int r = 0; r < 10; r += 2) { const c2 d = v[r] - bc(mean), e = v[r + 1] - bc(mean); q0 = fm(d, d, q0); q1 = fm(e, e, q1); }
    const float mb = bfr(mean);
    c2 d[10];
#pragma unroll
    for (int r = 0; r < 10; ++r) { const c2 x = v[r] - bc(mb); d[r] = unpk(pack2(x.x, x.y)); }
    const float var = hsum32(q0 + q1) * (1.f / 640.f);
    const float rs = __builtin_amdgcn_rcpf(bfr(sqrtf(bfr(bfr(var) + 1e-5f))));
#pragma unroll
    for (int r = 0; r < 10; ++r) { const c2 x = d[r] * bc(rs); o[r] = pack2(x.x, x.y); }
}

__device__ __forceinline__ __amdgpu_buffer_rsrc_t mkrsrc(const void* p, unsigned bytes) {
    return __builtin_amdgcn_make_buffer_rsrc((void*)p, 0, bytes, 0x00020000);
}
__device__ __forceinline__ unsigned xcc_id() { unsigned x; asm volatile("s_getreg_b32 %0, hwreg(HW_REG_XCC_ID)" : "=s"(x)); return x & 15u; }
#define VMWAIT() do { asm volatile("" ::: "memory"); __builtin_amdgcn_s_waitcnt(0x0F70); asm volatile("" ::: "memory"); } while (0)
#define BAR() asm volatile("s_waitcnt lgkmcnt(0)\n\ts_barrier" ::: "memory")
#define BAR0() asm volatile("s_barrier" ::: "memory")

#define FST(k) do {} while (0)
#define FSW(k) do {} while (0)
extern __shared__ __attribute__((aligned(16))) char smem[];
#define RST(k, lay, wv, sel) do {} while (0)
constexpr int R_C = 8 * 10 * 72 * 8;
constexpr float FSC = 0.5f / ((float)S_ * (float)D_);
__device__ __forceinline__ c2 ldv(const LAS void* p) { return *(const volatile LAS c2*)p; }
struct RowTw { Tw a, b; };
__device__ __forceinline__ void row_tw(RowTw& t, int tid) { const int i = tid % 80; ldtw(t.a, TWR2, i); ldtw(t.b, TWR3, i); }
template <int NF, bool LATE> __device__ __forceinline__ void a1_unit(__amdgpu_buffer_rsrc_t rx, int row0, __amdgpu_buffer_rsrc_t rws, __amdgpu_buffer_rsrc_t rwin, RowTw& tw, int tid, unsigned (&fs)[12]) {
    const int wave = __builtin_amdgcn_readfirstlane(tid >> 6), lane = tid & 63;
    LAS c2* A = (LAS c2*)smem; LAS c2* C = (LAS c2*)(smem + R_C);
    FST(0);
    if (wave < NF) {
        unsigned d[10];
        for (unsigned spins = 0;;) {
            asm volatile("" ::: "memory");
#pragma unroll
            for (int r = 0; r < 10; ++r) d[r] = __builtin_amdgcn_raw_buffer_load_b32(rx, (lane & 31) * 4u + (lane >> 5) * (D_ * 2u) + r * 128u, (unsigned)(row0 + 2 * wave) * (D_ * 2), 16);
            if (!LATE) break;
            unsigned m = d[0];
#pragma unroll
            for (int r = 1; r < 10; ++r) m = max(m, d[r]);
            if (!__any(m == ~0u) || spin_fail(spins, rwin)) break;
            __builtin_amdgcn_s_sleep(PS1);
        }
        c2 u[10];
#pragma unroll
        for (int r = 0; r < 10; ++r) u[r] = unpk(d[r]);
        FST(1);
        if (LATE) RST(16, 0, wave, 0);
        ln_row(u, d);
        FST(2);
#pragma unroll
        for (int r = 0; r < 10; ++r) { u[r] = unpk(d[r]); swap32(u[r]); }
        dft10<-1>(u);
#pragma unroll
        for (int r = 0; r < 10; ++r) A[(wave * 10 + r) * 72 + (lane & 31) + 36 * (lane >> 5)] = u[r];
        FST(3);
    }
    BAR();
    if (NF == 4 && !LATE) row_tw(tw, tid);
    FST(4);
#pragma unroll
    for (int c = 0; c < (NF > 4 ? 2 : 1); ++c) {
        if (tid < (NF > 4 ? (c ? 160 : 480) : 80 * NF)) {
            const int K = (tid >> 3) + 60 * c, m = tid & 7;
            LAS c2* a = A + K * 72;
            c2 u[8];
#pragma unroll
            for (int q = 0; q < 8; ++q) u[q] = ldv(a + (m >> 1) + 36 * (m & 1) + 4 * q);
            twid<-1>(u, tw.a);
            dft8<-1>(u);
#pragma unroll
            for (int p = 0; p < 8; ++p) a[9 * p + m] = u[p];
            asm volatile("" ::: "memory");
#pragma unroll
            for (int q = 0; q < 8; ++q) u[q] = ldv(a + 9 * m + q);
            twid<-1>(u, tw.b);
            dft8<-1>(u);
            LAS c2* o = C + (K / 10) * 638 + K + 10 * m;
#pragma unroll
            for (int p = 0; p < 8; ++p) o[80 * p] = u[p];
        }
    }
    FST(5);
    BAR();
    FST(6);
#pragma unroll
    for (int c = 0; c < (NF > 4 ? 5 : 3); ++c) {
        const int it = tid + 512 * c, kp = it / (2 * NF), row = it % (2 * NF);
        if (NF == 4 && !LATE && row0 < 1600) for (int i = 0; i < (c ? 8 : wave); ++i) __builtin_amdgcn_s_sleep(TRK);
        if (NF > 4 || c < 2 || tid < 256) {
            const LAS c2* z = C + (row >> 1) * 648;
            const f32x4 zk = *(const LAS f32x4*)(z + 2 * kp);
            const c2 n0 = ldv(z + (kp ? 640 - 2 * kp : 320)), n1 = ldv(z + 639 - 2 * kp);
            const bool odd = it & 1;
            f32x4 o = odd ? (f32x4){zk[1] + n0.y, n0.x - zk[0], zk[3] + n1.y, n1.x - zk[2]} : (f32x4){zk[0] + n0.x, zk[1] - n0.y, zk[2] + n1.x, zk[3] - n1.y};
            if (c == 0 && kp == 0) { o[0] = 2.f * (odd ? zk[1] : zk[0]); o[1] = 2.f * (odd ? n0.y : n0.x); }
            u32x4 ob = __builtin_bit_cast(u32x4, o * FSC);
            if (LATE) { ob[1] = nosent1(ob[1]); ob[3] = nosent1(ob[3]); }
            st128(ob, rws, kp * (LATE ? GST : FST_) + row * 16u, (LATE ? O_FL - (S_ - 256) * 16u : O_F) + (unsigned)row0 * 16u, 16);
        }
    }
    FST(7);
}
template <class FG> __device__ __forceinline__ void a3_unit(__amdgpu_buffer_rsrc_t rx, int row0, int layer, __amdgpu_buffer_rsrc_t rws, __amdgpu_buffer_rsrc_t rwin, const RowTw& tw, int tid, FG go, unsigned (&fs)[12]) {
    const int wave = __builtin_amdgcn_readfirstlane(tid >> 6), lane = tid & 63;
    LAS c2* A = (LAS c2*)smem; LAS c2* C = (LAS c2*)(smem + R_C);
    const unsigned n0 = (lane & 7) | ((lane & 24) << 1);
    c2 x[10];
    if (wave < 4) {
        const unsigned base = (unsigned)(row0 + 2 * wave) * (D_ * 2), vo = n0 * 2u + (lane >> 5) * (D_ * 2u);
#pragma unroll
        for (int r = 0; r < 10; ++r) x[r] = (c2){bf2f(__builtin_amdgcn_raw_buffer_load_b16(rx, vo + 128u * r, base, 16)), bf2f(__builtin_amdgcn_raw_buffer_load_b16(rx, vo + 16u + 128u * r, base, 16))};
    }
    const bool early = row0 < 256;
    if (!early) go();
    else if (TG) {
        if (wave == 7) { const unsigned vo[3] = {lane * 128u, lane * 128u + 8192u, min(lane, 31) * 128u + 16384u}; wait_told(rwin, vo, W_COL + (unsigned)layer * W_LAYER + 4u, 1); }
        BAR0();
    }
    FST(0);
    u32x4 fv[3];
    {
        const unsigned vo = (tid >> 3) * (early ? GST : FST_) + (tid & 7) * 16u, st = (early ? GST : FST_) * 64u, so = (early ? O_FL + (unsigned)(2 - layer) * (NCOL * GST) : O_F) + (unsigned)row0 * 16u;
#pragma unroll
        for (int c = 0; c < 3; ++c) fv[c] = __builtin_amdgcn_raw_buffer_load_b128(rws, vo, c < 2 || wave < 4 ? so + c * st : 0x40000000u, 16);
        if (early) for (unsigned spins = 0;;) {
            if (!__any(max(max(smax(fv[0]), smax(fv[1])), smax(fv[2])) == ~0u) || spin_fail(spins, rwin)) break;
            __builtin_amdgcn_s_sleep(PS3);
            asm volatile("" ::: "memory");
            u32x4 x[3];
#pragma unroll
            for (int c = 0; c < 3; ++c) x[c] = __builtin_amdgcn_raw_buffer_load_b128(rws, smax(fv[c]) == ~0u ? vo : 0x40000000u, so + c * st, 16);
#pragma unroll
            for (int c = 0; c < 3; ++c) if (smax(fv[c]) == ~0u) fv[c] = x[c];
        }
    }
#pragma unroll
    for (int c = 0; c < 3; ++c) {
        const int it = tid + 512 * c, kp = it >> 3;
        if (c < 2 || tid < 256) {
            const bool odd = it & 1;
            const f32x4 v = __builtin_bit_cast(f32x4, fv[c]), p = {dppf<0xB1>(v[0]), dppf<0xB1>(v[1]), dppf<0xB1>(v[2]), dppf<0xB1>(v[3])};
            c2 z0 = odd ? (c2){p[0] + v[1], v[0] - p[1]} : (c2){v[0] - p[1], p[0] + v[1]};
            const c2 z1 = odd ? (c2){p[2] + v[3], v[2] - p[3]} : (c2){v[2] - p[3], p[2] + v[3]};
            int i0 = odd ? 640 - 2 * kp : 2 * kp;
            if (c == 0 && kp == 0) { z0 = odd ? (c2){p[1], v[1]} : (c2){v[0], p[0]}; i0 >>= 1; }
            LAS c2* z = C + ((it >> 1) & 3) * 648;
            z[i0] = z0; z[odd ? 639 - 2 * kp : 2 * kp + 1] = z1;
        }
    }
    FST(1);
    BAR();
    if (early && TH && wave == 7) tell_at(rwin, W_H + (unsigned)layer * W_LAYER + (unsigned)((row0 >> 5) * 128 + 16 + ((row0 >> 3) & 3) * 4), layer ? TH : TH0);
    if (early) RST(3, layer, wave, 0);
    FST(2);
    if (tid < 320) {
        const int K = tid >> 3, m = tid & 7;
        LAS c2* a = A + K * 72;
        const LAS c2* o = C + (K / 10) * 638 + K + 10 * m;
        c2 u[8];
#pragma unroll
        for (int p = 0; p < 8; ++p) u[p] = ldv(o + 80 * p);
        dft8<1>(u);
        twid<1>(u, tw.b);
#pragma unroll
        for (int q = 0; q < 8; ++q) a[9 * q + m] = u[q];
        asm volatile("" ::: "memory");
#pragma unroll
        for (int p = 0; p < 8; ++p) u[p] = ldv(a + 9 * m + p);
        dft8<1>(u);
        twid<1>(u, tw.a);
#pragma unroll
        for (int q = 0; q < 8; ++q) a[m + 8 * (q >> 1) + 32 * (q & 1)] = u[q];
    }
    FST(3);
    BAR();
    FST(4);
    if (wave < 4) {
        c2 u[10];
#pragma unroll
        for (int r = 0; r < 10; ++r) u[r] = ldv(A + (wave * 10 + r) * 72 + lane);
        dft10<1>(u);
        unsigned x1[10], h[10];
#pragma unroll
        for (int r = 0; r < 10; ++r) {
            swap32(u[r]);
            const c2 y = x[r] + unpk(pack2(u[r].x, u[r].y));
            x1[r] = pack2(y.x, y.y);
            u[r] = unpk(x1[r]);
        }
        FST(5);
        ln_row(u, h);
        FST(6);
        LAS char* st = (LAS char*)C + wave * 5120;
        LAS char* hp = st + (lane >> 5) * 1280 + (n0 & 48) * 2 + pi16(n0 & 15) * 2;
#pragma unroll
        for (int r = 0; r < 10; ++r) *(LAS unsigned*)(hp + 128 * r) = nosent1(h[r]);
#pragma unroll
        for (int c = 0; c < 3; ++c) {
            if (c < 2 || lane < 32) {
                const int id = c * 64 + lane, rr = id >= 80, ch = id - 80 * rr, row = (row0 + 2 * wave + rr) & 31;
                st128(*(const LAS u32x4*)(st + id * 16), rws, (unsigned)((ch >> 3) * 4096 + row * 128 + (((ch & 7) ^ (row & 7)) << 4)), O_H + (unsigned)((row0 >> 5) - NHF * layer) * 40960u, 16);
            }
        }
        if (early) RST(4, layer, wave, 0);
        LAS char* xp = st + 2560 + (lane >> 5) * 1280 + n0 * 2;
#pragma unroll
        for (int r = 0; r < 10; ++r) { *(LAS u16*)(xp + 128 * r) = (u16)x1[r]; *(LAS u16*)(xp + 128 * r + 16) = (u16)(x1[r] >> 16); }
#pragma unroll
        for (int c = 0; c < 3; ++c)
            if (c < 2 || lane < 32) st128(*(const LAS u32x4*)(st + 2560 + (c * 64 + lane) * 16), rws, (c * 64 + lane) * 16u, O_X1 + (unsigned)(row0 + 2 * wave) * (D_ * 2), 16);
        FST(7);
    }
}

constexpr int C_T = 8 * 544 * 8, C_B = C_T + (int)sizeof(TwCol), C_W = C_B + 32768, C_END = C_W + 8 * 516 * 4;
template <class FG> __device__ __forceinline__ void a2_unit(int kp, int late, __amdgpu_buffer_rsrc_t rws, __amdgpu_buffer_rsrc_t rwin, int tid, FG go, unsigned (&fs)[12]) {
    const int w = __builtin_amdgcn_readfirstlane(tid >> 6), l = tid & 63;
    const unsigned fb = O_F + (unsigned)kp * FST_, wb = O_WFT + (unsigned)kp * WST;
    const int b = (l >> 1) & 7;
    const int k0 = w + 8 * b + 128 * (l >> 4);
    c2 u[8];
    u32x4 wv[2];
    {
        f32x4 tv[3];
#pragma unroll
        for (int i = 0; i < 3; ++i) tv[i] = ((const f32x4*)&TWC)[min(tid + 512 * i, (int)sizeof(TwCol) / 16 - 1)];
        go(0);
#pragma unroll
        for (int i = 0; i < 3; ++i) ((LAS f32x4*)(smem + C_T))[min(tid + 512 * i, (int)sizeof(TwCol) / 16 - 1)] = tv[i];
        BAR();
        FST(0);
        u32x4 x[4];
#pragma unroll
        for (int r = 0; r < 4; ++r) x[r] = __builtin_amdgcn_raw_buffer_load_b128(rws, (tid >> 1) * 16u + (tid & 1) * 4096u, fb + r * 8192u, 16);
#pragma unroll
        for (int r = 0; r < 2; ++r) wv[r] = __builtin_amdgcn_raw_buffer_load_b128(rws, tid * 16u, wb + r * 8192u, 16);
#pragma unroll
        for (int r = 0; r < 4; ++r) {
            const f32x4 y = __builtin_bit_cast(f32x4, x[r]);
            const c2 z = {dppf<0xB1>(l & 1 ? y[0] : y[2]), dppf<0xB1>(l & 1 ? y[1] : y[3])};
            u[2 * r] = l & 1 ? z : (c2){y[0], y[1]};
            u[2 * r + 1] = l & 1 ? (c2){y[2], y[3]} : z;
        }
    }
    const LAS f32x4* t1 = (const LAS f32x4*)(smem + C_T) + (tid >> 1);
    const LAS f32x4* t2 = (const LAS f32x4*)(smem + C_T + sizeof(Tw7<256>)) + (l >> 1);
    const LAS f32x4* t3 = (const LAS f32x4*)(smem + C_T + sizeof(Tw7<256>) + sizeof(Tw7<32>)) + (l >> 4);
    LAS c2* e0 = (LAS c2*)smem + tid;
    LAS c2* e1 = (LAS c2*)smem + w * 544 + l;
    LAS c2* e2 = (LAS c2*)smem + w * 544 + 2 * (l >> 4) + (l & 1);
    Tw tw;
    ldtw<256>(tw, t1);
    if (late) {
        go(2);
        FST(1);
        for (unsigned spins = 0;;) {
            asm volatile("" ::: "memory");
            const u32x2 x = __builtin_amdgcn_raw_buffer_load_b64(rws, tid * 8u, O_FL + (unsigned)kp * GST, 16);
            u[7] = __builtin_bit_cast(c2, x);
            if (!__any(x[1] == ~0u) || spin_fail(spins, rwin)) break;
            if (w < 3) __builtin_amdgcn_s_sleep(16); else __builtin_amdgcn_s_sleep(PS2);
        }
    }
    FST(2);
    dft8<-1>(u);
    twid<-1>(u, tw);
    if (kp == 0) {
#pragma unroll
        for (int i = 0; i < 4; ++i)
            asm volatile("s_mov_b32 m0, %3\n\ts_nop 3\n\tbuffer_load_dwordx4 %0, %1, %2 offen sc1 lds" :: "v"(l * 16u), "s"(rws), "s"(O_WFT + NCOL * WST + (w * 4 + i) * 1024u), "s"(C_B + (w * 4 + i) * 1024u), "v"(u[7]) : "m0", "memory");
    }
#pragma unroll
    for (int r = 0; r < 8; ++r) e0[544 * r] = u[r];
    ldtw<32>(tw, t2);
    BAR();
    if (A2P == 1 && w < 4) __builtin_amdgcn_s_setprio(2);
    if (A2P == 2 && (w & 1)) __builtin_amdgcn_s_setprio(2);
    if (A2P == 3 && w >= 4) __builtin_amdgcn_s_sleep(4);
    if (TG && w == 0) tell_at(rwin, W_COL + (unsigned)late * W_LAYER + (unsigned)kp * 128u + 4u, kp ? TG : TG + 30);
#pragma unroll
    for (int r = 0; r < 8; ++r) u[r] = ldv(e1 + 64 * r);
    dft8<-1>(u);
    twid<-1>(u, tw);
#pragma unroll
    for (int r = 0; r < 8; ++r) e1[68 * r] = u[r];
    asm volatile("" ::: "memory");
#pragma unroll
    for (int r = 0; r < 8; ++r) u[r] = ldv(e2 + 68 * b + 8 * r);
    ldtw<4>(tw, t3);
    dft8<-1>(u);
    twid<-1>(u, tw);
    c2 f[8];
#pragma unroll
    for (int r = 0; r < 8; ++r) f[r] = unpk(wv[r >> 2][r & 3]);
    asm volatile("s_nop 1" : "+v"(u[1]), "+v"(u[2]), "+v"(u[3]), "+v"(u[4]), "+v"(u[5]), "+v"(u[6]), "+v"(u[7]));
#pragma unroll
    for (int r = 0; r < 4; ++r) swapl<5>(u[r], u[r + 4]);
#pragma unroll
    for (int r = 0; r < 4; ++r) swapl<4>(u[(r & 1) + 2 * (r & 2)], u[(r & 1) + 2 * (r & 2) + 2]);
    dft4<-1>(u[0], u[2], u[4], u[6]); dft4<-1>(u[1], u[3], u[5], u[7]);
    FST(3);
    if (kp == 0) {
        LAS c2* z = (LAS c2*)(smem + C_W);
#pragma unroll
        for (int r = 0; r < 8; ++r) if (!(l & 1)) z[w * 258 + b + 16 * (l >> 4) + 8 * (r & 1) + 64 * (r >> 1)] = u[r];
        VMWAIT();
        BAR();
#pragma unroll
        for (int r = 0; r < 8; ++r) {
            const int kn = (S_ - k0 - 64 * (r & 1) - 512 * (r >> 1)) & (S_ - 1);
            const f32x4 ab = ((const LAS f32x4*)(smem + C_B))[256 * r + (tid >> 1)];
            const c2 zn = z[(kn & 7) * 258 + (kn >> 3)], g = {ab[2], ab[3]}, h = l & 1 ? f[r] : (c2){ab[0], ab[1]};
            const c2 y = cfin<1>(g, zn, g * bc(zn.x));
            u[r] = cfin<-1>(u[r], h, fm(u[r], bc(h.x), l & 1 ? bc(0.f) : y));
        }
    } else {
#pragma unroll
        for (int r = 0; r < 8; ++r) u[r] = cfin<-1>(u[r], f[r], u[r] * bc(f[r].x));
    }
    FST(4);
    dft4<1>(u[0], u[2], u[4], u[6]); dft4<1>(u[1], u[3], u[5], u[7]);
#pragma unroll
    for (int r = 0; r < 4; ++r) swapl<4>(u[(r & 1) + 2 * (r & 2)], u[(r & 1) + 2 * (r & 2) + 2]);
#pragma unroll
    for (int r = 0; r < 4; ++r) swapl<5>(u[r], u[r + 4]);
    twid<1>(u, tw);
    dft8<1>(u);
#pragma unroll
    for (int r = 0; r < 8; ++r) e2[66 * b + 8 * r] = u[r];
    asm volatile("" ::: "memory");
    ldtw<32>(tw, t2);
#pragma unroll
    for (int r = 0; r < 8; ++r) u[r] = ldv(e1 + 66 * r);
    twid<1>(u, tw);
    dft8<1>(u);
#pragma unroll
    for (int r = 0; r < 8; ++r) e1[64 * r] = u[r];
    ldtw<256>(tw, t1);
    __builtin_amdgcn_s_setprio(0);
    FST(7);
    BAR();
    FST(10);
#pragma unroll
    for (int r = 0; r < 8; ++r) u[r] = ldv(e0 + 544 * r);
    twid<1>(u, tw);
    dft8<1>(u);
    FST(5);
    __builtin_amdgcn_raw_buffer_store_b64((u32x2){__float_as_uint(u[0].x), nosent1(__float_as_uint(u[0].y))}, rws, tid * 8u, O_FL + (unsigned)((2 - late) * NCOL + kp) * GST, 16);
#pragma unroll
    for (int r = 1; r < 8; ++r) __builtin_amdgcn_raw_buffer_store_b64(__builtin_bit_cast(u32x2, u[r]), rws, tid * 8u, fb + r * 4096u, 16);
    FST(6);
}

struct Args { char* ws; char* win; const u16 *x, *wr, *wi, *mem0, *wq, *wk, *wv, *wo; u16* out; };
__device__ __forceinline__ const u16* arg(int i) {
    const __attribute__((address_space(4))) u64* p = (const __attribute__((address_space(4))) u64*)__builtin_amdgcn_kernarg_segment_ptr();
    asm volatile("" : "+s"(p));
    return (const u16*)p[i];
}
enum { A_X = 2, A_WR, A_WI, A_MEM0, A_WQ, A_WK, A_WV, A_WO, A_OUT };
#define MXV 0x5556ull
#define MXQ 0x1554ull
#define NTAIL 10
static_assert(NTAIL >= 3, "the owners release segments up to NSEG - 4");
constexpr int TAILS = NSEG - NTAIL;
static_assert((MXV & 31) == 22 && (MXQ & 31) == 20, "segment sets");
constexpr int NREC = 96 + NOWN, NQT = NREC + NOWN, NXT = NQT + 32, NWRK = NXT + 32;
#define SST(k) do {} while (0)
#define LSEL 0
#define RT(k) do {} while (0)
#define RTS() do {} while (0)
#define B3ATTR
#define MTA(k)
#define MTRT() do {} while (0)
#define MT(k) do {} while (0)
#define MTS() do {} while (0)
#define FC(x) do {} while (0)
#define DQC 1200
#define DVC 1200
#define M1 44
#define M2 226
#define PEN 280
#define DZP 55
#define LT0 342
#define SAD 1
#define PEN1 230
#define PEN0 330
#define TSLP 1
#define OPR 2
#define BSPARSE 0
#define ZLAG 4
#define TKP 0
#define DPS 12
#define DXT 3000
#define LTM 12
#define XS0 10
#define XSU 8
#define XSM 32
#define LTU 4
#define LTS 3
#define XSS 3
#define TKS 3
#define LT1 450
#define FDL 2
#define ZLA 3
#define NORND 0
#define LTX 40
#define TKE 200
#define DVO 4000
#define ZPS 2
#define HLA 8
#define QB2 2
#define NB2 6
#define DZR 24
#define ML 150
#define NB1 3
#define DPT 1
#define DDMA 8
#define DZ2 800
#define QEARLY 2
#define OSLP 32
#define OSLP2 1
#define WSLP 64
#define DO0 4000
#define DW0 1200
#define DQT 2000
#define ZEARLY 6
#define HSLP 4
#define ASLP 4
#define CSLP 4
#define A1S 0
#define OSLP3 12
#define DVT 860
#define VSL 0
#define WARM 1
constexpr unsigned CODE_BYTES = 96000;
#define DTK 4
#define OSL4 10
#define OAUX 0
#define TSL0 6
#define SE 6
#define XE 12
#define PENS 20
#define CF 190
#define HSL2 16
#define DH1 10
#define DZ0 400
#define FZP 1
__device__ __forceinline__ unsigned fz_at(unsigned h, unsigned g) { return FZP ? g * 1024u + (h >> 2) * 128u + (h & 3u) * 4u : h * 128u + g * 4u; }
#define LKE 0
#define DW4 6
#define DBIG 580
#define PENH 280
#define ROT 0
#define VREV 0
#define DGV 0
#define VOFF0 "0"
#define VOFF1 "1024"
#define VOFF2 "2048"
#define VOFF3 "3072"
#define VOFF4 "4096"
#define VOFF5 "5120"
#define VOFF6 "6144"
#define VOFF7 "7168"
#define VOFF8 "8192"
#define VOFF9 "9216"
#define STK 1
#define STX 0
#define UFQ 14
#define DQ0 36
#define QSL 6
#define OST0 40
#define NPR 3
#define DQA 1300
#define DQA0 1400
#define DIE 0
#define ALN ".p2align 6\n\t"
#define SB() __builtin_amdgcn_sched_barrier(0)
#define PIN1(a) asm volatile("" : "+v"(a) :: "memory")
#define PIN2(a, b) asm volatile("" : "+v"(a), "+v"(b) :: "memory")
__device__ __forceinline__ unsigned fresh_lane() { unsigned l; asm volatile("v_mbcnt_lo_u32_b32 %0, -1, 0\n\tv_mbcnt_hi_u32_b32 %0, -1, %0" : "=v"(l)); return l; }
#define LANE0 (min(fresh_lane(), 1u) << 30)
__device__ __forceinline__ void wait_until(unsigned long long t) { while ((long long)(__builtin_amdgcn_s_memtime() - t) < 0) __builtin_amdgcn_s_sleep(1); }
struct Clk { unsigned ta, ma, cpq, qpc; };
__device__ __forceinline__ int sx24(unsigned v) { return (int)(v << 8) >> 8; }
#define WAIT_FINE ALN \
        "s_memrealtime vcc\n\t" \
        "s_memtime s[100:101]\n\t" \
        "s_brev_b32 %[fi], -2\n\t" \
        "s_waitcnt lgkmcnt(0)\n\t" \
        "s_mov_b32 %[rp], vcc_lo\n\t" \
        "s_lshl_b32 vcc_hi, vcc_lo, 2\n\t" \
        "s_sub_u32 %[x], %[t], vcc_hi\n\t" \
        "s_bfe_i32 %[x], %[x], 0x180000\n\t" \
        "s_min_i32 %[x], %[x], 4000\n\t" \
        "s_max_i32 %[x], %[x], -4000\n\t" \
        "s_add_u32 %[t], vcc_hi, %[x]\n\t" \
        "s_mul_i32 %[x], %[x], %[cq]\n\t" \
        "s_ashr_i32 %[x], %[x], 8\n\t" \
        "s_sub_u32 %[tm], %[t], %[ta]\n\t" \
        "s_bfe_i32 %[tm], %[tm], 0x180000\n\t" \
        "s_min_i32 %[tm], %[tm], 1000000\n\t" \
        "s_max_i32 %[tm], %[tm], -1000000\n\t" \
        "s_mul_i32 %[tm], %[tm], %[cq]\n\t" \
        "s_ashr_i32 %[tm], %[tm], 8\n\t" \
        "s_add_u32 %[tm], %[tm], %[ma]\n\t" \
        "s_sub_u32 %[tm], %[tm], s100\n\t" \
        "s_lshr_b32 vcc_hi, %[cq], 4\n\t" \
        "s_sub_u32 vcc_hi, %[x], vcc_hi\n\t" \
        "s_max_i32 %[tm], %[tm], vcc_hi\n\t" \
        "s_min_i32 %[tm], %[tm], %[x]\n\t" \
        "s_add_u32 %[tm], %[tm], s100\n" \
        "1:\n\t" \
        "s_memrealtime vcc\n\t" \
        "s_memtime s[100:101]\n\t" \
        "s_waitcnt lgkmcnt(0)\n\t" \
        "s_cmp_eq_u32 vcc_lo, %[rp]\n\t" \
        "s_cbranch_scc1 2f\n\t" \
        "s_mov_b32 %[rp], vcc_lo\n\t" \
        "s_lshl_b32 %[x], vcc_lo, 2\n\t" \
        "s_sub_u32 %[x], %[t], %[x]\n\t" \
        "s_bfe_i32 %[x], %[x], 0x180000\n\t" \
        "s_mul_i32 %[x], %[x], %[cq]\n\t" \
        "s_ashr_i32 %[x], %[x], 8\n\t" \
        "s_add_u32 %[x], %[x], s100\n\t" \
        "s_sub_u32 s101, %[x], %[tm]\n\t" \
        "s_cmp_lt_i32 s101, %[fi]\n\t" \
        "s_cselect_b32 %[tm], %[x], %[tm]\n\t" \
        "s_mov_b32 %[fi], 0\n" \
        "2:\n\t" \
        "s_sub_u32 %[x], %[tm], s100\n\t" \
        "s_cmp_gt_i32 %[x], 260\n\t" \
        "s_cbranch_scc1 1b\n\t" \
        "s_cmp_eq_u32 %[fi], 0\n\t" \
        "s_cselect_b32 %[ta], %[t], %[ta]\n\t" \
        "s_cselect_b32 %[ma], %[tm], %[ma]\n" \
        "3:\n\t" \
        "s_memtime s[100:101]\n\t" \
        "s_waitcnt lgkmcnt(0)\n\t" \
        "s_sub_u32 %[x], %[tm], s100\n\t" \
        "s_cmp_gt_i32 %[x], 92\n\t" \
        "s_cbranch_scc1 3b\n\t" \
        "s_max_i32 %[x], %[x], 0\n\t" \
        "s_andn2_b32 %[x], %[x], 3\n\t" \
        "s_sub_u32 %[x], 104, %[x]\n\t" \
        "s_getpc_b64 vcc\n\t" \
        "s_add_u32 vcc_lo, vcc_lo, %[x]\n\t" \
        "s_addc_u32 vcc_hi, vcc_hi, 0\n\t" \
        "s_setpc_b64 vcc\n\t" \
        ".rept 23\n\t" \
        "s_nop 0\n\t" \
        ".endr\n\t"
#define WAIT_FINE_OUT [tm] "=&s"(tm), [rp] "=&s"(rp), [x] "=&s"(x), [fi] "=&s"(fi), [t] "+s"(t), [ta] "+s"(c.ta), [ma] "+s"(c.ma)
__device__ __forceinline__ void wait_fine(unsigned t, Clk& c) {
    unsigned tm, rp, x, fi;
    asm volatile(WAIT_FINE : WAIT_FINE_OUT : [cq] "s"(c.cpq) : "vcc", "scc", "s100", "s101", "memory");
}
__device__ __forceinline__ void wait_fine_bar(unsigned t, Clk& c) {
    unsigned tm, rp, x, fi;
    asm volatile(WAIT_FINE "s_barrier" : WAIT_FINE_OUT : [cq] "s"(c.cpq) : "vcc", "scc", "s100", "s101", "memory");
}
__device__ __forceinline__ void wait_cyc_bar(unsigned tm) {
    unsigned x;
    asm volatile(
        ALN
        "3:\n\t"
        "s_memtime s[100:101]\n\t"
        "s_waitcnt lgkmcnt(0)\n\t"
        "s_sub_u32 %0, %1, s100\n\t"
        "s_cmp_gt_i32 %0, 92\n\t"
        "s_cbranch_scc1 3b\n\t"
        "s_max_i32 %0, %0, 0\n\t"
        "s_andn2_b32 %0, %0, 3\n\t"
        "s_sub_u32 %0, 104, %0\n\t"
        "s_getpc_b64 vcc\n\t"
        "s_add_u32 vcc_lo, vcc_lo, %0\n\t"
        "s_addc_u32 vcc_hi, vcc_hi, 0\n\t"
        "s_setpc_b64 vcc\n\t"
        ".rept 23\n\t"
        "s_nop 0\n\t"
        ".endr\n\t"
        "s_barrier"
        : "=&s"(x) : "s"(tm) : "vcc", "scc", "s100", "s101", "memory");
}
__device__ __forceinline__ unsigned cyc_at(const Clk& c, int d) {
    d = min(d, 20000);
    asm volatile("" : "+s"(d));
    return c.ma + (unsigned)((max(d, -4000) * (int)c.cpq) >> 8);
}
__device__ __forceinline__ void clk_rate(Clk& c, unsigned ta0, unsigned ma0) {
    const int dq = sx24(c.ta - ta0);
    if (dq > 200) {
        const float r = fminf(fmaxf((float)(c.ma - ma0) * __builtin_amdgcn_rcpf((float)dq), 1.f), 8.f);
        asm volatile("s_nop 1\n\tv_readfirstlane_b32 %0, %2\n\tv_readfirstlane_b32 %1, %3\n\ts_nop 3" : "=s"(c.cpq), "=s"(c.qpc)
                     : "v"((unsigned)(256.f * r + 0.5f)), "v"((unsigned)(65536.f * __builtin_amdgcn_rcpf(r))));
    }
}
__device__ __forceinline__ void clk_init(Clk& c) {
    c.ta = 4u * (unsigned)__builtin_amdgcn_s_memrealtime(); c.ma = (unsigned)__builtin_amdgcn_s_memtime();
    for (int i = 0; i < 2; ++i) {
        const unsigned ta0 = c.ta, ma0 = c.ma;
        wait_fine(c.ta + (i ? 360u : 60u), c);
        clk_rate(c, ta0, ma0);
    }
}
__device__ __forceinline__ unsigned sched_lane(int s) { const unsigned l = fresh_lane(); return (l & 31u) * 128u + (l >> 5) * 4u; }
__device__ __forceinline__ int read_sched(__amdgpu_buffer_rsrc_t rwin, int s, unsigned vo, unsigned to, unsigned f, unsigned g0, int& lt, int& xs, unsigned& fc) {
    int d = (int)((f - g0) & 0xFFFFFFu);
    if (__builtin_expect(__any((f >> 24) != (unsigned)s) || s == 0, 0)) {
        for (unsigned spins = 0; __any((f >> 24) != (unsigned)s);) {
            FC(fc);
            if (spin_fail(spins, rwin)) break;
            if (s == 0) __builtin_amdgcn_s_sleep(TSL0); else __builtin_amdgcn_s_sleep(TSLP);
            asm volatile("" ::: "memory");
            if ((f >> 24) != (unsigned)s) f = __builtin_amdgcn_raw_buffer_load_b32(rwin, vo, to, 16);
        }
        const unsigned f0 = __builtin_amdgcn_readfirstlane(f) - 0x100000u;
        d = (int)((f - f0) & 0xFFFFFFu) + (int)((f0 - g0) & 0xFFFFFFu);
    }
    d = max(d, __builtin_amdgcn_update_dpp(0, d, 0xB1, 0xF, 0xF, true));
    d = max(d, __builtin_amdgcn_update_dpp(0, d, 0x4E, 0xF, 0xF, true));
    d = max(d, __builtin_amdgcn_update_dpp(0, d, 0x141, 0xF, 0xF, true));
    d = max(d, __builtin_amdgcn_update_dpp(0, d, 0x140, 0xF, 0xF, true));
    d = max(d, __builtin_amdgcn_update_dpp(0, d, 0x142, 0xA, 0xF, true));
    d = max(__builtin_amdgcn_readlane(d, 16), __builtin_amdgcn_readlane(d, 48));
    const int l0 = lt, x0 = xs;
    d -= 0x400000;
    const bool miss = d > l0 + min(x0 + LTM, LTX);
    if (s >= SAD) { lt = miss ? l0 : min(max(d, l0 - 1 - ((l0 - d) >> LTS)), l0 + LTU); xs = miss ? min(x0 + XSU, XSM) : max(x0 - 1 - (x0 >> XSS), 0); }
    if (s == 1) lt = LT0;
    if (s < SE) xs = max(xs, XE);
    return 0x400000 + (s < SAD ? d + (s ? PENS : PEN0) : miss ? d + (d < DBIG ? PENH : PEN) : l0 + x0) + M1 + M2 + x0;
}
constexpr int QS = 272;
constexpr int TQS = 1152;
constexpr int L_Q = 10240, L_K = L_Q + 2 * TQS, L_S = L_K + 2 * TQS, L_TOK = L_S + 256, L_H = L_TOK + 49152, L_END = L_H + 2 * 40960;
static_assert(C_END <= L_END && L_END == 146176, "LDS");
typedef __attribute__((ext_vector_type(4))) short v4i16_t;
__device__ __forceinline__ u32x2 ldtr(const char* p) { return __builtin_bit_cast(u32x2, __builtin_amdgcn_ds_read_tr16_b64_v4i16((LAS v4i16_t*)p)); }
__device__ __forceinline__ u32x2 ldtr1(const LAS char* p) { return __builtin_bit_cast(u32x2, __builtin_amdgcn_ds_read_tr16_b64_v4i16((LAS v4i16_t*)p)); }
__device__ __forceinline__ u32x4 ldtr2(const LAS char* p0, const LAS char* p1) {
    const u32x2 x = __builtin_bit_cast(u32x2, __builtin_amdgcn_ds_read_tr16_b64_v4i16((LAS v4i16_t*)p0)), y = __builtin_bit_cast(u32x2, __builtin_amdgcn_ds_read_tr16_b64_v4i16((LAS v4i16_t*)p1));
    return (u32x4){x[0], x[1], y[0], y[1]};
}
__device__ __forceinline__ u32x4 ldtr2(const char* p0, const char* p1) { const u32x2 x = ldtr(p0), y = ldtr(p1); return (u32x4){x[0], x[1], y[0], y[1]}; }

__device__ __forceinline__ unsigned poll_lim(int& budget) {
    --budget;
    unsigned lim = budget > 0 ? 0xFFFFFFFEu : 0xFFFFFFFFu;
    asm volatile("" : "+s"(lim));
    return lim;
}
#define GATHER \
        "buffer_load_dwordx4 %[vo], %[rw], %[s0] offen sc1 lds\n\t" \
        "buffer_load_dwordx4 %[vo], %[rw], %[s0] offen offset:1024 sc1 lds\n\t" \
        "buffer_load_dwordx4 %[vo], %[rw], %[s0] offen offset:2048 sc1 lds\n\t" \
        "buffer_load_dwordx4 %[vo], %[rw], %[s0] offen offset:3072 sc1 lds\n\t" \
        "s_movk_i32 m0, 0x1000\n\t" \
        "s_nop 0\n\t" \
        "buffer_load_dwordx4 %[vo], %[rw], %[s4] offen sc1 lds\n\t" \
        "buffer_load_dwordx4 %[vo], %[rw], %[s4] offen offset:1024 sc1 lds\n\t" \
        "buffer_load_dwordx4 %[vo], %[rw], %[s4] offen offset:2048 sc1 lds\n\t" \
        "buffer_load_dwordx4 %[vo], %[rw], %[s4] offen offset:3072 sc1 lds\n\t" \
        "s_movk_i32 m0, 0x2000\n\t" \
        "s_nop 0\n\t" \
        "buffer_load_dwordx4 %[vo], %[rw], %[s1] offen sc1 lds\n\t" \
        "buffer_load_dwordx4 %[vo], %[rw], %[s1] offen offset:1024 sc1 lds"
#define GATHER_R \
        "buffer_load_dwordx4 %[vo], %[rw], %[s1] offen offset:1024 sc1 lds\n\t" \
        "buffer_load_dwordx4 %[vo], %[rw], %[s1] offen sc1 lds\n\t" \
        "s_movk_i32 m0, 0x1000\n\t" \
        "s_nop 0\n\t" \
        "buffer_load_dwordx4 %[vo], %[rw], %[s4] offen offset:3072 sc1 lds\n\t" \
        "buffer_load_dwordx4 %[vo], %[rw], %[s4] offen offset:2048 sc1 lds\n\t" \
        "buffer_load_dwordx4 %[vo], %[rw], %[s4] offen offset:1024 sc1 lds\n\t" \
        "buffer_load_dwordx4 %[vo], %[rw], %[s4] offen sc1 lds\n\t" \
        "s_movk_i32 m0, 0\n\t" \
        "s_nop 0\n\t" \
        "buffer_load_dwordx4 %[vo], %[rw], %[s0] offen offset:3072 sc1 lds\n\t" \
        "buffer_load_dwordx4 %[vo], %[rw], %[s0] offen offset:2048 sc1 lds\n\t" \
        "buffer_load_dwordx4 %[vo], %[rw], %[s0] offen offset:1024 sc1 lds\n\t" \
        "buffer_load_dwordx4 %[vo], %[rw], %[s0] offen sc1 lds"
#define GATHER_IN [vo] "v"(vo), [rw] "s"(rwin), [s0] "s"(so0), [s4] "s"(so0 + 2u * PAIR), [s1] "s"(so0 + 4u * PAIR)
__device__ __forceinline__ void gather_dma(__amdgpu_buffer_rsrc_t rwin, unsigned vo, unsigned so0, bool rev) {
    if (rev) asm volatile("s_movk_i32 m0, 0x2000\n\ts_nop 0\n\t" GATHER_R :: GATHER_IN : "m0", "memory");
    else asm volatile("s_mov_b32 m0, 0\n\ts_nop 0\n\t" GATHER :: GATHER_IN : "m0", "memory");
}
__device__ __forceinline__ void gather_at(unsigned t, Clk& c, __amdgpu_buffer_rsrc_t rwin, unsigned vo, unsigned so0, bool rev) {
    unsigned tm, rp, x, fi;
    if (rev) asm volatile("s_movk_i32 m0, 0x2000\n\t" WAIT_FINE GATHER_R : WAIT_FINE_OUT : [cq] "s"(c.cpq), GATHER_IN : "vcc", "scc", "s100", "s101", "m0", "memory");
    else asm volatile("s_mov_b32 m0, 0\n\t" WAIT_FINE GATHER : WAIT_FINE_OUT : [cq] "s"(c.cpq), GATHER_IN : "vcc", "scc", "s100", "s101", "m0", "memory");
}
__device__ __forceinline__ void dma4k(__amdgpu_buffer_rsrc_t r, unsigned vo, unsigned so, unsigned lds) {
    asm volatile(
        "s_mov_b32 m0, %3\n\t"
        "s_nop 0\n\t"
        "buffer_load_dwordx4 %0, %1, %2 offen sc1 lds\n\t"
        "buffer_load_dwordx4 %0, %1, %2 offen offset:1024 sc1 lds\n\t"
        "buffer_load_dwordx4 %0, %1, %2 offen offset:2048 sc1 lds\n\t"
        "buffer_load_dwordx4 %0, %1, %2 offen offset:3072 sc1 lds"
        :: "v"(vo), "s"(r), "s"(so), "s"(lds) : "m0", "memory");
}
__device__ __forceinline__ int bs2(int c) { return (c & ~3) | ((c & 1) << 1) | ((c >> 1) & 1); }
__device__ __forceinline__ void gather_mem0(char* memL, int tid, int nt = 512) {
    const u16* mem0 = arg(A_MEM0);
    for (int c = tid; c < 640; c += nt) {
        const u32x2* src = (const u32x2*)(mem0 + ((c & 6) + ((c >> 3) & 1)) * D_ + (c >> 4) * 16 + (c & 1) * 4);
        const u32x2 x = src[0], y = src[2];
        *(u32x4*)(memL + c * 16) = (u32x4){(x[0] & 0xFFFFu) | (y[0] << 16), (x[0] >> 16) | (y[0] & 0xFFFF0000u), (x[1] & 0xFFFFu) | (y[1] << 16), (x[1] >> 16) | (y[1] & 0xFFFF0000u)};
    }
}
__device__ __forceinline__ f32x2 hadd(f32x4 c) { return (f32x2){c[0], c[1]} + (f32x2){c[2], c[3]}; }
__device__ __forceinline__ unsigned hpack(f32x4 c) { const f32x2 y = hadd(c); return pack2(y[0], y[1]); }
__device__ __forceinline__ u32x8 cat8(u32x4 lo, u32x4 hi) { return (u32x8){lo[0], lo[1], lo[2], lo[3], hi[0], hi[1], hi[2], hi[3]}; }
__device__ __forceinline__ u32x4 half8(const u32x8& v, int u) { return u ? (u32x4){v[4], v[5], v[6], v[7]} : (u32x4){v[0], v[1], v[2], v[3]}; }
__device__ __forceinline__ void dma1k(__amdgpu_buffer_rsrc_t r, unsigned vo, unsigned so, unsigned lds) {
    asm volatile("s_mov_b32 m0, %3\n\ts_nop 0\n\tbuffer_load_dwordx4 %0, %1, %2 offen sc1 lds" :: "v"(vo), "s"(r), "s"(so), "s"(lds) : "m0", "memory");
}
template <class FM> __device__ __forceinline__ void load_w(u32x8 (&wt)[2][10], const u16* src, unsigned rs, bool perm, unsigned b, unsigned c, int w, FM mid) {
    const __amdgpu_buffer_rsrc_t r = mkrsrc(src, 0x7FFFFFFFu);
    const unsigned l = fresh_lane(), q = l >> 4, lds = (unsigned)(w >> 2) * 65536u, v = w & 3;
    const unsigned vo0 = (perm ? ((q & 1u) << 3) | ((q & 2u) << 1) : q) * rs + ((l & 15u) ^ (q << 1)) * 16u, vo1 = vo0 ^ 128u, a = perm ? rs : 4u * rs;
    const LAS char* p0 = (const LAS char*)(size_t)(lds + q * 2048u + ((l >> 2) & 3u) * 256u + ((2u * v) ^ ((l >> 2) & 7u)) * 32u + (l & 3u) * 8u);
    const LAS char* p1 = (const LAS char*)((size_t)p0 ^ 32u);
    auto issue = [&](int g) {
#pragma unroll
        for (int i = 0; i < 4; ++i)
            asm volatile("s_mov_b32 m0, %3\n\ts_nop 3\n\tbuffer_load_dwordx4 %0, %1, %2 offen lds" :: "v"(i < 2 ? vo0 : vo1), "s"(r), "s"(i * a + (g & 1) * b + (g >> 1) * c), "s"(lds + (g & 3) * 16384u + v * 4096u + i * 1024u) : "m0", "memory");
    };
#pragma unroll
    for (int g = 0; g < 3; ++g) issue(g);
#pragma unroll
    for (int g = 0; g < 10; ++g) {
        asm volatile("s_waitcnt vmcnt(%0)\n\ts_barrier" :: "n"(g < 8 ? 8 : 4 * (9 - g)) : "memory");
        if (g + 3 < 10) issue(g + 3);
        const int o = (g & 3) * 16384;
        wt[0][g] = cat8(ldtr2(p0 + o, p0 + o + 1024), ldtr2(p0 + o + 8192, p0 + o + 9216));
        wt[1][g] = cat8(ldtr2(p1 + o, p1 + o + 1024), ldtr2(p1 + o + 8192, p1 + o + 9216));
        asm volatile("s_waitcnt lgkmcnt(0)" : "+v"(wt[0][g]), "+v"(wt[1][g]) :: "memory");
        if (g == 6) mid();
    }
    BAR0();
}
__device__ __forceinline__ int tile_of(int o) { return 8 * ((o % 10) >> 1) + 2 * (o / 10) + (o & 1); }
__device__ __forceinline__ void wait_tags(__amdgpu_buffer_rsrc_t rwin, int g0, int g1, unsigned n1, u64 val, bool boot) {
    const unsigned l = fresh_lane();
    unsigned o[4];
#pragma unroll
    for (int g = 0; g < 4; ++g) o[g] = g >= g0 && g < g1 && (g + 1 < g1 || l < n1) ? l * 128u + g * 8192u : 0x40000000u;
    for (unsigned spins = 0;;) {
        asm volatile("" ::: "memory");
        u32x2 v[4];
#pragma unroll
        for (int g = 0; g < 4; ++g) v[g] = __builtin_amdgcn_raw_buffer_load_b64(rwin, o[g], W_A1, 16);
#pragma unroll
        for (int g = 0; g < 4; ++g) if (__builtin_bit_cast(u64, v[g]) == val) o[g] = 0x40000000u;
        if (!__any((o[0] & o[1] & o[2] & o[3]) != 0x40000000u)) break;
        if (boot ? ++spins > SPIN_CAP : spin_fail(spins, rwin)) break;
        __builtin_amdgcn_s_sleep(ASLP);
    }
}
template <int N> __device__ __forceinline__ void stage_h(__amdgpu_buffer_rsrc_t rws, unsigned src, unsigned lds, unsigned st) {
    const unsigned l = fresh_lane() * 16u;
#pragma unroll
    for (int i = 0; i < N; ++i) dma1k(rws, l, src + i * st, lds + i * st);
}
template <int N> __device__ __forceinline__ void check_h(__amdgpu_buffer_rsrc_t rws, __amdgpu_buffer_rsrc_t rwin, unsigned src, unsigned lds, unsigned st) {
    const LAS char* p = (const LAS char*)smem + lds + fresh_lane() * 16u;
    for (unsigned spins = 0;;) {
        unsigned m = 0;
#pragma unroll
        for (int i = 0; i < N; ++i) { const u32x4 x = *(const volatile LAS u32x4*)(p + i * st); m = max(max(m, max(x[0], x[1])), max(x[2], x[3])); }
        if (__builtin_expect(!__any(m == ~0u), 1) || spin_fail(spins, rwin)) break;
        stage_h<N>(rws, src, lds, st);
        VMWAIT();
    }
}
__device__ __forceinline__ void wait_h(__amdgpu_buffer_rsrc_t rwin, unsigned fo, int seg, int& hrdy) {
    unsigned lim = 63u;
    if (__builtin_expect(hrdy <= seg, 0)) for (unsigned spins = 0;;) {
        asm volatile("" ::: "memory");
        const u32x4 f = __builtin_amdgcn_raw_buffer_load_b128(rwin, min(fresh_lane(), lim) * 128u, W_H + fo, 16);
        const unsigned long long miss = __ballot((f[0] | f[1] | f[2] | f[3]) != 0u);
        hrdy = miss ? __builtin_ctzll(miss) : (int)lim + 1;
        if (hrdy > seg) break;
        if (spin_fail(spins, rwin)) break;
        lim = (unsigned)min(seg + HLA, 63);
        if (seg < 8) __builtin_amdgcn_s_sleep(HSLP); else __builtin_amdgcn_s_sleep(HSL2);
    }
}
template <int CTRL> __device__ __forceinline__ unsigned dppu(unsigned v) { return (unsigned)__builtin_amdgcn_mov_dpp((int)v, CTRL, 0xF, 0xF, true); }
template <int CTRL> __device__ __forceinline__ u32x4 ror4(u32x4 v) { return (u32x4){dppu<CTRL>(v[0]), dppu<CTRL>(v[1]), dppu<CTRL>(v[2]), dppu<CTRL>(v[3])}; }
template <int N> struct IC { static constexpr int v = N; };
__device__ __forceinline__ float xmax16(float v) {
    auto q = __builtin_amdgcn_permlane16_swap(__float_as_uint(v), __float_as_uint(v), false, false);
    return fmx(__uint_as_float(q[0]), __uint_as_float(q[1]));
}
__device__ __forceinline__ float xsum16(float v) {
    auto q = __builtin_amdgcn_permlane16_swap(__float_as_uint(v), __float_as_uint(v), false, false);
    return __uint_as_float(q[0]) + __uint_as_float(q[1]);
}
__device__ __forceinline__ float addswap16(float a, float b) {
    auto r = __builtin_amdgcn_permlane16_swap(__float_as_uint(a), __float_as_uint(b), false, false);
    return __uint_as_float(r[0]) + __uint_as_float(r[1]);
}
#define LANES() \
    const int lane = (int)fresh_lane(), tid = w * 64 + lane, fr = lane & 15, fq = lane >> 4; \
    const int sidx = (lane & 2) ? 0xEEEE : 0x4444; \
    const int ma0 = fq * 256 + (fr & 1) * 128 + (fr >> 1) * 16; \
    const int prow = 16 * ((fr >> 2) & 1) + 4 * (fr >> 3) + (fr & 3), hoff = fr * 128; \
    (void)tid; (void)sidx; (void)ma0; (void)prow; (void)hoff

__device__ __forceinline__ int seg_after(unsigned long long m, int j) { m = j >= 63 ? 0ull : m >> (j + 1); return m ? j + 1 + __builtin_ctzll(m) : NSEG; }
__device__ __forceinline__ int seg_done(unsigned long long m, int f0, int f1, unsigned long long x = 0) { return min(seg_after(~m, f0), seg_after(m | x, f1)) - 1; }
constexpr unsigned long long MXA = 1ull << 40;
template <int OFF> __device__ __forceinline__ void ldfrag(u32x4& d, const LAS char* p) { asm volatile("ds_read_b128 %0, %1 offset:%2" : "=v"(d) : "v"(p), "n"(OFF) : "memory"); }
template <int U, class FM, class FV> __device__ __forceinline__ void project(const u32x8 (&wt)[2][10], f32x4 (&tc)[2][2], u32x4 (&bb)[8], const LAS char* c0, const LAS char* c1, const LAS char* n0, const LAS char* n1, FM mid, FV vm) {
    constexpr int t = U >> 2, k = U & 3, v = (U + 8) % 40;
    if (U == 32) mid();
    vm(IC<U>());
    const LAS char* p = U < 32 ? (k >> 1 ? c1 : c0) : (k >> 1 ? n1 : n0);
    if (U < 2) asm volatile("s_nop 0\n\ts_waitcnt lgkmcnt(7)\n\tv_mfma_f32_16x16x32_bf16 %0, %3, %2, 0\n\tv_mfma_f32_16x16x32_bf16 %1, %4, %2, 0\n\tds_read_b128 %2, %5 offset:%6"
                            : "=&v"(tc[0][k & 1]), "=&v"(tc[1][k & 1]), "+v"(bb[U & 7]) : "v"(half8(wt[0][t], k >> 1)), "v"(half8(wt[1][t], k >> 1)), "v"(p), "n"((v >> 2) * 4096 + (k & 1) * 2048) : "memory");
    else asm volatile(".rept %7\n\ts_nop 0\n\t.endr\n\ts_waitcnt lgkmcnt(7)\n\tv_mfma_f32_16x16x32_bf16 %0, %3, %2, %0\n\tv_mfma_f32_16x16x32_bf16 %1, %4, %2, %1\n\tds_read_b128 %2, %5 offset:%6"
                      : "+v"(tc[0][k & 1]), "+v"(tc[1][k & 1]), "+v"(bb[U & 7]) : "v"(half8(wt[0][t], k >> 1)), "v"(half8(wt[1][t], k >> 1)), "v"(p), "n"((v >> 2) * 4096 + (k & 1) * 2048), "n"(U < 8 ? 1 : 0) : "memory");
    if constexpr (U < 39) project<U + 1>(wt, tc, bb, c0, c1, n0, n1, mid, vm);
    else asm volatile("s_nop 7" : "+v"(tc[0][0]), "+v"(tc[0][1]), "+v"(tc[1][0]), "+v"(tc[1][1]));
}
__device__ __forceinline__ void project_end(u32x4 (&bb)[8]) {
    asm volatile("s_waitcnt lgkmcnt(0)" : "+v"(bb[0]), "+v"(bb[1]), "+v"(bb[2]), "+v"(bb[3]), "+v"(bb[4]), "+v"(bb[5]), "+v"(bb[6]), "+v"(bb[7]) :: "memory");
}
__device__ __forceinline__ void project0(u32x4 (&bb)[8], const LAS char* c0, const LAS char* c1) {
    ldfrag<0>(bb[0], c0); ldfrag<2048>(bb[1], c0); ldfrag<0>(bb[2], c1); ldfrag<2048>(bb[3], c1);
    ldfrag<4096>(bb[4], c0); ldfrag<6144>(bb[5], c0); ldfrag<4096>(bb[6], c1); ldfrag<6144>(bb[7], c1);
    project_end(bb);
}

__global__ __launch_bounds__(512, 2) B3ATTR void mega_kernel(Args a) {
    char* memL = smem;
    char* tokL = smem + L_TOK;
    char* hL = smem + L_H;
    int w = __builtin_amdgcn_readfirstlane(threadIdx.x >> 6);
    __amdgpu_buffer_rsrc_t rwin = mkrsrc(a.win, W_END);
    char* ws = a.ws;
    __amdgpu_buffer_rsrc_t rws = mkrsrc(ws, (unsigned)OFF_WIN);
    auto fresh_rsrc = [&]() INL { ws = (char*)arg(0); rws = mkrsrc(ws, (unsigned)OFF_WIN); rwin = mkrsrc(arg(1), W_END); };
    const char* pc;
    asm volatile("s_getpc_b64 %0" : "=s"(pc));
    unsigned fs[12] = {};
    const u32x2 tg0 = __builtin_amdgcn_raw_buffer_load_b64(rws, 0u, 0u, 16);
    u64 tag;
    u32x2 rot0;
    {
        const int tid = threadIdx.x;
        if (blockIdx.x < 64) {
            const unsigned j = blockIdx.x;
            if (j == 0 && w == 0) __builtin_amdgcn_raw_buffer_store_b64(__builtin_bit_cast(u32x2, (__builtin_bit_cast(u64, tg0) & ~15ull) + 16ull + (xcc_id() & 7u)), rws, LANE0, 128u, 16);
            const unsigned g0 = (j * 512u + tid) * 16u;
#pragma unroll
            for (int i = 0; i < 3; ++i) st128(SENT, mkrsrc(ws + O_ARM1, O_H + NHF * 40960u - O_ARM1), g0 + i * 524288u, 0, 16);
#pragma unroll
            for (int i = 0; i < 7; ++i) {
                const unsigned g = g0 + i * 524288u;
                st128(SENT, mkrsrc(a.win, W_END + 2 * PAIR), g + (g < W_ARM - W_LAYER ? 0u : W_ZM - (W_ARM - W_LAYER)), 0, 16);
            }
#pragma unroll
            for (int i = 0; i < 3; ++i) st128(SENT, mkrsrc(ws + O_Z, ZTSEG * 2), g0 + i * 524288u, 0, 16);
            static_assert(W_ARM - W_LAYER + W_END + 2 * PAIR - W_ZM <= 7u * 524288u && O_ARM1 + 3u * 524288u >= O_H + NHF * 40960u && ZTSEG * 2 <= 3u * 524288u, "arming");
        } else if (blockIdx.x >= 128) {
            RowTw tw;
            row_tw(tw, tid);
            a1_unit<8, false>(mkrsrc(a.x, S_ * D_ * 2), (blockIdx.x - 128) * 16, rws, rwin, tw, tid, fs);
        } else {
            const unsigned j = blockIdx.x - 64;
            FST(0);
            auto row = [&](unsigned i) { return (j & 7u) + 8u * (i & 15u) + 128u * ((j >> 3) & 3u) + 512u * (((j >> 4) & 2u) + ((i >> 4) & 1u)); };
            u32x4 v[6];
#pragma unroll
            for (int i = 0; i < 6; ++i) {
                const unsigned id = min(tid + 512 * i, 64 * 41 - 1), r = id / 41u;
                v[i] = ((const u32x4*)(r < 32u ? a.wr : a.wi))[row(r) * (DF * 2) / 16u + id - r * 41u];
            }
            u16 n[4];
            if (w == 7) {
                const size_t kn = (size_t)((S_ - row(tid)) & (S_ - 1)) * DF;
                n[0] = a.wr[kn]; n[1] = a.wi[kn]; n[2] = a.wr[kn + 320]; n[3] = a.wi[kn + 320];
            }
            const unsigned wo = WARM ? (w * 8u + (j >> 3)) * 8192u : 0x40000000u;
            const unsigned x0 = __builtin_amdgcn_raw_buffer_load_b32(mkrsrc(pc, CODE_BYTES), (tid & 63) * 128u, wo, 0), x1 = __builtin_amdgcn_raw_buffer_load_b32(mkrsrc(&TWC, sizeof(TwCol)), (tid & 63) * 128u, wo, 0);
#pragma unroll
            for (int i = 0; i < 6; ++i) ((u32x4*)smem)[min(tid + 512 * i, 64 * 41 - 1)] = v[i];
            FST(1);
            BAR();
            FST(2);
            const u16* q0 = (const u16*)smem + (j & 7u);
            u32x4 o[5];
#pragma unroll
            for (int i = 0; i < 5; ++i) {
                const u16* q = q0 + ((tid >> 1) & 7) * 328 + (tid >> 4) * 2 + (tid & 1) + 64 * i;
#pragma unroll
                for (int e = 0; e < 4; ++e) o[i][e] = (unsigned)q[e * 8 * 328] | ((unsigned)q[(32 + e * 8) * 328] << 16);
            }
#pragma unroll
            for (int i = 0; i < 5; ++i) st128(o[i], rws, (tid >> 4) * WST + (tid & 15) * 16u, O_WFT + i * 32u * WST + (j >> 5) * 8192u + (j & 7u) * 1024u + ((j >> 3) & 3u) * 256u, 16);
            if (w == 7) {
                const u16* q = q0 + (tid & 31) * 328;
                const unsigned k = row(tid);
                const c2 h0 = (c2){bf2f(q[0]) + bf2f(n[0]), bf2f(q[32 * 328]) - bf2f(n[1])} * bc(0.25f), h1 = (c2){bf2f(q[320]) + bf2f(n[2]), bf2f(q[32 * 328 + 320]) - bf2f(n[3])} * bc(0.25f);
                st128(__builtin_bit_cast(u32x4, (f32x4){h0.x + h1.x, h0.y + h1.y, h0.x - h1.x, h0.y - h1.y}), rws, (((k >> 8) & 6u) * 256u + (k & 64u) * 4u + (k & 7u) * 32u + ((k >> 4) & 24u) + ((k >> 3) & 7u)) * 16u, O_WFT + NCOL * WST, 16);
            }
            FST(3);
            asm volatile("" :: "v"(x0), "v"(x1));
            FST(4);
        }
        rot0 = __builtin_amdgcn_raw_buffer_load_b64(rws, 0u, 128u, 16);
        VMWAIT();
        FST(8);
        BAR0();
        FST(9);
    }
    tag = (((u64)__builtin_amdgcn_readfirstlane(tg0[1]) << 32) | (__builtin_amdgcn_readfirstlane(tg0[0]) & ~15u)) + 16ull;
    if (w == 0) __builtin_amdgcn_raw_buffer_store_b64(__builtin_bit_cast(u32x2, tag + 8ull), rwin, LANE0, W_A1 + blockIdx.x * 128u, 16);
    if (blockIdx.x >= 128) FSW(0); else if (blockIdx.x >= 64) FSW(6);
    unsigned vx;
    int bid;
    auto set_role = [&](unsigned x0) INL {
        vx = x0;
        const int j = blockIdx.x >> 3, x = vx & 3;
        if (!(vx & 4)) bid = j < 8 ? 8 * x + j : j < 16 ? 24 + 8 * x + j : j < 26 ? 80 + 10 * x + j : j < 30 ? NWRK + 4 * (j - 26) + x : NREC - 22 + 10 * x + j;
        else bid = j < 8 ? 64 + 8 * x + j : j < 16 ? NQT - 8 + 8 * x + j : j < 24 ? NREC - 16 + 10 * x + j : NXT - 24 + 8 * x + j;
    };
    auto get_rot = [&]() INL {
        unsigned r = 0;
        for (unsigned spins = 0; spins < SPIN_CAP; ++spins) {
            const u64 x = __builtin_bit_cast(u64, rot0) - tag;
            r = __builtin_amdgcn_readfirstlane((unsigned)x);
            if (__all(x < 8ull)) break;
            __builtin_amdgcn_s_sleep(2);
            asm volatile("" ::: "memory");
            rot0 = __builtin_amdgcn_raw_buffer_load_b64(rws, 0u, 128u, 16);
        }
        return (r + blockIdx.x + 4u * DIE) & 7u;
    };
    set_role(blockIdx.x < 64 ? (xcc_id() ^ (4u * DIE)) & 7u : get_rot());
    unsigned long long tref = 0;
    int layer = 0;
    unsigned fo = 0;
    unsigned nh = 0, nz = 0, mc = 0, qz = 0, mo = 0, tro = 0;
    auto near_init = [&]() INL {
        unsigned f;
        for (unsigned spins = 0;;) {
            asm volatile("" ::: "memory");
            f = __builtin_amdgcn_raw_buffer_load_b32(rwin, (fresh_lane() & 15u) * 4u, W_NEAR, 16);
            if (!__any(f == ~0u) || spin_fail(spins, rwin)) break;
            __builtin_amdgcn_s_sleep(8);
        }
        const unsigned h = (unsigned)__ballot(f & 1u), q = (unsigned)__ballot(f & 2u) ^ 0xAAAAu;
        auto most = [&](unsigned m) { return (__builtin_popcount(m & 0x0F0Fu) + 3u) >> 3; };
        nh = most(h) * HALF; nz = most(h >> 4) * HALF;
        mc = (vx & 3u) ^ most(q) ^ (STK ? 2u * most((unsigned)__ballot(f & 4u) ^ 0x3333u) : STX);
        qz = most(q >> 4);
        mo = W_MEM + (mc & 1u) * PAIR + nh + (mc >> 1) * 4096u;
        tro = mo - W_MEM + W_T;
    };
    auto zpair = [&](unsigned x, unsigned k) { return (((x >> 1) * 10u + k) * 2u + ((x & 1u) ^ qz)) * PAIR + nz; };
    auto two = [&]() { const unsigned l = fresh_lane(); return l < 4 ? (l >> 1) * PAIR + (l & 1) * 4096u + nh : 0x40000000u; };
    const f32x4 z4 = {0.f, 0.f, 0.f, 0.f};
    int hrdy = 0, budget = (int)SPIN_CAP;
    unsigned fc0 = 0, fc1 = 0, fc2 = 0;
    Clk ck = {0u, 0u, 1600u, 10486u};
    SST(5);
#define xt (bid >= NXT && bid < NWRK)
#define qx (bid >= NQT && bid < NWRK)
#define xq (bid >= NREC && bid < NQT && (bid - NREC) % 10 < 8)
    unsigned tpp = 0;
    int lt = LT1, xs = XS0;
    struct Tk { unsigned vo, tw, sl; const LAS char* sp; };
    auto tk_init = [&]() {
        const unsigned l = fresh_lane();
        Tk k = {l * 16u, two(), (l & 31u) * 128u + 8u - (l >> 5) * 4u, (const LAS char*)(memL + l * 64 + 4 + ((l >> 1) & 7) * 8)};
        PIN2(k.vo, k.tw); PIN2(k.sl, k.sp);
        return k;
    };
    auto sched2 = [&](Tk& k, int s, unsigned f) INL {
        const unsigned g0 = tpp - 0x400000u;
        tpp = g0 + (unsigned)read_sched(rwin, s, k.sl, tro + (unsigned)(s & 3) * 2u * PAIR, f, g0, lt, xs, fc1);
        k.sl &= ~12u;
        RT(6);
    };
    auto sched = [&](Tk& k, int s) INL { sched2(k, s, __builtin_amdgcn_raw_buffer_load_b32(rwin, k.sl, tro + (unsigned)(s & 3) * 2u * PAIR, 16)); };
    unsigned it = 0;
    auto fetch = [&](const Tk& k, int s, bool first, unsigned lim, unsigned dc, unsigned fo, bool tell) INL {
        const unsigned sb = (unsigned)(s & 3) * 6u * PAIR, ta0 = ck.ta, mm0 = ck.ma;
        if (__builtin_expect(s > 0, 1)) {
            if (__builtin_expect(first, 1)) { const unsigned te = cyc_at(ck, sx24(tpp - TKE - ck.ta)); while ((int)((unsigned)__builtin_amdgcn_s_memtime() - te) < 0) __builtin_amdgcn_s_sleep(1); }
            if (__builtin_expect(first, 1)) gather_at(tpp + (tell ? 0 : DGV), ck, rwin, k.vo, mo + sb, VREV && !tell); else { const unsigned c = (mc + 1u + (it & 1u)) & 3u; gather_dma(rwin, k.vo, W_MEM + (c & 1u) * PAIR + (c >> 1) * 4096u + nh + sb, VREV && !tell); }
        }
        __builtin_amdgcn_s_setprio(0);
        RT(1);
        clk_rate(ck, ta0, mm0);
        unsigned tb = ck.ma - dc, tg = (unsigned)s << 24, so = W_T + (unsigned)(s & 3) * 2u * PAIR + fo;
        asm volatile("" : "+s"(tb), "+s"(tg), "+s"(so));
        asm volatile(ALN "s_waitcnt vmcnt(%1) lgkmcnt(0)\n\ts_barrier\n\ts_waitcnt vmcnt(%2)\n\ts_barrier\n\ts_waitcnt vmcnt(0)\n\ts_memtime %0\n\ts_barrier"
                     : "=s"(tref) : "n"(10 - NB1), "n"(10 - NB2) : "memory");
        MT(1);
        if (!tell) { RT(3); return; }
        unsigned x0 = *(const LAS unsigned*)k.sp, x1 = *(const LAS unsigned*)(k.sp + 4096), x2 = *(const LAS unsigned*)(k.sp + 6144);
        __builtin_amdgcn_s_waitcnt(0xC07F);
        asm volatile("" : "+s"(tref) :: "memory");
        const unsigned fw = ((ck.ta + ((((unsigned)tref - tb) * ck.qpc) >> 16)) & 0xFFFFFFu) | tg;
        __builtin_amdgcn_raw_buffer_store_b32(fw, rwin, k.tw, __any(max(max(x0, x1), x2) > lim) ? 0x40000000u : so, 16);
        RT(3);
    };
    auto rout = [&]() { return mkrsrc(layer ? arg(A_OUT) : (const u16*)(ws + OFF_X2), S_ * D_ * 2); };
    auto rec_end = [&](unsigned g) INL {
        if (g) for (unsigned spins = 0;;) {
            asm volatile("" ::: "memory");
            if (__builtin_amdgcn_readfirstlane(__builtin_amdgcn_raw_buffer_load_b32(rws, 0u, g == 3 ? O_FL + (unsigned)(bid & 127) * GST + 4092u : O_FL - g * SEG * D_ * 2u + (unsigned)bid * 128u, 16)) != -1 || spin_fail(spins, rwin)) break;
            __builtin_amdgcn_s_sleep(16);
        }
    };
    auto mytid = [&]() { return w * 64 + (int)fresh_lane(); };
    auto new_layer = [&]() { fo = (unsigned)layer * W_LAYER; asm volatile("" : "+s"(fo), "+s"(w), "+s"(bid), "+s"(nh), "+s"(nz), "+s"(mc), "+s"(qz), "+s"(tag)); hrdy = 0; tpp = 0; lt = LT1; xs = XS0; };
    auto load_qv = [&](u32x8 (&wt)[2][10], int hh, bool v, auto mid) INL {
        const bool o = v && w >= 4;
        load_w(wt, arg(A_WQ + 2 * v + (w >> 2)) + (o ? (hh * 128 + (w & 3) * 32) * D_ : hh * 128 + (w & 3) * 16 * HD), o ? D_ * 2 : HD * 2, !o, o ? 16 * D_ * 2 : 64 * HD * 2, o ? 256 : 128 * HD * 2, w, mid);
    };
    auto hseg = [&](int j) { return O_H + (unsigned)(j - NHF * layer) * 40960u; };
    auto stage2 = [&](int j0, int j1, bool vt) INL {
        if (w == 0) {
            if (j1 >= NHF) wait_h(rwin, fo, j1, hrdy);
            else if (TH) { const unsigned vo[1] = {(fresh_lane() & 3u) * 4u}; wait_told(rwin, vo, W_H + fo + (unsigned)j0 * 128u + 16u, 1); }
            else for (unsigned spins = 0;;) {
                asm volatile("" ::: "memory");
                const unsigned l = fresh_lane();
                if (!__any(__builtin_amdgcn_raw_buffer_load_b32(rws, (l & 31u) * 128u + (l >> 5) * (unsigned)(j1 - j0) * 40960u, hseg(j0) + 36864u, 16) == ~0u) || spin_fail(spins, rwin)) break;
                __builtin_amdgcn_s_sleep(HSLP);
            }
            *(volatile LAS int*)(smem + L_S + 32) = hrdy;
        }
        BAR();
        hrdy = __builtin_amdgcn_readfirstlane(*(volatile LAS int*)(smem + L_S + 32));
        SST(2);
        if (j0 == 1 && vt) __builtin_amdgcn_s_sleep(DH1);
        stage_h<5>(rws, hseg(j0) + w * 1024u, L_H + w * 1024u, 8192u);
        if (!vt) {
            stage_h<5>(rws, hseg(j1) + w * 1024u, L_H + 40960u + w * 1024u, 8192u);
            asm volatile("s_waitcnt vmcnt(5)" ::: "memory");
        } else if (w >= 4 && j1 != j0) {
            stage_h<10>(rws, hseg(j1) + (w - 4) * 1024u, L_H + 40960u + (w - 4) * 1024u, 4096u);
            asm volatile("s_waitcnt vmcnt(10)" ::: "memory");
        } else VMWAIT();
        check_h<5>(rws, rwin, hseg(j0) + w * 1024u, L_H + w * 1024u, 8192u);
        BAR0();
        SST(3);
        if (vt && w >= 4 && j1 != j0) {
            VMWAIT();
            check_h<10>(rws, rwin, hseg(j1) + (w - 4) * 1024u, L_H + 40960u + (w - 4) * 1024u, 4096u);
        }
    };
#define WO(eb, nt) wt[(nt) & 1][((nt) & ~1) + (eb)]
    unsigned zb = W_ZM;
    auto probe = [&]() INL {
        {
            unsigned* best = (unsigned*)smem;
            const int tid = mytid();
            const __amdgpu_buffer_rsrc_t rw = mkrsrc(a.win + (bid & 4 ? W_END : W_PROBE), 2 * PAIR);
            const unsigned base = (unsigned)(bid & 8) * 64u + w * 1024u;
            if (tid < 8) best[tid] = 100000u;
            if (w == 0) wait_tags(rwin, 0, 1, 64u, tag + 8ull, true);
            BAR();
            unsigned mn[4] = {~0u, ~0u, ~0u, ~0u};
            for (int r = 0; r < NPR; ++r) {
#pragma unroll
                for (int i = 0; i < 4; ++i) {
                    const unsigned long long t1 = __builtin_amdgcn_s_memtime();
                    asm volatile("s_waitcnt lgkmcnt(0)" ::: "memory");
                    unsigned v = __builtin_amdgcn_raw_buffer_load_b32(rw, base + r * 128u + i * HALF, 0, 16);
                    asm volatile("s_waitcnt vmcnt(0)" : "+v"(v) :: "memory");
                    const unsigned long long t2 = __builtin_amdgcn_s_memtime();
                    mn[i] = min(mn[i], (unsigned)(t2 - t1));
                }
            }
            if ((tid & 63) == 0) {
#pragma unroll
                for (int i = 0; i < 4; ++i) atomicMin(&best[i + (w & 4)], mn[i]);
            }
            BAR();
            const unsigned h = min(min(best[1], best[3]), min(best[5], best[7])) < min(min(best[0], best[2]), min(best[4], best[6])), q = best[h + 2] + best[h + 6] < best[h] + best[h + 4], k = best[h + 4] + best[h + 6] < best[h] + best[h + 2];
            if (w == 0) __builtin_amdgcn_raw_buffer_store_b32(h + 2u * q + 4u * k, rwin, LANE0, W_NEAR + (unsigned)(bid & 15) * 4u, 16);
            BAR();
        }
    };
    auto vo_role = [&](const u32x8 (&wt)[2][10]) INL {
        const int hh = bid & 31;
        auto steps = [&](auto cc) INL {
            constexpr int C = decltype(cc)::v & 1;
            constexpr bool TK = decltype(cc)::v == 3;
            LANES();
            Tk tk = {0u, 0u, 0x40000000u, nullptr};
            if (TK) { tk = tk_init(); __builtin_amdgcn_s_setprio(TKP); }
            const unsigned zi = (unsigned)(hh * 128 + fr * 8) * 2u;
            unsigned zo0 = zpair(w & 3, fq) + zi, zo1 = zo0 + 8 * PAIR, zo2 = zpair(w & 3, 8 + (fq >> 1)) + zi + (fq & 1) * 8;
            const LAS char* va = (const LAS char*)(smem + L_Q + (fr & 1) * 544 + ((fr >> 1) & 1) * 1152 + (16 * fq + 2 * (fr >> 2)) * 4);
            LAS char* vw = (LAS char*)(smem + L_Q + (fq & 1) * 544 + (fq >> 1) * 1152 + (16 * w + bs2(fr)) * 4);
            const LAS char* mp0 = (const LAS char*)(memL + ma0);
            PIN2(zo0, zo1); PIN1(zo2);
            if (C == 0) { PIN2(mp0, vw); } else PIN1(va);
            u32x4 za = SENT, zc = SENT;
            u32x2 ze = {0u, 0u};
            auto put_z2 = [&](int s) {
                const unsigned z2b = s < NSEG ? O_Z2 + (unsigned)s * (NOWN * 8192u) + (unsigned)(10 * (w & 3) * 8192 + hh * 256) : 0x40000000u, l2 = fresh_lane();
                st128(za, rws, (l2 >> 4) * 8192u + (l2 & 15u) * 16u, z2b, 16);
                st128(zc, rws, (l2 >> 4) * 8192u + (l2 & 15u) * 16u, z2b + 4u * 8192u, 16);
                __builtin_amdgcn_raw_buffer_store_b64(ze, rws, (l2 >> 5) * 8192u + (l2 & 15u) * 16u + ((l2 >> 4) & 1u) * 8u, z2b + 8u * 8192u, 16);
            };
            for (int s = 0; s < NSEG; ++s) {
                RT(0);
                bool redo = false;
                unsigned sf = 0;
                do {
                    ++it;
                    if (C == 0) {
                        const unsigned lim = s > 0 ? poll_lim(budget) : ~0u;
                        asm volatile(
                            "v_mov_b64 v[200:201], 0\n\t"
                            "v_mov_b64 v[202:203], 0\n\t"
                            "v_mov_b64 v[204:205], 0\n\t"
                            "v_mov_b64 v[206:207], 0\n\t"
                            "v_mov_b32 v208, 0\n\t"
                            "s_setprio 2\n\t"
                            ALN
                            "s_waitcnt lgkmcnt(0)\n\t"
                            "s_barrier\n\t"
                            MTA(0)
                            "ds_read_b128 v[212:215], %[mp] offset:" VOFF0 "\n\t"
                            "ds_read_b128 v[216:219], %[mp] offset:" VOFF1 "\n\t"
                            "ds_read_b128 v[220:223], %[mp] offset:" VOFF2 "\n\t"
                            "s_barrier\n\t"
                            "ds_read_b128 v[224:227], %[mp] offset:" VOFF3 "\n\t"
                            "ds_read_b128 v[228:231], %[mp] offset:" VOFF4 "\n\t"
                            "ds_read_b128 v[232:235], %[mp] offset:" VOFF5 "\n\t"
                            "s_waitcnt lgkmcnt(5)\n\t"
                            "v_smfmac_f32_16x16x64_bf16 v[200:203], v[212:215], %[w00], %[ix]\n\t"
                            "s_waitcnt lgkmcnt(4)\n\t"
                            "v_smfmac_f32_16x16x64_bf16 v[200:203], v[216:219], %[w01], %[ix]\n\t"
                            "s_barrier\n\t"
                            MTA(1)
                            "ds_read_b128 v[236:239], %[mp] offset:" VOFF6 "\n\t"
                            "s_waitcnt lgkmcnt(4)\n\t"
                            "v_smfmac_f32_16x16x64_bf16 v[200:203], v[220:223], %[w02], %[ix]\n\t"
                            "ds_read_b128 v[240:243], %[mp] offset:" VOFF7 "\n\t"
                            "s_waitcnt lgkmcnt(4)\n\t"
                            "v_smfmac_f32_16x16x64_bf16 v[200:203], v[224:227], %[w03], %[ix]\n\t"
                            "ds_read_b128 v[244:247], %[mp] offset:" VOFF8 "\n\t"
                            "s_waitcnt lgkmcnt(4)\n\t"
                            "v_smfmac_f32_16x16x64_bf16 v[200:203], v[228:231], %[w04], %[ix]\n\t"
                            "ds_read_b128 v[248:251], %[mp] offset:" VOFF9 "\n\t"
                            "s_waitcnt lgkmcnt(4)\n\t"
                            "v_smfmac_f32_16x16x64_bf16 v[200:203], v[232:235], %[w05], %[ix]\n\t"
                            "s_waitcnt lgkmcnt(3)\n\t"
                            "v_smfmac_f32_16x16x64_bf16 v[200:203], v[236:239], %[w06], %[ix]\n\t"
                            "s_waitcnt lgkmcnt(2)\n\t"
                            "v_smfmac_f32_16x16x64_bf16 v[200:203], v[240:243], %[w07], %[ix]\n\t"
                            "s_waitcnt lgkmcnt(1)\n\t"
                            "v_smfmac_f32_16x16x64_bf16 v[200:203], v[244:247], %[w08], %[ix]\n\t"
                            "s_waitcnt lgkmcnt(0)\n\t"
                            "v_smfmac_f32_16x16x64_bf16 v[200:203], v[248:251], %[w09], %[ix]\n\t"
                            "s_nop 2\n\t"
                            "v_smfmac_f32_16x16x64_bf16 v[204:207], v[212:215], %[w10], %[ix]\n\t"
                            "s_nop 2\n\t"
                            "v_smfmac_f32_16x16x64_bf16 v[204:207], v[216:219], %[w11], %[ix]\n\t"
                            "v_add_f32 v200, v200, v202\n\t"
                            "v_add_f32 v201, v201, v203\n\t"
                            "v_cvt_pk_bf16_f32 v200, v200, v201\n\t"
                            "ds_write_b32 %[vw], v200\n\t"
                            MTA(2)
                            "v_smfmac_f32_16x16x64_bf16 v[204:207], v[220:223], %[w12], %[ix]\n\t"
                            "v_max3_u32 v208, v208, v212, v213\n\t"
                            "v_max3_u32 v208, v208, v214, v215\n\t"
                            "v_smfmac_f32_16x16x64_bf16 v[204:207], v[224:227], %[w13], %[ix]\n\t"
                            "v_max3_u32 v208, v208, v216, v217\n\t"
                            "v_max3_u32 v208, v208, v218, v219\n\t"
                            "v_smfmac_f32_16x16x64_bf16 v[204:207], v[228:231], %[w14], %[ix]\n\t"
                            "v_max3_u32 v208, v208, v220, v221\n\t"
                            "v_max3_u32 v208, v208, v222, v223\n\t"
                            "v_smfmac_f32_16x16x64_bf16 v[204:207], v[232:235], %[w15], %[ix]\n\t"
                            "v_max3_u32 v208, v208, v224, v225\n\t"
                            "v_max3_u32 v208, v208, v226, v227\n\t"
                            "v_smfmac_f32_16x16x64_bf16 v[204:207], v[236:239], %[w16], %[ix]\n\t"
                            "v_max3_u32 v208, v208, v228, v229\n\t"
                            "v_max3_u32 v208, v208, v230, v231\n\t"
                            "v_smfmac_f32_16x16x64_bf16 v[204:207], v[240:243], %[w17], %[ix]\n\t"
                            "s_waitcnt lgkmcnt(0)\n\t"
                            "s_barrier\n\t"
                            MTA(5)
                            "v_smfmac_f32_16x16x64_bf16 v[204:207], v[244:247], %[w18], %[ix]\n\t"
                            "v_max3_u32 v208, v208, v232, v233\n\t"
                            "v_max3_u32 v208, v208, v234, v235\n\t"
                            "v_smfmac_f32_16x16x64_bf16 v[204:207], v[248:251], %[w19], %[ix]\n\t"
                            "v_max3_u32 v208, v208, v236, v237\n\t"
                            "v_max3_u32 v208, v208, v238, v239\n\t"
                            "v_max3_u32 v208, v208, v240, v241\n\t"
                            "v_max3_u32 v208, v208, v242, v243\n\t"
                            "v_max3_u32 v208, v208, v244, v245\n\t"
                            "v_max3_u32 v208, v208, v246, v247\n\t"
                            "v_max3_u32 v208, v208, v248, v249\n\t"
                            "v_max3_u32 v208, v208, v250, v251\n\t"
                            "v_cmp_lt_u32 vcc, %[lim], v208\n\t"
                            "v_add_f32 v204, v204, v206\n\t"
                            "v_add_f32 v205, v205, v207\n\t"
                            "v_cvt_pk_bf16_f32 v204, v204, v205\n\t"
                            "ds_write_b32 %[vw], v204 offset:256\n\t"
                            MTA(3)
                            "s_cbranch_vccz 1f\n\t"
                            "v_mov_b32 v208, %[it]\n\t"
                            "v_mov_b32 v209, 0\n\t"
                            "ds_write_b32 v209, v208 offset:%[ls]\n\t"
                            "1:\n\t"
                            "s_waitcnt vmcnt(0) lgkmcnt(0)\n\t"
                            "s_barrier\n\t"
                            "s_setprio 0\n\t"
                            :: [mp] "v"(mp0), [vw] "v"(vw), [ix] "v"(sidx), [lim] "s"(lim), [it] "s"(it), [ls] "n"(L_S),
                              [w00] "v"(wt[0][VREV ? 9 - 0 : 0]), [w01] "v"(wt[0][VREV ? 9 - 1 : 1]), [w02] "v"(wt[0][VREV ? 9 - 2 : 2]), [w03] "v"(wt[0][VREV ? 9 - 3 : 3]), [w04] "v"(wt[0][VREV ? 9 - 4 : 4]), [w05] "v"(wt[0][VREV ? 9 - 5 : 5]), [w06] "v"(wt[0][VREV ? 9 - 6 : 6]), [w07] "v"(wt[0][VREV ? 9 - 7 : 7]), [w08] "v"(wt[0][VREV ? 9 - 8 : 8]), [w09] "v"(wt[0][VREV ? 9 - 9 : 9]), [w10] "v"(wt[1][VREV ? 9 - 0 : 0]), [w11] "v"(wt[1][VREV ? 9 - 1 : 1]), [w12] "v"(wt[1][VREV ? 9 - 2 : 2]), [w13] "v"(wt[1][VREV ? 9 - 3 : 3]), [w14] "v"(wt[1][VREV ? 9 - 4 : 4]), [w15] "v"(wt[1][VREV ? 9 - 5 : 5]), [w16] "v"(wt[1][VREV ? 9 - 6 : 6]), [w17] "v"(wt[1][VREV ? 9 - 7 : 7]), [w18] "v"(wt[1][VREV ? 9 - 8 : 8]), [w19] "v"(wt[1][VREV ? 9 - 9 : 9])
                            : "v200", "v201", "v202", "v203", "v204", "v205", "v206", "v207", "v208", "v209", "v212", "v213", "v214", "v215", "v216", "v217", "v218", "v219", "v220", "v221", "v222", "v223", "v224", "v225", "v226", "v227", "v228", "v229", "v230", "v231", "v232", "v233", "v234", "v235", "v236", "v237", "v238", "v239", "v240", "v241", "v242", "v243", "v244", "v245", "v246", "v247", "v248", "v249", "v250", "v251", "vcc", "memory");
                        MT(4);
                        redo = __builtin_amdgcn_readfirstlane(*(volatile LAS unsigned*)(smem + L_S)) == it;
                        if (redo) FC(fc0);
                    } else {
                        f32x16 ca, cb;
                        f32x8 cc;
                        asm volatile("v_mov_b64 v[212:213], 0\n\tv_mov_b64 v[214:215], 0\n\tv_mov_b64 v[216:217], 0\n\tv_mov_b64 v[218:219], 0\n\tv_mov_b64 v[220:221], 0\n\tv_mov_b64 v[222:223], 0\n\tv_mov_b64 v[224:225], 0\n\tv_mov_b64 v[226:227], 0\n\tv_mov_b64 v[228:229], 0\n\tv_mov_b64 v[230:231], 0\n\tv_mov_b64 v[232:233], 0\n\tv_mov_b64 v[234:235], 0\n\tv_mov_b64 v[236:237], 0\n\tv_mov_b64 v[238:239], 0\n\tv_mov_b64 v[240:241], 0\n\tv_mov_b64 v[242:243], 0\n\tv_mov_b64 v[244:245], 0\n\tv_mov_b64 v[246:247], 0\n\tv_mov_b64 v[248:249], 0\n\tv_mov_b64 v[250:251], 0\n\t" : "={v[212:227]}"(ca), "={v[228:243]}"(cb), "={v[244:251]}"(cc));
                        unsigned sr = tro + (unsigned)(s & 3) * 2u * PAIR;
                        asm volatile("" : "+s"(sr));
                        if (TK) fetch(tk, s, !redo, s > 0 ? poll_lim(budget) : ~0u, 0u, 0u, false);
                        else asm volatile(ALN "s_waitcnt lgkmcnt(0)\n\ts_barrier\n\ts_barrier\n\ts_barrier" ::: "memory");
                        unsigned vd, vv, zs = zb;
                        u32x4 af1;
                        asm volatile(
                            "s_setprio 1\n\t"
                            "s_waitcnt lgkmcnt(0)\n\t"
                            "s_barrier\n\t"
                            "ds_read_b64_tr_b16 v[192:193], %[va]\n\t"
                            "ds_read_b64_tr_b16 v[194:195], %[va] offset:32\n\t"
                            "s_waitcnt lgkmcnt(0)\n\t"
                            "v_smfmac_f32_16x16x64_bf16 v[212:215], v[192:195], %[w0], %[ix]\n\t"
                            "v_smfmac_f32_16x16x64_bf16 v[220:223], v[192:195], %[w2], %[ix]\n\t"
                            "v_smfmac_f32_16x16x64_bf16 v[216:219], v[192:195], %[w1], %[ix]\n\t"
                            "v_smfmac_f32_16x16x64_bf16 v[224:227], v[192:195], %[w3], %[ix]\n\t"
                            "s_barrier\n\t"
                            MTA(4)
                            "ds_read_b64_tr_b16 v[196:197], %[va] offset:256\n\t"
                            "v_smfmac_f32_16x16x64_bf16 v[228:231], v[192:195], %[w4], %[ix]\n\t"
                            "ds_read_b64_tr_b16 v[198:199], %[va] offset:288\n\t"
                            "v_smfmac_f32_16x16x64_bf16 v[236:239], v[192:195], %[w6], %[ix]\n\t"
                            "ds_read_b32 v210, %[vz] offset:%[ls]\n\t"
                            "v_smfmac_f32_16x16x64_bf16 v[232:235], v[192:195], %[w5], %[ix]\n\t"
                            "v_smfmac_f32_16x16x64_bf16 v[240:243], v[192:195], %[w7], %[ix]\n\t"
                            "v_smfmac_f32_16x16x64_bf16 v[244:247], v[192:195], %[w8], %[ix]\n\t"
                            "v_smfmac_f32_16x16x64_bf16 v[248:251], v[192:195], %[w9], %[ix]\n\t"
                            "buffer_load_dword %[sf], %[sl], %[rw], %[sr] offen sc1\n\t"
                            : [sf] "=&v"(sf), "+{v[212:227]}"(ca), "+{v[228:243]}"(cb), "+{v[244:251]}"(cc), "=&{v[196:199]}"(af1), "=&{v210}"(vv)
                            : [va] "v"(va), [vz] "v"(0u), [ls] "n"(L_S), [ix] "v"(sidx), [sl] "v"(tk.sl), [sr] "s"(sr), [rw] "s"(rwin),
                              [w0] "v"(WO(0, 0)), [w1] "v"(WO(0, 1)), [w2] "v"(WO(0, 2)), [w3] "v"(WO(0, 3)), [w4] "v"(WO(0, 4)), [w5] "v"(WO(0, 5)), [w6] "v"(WO(0, 6)), [w7] "v"(WO(0, 7)), [w8] "v"(WO(0, 8)), [w9] "v"(WO(0, 9))
                            : "v192", "v193", "v194", "v195", "memory");
                        asm volatile(
                            "s_waitcnt lgkmcnt(0)\n\t"
                            "v_smfmac_f32_16x16x64_bf16 v[212:215], v[196:199], %[w0], %[ix]\n\t"
                            "v_readfirstlane_b32 %[vd], v210\n\t"
                            "v_smfmac_f32_16x16x64_bf16 v[220:223], v[196:199], %[w2], %[ix]\n\t"
                            "s_cmp_eq_u32 %[vd], %[it]\n\t"
                            "s_cselect_b32 %[zs], 2.0, %[zs]\n\t"
                            "v_smfmac_f32_16x16x64_bf16 v[216:219], v[196:199], %[w1], %[ix]\n\t"
                            "s_nop 1\n\t"
                            "v_smfmac_f32_16x16x64_bf16 v[224:227], v[196:199], %[w3], %[ix]\n\t"
                            "v_add_f32 v212, v212, v214\n\t"
                            "v_smfmac_f32_16x16x64_bf16 v[228:231], v[196:199], %[w4], %[ix]\n\t"
                            "v_add_f32 v213, v213, v215\n\t"
                            "v_smfmac_f32_16x16x64_bf16 v[236:239], v[196:199], %[w6], %[ix]\n\t"
                            "v_add_f32 v220, v220, v222\n\t"
                            "v_smfmac_f32_16x16x64_bf16 v[232:235], v[196:199], %[w5], %[ix]\n\t"
                            "v_add_f32 v221, v221, v223\n\t"
                            "v_smfmac_f32_16x16x64_bf16 v[240:243], v[196:199], %[w7], %[ix]\n\t"
                            "v_cvt_pk_bf16_f32 v200, v212, v213\n\t"
                            "v_smfmac_f32_16x16x64_bf16 v[244:247], v[196:199], %[w8], %[ix]\n\t"
                            "v_cvt_pk_bf16_f32 v201, v220, v221\n\t"
                            "v_smfmac_f32_16x16x64_bf16 v[248:251], v[196:199], %[w9], %[ix]\n\t"
                            "v_add_f32 v216, v216, v218\n\t"
                            "v_add_f32 v217, v217, v219\n\t"
                            "v_add_f32 v224, v224, v226\n\t"
                            "v_add_f32 v225, v225, v227\n\t"
                            "v_cvt_pk_bf16_f32 v202, v216, v217\n\t"
                            "v_cvt_pk_bf16_f32 v203, v224, v225\n\t"
                            "v_permlane32_swap_b32 v200, v201\n\t"
                            "v_pk_add_f32 v[228:229], v[228:229], v[230:231]\n\t"
                            "v_permlane32_swap_b32 v202, v203\n\t"
                            "v_min_u32 v201, -2, v201\n\t"
                            "v_pk_add_f32 v[236:237], v[236:237], v[238:239]\n\t"
                            "v_min_u32 v203, -2, v203\n\t"
                            "v_permlane16_swap_b32 v200, v202\n\t"
                            "v_pk_add_f32 v[232:233], v[232:233], v[234:235]\n\t"
                            "v_permlane16_swap_b32 v201, v203\n\t"
                            "v_pk_add_f32 v[240:241], v[240:241], v[242:243]\n\t"
                            MTA(5)
                            "buffer_store_dwordx4 v[200:203], %[z0], %[rw], %[zs] offen sc1\n\t"
                            "v_cvt_pk_bf16_f32 v204, v228, v229\n\t"
                            "v_cvt_pk_bf16_f32 v205, v236, v237\n\t"
                            "v_cvt_pk_bf16_f32 v206, v232, v233\n\t"
                            "v_cvt_pk_bf16_f32 v207, v240, v241\n\t"
                            "v_permlane32_swap_b32 v204, v205\n\t"
                            "v_pk_add_f32 v[244:245], v[244:245], v[246:247]\n\t"
                            "v_permlane32_swap_b32 v206, v207\n\t"
                            "v_min_u32 v205, -2, v205\n\t"
                            "v_pk_add_f32 v[248:249], v[248:249], v[250:251]\n\t"
                            "v_min_u32 v207, -2, v207\n\t"
                            "v_permlane16_swap_b32 v204, v206\n\t"
                            "v_cvt_pk_bf16_f32 v208, v244, v245\n\t"
                            "v_permlane16_swap_b32 v205, v207\n\t"
                            "v_cvt_pk_bf16_f32 v209, v248, v249\n\t"
                            "buffer_store_dwordx4 v[204:207], %[z1], %[rw], %[zs] offen sc1\n\t"
                            "s_nop 0\n\t"
                            "v_permlane32_swap_b32 v208, v209\n\t"
                            "v_min_u32 v209, -2, v209\n\t"
                            "buffer_store_dwordx2 v[208:209], %[z2], %[rw], %[zs] offen sc1\n\t"
                            : [vd] "=&s"(vd), [zs] "+s"(zs), "=&{v[200:203]}"(za), "=&{v[204:207]}"(zc), "=&{v[208:209]}"(ze), "+{v[212:227]}"(ca), "+{v[228:243]}"(cb), "+{v[244:251]}"(cc), "+v"(sf)
                            : [ix] "v"(sidx), [it] "s"(it), [rw] "s"(rwin), [z0] "v"(zo0), [z1] "v"(zo1), [z2] "v"(zo2), "{v[196:199]}"(af1), "{v210}"(vv),
                              [w0] "v"(WO(1, 0)), [w1] "v"(WO(1, 1)), [w2] "v"(WO(1, 2)), [w3] "v"(WO(1, 3)), [w4] "v"(WO(1, 4)), [w5] "v"(WO(1, 5)), [w6] "v"(WO(1, 6)), [w7] "v"(WO(1, 7)), [w8] "v"(WO(1, 8)), [w9] "v"(WO(1, 9))
                            : "scc", "memory");
                        redo = vd == it;
                        RT(4);
                        MT(6);
                        tref = __builtin_amdgcn_s_memtime();
                        __builtin_amdgcn_s_setprio(TKP);
                        asm volatile("s_waitcnt vmcnt(3)" : "+v"(sf) :: "memory");
                    }
                } while (__builtin_expect(redo, 0));
                if (s < 8) SST(8 + s);
                if (C == 0) {
                    if (s > 0 && s + 1 < NSEG) {
                        __builtin_amdgcn_s_sleep(DZR);
                        const unsigned zq = zb == W_ZM ? W_ZM + 2u * NOWN * PAIR : zb - NOWN * PAIR;
                        unsigned s1;
                        asm volatile("v_mov_b32 %0, -1" : "=v"(s1));
                        st128(((u32x4){s1, s1, s1, s1}), rwin, zo0, zq, 16);
                        st128(((u32x4){s1, s1, s1, s1}), rwin, zo1, zq, 16);
                        __builtin_amdgcn_raw_buffer_store_b64((u32x2){s1, s1}, rwin, zo2, zq, 16);
                    }
                } else {
                    if (s > TAILS) __builtin_amdgcn_raw_buffer_store_b32((unsigned)s - 1u, rwin, 0u, W_FZ2 + fo + (unsigned)(hh * 128 + (w & 3) * 4), 16);
                    if (TK && __builtin_expect(s == 0, 0)) { wait_h(rwin, fo, 5, hrdy); clk_init(ck); }
                    if (TK && s + 1 < NSEG) sched2(tk, s, sf);
                    if (!TK) wait_until(tref + DZ2);
                    if (s + 2 < NSEG) put_z2(s);
                }
                if (budget <= 0) set_tmo(rwin);
                zb = zb == W_ZM + 2u * NOWN * PAIR ? W_ZM : zb + NOWN * PAIR;
                RT(5);
                RTS();
                MT(7);
                MTS();
            }
        };
        const int tid = mytid();
        gather_mem0(memL, tid);
        if (tid < 8) *(unsigned*)(smem + L_S + tid * 4) = 0u;
        if (w < 4) steps(IC<0>()); else if (w < 7) steps(IC<1>()); else steps(IC<3>());
    };
    auto vt_role = [&](const u32x8 (&wt)[2][10], int hh, unsigned long long mv) INL {
        constexpr bool vt = true;
        const int jl = 63 - __builtin_clzll(mv);
        auto nextv = [&](int j) { return min(seg_after(mv, j), jl); };
        const int jv1 = nextv(-1), jv2 = nextv(jv1);
        if (w == 0) *(unsigned*)(smem + L_S + (fresh_lane() & 7u) * 4) = 0u;
        stage2(jv1, jv2, true);
        {
            LANES();
            const int ni = __builtin_popcountll(mv) + 1;
            if (w < 4) {
                const LAS char* c0 = (const LAS char*)(hL + hoff + ((fq ^ (fr & 7)) << 4));
                const LAS char* c1 = (const LAS char*)(hL + hoff + (((4 + fq) ^ (fr & 7)) << 4));
                const LAS char* n0 = c0 + 40960; const LAS char* n1 = c1 + 40960;
                u32x4 bb[8];
                project0(bb, c0, c1);
                for (int i = 1; i < ni; ++i) {
                    if (vt) MT(0);
                    f32x4 tc[2][2];
                    project<0>(wt, tc, bb, c0, c1, n0, n1,
                        [&]() {
                            MT(1);
                            if (i == 1) SST(16);
                            asm volatile("s_waitcnt vmcnt(0) lgkmcnt(0)\n\ts_barrier" ::: "memory");
                            if (i == 1) SST(17);
                            MT(2);
                        }, [](auto) {});
                    { const LAS char* x = c0; c0 = n0; n0 = x; x = c1; c1 = n1; n1 = x; }
                    char* tb = tokL + (i & 1) * 32 * QS + (16 * w + 4 * fq) * 2;
#pragma unroll
                    for (int g = 0; g < 2; ++g)
#pragma unroll
                        for (int tt = 0; tt < 2; ++tt) *(u32x2*)(tb + (prow + 8 * tt) * QS + g * 128) = (u32x2){pack2(tc[g][tt][0], tc[g][tt][1]), pack2(tc[g][tt][2], tc[g][tt][3])};
                    asm volatile("" ::: "memory");
                    *(volatile LAS int*)(smem + L_S + 16 + w * 4) = i;
                    if (i == 1) SST(18);
                    if (__builtin_expect(i == 1, 0)) __builtin_amdgcn_s_sleep(VSL);
                    if (vt) { MT(3); const int s = i & 63; MTRT(); MTS(); }
                }
                project_end(bb);
            } else {
                __builtin_amdgcn_s_setprio(OPR);
                const unsigned hv = lane * 16u, fz_ = W_FZ + fo + fz_at(hh, w - 4) + (jv1 ? 4096u : 0u);
                unsigned fp = ~0u;
                unsigned fzp = 0x40000000u;
                int jo = jv1, jh = jv2;
                for (int i = 1; i < ni; ++i, jo = nextv(jo)) {
                    const int b = i + (i >= ZLAG);
                    if (__builtin_expect(i == 1 || i == ZLAG, 0)) {
                        VMWAIT();
                        if (i == ZLAG) {
                            jh = nextv(jh);
                            wait_h(rwin, fo, jh, hrdy);
                            BAR0();
                            for (int t = 0; t < 10; ++t) dma1k(rws, hv, hseg(jh) + (unsigned)((w - 4) * 1024 + t * 4096), (unsigned)(L_H + ((i + 1) & 1) * 40960 + (w - 4) * 1024 + t * 4096));
                            VMWAIT();
                        }
                    }
                    jh = nextv(jh);
                    if (b < ni) BAR0();
                    if (__builtin_expect(jo + layer == 0 && w == 7, 0)) near_init();
                    wait_h(rwin, fo, jh, hrdy);
                    if (i == 1) SST(19);
                    if (vt) MT(1);
                    for (;;) {
                        const u32x4 f = *(volatile LAS u32x4*)(smem + L_S + 16);
                        if (__builtin_amdgcn_readfirstlane(min(min(f[0], f[1]), min(f[2], f[3]))) >= (unsigned)i) break;
                        __builtin_amdgcn_s_sleep(1);
                    }
                    asm volatile("" ::: "memory");
                    if (i == 1) SST(20);
                    if (__builtin_expect(jo == 0 && w == 7, 0)) __builtin_amdgcn_raw_buffer_store_b32((4u * (unsigned)__builtin_amdgcn_s_memrealtime() + (layer ? DVT : DVT0)) & 0xFFFFFFu, rwin, two(), W_T + (unsigned)hh * 128u + 4u, 16);
                    if (vt) MT(0);
                    u32x4 vf[2][4];
#pragma unroll
                    for (int t = 0; t < 2; ++t)
#pragma unroll
                        for (int ks = 0; ks < 4; ++ks) vf[t][ks] = *(const u32x4*)(tokL + (i & 1) * 32 * QS + (16 * t + fr) * QS + ks * 64 + fq * 16);
                    const unsigned ztb = O_Z + (unsigned)((jo - layer) * (int)ZTSEG) * 2u + (unsigned)(10 * (w - 4) * H_ + hh) * 1024u;
                    f32x4 c[2][2];
                    u32x4 r;
                    unsigned hs = hseg(jh) + (unsigned)((w - 4) * 1024), hd = (unsigned)(L_H + ((b + 1) & 1) * 40960 + (w - 4) * 1024);
                    asm volatile("" : "+s"(hs), "+s"(hd));
#pragma unroll
                    for (int nt = 0; nt < 11; ++nt) {
                        auto& cc = c[nt & 1]; auto& cp = c[(nt + 1) & 1];
#pragma unroll
                        for (int ks = 0; ks < 4; ++ks) {
                            if (nt < 10) { cc[0] = mfma32(vf[0][ks], half8(WO(ks >> 1, nt < 10 ? nt : 0), ks & 1), ks ? cc[0] : z4); PIN1(cc[0]); }
                            if (nt > 0 && ks >= 1 && ks < 3) { r[2 * ks - 2] = pack2(cp[ks - 1][0], cp[ks - 1][1]); PIN1(r); }
                            if (nt < 10) { cc[1] = mfma32(vf[1][ks], half8(WO(ks >> 1, nt < 10 ? nt : 0), ks & 1), ks ? cc[1] : z4); PIN1(cc[1]); }
                            if (nt > 0 && ks >= 1 && ks < 3) { r[2 * ks - 1] = nosent1(pack2(cp[ks - 1][2], cp[ks - 1][3])); PIN1(r); }
                            if (nt > 0 && ks == 2) st128(r, rws, lane * 16u, ztb + (unsigned)((nt - 1) * H_) * 1024u, 16);
                            if (nt < 5 && !(ks & 1)) dma1k(rws, hv, hs + (2 * nt + (ks >> 1)) * 4096u, hd + (2 * nt + (ks >> 1)) * 4096u);
                            if (nt == 5 && ks == 0) {
                                asm volatile("s_waitcnt vmcnt(14)" ::: "memory");
                                __builtin_amdgcn_raw_buffer_store_b32(fp, rwin, 0u, fzp, 16);
                            }
                            SB();
                        }
                    }
                    if (vt) MT(2);
                    if (i == 1) SST(21);
                    fp = (unsigned)jo;
                    fzp = fz_;
                    if (__builtin_expect(jo <= ZEARLY || i + 1 == ni, 0)) {
                        VMWAIT();
                        __builtin_amdgcn_raw_buffer_store_b32(fp, rwin, 0u, fz_, 16);
                    } else asm volatile("s_waitcnt vmcnt(7)" ::: "memory");
                    if (jo < 8) SST(8 + jo);
                    if (vt && (!BSPARSE || jo % 6 == 2)) { const int s = jo; RT(0); RTS(); }
                    if (vt) { MT(3); const int s = i & 63; MTRT(); MTS(); }
                }
            }
            __builtin_amdgcn_s_setprio(0);
            VMWAIT();
            BAR();
        }
    };
    auto qt_role = [&](const u32x8 (&wt)[2][10], int hh, auto qq) INL {
        constexpr bool qt = decltype(qq)::v;
        const unsigned long long mq = bid < NQT ? MXQ : ~MXQ;
        const int jl = 63 - __builtin_clzll(mq);
        auto nextq = [&](int j) { return min(seg_after(mq, j), jl); };
        const int jq0 = qt ? nextq(0) : 0, jq1 = qt ? nextq(jq0) : 0;
        if (qt && jq0 + layer == 1 && w == 7) near_init();
        stage2(jq0, jq1, !qt);
        if (qt && jq0 == 1 && w == 7) __builtin_amdgcn_raw_buffer_store_b32((4u * (unsigned)__builtin_amdgcn_s_memrealtime() + (layer ? DQA : DQA0)) & 0xFFFFFFu, rwin, two(), W_T + (unsigned)hh * 128u + 8u, 16);
        auto check2 = [&]() INL { VMWAIT(); check_h<5>(rws, rwin, hseg(jq1) + w * 1024u, L_H + 40960u + w * 1024u, 8192u); };
        {
            LANES();
            const unsigned to0 = (unsigned)((w ^ 4) >> 2) * 8192u + (unsigned)((w & 3) * 64 + (lane ^ ((fq & 1) * (w < 4 ? 8 : 5)))) * 16u, to1 = to0 + 4096u, hv = lane * 16u;
            const LAS char* c0 = (const LAS char*)(hL + hoff + ((fq ^ (fr & 7)) << 4));
            const LAS char* c1 = (const LAS char*)(hL + hoff + (((4 + fq) ^ (fr & 7)) << 4));
            u32x4 bb[8], g0 = SENT, g1 = SENT;
            f32x4 tc[2][2];
            auto pack = [&]() {
                g0 = (u32x4){pack2(tc[0][0][0], tc[0][0][1]), pack2(tc[0][0][2], tc[0][0][3]), pack2(tc[1][0][0], tc[1][0][1]), pack2(tc[1][0][2], tc[1][0][3])};
                g1 = (u32x4){pack2(tc[0][1][0], tc[0][1][1]), pack2(tc[0][1][2], tc[0][1][3]), pack2(tc[1][1][0], tc[1][1][1]), pack2(tc[1][1][2], tc[1][1][3])};
            };
            const LAS char* n0 = c0 + 40960; const LAS char* n1 = c1 + 40960;
            if (!qt) {
                project0(bb, c0, c1);
                project<0>(wt, tc, bb, c0, c1, c0, c1, []() {}, [](auto) {});
                pack();
                *(u32x4*)(tokL + to0) = g0; *(u32x4*)(tokL + to1) = g1;
                project_end(bb);
            } else {
                project0(bb, c0, c1);
                unsigned hs = hseg(jq1) + (unsigned)(w * 1024), hd = (unsigned)(L_H + 40960 + w * 1024), fv = lane == 0 ? 0u : 0x40000000u, fj = 0;
                const unsigned fq_ = W_FQ + fo + (unsigned)(hh * 128 + (jq0 > 1 ? 4096 : 0) + w * 4);
                for (int j = jq0, jn = jq1;; j = jn, jn = nextq(jn)) {
                    MT(0);
                    project<0>(wt, tc, bb, c0, c1, n0, n1,
                        [&]() {
                            if (__builtin_expect(j == jq0, 0)) check2();
                            wait_h(rwin, fo, nextq(jn), hrdy);
                            hs = hseg(nextq(jn)) + (unsigned)(w * 1024); hd = (unsigned)(2 * L_H + 40960 + 2 * w * 1024) - hd;
                            asm volatile("" : "+s"(hs), "+s"(hd));
                            MT(1);
                            VMWAIT();
                            BAR();
                            MT(2);
                        },
                        [&](auto uu) {
                            constexpr int u = decltype(uu)::v;
                            if (u == 34 || u == 38 || u == 2 || u == 6 || u == 10) { constexpr int t = ((u + 6) % 40) / 4; dma1k(rws, hv, hs + t * 8192u, hd + t * 8192u); }
                            if (u == UFQ) { asm volatile("s_waitcnt vmcnt(3)" ::: "memory"); __builtin_amdgcn_raw_buffer_store_b32(fj, rwin, fv, fq_, 16); asm volatile("" ::: "memory"); }
                        });
                    pack();
                    const unsigned qs = O_QK + (unsigned)(j * H_ + hh) * 16384u;
                    st128(g0, rws, to0, qs, 16);
                    st128(g1, rws, to1, qs, 16);
                    { const LAS char* x = c0; c0 = n0; n0 = x; x = c1; c1 = n1; n1 = x; }
                    fj = (unsigned)j;
                    PIN1(fj);
                    if (__builtin_expect(j <= QEARLY || j == jl, 0)) {
                        VMWAIT();
                        __builtin_amdgcn_raw_buffer_store_b32(fj, rwin, fv, fq_, 16);
                        if (j == jl) break;
                    }
                    MT(3);
                    if (j < 8) SST(8 + j);
                    if (!BSPARSE || j % 6 == 2) { const int s = j; RT(0); RTS(); MTRT(); MTS(); }
                }
                project_end(bb);
                VMWAIT();
                BAR0();
            }
        }
        SST(4);
    };
    auto qk_role = [&](const u32x8 (&wt)[2][10]) INL {
        const int hh = bid;
        int qrdy = 0;
        auto steps = [&](auto cc) INL {
        constexpr int C = decltype(cc)::v >> 1, R = decltype(cc)::v & 1;
        LANES();
        const LAS char* ka = (const LAS char*)(smem + L_K + (fr & 1) * TQS + (4 * fq + (fr >> 2)) * 8);
        const LAS char* qa = (const LAS char*)(smem + L_Q + (fr & 1) * 544 + ((fr >> 1) & 1) * 1152 + (16 * fq + 2 * (fr >> 2)) * 4);
        LAS char* qw = (LAS char*)(C < 2 ? smem + L_Q + (fq & 1) * 544 + (fq >> 1) * 1152 + (32 * w + 8 * (fr >> 2) + bs2(fr & 3)) * 4
                                         : smem + L_K + (fq >> 1) * TQS + (32 * (w & 3) + fr) * 8 + (fq & 1) * 4);
        const LAS char* mp0 = (const LAS char*)(memL + ma0);
        const int z = 16 * (fr >> 3) + 4 * (fr & 3) + 2 * ((fr >> 2) & 1);
        const int ko = ((z >> 4) * 256 + fq * 16 + ((z & 15) ^ ((fq & 1) * 5))) * 16;
        unsigned pa0 = 2u * (hh >> 3) * PAIR + (unsigned)(C * 4096 + (hh & 7) * 512 + R * 256 + lane * 4);
        unsigned pa2 = ((8u + (fr >> 3)) * PAIR + C * 4096u + (unsigned)((hh >> 1) * 256 + R * 128 + (hh & 1) * 64 + fq * 16 + (fr & 6) * 2)) | (fr & 1) << 30;
        PIN2(ka, qw); PIN1(mp0);
        constexpr bool TK = C == 2 && R == 1;
        Tk tk = {0u, 0u, 0x40000000u, nullptr};
        if (TK) { tk = tk_init(); __builtin_amdgcn_s_setprio(TKP); }
        if (C < 2) { PIN2(qa, pa0); PIN1(pa2); }
        int qf_ = 0;
        int tbo = 0;
        for (int s = 0; s < NSEG; ++s) {
            RT(0);
            const char* tb = tokL + tbo;
            unsigned pb = __builtin_amdgcn_readfirstlane(s + 1 < NSEG ? p_slot(s) + nh : 0x40000000u);
            asm volatile("" : "+s"(pb));
            bool redo = false;
            unsigned sf = 0;
            if (s <= 1) BAR();
            u32x8 kx[2][2];
            if (C < 2) {
#pragma unroll
                for (int u = 0; u < 4; ++u) kx[u >> 1][u & 1] = cat8(*(const u32x4*)(tb + (ko ^ ((u >> 1) * 16)) + (u & 1) * 2048), *(const u32x4*)(tb + (ko ^ ((u >> 1) * 16)) + (u & 1) * 2048 + 1024));
                PIN2(kx[0][0], kx[0][1]); PIN2(kx[1][0], kx[1][1]);
            }
            do {
                ++it;
                f32x4 a0 = zero4(), a1 = zero4();
                PIN2(a0, a1);
                u32x4 m[10];
                if (C < 2) {
                    __builtin_amdgcn_s_setprio(2);
                    asm volatile(ALN ::: "memory");
                    BAR();
                    MT(0);
#pragma unroll
                    for (int t = 0; t < NB1; ++t) { m[t] = *(const LAS u32x4*)(mp0 + t * 1024); SB(); }
                    BAR0();
#pragma unroll
                    for (int t = 0; t < 10; ++t) {
                        if (t == QB2) { BAR0(); MT(1); }
                        if (t + NB1 < 10) m[t + NB1] = *(const LAS u32x4*)(mp0 + (t + NB1) * 1024);
                        SB();
                        a0 = smfmac(m[t], wt[0][t], a0, sidx); a1 = smfmac(m[t], wt[1][t], a1, sidx);
                        PIN2(a0, a1);
                    }
                } else {
                    unsigned mx = 0;
                    asm volatile("" : "+{v[200:203]}"(a0), "+{v[204:207]}"(a1), "+{v208}"(mx));
                    unsigned lim = s > 0 ? poll_lim(budget) : ~0u, sr = tro + (unsigned)(s & 3) * 2u * PAIR;
                    asm volatile("" : "+s"(lim), "+s"(sr));
                    if (TK) fetch(tk, s, !redo, lim, DQC, (unsigned)hh * 128u, true);
                    else asm volatile(ALN "s_waitcnt lgkmcnt(0)\n\ts_barrier\n\ts_barrier\n\ts_barrier" ::: "memory");
                    asm volatile(
                        "ds_read_b128 v[212:215], %[mp] offset:0\n\t"
                        "ds_read_b128 v[216:219], %[mp] offset:1024\n\t"
                        "ds_read_b128 v[220:223], %[mp] offset:2048\n\t"
                        "ds_read_b128 v[224:227], %[mp] offset:3072\n\t"
                        "ds_read_b128 v[228:231], %[mp] offset:4096\n\t"
                        "ds_read_b128 v[232:235], %[mp] offset:5120\n\t"
                        "ds_read_b128 v[236:239], %[mp] offset:6144\n\t"
                        "ds_read_b128 v[240:243], %[mp] offset:7168\n\t"
                        "ds_read_b128 v[244:247], %[mp] offset:8192\n\t"
                        "ds_read_b128 v[248:251], %[mp] offset:9216\n\t"
                        MTA(4)
                        "s_waitcnt lgkmcnt(9)\n\t"
                        "v_smfmac_f32_16x16x64_bf16 v[200:203], v[212:215], %[w00], %[ix]\n\t"
                        "v_max3_u32 v208, v208, v212, v213\n\t"
                        "v_smfmac_f32_16x16x64_bf16 v[204:207], v[212:215], %[w10], %[ix]\n\t"
                        "s_waitcnt lgkmcnt(8)\n\t"
                        "v_max3_u32 v208, v208, v214, v215\n\t"
                        "v_smfmac_f32_16x16x64_bf16 v[200:203], v[216:219], %[w01], %[ix]\n\t"
                        "v_max3_u32 v208, v208, v216, v217\n\t"
                        "v_smfmac_f32_16x16x64_bf16 v[204:207], v[216:219], %[w11], %[ix]\n\t"
                        "s_waitcnt lgkmcnt(7)\n\t"
                        "v_max3_u32 v208, v208, v218, v219\n\t"
                        "v_smfmac_f32_16x16x64_bf16 v[200:203], v[220:223], %[w02], %[ix]\n\t"
                        "v_max3_u32 v208, v208, v220, v221\n\t"
                        "v_smfmac_f32_16x16x64_bf16 v[204:207], v[220:223], %[w12], %[ix]\n\t"
                        "s_waitcnt lgkmcnt(6)\n\t"
                        "v_max3_u32 v208, v208, v222, v223\n\t"
                        "v_smfmac_f32_16x16x64_bf16 v[200:203], v[224:227], %[w03], %[ix]\n\t"
                        "v_max3_u32 v208, v208, v224, v225\n\t"
                        "v_smfmac_f32_16x16x64_bf16 v[204:207], v[224:227], %[w13], %[ix]\n\t"
                        "s_waitcnt lgkmcnt(0)\n\t"
                        "v_max3_u32 v208, v208, v226, v227\n\t"
                        "v_smfmac_f32_16x16x64_bf16 v[200:203], v[228:231], %[w04], %[ix]\n\t"
                        "v_max3_u32 v208, v208, v228, v229\n\t"
                        "v_smfmac_f32_16x16x64_bf16 v[204:207], v[228:231], %[w14], %[ix]\n\t"
                        "v_max3_u32 v208, v208, v230, v231\n\t"
                        "v_smfmac_f32_16x16x64_bf16 v[200:203], v[232:235], %[w05], %[ix]\n\t"
                        "v_max3_u32 v208, v208, v232, v233\n\t"
                        "v_smfmac_f32_16x16x64_bf16 v[204:207], v[232:235], %[w15], %[ix]\n\t"
                        "v_max3_u32 v208, v208, v234, v235\n\t"
                        "v_smfmac_f32_16x16x64_bf16 v[200:203], v[236:239], %[w06], %[ix]\n\t"
                        "v_max3_u32 v208, v208, v236, v237\n\t"
                        "v_smfmac_f32_16x16x64_bf16 v[204:207], v[236:239], %[w16], %[ix]\n\t"
                        "v_max3_u32 v208, v208, v238, v239\n\t"
                        "v_smfmac_f32_16x16x64_bf16 v[200:203], v[240:243], %[w07], %[ix]\n\t"
                        "v_max3_u32 v208, v208, v240, v241\n\t"
                        "v_smfmac_f32_16x16x64_bf16 v[204:207], v[240:243], %[w17], %[ix]\n\t"
                        "v_max3_u32 v208, v208, v242, v243\n\t"
                        "v_smfmac_f32_16x16x64_bf16 v[200:203], v[244:247], %[w08], %[ix]\n\t"
                        "v_max3_u32 v208, v208, v244, v245\n\t"
                        "v_smfmac_f32_16x16x64_bf16 v[204:207], v[244:247], %[w18], %[ix]\n\t"
                        "v_max3_u32 v208, v208, v246, v247\n\t"
                        "v_smfmac_f32_16x16x64_bf16 v[200:203], v[248:251], %[w09], %[ix]\n\t"
                        "v_max3_u32 v208, v208, v248, v249\n\t"
                        "v_smfmac_f32_16x16x64_bf16 v[204:207], v[248:251], %[w19], %[ix]\n\t"
                        "v_max3_u32 v208, v208, v250, v251\n\t"
                        "buffer_load_dword %[sf], %[sl], %[rw], %[sr] offen sc1\n\t"
                        "v_cmp_lt_u32 vcc, %[lim], v208\n\t"
                        "s_nop 3\n\t"
                        "v_pk_add_f32 v[200:201], v[200:201], v[202:203]\n\t"
                        "v_pk_add_f32 v[204:205], v[204:205], v[206:207]\n\t"
                        "v_cvt_pk_bf16_f32 v200, v200, v201\n\t"
                        "v_cvt_pk_bf16_f32 v204, v204, v205\n\t"
                        "ds_write2_b32 %[qw], v200, v204 offset1:32\n\t"
                        MTA(2)
                        "s_cbranch_vccz 1f\n\t"
                        "v_mov_b32 v208, %[it]\n\t"
                        "v_mov_b32 v209, 0\n\t"
                        "ds_write_b32 v209, v208 offset:%[ls]\n\t"
                        "1:\n\t"
                        "s_waitcnt vmcnt(%[nv]) lgkmcnt(0)\n\t"
                        "s_barrier\n\t"
                        "s_waitcnt vmcnt(0)\n\t"
                        : [sf] "=&v"(sf), "+v"(qf_), "+{v[200:203]}"(a0), "+{v[204:207]}"(a1), "+{v208}"(mx)
                        : [mp] "v"(mp0), [qw] "v"(qw), [ix] "v"(sidx), [lim] "s"(lim), [it] "s"(it), [ls] "n"(L_S), [sl] "v"(tk.sl), [sr] "s"(sr), [rw] "s"(rwin), [nv] "n"(TK ? 2 : 1),
                          [w00] "v"(wt[0][0]), [w01] "v"(wt[0][1]), [w02] "v"(wt[0][2]), [w03] "v"(wt[0][3]), [w04] "v"(wt[0][4]), [w05] "v"(wt[0][5]), [w06] "v"(wt[0][6]), [w07] "v"(wt[0][7]), [w08] "v"(wt[0][8]), [w09] "v"(wt[0][9]), [w10] "v"(wt[1][0]), [w11] "v"(wt[1][1]), [w12] "v"(wt[1][2]), [w13] "v"(wt[1][3]), [w14] "v"(wt[1][4]), [w15] "v"(wt[1][5]), [w16] "v"(wt[1][6]), [w17] "v"(wt[1][7]), [w18] "v"(wt[1][8]), [w19] "v"(wt[1][9])
                        : "v209", "v212", "v213", "v214", "v215", "v216", "v217", "v218", "v219", "v220", "v221", "v222", "v223", "v224", "v225", "v226", "v227", "v228", "v229", "v230", "v231", "v232", "v233", "v234", "v235", "v236", "v237", "v238", "v239", "v240", "v241", "v242", "v243", "v244", "v245", "v246", "v247", "v248", "v249", "v250", "v251", "vcc", "memory");
                }
                if (C < 2) {
                    *(LAS unsigned*)qw = hpack(a0); *(LAS unsigned*)(qw + 16) = hpack(a1);
                    SB();
                    MT(2);
                    unsigned vd;
#define SC0 "v[212:215]"
#define SC1 "v[216:219]"
#define SC2 "v[220:223]"
#define DPP(op, d, ctl) op " " d ", " d ", " d " " ctl " row_mask:0xf bank_mask:0xf\n\t"
#define TAIL(A0, A2, B0, B2, C0, C2) \
                        "v_mov_b64 v[212:213], 0\n\tv_mov_b64 v[214:215], 0\n\tv_mov_b64 v[216:217], 0\n\tv_mov_b64 v[218:219], 0\n\tv_mov_b64 v[220:221], 0\n\tv_mov_b64 v[222:223], 0\n\t" \
                        "s_setprio 3\n\t" \
                        "s_waitcnt lgkmcnt(0)\n\t" \
                        "s_barrier\n\t" MTA(3) \
                        "ds_read_b64_tr_b16 v[224:225], %[qa]\n\t" \
                        "ds_read_b64_tr_b16 v[226:227], %[qa] offset:32\n\t" \
                        "ds_read_b64_tr_b16 v[228:229], %[qa] offset:256\n\t" \
                        "ds_read_b64_tr_b16 v[230:231], %[qa] offset:288\n\t" \
                        "ds_read_b64_tr_b16 v[232:233], %[ka]\n\t" \
                        "ds_read_b64_tr_b16 v[234:235], %[ka] offset:128\n\t" \
                        "ds_read_b64_tr_b16 v[236:237], %[ka] offset:256\n\t" \
                        "ds_read_b64_tr_b16 v[238:239], %[ka] offset:384\n\t" \
                        "ds_read_b64_tr_b16 v[240:241], %[ka] offset:512\n\t" \
                        "ds_read_b64_tr_b16 v[242:243], %[ka] offset:640\n\t" \
                        "s_waitcnt lgkmcnt(8)\n\t" \
                        "v_smfmac_f32_16x16x64_bf16 " SC0 ", v[224:227], %[k00], %[ix]\n\t" \
                        "ds_read_b64_tr_b16 v[244:245], %[ka] offset:768\n\t" \
                        "v_smfmac_f32_16x16x64_bf16 " SC1 ", v[224:227], %[k10], %[ix]\n\t" \
                        "ds_read_b64_tr_b16 v[246:247], %[ka] offset:896\n\t" \
                        "s_waitcnt lgkmcnt(8)\n\t" \
                        "v_smfmac_f32_16x16x64_bf16 " SC0 ", v[228:231], %[k01], %[ix]\n\t" \
                        "ds_read_b32 v248, %[vz] offset:%[ls]\n\t" \
                        "v_smfmac_f32_16x16x64_bf16 " SC1 ", v[228:231], %[k11], %[ix]\n\t" \
                        "s_waitcnt lgkmcnt(5)\n\t" \
                        "v_smfmac_f32_16x16x64_bf16 " SC2 ", v[224:227], v[232:239], %[ix]\n\t" \
                        "s_waitcnt lgkmcnt(1)\n\t" \
                        "v_smfmac_f32_16x16x64_bf16 " SC2 ", v[228:231], v[240:247], %[ix]\n\t" \
                        "s_waitcnt lgkmcnt(0)\n\t" \
                        MTA(4) "v_readfirstlane_b32 %[vd], v248\n\t" \
                        "v_add_f32 v232, " A0 ", " A2 "\n\t" \
                        "s_nop 0\n\t" \
                        "v_add_f32 v233, " B0 ", " B2 "\n\t" \
                        "v_max_f32 v235, v232, v233\n\t" \
                        "s_cmp_eq_u32 %[vd], %[it]\n\t" \
                        "s_cselect_b32 %[p0], 2.0, %[p0]\n\t" \
                        DPP("v_max_f32_dpp", "v235", "row_ror:8") \
                        "v_add_f32 v234, " C0 ", " C2 "\n\t" \
                        "v_max_f32 v235, v235, v234\n\t" \
                        "s_add_u32 %[p1], %[p0], 0x4000\n\t" \
                        "s_nop 0\n\t" DPP("v_max_f32_dpp", "v235", "row_ror:4") \
                        "s_nop 1\n\t" DPP("v_max_f32_dpp", "v235", "row_ror:2") \
                        "s_nop 1\n\t" DPP("v_max_f32_dpp", "v235", "row_ror:1") \
                        "v_mul_f32 v235, 0xbe0293ee, v235\n\t" \
                        "v_fmamk_f32 v232, v232, 0x3e0293ee, v235\n\t" \
                        "v_fmamk_f32 v233, v233, 0x3e0293ee, v235\n\t" \
                        "v_exp_f32 v232, v232\n\t" \
                        "v_exp_f32 v233, v233\n\t" \
                        "v_fmamk_f32 v234, v234, 0x3e0293ee, v235\n\t" \
                        "v_add_f32 v235, v232, v233\n\t" \
                        "v_exp_f32 v234, v234\n\t" \
                        "s_nop 0\n\t" DPP("v_add_f32_dpp", "v235", "row_ror:8") \
                        "v_add_f32 v235, v235, v234\n\t" \
                        "v_mov_b32_dpp v236, v234 quad_perm:[1,1,3,3] row_mask:0xf bank_mask:0xf\n\t" \
                        "s_nop 0\n\t" DPP("v_add_f32_dpp", "v235", "row_ror:4") \
                        "s_nop 1\n\t" DPP("v_add_f32_dpp", "v235", "row_ror:2") \
                        "s_nop 1\n\t" DPP("v_add_f32_dpp", "v235", "row_ror:1") \
                        "v_rcp_f32 v235, v235\n\t" \
                        "s_nop 0\n\t" \
                        "v_mul_f32_e64 v232, v232, v235 clamp\n\t" \
                        "v_mul_f32_e64 v233, v233, v235 clamp\n\t" \
                        "v_cvt_pk_bf16_f32 v232, v232, v233\n\t" \
                        MTA(5) "buffer_store_dword v232, %[pa], %[rw], %[p0] offen sc1\n\t" \
                        "v_mul_f32_e64 v234, v234, v235 clamp\n\t" \
                        "v_mul_f32_e64 v236, v236, v235 clamp\n\t" \
                        "buffer_store_dword v232, %[pa], %[rw], %[p1] offen sc1\n\t" \
                        "v_cvt_pk_bf16_f32 v234, v234, v236\n\t" \
                        "buffer_store_dword v234, %[pm], %[rw], %[p0] offen sc1\n\t" \
                        "s_setprio 0"
#define TAILIO : [vd] "=&s"(vd), [p0] "+s"(ps), [p1] "=&s"(ps1) \
                        : [qa] "v"(qa), [ka] "v"(ka), [vz] "v"(0u), [ls] "n"(L_S), [ix] "v"(sidx), [it] "s"(it), [pa] "v"(pa0), [pm] "v"(pa2), [rw] "s"(rwin), \
                          [k00] "v"(kx[0][0]), [k01] "v"(kx[0][1]), [k10] "v"(kx[1][0]), [k11] "v"(kx[1][1]) \
                        : "v212", "v213", "v214", "v215", "v216", "v217", "v218", "v219", "v220", "v221", "v222", "v223", "v224", "v225", "v226", "v227", "v228", "v229", "v230", "v231", "v232", \
                          "v233", "v234", "v235", "v236", "v237", "v238", "v239", "v240", "v241", "v242", "v243", "v244", "v245", "v246", "v247", "v248", "scc", "memory"
                    unsigned ps = pb, ps1;
                    if (R == 0) asm volatile(TAIL("v212", "v214", "v216", "v218", "v220", "v222") TAILIO);
                    else asm volatile(TAIL("v213", "v215", "v217", "v219", "v221", "v223") TAILIO);
                    redo = vd == it;
                    RT(4);
                    MT(6);
                } else {
                    MT(3);
                    if (TK) { __builtin_amdgcn_s_setprio(TKP); if (TKS) __builtin_amdgcn_s_sleep(TKS); }
                    redo = __builtin_amdgcn_readfirstlane(*(volatile LAS unsigned*)(smem + L_S)) == it;
                    if (redo) FC(fc0);
                }
            } while (__builtin_expect(redo, 0));
            if (s < 8) SST(8 + s);
            if (s >= 56) SST(s - 40);
            if (C + R == 1) {
                if (s + layer > 0) {
                    __builtin_amdgcn_s_sleep(DDMA);
                    unsigned l2 = fresh_lane(), s1;
                    asm volatile("v_mov_b32 %0, -1" : "=v"(s1));
                    const unsigned base = p_slot(s - 1) + nh;
                    asm volatile("s_waitcnt vmcnt(3)" ::: "memory");
                    st128(((u32x4){s1, s1, s1, s1}), rwin, (l2 >> 5) * PAIR + (l2 & 31u) * 16u, base + (unsigned)(2 * (hh >> 3)) * PAIR + (unsigned)(R * 4096 + (hh & 7) * 512), 16);
                    if (R) __builtin_amdgcn_raw_buffer_store_b64((u32x2){s1, s1}, rwin, (l2 >> 5) * PAIR + (l2 & 16u) * 256u + (l2 & 8u) * 16u + (l2 & 7u) * 8u, base + 8u * PAIR + (unsigned)((hh >> 1) * 256 + (hh & 1) * 64), 16);
                    else st128(((u32x4){s1, s1, s1, s1}), rwin, two(), W_T + (unsigned)((s + 2) & 3) * 2u * PAIR + (unsigned)hh * 128u, 16);
                }
            } else if (TK) {
                if (s + 1 < NSEG) sched2(tk, s, sf);
            } else if (C == 2 && w == 4) {
                if (s + 2 < NSEG) {
                    auto done = [&](int f) INL {
                        f = min(f, __builtin_amdgcn_update_dpp(f, f, 0xB1, 0xF, 0xF, false)); f = min(f, __builtin_amdgcn_update_dpp(f, f, 0x4E, 0xF, 0xF, false));
                        f = min(f, __builtin_amdgcn_update_dpp(f, f, 0x141, 0xF, 0xF, false));
                        return seg_done(MXQ, __builtin_amdgcn_readlane(f, 0), __builtin_amdgcn_readlane(f, 8));
                    };
                    qrdy = max(qrdy, done(qf_));
                    const int sq = s + 2;
                    if (__builtin_expect(s == 0, 0)) __builtin_amdgcn_s_sleep(DQ0);
                    if (__builtin_expect(qrdy < sq, 0)) for (unsigned spins = 0;;) {
                        asm volatile("" ::: "memory");
                        const int f = (int)__builtin_amdgcn_raw_buffer_load_b32(rwin, (unsigned)((lane & 8) * 512 + (lane & 7) * 4), W_FQ + fo + (unsigned)hh * 128u, 16);
                        qrdy = done(f);
                        if (qrdy >= sq) break;
                        FC(fc2);
                        if (spin_fail(spins, rwin)) break;
                        __builtin_amdgcn_s_sleep(QSL);
                    }
                    const unsigned so = O_QK + (unsigned)(((s + 2) * H_ + hh) * 16384), lo = (unsigned)(L_TOK + (tbo ? tbo - 16384 : 32768)), l2 = fresh_lane();
                    if (__builtin_expect(s == 0, 0)) {
                        for (int i = 0; i < 4; ++i) dma4k(rws, l2 * 16u, so - H_ * 16384u + i * 4096u, lo - 16384u + i * 4096u);
                    }
#pragma unroll
                    for (int i = 0; i < 4; ++i) dma4k(rws, l2 * 16u, so + i * 4096u, lo + i * 4096u);
                    if (__builtin_expect(s == 0, 0)) asm volatile("s_waitcnt vmcnt(16)" ::: "memory");
                    if (qrdy < min(s + 5, NSEG - 1))
                        asm volatile("buffer_load_dword %0, %1, %2, %3 offen sc1" : "+v"(qf_) : "v"((l2 & 8u) * 512u + (l2 & 7u) * 4u), "s"(rwin), "s"(W_FQ + fo + (unsigned)hh * 128u) : "memory");
                }
            } else {
                asm volatile("s_waitcnt vmcnt(3)" ::: "memory");
                if (s > TAILS) __builtin_amdgcn_raw_buffer_store_b32((unsigned)s - 1u, rwin, 0u, W_FPT + fo + (unsigned)(hh * 128 + (w & 3) * 4), 16);
                if (C == 2 && s + 1 < NSEG) __builtin_amdgcn_s_sleep(DPT);
                const float scale = 0.08838834764831845f, l2e = 1.44269504088896340736f * (NORND ? scale : 1.f);
                const int tq = (w >> 1) & 1, qh = w & 1;
                f32x4 sc[3] = {z4, z4, z4};
                u32x4 kt[2][4], qf[4], kf[4];
                const unsigned l2 = fresh_lane(), kto = ((l2 & 8u) * 512u + (l2 & 51u) * 16u + (l2 & 4u) * 32u) ^ ((l2 & 16u) * 5u), qto = ((l2 & 55u) * 16u + qh * 128u) ^ ((l2 & 16u) * 8u);
#pragma unroll
                for (int ks = 0; ks < 4; ++ks) {
                    qf[ks] = *(const u32x4*)(tb + 8192 + (tq * 4 + ks) * 1024 + qto);
                    kt[0][ks] = *(const u32x4*)(tb + ks * 1024 + kto);
                    kt[1][ks] = *(const u32x4*)(tb + ks * 1024 + (kto ^ 64u));
                    SB();
                }
#pragma unroll
                for (int ks = 0; ks < 4; ++ks) {
                    sc[0] = mfma32(kt[0][ks], qf[ks], sc[0]); PIN1(sc[0]);
                    kf[ks] = ldtr2(ka + ks * 256, ka + ks * 256 + 128);
                    SB();
                    sc[1] = mfma32(kt[1][ks], qf[ks], sc[1]); PIN1(sc[1]);
                }
#pragma unroll
                for (int ks = 0; ks < 4; ++ks) sc[2] = mfma32(kf[ks], qf[ks], sc[2]);
                const bool hi = (l2 & 8u) != 0u;
                float v[6];
#pragma unroll
                for (int r = 0; r < 4; ++r) v[r] = hi ? sc[1][r] : sc[0][r];
                v[4] = hi ? sc[2][2] : sc[2][0]; v[5] = hi ? sc[2][3] : sc[2][1];
#pragma unroll
                for (int r = 0; r < 6; r += 2) {
                    if (NORND) break;
                    const unsigned b0 = pack2(v[r], v[r + 1]);
                    const unsigned b1 = pack2(lo2f(b0) * scale, hi2f(b0) * scale);
                    v[r] = lo2f(b1); v[r + 1] = hi2f(b1);
                }
                const int lim = 8 * (w & 3) + (int)(l2 & 7u) - 8 * (int)(l2 >> 4) - (int)((l2 & 8u) >> 1);
#pragma unroll
                for (int r = 0; r < 4; ++r) if (r > lim) v[r] = NORND ? -1000000000.0f / scale : bfr(-1000000000.0f);
                float mx = fmx(fmx(fmx(fmx(fmx(v[0], v[1]), v[2]), v[3]), v[4]), v[5]);
                mx = xmax3216(fmx(mx, ror8f(mx)));
                const float mb = -mx * l2e;
#pragma unroll
                for (int r = 0; r < 6; ++r) v[r] = __builtin_amdgcn_exp2f(fmaf(v[r], l2e, mb));
                float sum = fmaf(v[4] + v[5], 0.5f, (v[0] + v[1]) + (v[2] + v[3]));
                sum = xsum3216(sum + ror8f(sum));
                const float inv = __builtin_amdgcn_rcpf(sum);
                const unsigned p0 = pack2(v[0] * inv, v[1] * inv), p1 = nosent1(pack2(v[2] * inv, v[3] * inv)), p2 = nosent1(pack2(v[4] * inv, v[5] * inv));
                const unsigned base = (s + 2 < NSEG ? O_PT : W_PL + fo - (NSEG - 2) * PTSEG * 2u) + (unsigned)(s * PTSEG) * 2u;
                const __amdgpu_buffer_rsrc_t rp = s + 2 < NSEG ? rws : rwin;
                const unsigned oob = (l2 & 8u) << 27;
                st128(((u32x4){p0, p1, dppu<0x128>(p0), dppu<0x128>(p1)}), rp, (l2 * 16u) | oob, base + (unsigned)((hh * 2 + tq) * 1024 + qh * 128), 16);
                __builtin_amdgcn_raw_buffer_store_b64((u32x2){p2, dppu<0x128>(p2)}, rp, ((l2 & 7u) * 16u + (l2 >> 4) * 8u) | oob | (l2 & 32u) << 25, base + 65536u + (unsigned)((tq * 32 + hh) * 256 + qh * 128), 16);
            }
            tbo = tbo == 32768 ? 0 : tbo + 16384;
            if (C == 2 && __builtin_expect(budget <= 0, 0)) set_tmo(rwin);
            RT(5);
            RTS();
            MT(7);
            MTS();
        }
        };
        if (w) gather_mem0(memL, mytid() - 64, 448);
        else *(unsigned*)(smem + L_S) = 0u;
        if (layer == 0) near_init();
        qt_role(wt, hh, IC<0>());
        if (w == 7) { ck.ta = 4u * (unsigned)__builtin_amdgcn_s_memrealtime() + 8u; ck.ma = (unsigned)__builtin_amdgcn_s_memtime(); }
        if (w == 0) steps(IC<0>()); else if (w == 1) steps(IC<1>()); else if (w == 2) steps(IC<2>()); else if (w == 3) steps(IC<3>()); else if (w < 7) steps(IC<4>()); else steps(IC<5>());
    };
    auto own_role = [&]() INL {
        const unsigned long long tstart = __builtin_amdgcn_s_memtime();
        if (layer == 0) near_init();
        LANES();
        const int o = bid - 96, hg = (w + ROT * o) & 3, ot = tile_of(o);
        const unsigned ztoff = (unsigned)((o * H_ + 8 * hg) * 64 + lane) * 16u;
        const unsigned zoff = zpair(o / 10, o % 10) + (unsigned)(lane * 16);
        const unsigned poff = (2u * hg + (mc & 1u)) * PAIR + nh + (mc >> 1) * 4096u + (unsigned)((fq >> 1) * 512 + (fr >> 2) * 128 + (fr & 1) * 64 + (fq & 1) * 32 + (fr & 2) * 8);
        const unsigned pmoff = (8u + (mc & 1u)) * PAIR + nh + (mc >> 1) * 4096u + (unsigned)(fq * 256 + (fr >> 3) * 128 + (fr & 2) * 32 + (fr & 4) * 8 + (fr & 1) * 16);
        const unsigned moff = (unsigned)(ot >> 4) * 2u * PAIR + nh + (unsigned)((ot & 15) * 256 + lane * 4);
        constexpr unsigned mc2 = 4096u;
        char* op = smem + 8192;
        if (w == 4 && lane < 2) *(volatile LAS int*)(smem + 4096 + lane * 4) = -1;
        BAR();
        SST(2);
        if (w == 7) clk_init(ck);
        wait_until(tstart + DO0);
        if (w < 4) {
            u32x8 zf[4];
            int zr = -1;
            unsigned zb = W_ZM + (unsigned)layer * (NOWN * PAIR), zso = O_Z - (unsigned)layer * (unsigned)(ZTSEG * 2);
            const unsigned ra = (unsigned)((ot >> 4) * 2 + (fq & 1)) * PAIR + nh + (unsigned)((fq >> 1) * 4096 + (ot & 15) * 256 + fr * 16);
            int np = 0;
            auto piece = [&]() INL {
                asm volatile("s_mov_b32 m0, %3\n\ts_nop 3\n\tbuffer_load_dwordx4 %0, %1, %2 offen sc1 lds\n\tbuffer_load_dwordx4 %0, %1, %2 offen offset:1024 sc1 lds"
                             :: "v"(ztoff), "s"(rws), "s"(O_Z + (unsigned)((NSEG - 1 - layer) * (int)ZTSEG) * 2u + np * 2048u), "s"(32768 + hg * 8192 + np * 2048) : "m0", "memory");
                ++np;
            };
            for (int s = 0; s + 1 < NSEG; ++s) {
                const unsigned pb = p_slot(s);
                const int sz = s;
                while (__builtin_expect(zr < sz, 0)) {
                    __builtin_amdgcn_s_sleep(1);
                    zr = __builtin_amdgcn_readfirstlane(*(volatile LAS int*)(smem + 4096));
                }
                if (s == 0) SST(1);
                asm volatile(
                    "1:\n\t"
                    "buffer_load_dwordx4 v[160:163], %[zt], %[rs], %[so] offen sc1\n\t"
                    "buffer_load_dwordx4 v[164:167], %[zt], %[rs], %[so] offen offset:1024 sc1\n\t"
                    "buffer_load_dwordx4 v[168:171], %[zt], %[rs], %[so] offen offset:2048 sc1\n\t"
                    "buffer_load_dwordx4 v[172:175], %[zt], %[rs], %[so] offen offset:3072 sc1\n\t"
                    "buffer_load_dwordx4 v[176:179], %[zt], %[rs], %[s4] offen sc1\n\t"
                    "buffer_load_dwordx4 v[180:183], %[zt], %[rs], %[s4] offen offset:1024 sc1\n\t"
                    "buffer_load_dwordx4 v[184:187], %[zt], %[rs], %[s4] offen offset:2048 sc1\n\t"
                    "buffer_load_dwordx4 v[188:191], %[zt], %[rs], %[s4] offen offset:3072 sc1\n\t"
                    "s_cmp_lg_u32 %[s], 0\n\t"
                    "s_cbranch_scc1 2f\n\t"
                    "s_waitcnt vmcnt(0)\n\t"
                    "v_max3_u32 v100, v161, v163, v165\n\t"
                    "v_max3_u32 v100, v100, v167, v169\n\t"
                    "v_max3_u32 v100, v100, v171, v173\n\t"
                    "v_max3_u32 v100, v100, v175, v177\n\t"
                    "v_max3_u32 v100, v100, v179, v181\n\t"
                    "v_max3_u32 v100, v100, v183, v185\n\t"
                    "v_max3_u32 v100, v100, v187, v189\n\t"
                    "v_max_u32 v100, v100, v191\n\t"
                    "v_cmp_eq_u32 vcc, -1, v100\n\t"
                    "s_sub_i32 %[tr], %[tr], 1\n\t"
                    "s_cbranch_vccz 2f\n\t"
                    "s_cmp_gt_i32 %[tr], 0\n\t"
                    "s_cbranch_scc1 1b\n\t"
                    "2:"
                    : "=&{v[160:167]}"(zf[0]), "=&{v[168:175]}"(zf[1]), "=&{v[176:183]}"(zf[2]), "=&{v[184:191]}"(zf[3]), [tr] "+s"(budget)
                    : [zt] "v"(ztoff), [rs] "s"(rws), [so] "s"(zso), [s4] "s"(zso + 4096u), [s] "s"(s) : "v100", "vcc", "scc", "memory");
                if (s == 0) SST(3);
                zso += (unsigned)(ZTSEG * 2);
                if (zr == NSEG - 1 && np < 4) piece();
                if (__builtin_expect(budget <= 0, 0)) set_tmo(rwin);
                RT(6);
                const unsigned spa = (unsigned)((s & 1) * 2048 + (w < 3 ? w * 512 : 0) + lane * 8);
#define OST(r)
                unsigned long long t0_, t1_, t2_, t3_, t4_ = 0, bad;
                int tries = budget;
                const unsigned sw = (unsigned)(((s & 3) + 1) & 4) * 1024u;
                unsigned po = poff ^ sw, pm = pmoff ^ sw, x0 = 4096u, x1 = 4096u;
                const unsigned mb = W_MEM + (unsigned)((s + 1) & 3) * 6u * PAIR;
                if (w < 3) asm volatile(
                    ALN
                    "s_barrier\n\t"
                    OST("%[t0]")
                    "v_mov_b64 v[96:97], 0\n\t"
                    "v_mov_b64 v[98:99], 0\n\t"
                    "v_mov_b32 v100, 0\n\t"
                    "s_barrier\n\t"
                    "1:\n\t"
                    "buffer_load_dwordx4 v[208:211], %[po], %[rw], %[pb] offen sc1\n\t"
                    "buffer_load_dwordx4 v[212:215], %[po], %[rw], %[pb] offen offset:1024 sc1\n\t"
                    "buffer_load_dwordx4 v[216:219], %[po], %[rw], %[pb] offen offset:2048 sc1\n\t"
                    "buffer_load_dwordx4 v[220:223], %[po], %[rw], %[pb] offen offset:3072 sc1\n\t"
                    OST("%[t1]")
                    "s_waitcnt vmcnt(3)\n\t"
                    OST("%[t3]")
                    "v_smfmac_f32_16x16x64_bf16 v[96:99], v[208:211], v[160:167], %[ix]\n\t"
                    "v_max3_u32 v100, v100, v208, v209\n\t"
                    "v_max3_u32 v100, v100, v210, v211\n\t"
                    "s_waitcnt vmcnt(2)\n\t"
                    "v_smfmac_f32_16x16x64_bf16 v[96:99], v[212:215], v[168:175], %[ix]\n\t"
                    "v_max3_u32 v100, v100, v212, v213\n\t"
                    "v_max3_u32 v100, v100, v214, v215\n\t"
                    "s_waitcnt vmcnt(1)\n\t"
                    "v_smfmac_f32_16x16x64_bf16 v[96:99], v[216:219], v[176:183], %[ix]\n\t"
                    "v_max3_u32 v100, v100, v216, v217\n\t"
                    "v_max3_u32 v100, v100, v218, v219\n\t"
                    "s_waitcnt vmcnt(0)\n\t"
                    "v_smfmac_f32_16x16x64_bf16 v[96:99], v[220:223], v[184:191], %[ix]\n\t"
                    "v_max3_u32 v100, v100, v220, v221\n\t"
                    "v_max3_u32 v100, v100, v222, v223\n\t"
                    "v_cmp_eq_u32 vcc, -1, v100\n\t"
                    OST("%[t2]")
                    "s_nop 4\n\t"
                    "v_pk_add_f32 v[96:97], v[96:97], v[98:99]\n\t"
                    "ds_write_b64 %[sp], v[96:97]\n\t"
                    "s_cbranch_vccz 2f\n\t"
                    "s_sub_i32 %[tr], %[tr], 1\n\t"
                    "s_cmp_lt_i32 %[tr], 1\n\t"
                    "s_cbranch_scc1 2f\n\t"
                    "v_xor_b32 %[po], %[x0], %[po]\n\t"
                    "s_xor_b32 %[x0], %[x0], 0x5000\n\t"
                    "s_waitcnt lgkmcnt(0)\n\t"
                    "v_mov_b64 v[96:97], 0\n\t"
                    "v_mov_b64 v[98:99], 0\n\t"
                    "v_mov_b32 v100, 0\n\t"
                    "s_branch 1b\n\t"
                    "2:\n\t"
                    "s_waitcnt lgkmcnt(0)\n\t"
                    "s_barrier\n\t"
                    "s_sleep 3\n\t"
                    : [tr] "+s"(tries), [po] "+v"(po), [x0] "+s"(x0), [t0] "=&s"(t0_), [t1] "=&s"(t1_), [t2] "=&s"(t2_), [t3] "=&s"(t3_)
                    : [sp] "v"(spa), [ix] "v"(sidx), [rw] "s"(rwin), [pb] "s"(pb), "{v[160:167]}"(zf[0]), "{v[168:175]}"(zf[1]), "{v[176:183]}"(zf[2]), "{v[184:191]}"(zf[3])
                    : "v96", "v97", "v98", "v99", "v100", "v208", "v209", "v210", "v211", "v212", "v213", "v214", "v215", "v216", "v217", "v218", "v219", "v220", "v221", "v222", "v223", "vcc", "scc", "memory");
                else asm volatile(
                    ALN
                    "s_barrier\n\t"
                    "buffer_load_dwordx4 v[128:131], %[zo], %[rw], %[zb] offen sc1\n\t"
                    "buffer_load_dwordx4 v[132:135], %[zo], %[rw], %[zb] offen offset:1024 sc1\n\t"
                    "buffer_load_dwordx4 v[136:139], %[zo], %[rw], %[zb] offen offset:2048 sc1\n\t"
                    "buffer_load_dwordx4 v[140:143], %[zo], %[rw], %[zb] offen offset:3072 sc1\n\t"
                    "buffer_load_dwordx4 v[144:147], %[zo], %[rw], %[z4] offen sc1\n\t"
                    "buffer_load_dwordx4 v[148:151], %[zo], %[rw], %[z4] offen offset:1024 sc1\n\t"
                    "buffer_load_dwordx4 v[152:155], %[zo], %[rw], %[z4] offen offset:2048 sc1\n\t"
                    "buffer_load_dwordx4 v[156:159], %[zo], %[rw], %[z4] offen offset:3072 sc1\n\t"
                    OST("%[t0]")
                    "v_mov_b64 v[96:97], 0\n\t"
                    "v_mov_b64 v[98:99], 0\n\t"
                    "v_mov_b32 v100, 0\n\t"
                    "s_barrier\n\t"
                    "s_sleep %[fd]\n\t"
                    "buffer_load_dwordx4 v[208:211], %[po], %[rw], %[pb] offen sc1\n\t"
                    "buffer_load_dwordx4 v[212:215], %[po], %[rw], %[pb] offen offset:1024 sc1\n\t"
                    "buffer_load_dwordx4 v[216:219], %[po], %[rw], %[pb] offen offset:2048 sc1\n\t"
                    "buffer_load_dwordx4 v[220:223], %[po], %[rw], %[pb] offen offset:3072 sc1\n\t"
                    "buffer_load_dwordx4 v[224:227], %[pm], %[rw], %[pb] offen sc1\n\t"
                    "buffer_load_dwordx4 v[228:231], %[pm], %[rw], %[pb] offen offset:1024 sc1\n\t"
                    "buffer_load_dwordx4 v[232:235], %[pm], %[rw], %[pb] offen offset:2048 sc1\n\t"
                    "buffer_load_dwordx4 v[236:239], %[pm], %[rw], %[pb] offen offset:3072 sc1\n\t"
                    OST("%[t1]")
                    "s_waitcnt vmcnt(8)\n\t"
                    "v_max3_u32 v101, v129, v131, v133\n\t"
                    "v_max3_u32 v101, v101, v135, v137\n\t"
                    "v_max3_u32 v101, v101, v139, v141\n\t"
                    "v_max3_u32 v101, v101, v143, v145\n\t"
                    "v_max3_u32 v101, v101, v147, v149\n\t"
                    "v_max3_u32 v101, v101, v151, v153\n\t"
                    "v_max3_u32 v101, v101, v155, v157\n\t"
                    "v_max_u32 v101, v101, v159\n\t"
                    "v_cmp_eq_u32 vcc, -1, v101\n\t"
                    "s_cbranch_vccz 3f\n\t"
                    "buffer_load_dwordx4 v[128:131], %[zo], %[rw], %[zb] offen sc1\n\t"
                    "buffer_load_dwordx4 v[132:135], %[zo], %[rw], %[zb] offen offset:1024 sc1\n\t"
                    "buffer_load_dwordx4 v[136:139], %[zo], %[rw], %[zb] offen offset:2048 sc1\n\t"
                    "buffer_load_dwordx4 v[140:143], %[zo], %[rw], %[zb] offen offset:3072 sc1\n\t"
                    "buffer_load_dwordx4 v[144:147], %[zo], %[rw], %[z4] offen sc1\n\t"
                    "buffer_load_dwordx4 v[148:151], %[zo], %[rw], %[z4] offen offset:1024 sc1\n\t"
                    "buffer_load_dwordx4 v[152:155], %[zo], %[rw], %[z4] offen offset:2048 sc1\n\t"
                    "buffer_load_dwordx4 v[156:159], %[zo], %[rw], %[z4] offen offset:3072 sc1\n\t"
                    "s_waitcnt vmcnt(0)\n\t"
                    "v_max3_u32 v101, v129, v131, v133\n\t"
                    "v_max3_u32 v101, v101, v135, v137\n\t"
                    "v_max3_u32 v101, v101, v139, v141\n\t"
                    "v_max3_u32 v101, v101, v143, v145\n\t"
                    "v_max3_u32 v101, v101, v147, v149\n\t"
                    "v_max3_u32 v101, v101, v151, v153\n\t"
                    "v_max3_u32 v101, v101, v155, v157\n\t"
                    "v_max_u32 v101, v101, v159\n\t"
                    "v_mov_b32 v100, v101\n\t"
                    "3:\n\t"
                    "v_permlane16_swap_b32 v128, v129\n\t"
                    "v_permlane16_swap_b32 v130, v131\n\t"
                    "v_permlane16_swap_b32 v132, v133\n\t"
                    "v_permlane16_swap_b32 v134, v135\n\t"
                    "v_permlane16_swap_b32 v136, v137\n\t"
                    "v_permlane16_swap_b32 v138, v139\n\t"
                    "v_permlane16_swap_b32 v140, v141\n\t"
                    "v_permlane16_swap_b32 v142, v143\n\t"
                    "v_permlane16_swap_b32 v144, v145\n\t"
                    "v_permlane16_swap_b32 v146, v147\n\t"
                    "v_permlane16_swap_b32 v148, v149\n\t"
                    "v_permlane16_swap_b32 v150, v151\n\t"
                    "v_permlane16_swap_b32 v152, v153\n\t"
                    "v_permlane16_swap_b32 v154, v155\n\t"
                    "v_permlane16_swap_b32 v156, v157\n\t"
                    "v_permlane16_swap_b32 v158, v159\n\t"
                    "s_mov_b32 vcc_lo, 0x00ff00ff\n\t"
                    "s_mov_b32 vcc_hi, 0x00ff00ff\n\t"
                    "s_waitcnt vmcnt(7)\n\t"
                    OST("%[t3]")
                    "v_smfmac_f32_16x16x64_bf16 v[96:99], v[208:211], v[160:167], %[ix]\n\t"
                    "v_max3_u32 v100, v100, v208, v209\n\t"
                    "v_max3_u32 v100, v100, v210, v211\n\t"
                    "s_waitcnt vmcnt(6)\n\t"
                    "v_smfmac_f32_16x16x64_bf16 v[96:99], v[212:215], v[168:175], %[ix]\n\t"
                    "v_max3_u32 v100, v100, v212, v213\n\t"
                    "v_max3_u32 v100, v100, v214, v215\n\t"
                    "s_waitcnt vmcnt(5)\n\t"
                    "v_smfmac_f32_16x16x64_bf16 v[96:99], v[216:219], v[176:183], %[ix]\n\t"
                    "v_max3_u32 v100, v100, v216, v217\n\t"
                    "v_max3_u32 v100, v100, v218, v219\n\t"
                    "s_waitcnt vmcnt(4)\n\t"
                    "v_smfmac_f32_16x16x64_bf16 v[96:99], v[220:223], v[184:191], %[ix]\n\t"
                    "v_max3_u32 v100, v100, v220, v221\n\t"
                    "v_max3_u32 v100, v100, v222, v223\n\t"
                    "s_waitcnt vmcnt(3)\n\t"
                    "v_smfmac_f32_16x16x64_bf16 v[96:99], v[224:227], v[128:135], %[ix]\n\t"
                    "v_max3_u32 v100, v100, v224, v225\n\t"
                    "v_max3_u32 v100, v100, v226, v227\n\t"
                    "s_waitcnt vmcnt(2)\n\t"
                    "v_smfmac_f32_16x16x64_bf16 v[96:99], v[228:231], v[136:143], %[ix]\n\t"
                    "s_barrier\n\t"
                    "ds_read_b64 v[102:103], %[sp]\n\t"
                    "ds_read_b64 v[104:105], %[sp] offset:512\n\t"
                    "ds_read_b64 v[106:107], %[sp] offset:1024\n\t"
                    "v_max3_u32 v100, v100, v228, v229\n\t"
                    "v_max3_u32 v100, v100, v230, v231\n\t"
                    "s_waitcnt vmcnt(1)\n\t"
                    "v_smfmac_f32_16x16x64_bf16 v[96:99], v[232:235], v[144:151], %[ix]\n\t"
                    "v_max3_u32 v100, v100, v232, v233\n\t"
                    "v_max3_u32 v100, v100, v234, v235\n\t"
                    "s_waitcnt vmcnt(0)\n\t"
                    "v_smfmac_f32_16x16x64_bf16 v[96:99], v[236:239], v[152:159], %[ix]\n\t"
                    "v_max3_u32 v100, v100, v236, v237\n\t"
                    "v_max3_u32 v100, v100, v238, v239\n\t"
                    OST("%[t2]")
                    "v_cmp_eq_u32 %[bad], -1, v100\n\t"
                    "s_waitcnt lgkmcnt(0)\n\t"
                    "v_add_f32 v102, v102, v104\n\t"
                    "v_add_f32 v103, v103, v105\n\t"
                    "v_add_f32 v102, v102, v106\n\t"
                    "v_add_f32 v103, v103, v107\n\t"
                    "v_add_f32 v96, v96, v102\n\t"
                    "v_add_f32 v97, v97, v103\n\t"
                    "v_add_f32 v96, v96, v98\n\t"
                    "v_add_f32 v97, v97, v99\n\t"
                    "s_cmp_lg_u64 %[bad], 0\n\t"
                    "s_cbranch_scc1 4f\n\t"
                    "2:\n\t"
                    "v_cndmask_b32_dpp v98, v97, v96, vcc row_ror:8 row_mask:0xf bank_mask:0xf\n\t"
                    "s_not_b64 vcc, vcc\n\t"
                    "v_cndmask_b32_dpp v99, v96, v97, vcc row_ror:8 row_mask:0xf bank_mask:0xf\n\t"
                    "v_cvt_pk_bf16_f32 v98, v98, v99\n\t"
                    "v_min_u32 v98, -2, v98\n\t"
                    "buffer_store_dword v98, %[mo], %[rw], %[m0] offen sc1\n\t"
                    "buffer_store_dword v98, %[mo], %[rw], %[m1] offen sc1\n\t"
                    "buffer_store_dword v98, %[mo], %[rw], %[m2] offen sc1\n\t"
                    "buffer_store_dword v98, %[mo], %[rw], %[m3] offen sc1\n\t"
                    OST("%[t4]")
                    "s_branch 5f\n\t"
                    "4:\n\t"
                    "s_sub_i32 %[tr], %[tr], 1\n\t"
                    "s_cmp_lt_i32 %[tr], 1\n\t"
                    "s_cbranch_scc1 2b\n\t"
                    "v_xor_b32 %[po], %[x0], %[po]\n\t"
                    "s_xor_b32 %[x0], %[x0], 0x5000\n\t"
                    "v_xor_b32 %[pm], %[x1], %[pm]\n\t"
                    "s_xor_b32 %[x1], %[x1], 0x5000\n\t"
                    "buffer_load_dwordx4 v[128:131], %[zo], %[rw], %[zb] offen sc1\n\t"
                    "buffer_load_dwordx4 v[132:135], %[zo], %[rw], %[zb] offen offset:1024 sc1\n\t"
                    "buffer_load_dwordx4 v[136:139], %[zo], %[rw], %[zb] offen offset:2048 sc1\n\t"
                    "buffer_load_dwordx4 v[140:143], %[zo], %[rw], %[zb] offen offset:3072 sc1\n\t"
                    "buffer_load_dwordx4 v[144:147], %[zo], %[rw], %[z4] offen sc1\n\t"
                    "buffer_load_dwordx4 v[148:151], %[zo], %[rw], %[z4] offen offset:1024 sc1\n\t"
                    "buffer_load_dwordx4 v[152:155], %[zo], %[rw], %[z4] offen offset:2048 sc1\n\t"
                    "buffer_load_dwordx4 v[156:159], %[zo], %[rw], %[z4] offen offset:3072 sc1\n\t"
                    "buffer_load_dwordx4 v[208:211], %[po], %[rw], %[pb] offen sc1\n\t"
                    "buffer_load_dwordx4 v[212:215], %[po], %[rw], %[pb] offen offset:1024 sc1\n\t"
                    "buffer_load_dwordx4 v[216:219], %[po], %[rw], %[pb] offen offset:2048 sc1\n\t"
                    "buffer_load_dwordx4 v[220:223], %[po], %[rw], %[pb] offen offset:3072 sc1\n\t"
                    "buffer_load_dwordx4 v[224:227], %[pm], %[rw], %[pb] offen sc1\n\t"
                    "buffer_load_dwordx4 v[228:231], %[pm], %[rw], %[pb] offen offset:1024 sc1\n\t"
                    "buffer_load_dwordx4 v[232:235], %[pm], %[rw], %[pb] offen offset:2048 sc1\n\t"
                    "buffer_load_dwordx4 v[236:239], %[pm], %[rw], %[pb] offen offset:3072 sc1\n\t"
                    "v_mov_b64 v[96:97], 0\n\t"
                    "v_mov_b64 v[98:99], 0\n\t"
                    "v_mov_b32 v100, 0\n\t"
                    "s_waitcnt vmcnt(0)\n\t"
                    "v_max3_u32 v101, v129, v131, v133\n\t"
                    "v_max3_u32 v101, v101, v135, v137\n\t"
                    "v_max3_u32 v101, v101, v139, v141\n\t"
                    "v_max3_u32 v101, v101, v143, v145\n\t"
                    "v_max3_u32 v101, v101, v147, v149\n\t"
                    "v_max3_u32 v101, v101, v151, v153\n\t"
                    "v_max3_u32 v101, v101, v155, v157\n\t"
                    "v_max_u32 v101, v101, v159\n\t"
                    "v_mov_b32 v100, v101\n\t"
                    "v_permlane16_swap_b32 v128, v129\n\t"
                    "v_permlane16_swap_b32 v130, v131\n\t"
                    "v_permlane16_swap_b32 v132, v133\n\t"
                    "v_permlane16_swap_b32 v134, v135\n\t"
                    "v_permlane16_swap_b32 v136, v137\n\t"
                    "v_permlane16_swap_b32 v138, v139\n\t"
                    "v_permlane16_swap_b32 v140, v141\n\t"
                    "v_permlane16_swap_b32 v142, v143\n\t"
                    "v_permlane16_swap_b32 v144, v145\n\t"
                    "v_permlane16_swap_b32 v146, v147\n\t"
                    "v_permlane16_swap_b32 v148, v149\n\t"
                    "v_permlane16_swap_b32 v150, v151\n\t"
                    "v_permlane16_swap_b32 v152, v153\n\t"
                    "v_permlane16_swap_b32 v154, v155\n\t"
                    "v_permlane16_swap_b32 v156, v157\n\t"
                    "v_permlane16_swap_b32 v158, v159\n\t"
                    "s_nop 1\n\t"
                    "v_smfmac_f32_16x16x64_bf16 v[96:99], v[208:211], v[160:167], %[ix]\n\t"
                    "v_max3_u32 v100, v100, v208, v209\n\t"
                    "v_max3_u32 v100, v100, v210, v211\n\t"
                    "v_smfmac_f32_16x16x64_bf16 v[96:99], v[212:215], v[168:175], %[ix]\n\t"
                    "v_max3_u32 v100, v100, v212, v213\n\t"
                    "v_max3_u32 v100, v100, v214, v215\n\t"
                    "v_smfmac_f32_16x16x64_bf16 v[96:99], v[216:219], v[176:183], %[ix]\n\t"
                    "v_max3_u32 v100, v100, v216, v217\n\t"
                    "v_max3_u32 v100, v100, v218, v219\n\t"
                    "v_smfmac_f32_16x16x64_bf16 v[96:99], v[220:223], v[184:191], %[ix]\n\t"
                    "v_max3_u32 v100, v100, v220, v221\n\t"
                    "v_max3_u32 v100, v100, v222, v223\n\t"
                    "v_smfmac_f32_16x16x64_bf16 v[96:99], v[224:227], v[128:135], %[ix]\n\t"
                    "v_max3_u32 v100, v100, v224, v225\n\t"
                    "v_max3_u32 v100, v100, v226, v227\n\t"
                    "v_smfmac_f32_16x16x64_bf16 v[96:99], v[228:231], v[136:143], %[ix]\n\t"
                    "v_max3_u32 v100, v100, v228, v229\n\t"
                    "v_max3_u32 v100, v100, v230, v231\n\t"
                    "v_smfmac_f32_16x16x64_bf16 v[96:99], v[232:235], v[144:151], %[ix]\n\t"
                    "v_max3_u32 v100, v100, v232, v233\n\t"
                    "v_max3_u32 v100, v100, v234, v235\n\t"
                    "v_smfmac_f32_16x16x64_bf16 v[96:99], v[236:239], v[152:159], %[ix]\n\t"
                    "v_max3_u32 v100, v100, v236, v237\n\t"
                    "v_max3_u32 v100, v100, v238, v239\n\t"
                    "v_cmp_eq_u32 %[bad], -1, v100\n\t"
                    "s_nop 5\n\t"
                    "v_add_f32 v96, v96, v102\n\t"
                    "v_add_f32 v97, v97, v103\n\t"
                    "v_add_f32 v96, v96, v98\n\t"
                    "v_add_f32 v97, v97, v99\n\t"
                    "s_cmp_lg_u64 %[bad], 0\n\t"
                    "s_cbranch_scc1 4b\n\t"
                    "s_branch 2b\n\t"
                    "5:\n\t"
                    : [tr] "+s"(tries), [po] "+v"(po), [pm] "+v"(pm), [x0] "+s"(x0), [x1] "+s"(x1), [bad] "=&s"(bad), [t0] "=&s"(t0_), [t1] "=&s"(t1_), [t2] "=&s"(t2_), [t3] "=&s"(t3_), [t4] "=&s"(t4_)
                    : [zo] "v"(zoff), [sp] "v"(spa), [ix] "v"(sidx), [rw] "s"(rwin), [zb] "s"(zb), [z4] "s"(zb + 4096u), [pb] "s"(pb), [mo] "v"(moff), [fd] "n"(FDL),
                      [m0] "s"(mb), [m1] "s"(mb + PAIR), [m2] "s"(mb + mc2), [m3] "s"(mb + mc2 + PAIR), "{v[160:167]}"(zf[0]), "{v[168:175]}"(zf[1]), "{v[176:183]}"(zf[2]), "{v[184:191]}"(zf[3])
                    : "v96", "v97", "v98", "v99", "v100", "v101", "v102", "v103", "v104", "v105", "v106", "v107", "v128", "v129", "v130", "v131", "v132", "v133", "v134", "v135", "v136", "v137", "v138", "v139", "v140", "v141", "v142", "v143", "v144", "v145", "v146", "v147", "v148", "v149", "v150", "v151", "v152", "v153", "v154", "v155", "v156", "v157", "v158", "v159", "v208", "v209", "v210", "v211", "v212", "v213", "v214", "v215", "v216", "v217", "v218", "v219", "v220", "v221", "v222", "v223", "v224", "v225", "v226", "v227", "v228", "v229", "v230", "v231", "v232", "v233", "v234", "v235", "v236", "v237", "v238", "v239", "vcc", "scc", "memory");
                budget = tries;
                if (w == 3) *(volatile LAS int*)(smem + 4100) = s;
                __builtin_amdgcn_s_sleep(DPS - 4);
                if (w == 3) {
                    st128(SENT, rwin, ra, W_MEM + (unsigned)(s & 3) * 6u * PAIR, 16);
                    if (lane == 0 && s > 1) __builtin_amdgcn_raw_buffer_store_b32((unsigned)s - 2u, rwin, 0u, W_GO + fo + (unsigned)o * 128u, 16);
                }
                for (;;) {
                    const u32x2 x = *(volatile LAS u32x2*)(smem + 4096);
                    zr = __builtin_amdgcn_readfirstlane(x[0]);
                    if (__builtin_amdgcn_readfirstlane(x[1]) == s) break;
                    __builtin_amdgcn_s_sleep(2);
                }
                zb = zb == W_ZM + 2u * NOWN * PAIR ? W_ZM : zb + NOWN * PAIR;
            }
            const unsigned zq = zb == W_ZM ? W_ZM + 2u * NOWN * PAIR : zb - NOWN * PAIR;
            while (np < 4) {
                while (zr < NSEG - 1) { __builtin_amdgcn_s_sleep(1); zr = __builtin_amdgcn_readfirstlane(*(volatile LAS int*)(smem + 4096)); }
                piece();
            }
            __builtin_amdgcn_s_sleep(DTK);
            for (int e = 0; e < 2; ++e) {
                f32x4 c[2];
                SST(16 + 3 * e);
                for (unsigned spins = 0;;) {
                    asm volatile("" ::: "memory");
                    SST(17 + 3 * e);
                    u32x4 pt[16], ptm[2][2], zm[2];
                    const unsigned pl = W_PL + fo + (unsigned)e * (PTSEG * 2u);
#pragma unroll
                    for (int q = 0; q < 2; ++q) zm[q] = __builtin_amdgcn_raw_buffer_load_b128(rwin, zoff + hg * 2048u + q * 1024u, e ? zb : zq, 16);
#pragma unroll
                    for (int q = 0; q < 16; ++q) pt[q] = __builtin_amdgcn_raw_buffer_load_b128(rwin, ((unsigned)(16 * hg) * 1024u + lane * 16u) | (q & 1 ? 0u : (lane & 32u) << 25), pl + q * 1024u, 16);
#pragma unroll
                    for (int q = 0; q < 4; ++q) ptm[q >> 1][q & 1] = __builtin_amdgcn_raw_buffer_load_b128(rwin, (unsigned)(hg * 2048 + lane * 16), pl + 65536u + (q >> 1) * 8192u + (q & 1) * 1024u, 16);
                    unsigned m = max(smax(zm[0]), smax(zm[1]));
#pragma unroll
                    for (int t = 0; t < 2; ++t) {
                        c[t] = z4;
#pragma unroll
                        for (int q = 0; q < 8; ++q) { c[t] = mfma32(half8(zf[q >> 1], q & 1), pt[2 * q + t], c[t]); m = max(m, smax(pt[2 * q + t])); }
#pragma unroll
                        for (int q = 0; q < 2; ++q) { c[t] = mfma32((u32x4){zm[q][0], zm[q][2], zm[q][1], zm[q][3]}, ptm[t][q], c[t]); m = max(m, smax(ptm[t][q])); }
                    }
                    if (!__any(m == 0xFFFFFFFFu)) break;
                    if (spin_fail(spins, rwin)) break;
                }
                SST(18 + 3 * e);
#pragma unroll
                for (int t = 0; t < 2; ++t) *(f32x4*)(op + e * 8192 + hg * 2048 + (16 * t + fr) * 64 + fq * 16) = c[t];
                BAR();
                VMWAIT();
#pragma unroll
                for (int q = 0; q < 4; ++q) zf[q] = cat8(*(const u32x4*)(smem + 32768 + hg * 8192 + q * 2048 + lane * 16), *(const u32x4*)(smem + 33792 + hg * 8192 + q * 2048 + lane * 16));
                if (e == 0) BAR0();
            }
            if (layer == 0) {
#pragma unroll
                for (int q = 0; q < 4; ++q) st128(SENT, rwin, zoff + hg * 2048u + (q & 1) * 1024u, q < 2 ? zb : zq, 16);
                if (w == 3) st128(SENT, rwin, ra, W_MEM + 3u * 6u * PAIR, 16);
            }
        } else if (w == 4) {
            const unsigned fl = fz_at(lane & 31, o / 10) + (lane & 32) * 128u;
            int zk = -1;
            auto note = [&](int f) INL {
                f = min(f, __builtin_amdgcn_update_dpp(f, f, 0xB1, 0xF, 0xF, false)); f = min(f, __builtin_amdgcn_update_dpp(f, f, 0x4E, 0xF, 0xF, false));
                f = min(f, __builtin_amdgcn_update_dpp(f, f, 0x141, 0xF, 0xF, false)); f = min(f, __builtin_amdgcn_update_dpp(f, f, 0x140, 0xF, 0xF, false));
                zk = seg_done(MXV, min(__builtin_amdgcn_readlane(f, 0), __builtin_amdgcn_readlane(f, 16)), min(__builtin_amdgcn_readlane(f, 32), __builtin_amdgcn_readlane(f, 48)), MXA);
                *(volatile LAS int*)(smem + 4096) = zk;
            };
            for (int s = 0; s < NSEG; ++s) {
                for (unsigned spins = 0; __builtin_expect(zk < s && s > 0, 0);) {
                    if (spins == 0) __builtin_amdgcn_s_sleep(DW4);
                    asm volatile("" ::: "memory");
                    note((int)__builtin_amdgcn_raw_buffer_load_b32(rwin, fl, W_FZ + fo, 16));
                    FC(fc2);
                    if (spin_fail(spins, rwin)) note(NSEG);
                    if (zk < s) __builtin_amdgcn_s_sleep(ZPS);
                }
                if (LKE && s > 0 && zk < (s + 8 >= NSEG ? NSEG - 1 : s + ZLA)) { __builtin_amdgcn_s_sleep(LKE); note((int)__builtin_amdgcn_raw_buffer_load_b32(rwin, fl, W_FZ + fo, 16)); }
                asm volatile("s_waitcnt lgkmcnt(0)\n\ts_barrier\n\ts_barrier" ::: "memory");
                int f = 0;
                const bool look = !LKE && zk < (s + 8 >= NSEG ? NSEG - 1 : s + ZLA);
                if (look) { __builtin_amdgcn_s_sleep(6); f = (int)__builtin_amdgcn_raw_buffer_load_b32(rwin, fl, W_FZ + fo, 16); }
                SB();
                BAR0();
                if (look) note(f);
            }
        } else if (w < 7) {
            const unsigned ooff = (unsigned)((S_ - (7 - w) * SEG + (lane >> 1)) * D_ + ot * 16 + (lane & 1) * 8) * 2u;
            const __amdgpu_buffer_rsrc_t ro = rout();
            for (int s = 0; s < 3 * NSEG - 3 + (w - 5); ++s) BAR0();
            if (w == 5) __builtin_amdgcn_s_sleep(DPS);
            const u32x4 xr = __builtin_amdgcn_raw_buffer_load_b128(rws, ooff, O_X1, 16);
            BAR0();
            BAR0();
            const char* src = op + (w - 5) * 8192 + lane * 32;
            f32x4 x = *(const f32x4*)src, y = *(const f32x4*)(src + 16);
#pragma unroll
            for (int ww = 1; ww < 4; ++ww) { x += *(const f32x4*)(src + ww * 2048); y += *(const f32x4*)(src + ww * 2048 + 16); }
            const u32x4 r = {pack2(lo2f(xr[0]) + bfr(x[0]), hi2f(xr[0]) + bfr(x[1])), pack2(lo2f(xr[1]) + bfr(x[2]), hi2f(xr[1]) + bfr(x[3])),
                             pack2(lo2f(xr[2]) + bfr(y[0]), hi2f(xr[2]) + bfr(y[1])), pack2(lo2f(xr[3]) + bfr(y[2]), hi2f(xr[3]) + bfr(y[3]))};
            st128(nosent4(r), ro, ooff, 0u, 16);
            RST(19 + w, layer, w, w & 3);
            if (w == 5) BAR0();
        } else {
            Tk tk = tk_init();
            __builtin_amdgcn_s_setprio(3);
            for (int s = 0; s < NSEG; ++s) {
                const unsigned ta0 = ck.ta, mm0 = ck.ma;
                unsigned tp = tpp + lt + xs + M1;
                if (s < SAD) {
                    if (s) wait_fine(tpp + CF, ck);
                    tp = M2 + xs + (s ? 0 : PEN1);
                    sched(tk, s);
                    tp = tpp - tp;
                    if (s == 0) { wait_fine(tp - DZ0, ck); *(volatile LAS int*)(smem + 4096) = 0; }
                }
                wait_fine_bar(tp - DZP, ck);
                wait_cyc_bar(cyc_at(ck, sx24(tp + (s + 1 < NSEG ? 0 : ML) - ck.ta)));
                clk_rate(ck, ta0, mm0);
                asm volatile("s_barrier ; 7" ::: "memory");
                if (s >= SAD && s + 1 < NSEG) sched(tk, s);
                RTS();
            }
        }
        __builtin_amdgcn_s_setprio(0);
        SST(23);
    };
    static_assert(NSEG % 3 == 1, "ring");
    auto a1_units = [&](auto late, int& u, int u1) INL {
        constexpr bool LATE = decltype(late)::v;
        int tid = mytid();
        RowTw tw;
        if (LATE) row_tw(tw, tid);
        for (; u < u1; u += !LATE ? 24 : u >= 232 && u < 240 ? 8 : 256) {
            if (!LATE) tid = mytid();
            if (w == 4) {
                for (unsigned spins = 0;;) {
                    asm volatile("" ::: "memory");
                    if (LATE) {
                        const int go = __builtin_amdgcn_readfirstlane(__builtin_amdgcn_raw_buffer_load_b32(rwin, 0u, W_GO + (unsigned)(u & 31) * 128u, 16)) - min((u >> 2) - 2, NSEG - 4);
                        if (go >= 0) break;
                        if (go < -3) __builtin_amdgcn_s_sleep(127);
                    } else if (!__any(__builtin_amdgcn_raw_buffer_load_b32(rwin, min(fresh_lane(), (unsigned)NOWN - 1u) * 4u, W_OUT + (unsigned)(u >> 2) * 256u, 16) != 0u)) break;
                    if (spin_fail(spins, rwin)) break;
                    if (u < 200) __builtin_amdgcn_s_sleep(WSLP); else __builtin_amdgcn_s_sleep(16);
                }
            }
            BAR();
            SST(24);
            a1_unit<4, LATE>(mkrsrc(ws + OFF_X2, S_ * D_ * 2), u * 8, rws, rwin, tw, tid, fs);
            if (!LATE) VMWAIT();
            FST(8);
            BAR0();
            FST(9);
            if (!LATE && w == 0) __builtin_amdgcn_raw_buffer_store_b64(__builtin_bit_cast(u32x2, tag + 9ull), rwin, LANE0, W_A1 + (unsigned)u * 128u, 16);
            SST(25);
            FSW(7);
            if (!LATE && u < 176) __builtin_amdgcn_s_sleep(A1S);
        }
    };
    auto out_role = [&](auto nt, int t0, int s0, auto between) INL {
        constexpr int NT = decltype(nt)::v, W0 = 8 - NT;
        const int o = s0 * 10 + (t0 >> 2), send = NSEG - 2;
        int rdy = -1, go = -1, zrdy = -1;
        for (int i = 0; i < s0; ++i) __builtin_amdgcn_s_sleep(OST0);
        for (int s = s0; s < send; s += 4) {
        LANES();
        const unsigned ztoff = (unsigned)((t0 * H_ + 4 * w) * 64 + lane) * 16u;
        const unsigned ptoff = (unsigned)(8 * w * 64 + lane) * 16u, ptmoff = 65536u + (unsigned)(4 * w * 16 + lane) * 16u;
        const unsigned ptoff0 = ptoff | (lane & 32u) << 25;
        const unsigned ooff = (unsigned)((lane >> 1) * D_ + tile_of(t0 + w - W0) * 16 + (lane & 1) * 8) * 2u;
        const unsigned fg = fo + (unsigned)((4 * w + (lane & 3)) * 128), gw = (unsigned)(t0 / 10) * 4u;
        const unsigned tfoff = fg + (lane < 16 ? W_FPT + (lane >> 2) * 4u : W_FZ2 + gw), zfoff = fo + W_FZ + fz_at(4 * w + (lane & 3), gw / 4u) + (lane & 32u) * 128u;
        auto wait_rdy = [&](int s) INL {
            for (unsigned spins = 0; rdy < s;) {
                asm volatile("" ::: "memory");
                if (s < TAILS || go < s - 2) {
                    go = (int)__builtin_amdgcn_readfirstlane(__builtin_amdgcn_raw_buffer_load_b32(rwin, 0u, W_GO + fo + (unsigned)o * 128u, 16));
                    rdy = max(rdy, go);
                    if (rdy < s) { if (s < TAILS || go < s - 3) __builtin_amdgcn_s_sleep(OSLP); else __builtin_amdgcn_s_sleep(OSLP3); }
                } else {
                    if (!__any((int)__builtin_amdgcn_raw_buffer_load_b32(rwin, tfoff, 0u, 16) < s)) rdy = s;
                    else __builtin_amdgcn_s_sleep(OSLP2);
                }
                if (rdy < s && spin_fail(spins, rwin)) break;
            }
        };
        const __amdgpu_buffer_rsrc_t ro = rout();
        u32x4 zf[NT][4];
        auto fetch_z = [&](int s) INL {
            for (unsigned spins = 0; zrdy < s;) {
                asm volatile("" ::: "memory");
                int f = (int)__builtin_amdgcn_raw_buffer_load_b32(rwin, zfoff, 0u, 16);
                f = min(f, __builtin_amdgcn_update_dpp(f, f, 0xB1, 0xF, 0xF, false)); f = min(f, __builtin_amdgcn_update_dpp(f, f, 0x4E, 0xF, 0xF, false));
                f = min(f, __builtin_amdgcn_update_dpp(f, f, 0x141, 0xF, 0xF, false));
                zrdy = seg_done(MXV, __builtin_amdgcn_readlane(f, 0), __builtin_amdgcn_readlane(f, 32));
                if (zrdy >= s || spin_fail(spins, rwin)) break;
                __builtin_amdgcn_s_sleep(OSLP);
            }
#pragma unroll
            for (int q = 0; q < 4 * NT; ++q) zf[q >> 2][q & 3] = __builtin_amdgcn_raw_buffer_load_b128(rws, ztoff + (q & 3) * 1024u, O_Z + (unsigned)((s - layer) * (int)ZTSEG) * 2u + (q >> 2) * (H_ * 1024u), 16);
        };
            u32x4 xr, zx[NT], pt[8], ptm[2];
            RT(0);
            fetch_z(s);
            if (NT > 1 && w >= W0 && layer == 0 && s >= 4 && s < 60) {
                asm volatile("s_waitcnt vmcnt(%0)" :: "n"(4 * NT) : "memory");
                __builtin_amdgcn_raw_buffer_store_b32(0u, rwin, LANE0, W_OUT + (unsigned)((s - 4) * 64 + t0 + w - W0) * 4u, 16);
            }
            if (w >= W0) xr = __builtin_amdgcn_raw_buffer_load_b128(rws, ooff, O_X1 + (unsigned)(s * SEG * D_ * 2), 16);
            if (s == 61) SST(27);
            wait_rdy(s);
            if (s == 61) SST(28);
            RT(1);
            const unsigned ptb = O_PT + (unsigned)(s * PTSEG) * 2u;
#pragma unroll
            for (int q = 0; q < 8; ++q) pt[q] = __builtin_amdgcn_raw_buffer_load_b128(rws, (q & 1 ? ptoff : ptoff0) + q * 1024u, ptb, 16);
#pragma unroll
            for (int q = 0; q < 2; ++q) ptm[q] = __builtin_amdgcn_raw_buffer_load_b128(rws, ptmoff + q * 8192u, ptb, 16);
#pragma unroll
            for (int q = 0; q < NT; ++q) zx[q] = __builtin_amdgcn_raw_buffer_load_b128(rws, tid * 16u, O_Z2 + (unsigned)(s * NOWN + t0 + q) * 8192u, 16);
            BAR0();
            f32x4 c[2 * NT];
#pragma unroll
            for (int q = 0; q < 4; ++q) {
#pragma unroll
                for (int u = 0; u < 2 * NT; ++u) c[u] = mfma32(zf[u >> 1][q], pt[2 * q + (u & 1)], q ? c[u] : z4);
                SB();
            }
#pragma unroll
            for (int u = 0; u < 2 * NT; ++u) {
                const int tq = u & 1, t = u >> 1;
                c[u] = mfma32((u32x4){zx[t][0], zx[t][2], zx[t][1], zx[t][3]}, ptm[tq], c[u]);
                *(f32x4*)(smem + w * (NT * 2048) + t * 2048 + (16 * tq + fr) * 64 + ((fq ^ ((fr >> 1) & 3)) << 4)) = c[u];
            }
            BAR();
            if (s == 61) SST(30);
            if (w >= W0) {
                const char* p0 = smem + (w - W0) * 2048 + (lane >> 1) * 64 + (((2 * (lane & 1)) ^ ((lane >> 2) & 3)) << 4);
                const char* p1 = smem + (w - W0) * 2048 + (lane >> 1) * 64 + (((2 * (lane & 1) + 1) ^ ((lane >> 2) & 3)) << 4);
                f32x4 x = *(const f32x4*)p0, y = *(const f32x4*)p1;
#pragma unroll
                for (int ww = 1; ww < 8; ++ww) { x += *(const f32x4*)(p0 + ww * (NT * 2048)); y += *(const f32x4*)(p1 + ww * (NT * 2048)); }
                const u32x4 r = {pack2(lo2f(xr[0]) + bfr(x[0]), hi2f(xr[0]) + bfr(x[1])), pack2(lo2f(xr[1]) + bfr(x[2]), hi2f(xr[1]) + bfr(x[3])),
                                 pack2(lo2f(xr[2]) + bfr(y[0]), hi2f(xr[2]) + bfr(y[1])), pack2(lo2f(xr[3]) + bfr(y[2]), hi2f(xr[3]) + bfr(y[3]))};
                if (layer) st128(r, ro, ooff, (unsigned)(s * SEG * D_ * 2), OAUX);
                else {
                    st128(nosent4(r), ro, ooff, (unsigned)(s * SEG * D_ * 2), 16);
                    if (NT == 1 && s < 56) { VMWAIT(); __builtin_amdgcn_raw_buffer_store_b32(0u, rwin, LANE0, W_OUT + (unsigned)(s * 64 + t0) * 4u, 16); }
                }
            } else __builtin_amdgcn_s_sleep(OSL4);
            if (s < 8 && NT > 1) SST(8 + s);
            if (s >= 56) SST(s - 40);
            RT(2);
            RTS();
            between(s);
        }
    };
    u32x8 wt[2][10];
#define PINW() asm volatile("" : "+{v[0:7]}"(wt[0][0]), "+{v[8:15]}"(wt[1][0]), "+{v[16:23]}"(wt[0][1]), "+{v[24:31]}"(wt[1][1]), "+{v[32:39]}"(wt[0][2]), "+{v[40:47]}"(wt[1][2]), "+{v[48:55]}"(wt[0][3]), "+{v[56:63]}"(wt[1][3]), "+{v[64:71]}"(wt[0][4]), "+{v[72:79]}"(wt[1][4]), "+{v[80:87]}"(wt[0][5]), "+{v[88:95]}"(wt[1][5]), "+{v[96:103]}"(wt[0][6]), "+{v[104:111]}"(wt[1][6]), "+{v[112:119]}"(wt[0][7]), "+{v[120:127]}"(wt[1][7]), "+{v[128:135]}"(wt[0][8]), "+{v[136:143]}"(wt[1][8]), "+{v[144:151]}"(wt[0][9]), "+{v[152:159]}"(wt[1][9]))
#define KILLW() asm volatile("" : "={v[0:7]}"(wt[0][0]), "={v[8:15]}"(wt[1][0]), "={v[16:23]}"(wt[0][1]), "={v[24:31]}"(wt[1][1]), "={v[32:39]}"(wt[0][2]), "={v[40:47]}"(wt[1][2]), "={v[48:55]}"(wt[0][3]), "={v[56:63]}"(wt[1][3]), "={v[64:71]}"(wt[0][4]), "={v[72:79]}"(wt[1][4]), "={v[80:87]}"(wt[0][5]), "={v[88:95]}"(wt[1][5]), "={v[96:103]}"(wt[0][6]), "={v[104:111]}"(wt[1][6]), "={v[112:119]}"(wt[0][7]), "={v[120:127]}"(wt[1][7]), "={v[128:135]}"(wt[0][8]), "={v[136:143]}"(wt[1][8]), "={v[144:151]}"(wt[0][9]), "={v[152:159]}"(wt[1][9]))
    if (blockIdx.x < 64) {
        for (;;) {
            load_qv(wt, bid & 31, bid >= 32, [&]() INL {
                rot0 = __builtin_amdgcn_raw_buffer_load_b64(rws, 0u, 128u, 16);
                if (w == 0) wait_tags(rwin, 0, 1, 64u, tag + 8ull, true);
                if (w == 7) clk_init(ck);
                if (w == 1) { const unsigned x = __builtin_amdgcn_raw_buffer_load_b32(mkrsrc(arg(A_MEM0), NM * D_ * 2), fresh_lane() * 160u, 0, 0); asm volatile("" :: "v"(x)); }
            });
            const unsigned x = get_rot();
            if (__builtin_expect(x == vx, 1)) break;
            set_role(x);
        }
        SST(1);
        if (TLBW) {
            const unsigned x = __builtin_amdgcn_raw_buffer_load_b32(rws, fresh_lane() << TLBW, ((blockIdx.x >> 3) + w * 8u) << (TLBW + 6) & 0x7FFFFFFFu, 0);
            asm volatile("" :: "v"(x));
        }
        PINW();
    } else {
        if (bid >= NWRK) probe();
        KILLW();
    }
    for (; layer < 2; ++layer) {
        new_layer();
        PINW();
        BAR();
        if (qx || (layer ? bid < 96 : (bid >= 32 && bid < 64) || (bid >= 104 && bid < NREC) || xq)) {
            const int ci = qx ? bid - 80 : bid < 96 ? bid : bid < NREC ? bid - 104 : 64 + (bid - NREC) / 10 * 8 + (bid - NREC) % 10;
            SST(26);
            a2_unit(ci >= 100 ? ci - 100 : ci + 60, layer, rws, rwin, mytid(), [&](int k) INL {
                if (k == 2) SST(28);
                else if (w == 0) {
                    if (layer && bid >= 64) for (unsigned spins = 0;;) {
                        asm volatile("" ::: "memory");
                        if (__all(__builtin_bit_cast(u64, __builtin_amdgcn_raw_buffer_load_b64(rwin, 0u, W_A1 + (unsigned)(200 + (bid & 15)) * 128u, 16)) == tag + 9ull) || spin_fail(spins, rwin)) break;
                        __builtin_amdgcn_s_sleep(100);
                    }
                    if (layer && bid >= 64) rec_end(GATE);
                    wait_tags(rwin, 0, 4, layer ? 32u : 64u, tag + 8ull + layer, !layer);
                    SST(27);
                }
            }, fs);
            SST(29);
            VMWAIT();
            FST(8);
            BAR0();
            FST(9);
            if (w == 0) {
                __builtin_amdgcn_raw_buffer_store_b32(0u, rwin, LANE0, W_COL + fo + (unsigned)ci * 128u, 16);
                if (bid == NQT && layer == 1) __builtin_amdgcn_raw_buffer_store_b64(__builtin_bit_cast(u32x2, tag), rws, LANE0, 0u, 16);
            }
            SST(30);
            FSW(1 + layer);
        }
#define NEXTB() do { PINW(); asm volatile("" : "+s"(bid), "+s"(w)); } while (0)
        NEXTB();
        if (bid < 32) { fresh_rsrc(); qk_role(wt); PINW(); }
        NEXTB();
        const int hh = bid < 96 ? bid & 31 : bid < NQT ? (bid - NREC) / 10 * 8 + (bid - NREC) % 10 : (bid - NQT) & 31;
        if (((qx || (bid >= 32 && bid < 64)) && layer == 0) || xq) { if (layer) { if (w == 0) rec_end(GATX); BAR0(); } load_qv(wt, hh, xt || bid < 64, []() {}); SST(1); PINW(); }
        NEXTB();
        if (bid >= 32 && bid < 64) { if (layer == 0) near_init(); fresh_rsrc(); vo_role(wt); PINW(); }
        NEXTB();
        if ((bid >= 64 && bid < 96) || xt) {
            fresh_rsrc();
            vt_role(wt, hh, xt ? MXV : ~MXV);
            if (xt) {
                out_role(IC<1>(), 5 * (hh & 7) + (hh & 1 ? 0 : 4), hh >> 3, [&](int s) INL {
                    if (s < 28) {
                        if (layer == 0) for (unsigned i = (s >> 2) * 2; i < (s >> 2) * 2 + 2; ++i)
                            st128(SENT, i < 7 ? mkrsrc(ws + O_ARM, O_ARM1 - O_ARM) : i < 12 ? mkrsrc(ws + O_Z - ZTSEG * 2, ZTSEG * 2) : mkrsrc(arg(1) + (W_ARM - W_LAYER) / 2, W_LAYER),
                                  (unsigned)(hh * 512 + mytid()) * 16u, (i < 7 ? i : i < 12 ? i - 7 : i - 12) * 262144u, 16);
                    } else if (s < 32) {
                        VMWAIT();
                        BAR0();
                        if (w >= 4) __builtin_amdgcn_raw_buffer_store_b32(NSEG - 1, rwin, LANE0, W_FZ + fo + fz_at(hh, w - 4) + 4096u, 16);
                    }
                });
                static_assert(O_ARM + 7u * 262144u >= O_ARM1 && ZTSEG * 2 <= 5u * 262144u && W_LAYER <= 262144u && !(MXV >> 40), "arming");
            }
            PINW();
        }
        NEXTB();
        if ((qx && !xt) || xq) { fresh_rsrc(); qt_role(wt, hh, IC<1>()); PINW(); }
        NEXTB();
        if (bid >= 96 && !qx) {
            const int fi = xq ? 256 : bid >= NWRK ? 255 - bid : bid >= NREC ? 8 + (bid - NREC) / 10 * 2 + (bid - NREC) % 10 : bid - 72, tid = mytid();
            RowTw tw;
            row_tw(tw, tid);
            if (layer == 0 && fi >= 16 && fi < 32) { if (w == 0) wait_tags(rwin, 0, 1, 64u, tag + 8ull, true); BAR0(); }
            if (layer && fi < 64) {
                for (unsigned spins = 0;;) {
                    asm volatile("" ::: "memory");
                    if (__all(__builtin_bit_cast(u64, __builtin_amdgcn_raw_buffer_load_b64(rwin, 0u, W_A1 + (unsigned)fi * 128u, 16)) == tag + 9ull) || spin_fail(spins, rwin)) break;
                    __builtin_amdgcn_s_sleep(8);
                }
            }
            bool cols = false;
            for (int u = fi; u < 256; u += 24) {
                a3_unit(mkrsrc(layer ? (const u16*)(ws + OFF_X2) : arg(A_X), S_ * D_ * 2), u * 8, layer, rws, rwin, tw, tid, [&]() INL {
                    if (cols) return;
                    cols = true;
                    if (w == 7) {
                        const unsigned l = fresh_lane();
                        unsigned o[3] = {l * 128u, l * 128u + 8192u, l < 32u ? l * 128u + 16384u : 0x40000000u};
                        for (unsigned spins = 0;;) {
                            asm volatile("" ::: "memory");
                            unsigned v[3];
#pragma unroll
                            for (int g = 0; g < 3; ++g) v[g] = __builtin_amdgcn_raw_buffer_load_b32(rwin, o[g], W_COL + fo, 16);
#pragma unroll
                            for (int g = 0; g < 3; ++g) if (v[g] == 0u) o[g] = 0x40000000u;
                            if (!__any((v[0] | v[1] | v[2]) != 0u) || spin_fail(spins, rwin)) break;
                            __builtin_amdgcn_s_sleep(CSLP);
                        }
                    }
                    BAR0();
                    SST(6);
                }, fs);
                VMWAIT();
                FST(8);
                BAR0();
                FST(9);
                if (w == 0) __builtin_amdgcn_raw_buffer_store_b32(0u, rwin, LANE0, W_H + fo + (unsigned)((u >> 2) * 128 + (u & 3) * 4), 16);
                if (u < 64) SST(7);
                FSW(u < 64 ? 3 + layer : 5);
                if (fi >= 24) break;
                if (u < 64) u += 40;
            }
            if (bid < NREC) { fresh_rsrc(); own_role(); }
            else if (xq) out_role(IC<4>(), 5 * (hh & 7) + (hh & 1), hh >> 3, [](int) {});
            else if (layer == 0) { int u = fi; a1_units(IC<0>(), u, 224); u = bid < NQT ? 192 + (bid - NREC) / 10 + 4 * ((bid - NREC) % 10) : bid < 248 ? bid - 8 : bid; a1_units(IC<1>(), u, 256); }
            KILLW();
        }
        NEXTB();
        SST(31);
    }
}

constexpr int L_DBG = 0;
extern "C" size_t dbg_offset(int i) {
    const size_t o[] = {OFF_WIN, W_TMO, W_NEAR, OFF_F, OFF_X1, OFF_X2, OFF_H, OFF_WFT};
    return o[i];
}

extern "C" void kernel_launch(void* const* d_in, const int* in_sizes, int n_in, void* d_out, int out_size, void* d_ws, size_t ws_size,
                              hipStream_t stream) {
    char* ws = (char*)d_ws;
    hipFuncSetAttribute((const void*)mega_kernel, hipFuncAttributeMaxDynamicSharedMemorySize, L_END + L_DBG);
    char* win = (char*)(((uintptr_t)(ws + OFF_WIN) + 0x1FFFFFu) & ~(uintptr_t)0x1FFFFFu);
    Args a{ws, win, (const u16*)d_in[0], (const u16*)d_in[1], (const u16*)d_in[2], (const u16*)d_in[4], (const u16*)d_in[5], (const u16*)d_in[6], (const u16*)d_in[7], (const u16*)d_in[8], (u16*)d_out};
    mega_kernel<<<256, 512, L_END + L_DBG, stream>>>(a);
}
```

```cpp
#include <hip/hip_runtime.h>
#include <hip/hip_bf16.h>
#include <stdint.h>

typedef unsigned short u16;
typedef unsigned long long u64;
typedef __attribute__((ext_vector_type(8))) __bf16 bf16x8;
typedef __attribute__((ext_vector_type(4))) short s16x4;
typedef __attribute__((ext_vector_type(4))) float f32x4;
typedef __attribute__((ext_vector_type(8))) float f32x8;
typedef __attribute__((ext_vector_type(16))) float f32x16;
typedef __attribute__((ext_vector_type(4))) unsigned u32x4;
typedef __attribute__((ext_vector_type(2))) unsigned u32x2;
typedef __attribute__((ext_vector_type(8))) unsigned u32x8;
typedef __attribute__((ext_vector_type(16))) __bf16 bf16x16;
typedef __attribute__((ext_vector_type(2))) unsigned short u16x2;
#define LAS __attribute__((address_space(3)))
#define INL __attribute__((always_inline))

constexpr int S_ = 2048, D_ = 640, DF = 321, H_ = 32, DH = 128, HD = 4096, NM = 8, SEG = 32, NSEG = 64;
constexpr int NOWN = 40;
constexpr int NCOL = 160;
constexpr size_t ZTSEG = (size_t)NOWN * H_ * 512;
constexpr unsigned PAIR = 16384, HALF = 8192;
constexpr unsigned W_MEM = 0, W_T = W_MEM + 24 * PAIR, W_P = 33 * PAIR, W_PROBE = 74 * PAIR, W_GO = W_PROBE + 2 * PAIR, W_FZ = W_GO + 5120, W_FPT = W_FZ + 8192, W_FZ2 = W_FPT + 4096,
                   W_FQ = W_FZ2 + 4096, W_H = W_FQ + 8192, W_COL = W_H + HALF, W_PL = W_COL + 20480, W_LAYER = W_PL + 2 * 81920 - W_GO, W_OUT = 32 * PAIR, W_MISC = W_OUT + 14336,
                   W_ARM = W_GO + 2 * W_LAYER, W_A1 = 106 * PAIR, W_ZM = 2u * 1024 * 1024, W_END = W_ZM + 120 * PAIR;
static_assert(W_ARM <= W_A1 && W_ARM % 1024 == 0, "window");
__device__ __forceinline__ unsigned p_slot(int s) { return W_P + (unsigned)((s & 3) * 10 + (((s & 3) + 1) >> 2)) * PAIR; }
constexpr unsigned W_TMO = W_MISC, W_NEAR = W_MISC + 128;
constexpr int PTSEG = 32768 + 8192;

#define FPAD 256
constexpr unsigned FST_ = S_ * 16 + FPAD, GST = 4096 + FPAD, WST = S_ * 8 + FPAD;
constexpr size_t al(size_t x) { return (x + 255) & ~(size_t)255; }
constexpr size_t OFF_CTL  = 0;
constexpr size_t OFF_WFT  = 4096 + 4 * 1024 * 1024;
constexpr size_t OFF_F    = OFF_WFT + al((size_t)(NCOL + 2) * WST);
constexpr size_t OFF_X1   = OFF_F + al((size_t)NCOL * FST_);
constexpr size_t OFF_X2   = OFF_X1 + al((size_t)S_ * D_ * 2);
constexpr size_t OFF_FL   = OFF_X2 + al((size_t)S_ * D_ * 2);
constexpr int NHF = 5;
constexpr size_t OFF_H    = OFF_FL + 3 * (size_t)NCOL * GST + NHF * 40960;
#define ZPAD 0
constexpr size_t OFF_Z    = OFF_H + al((size_t)S_ * D_ * 2) + ZTSEG * 2 + ZPAD;
constexpr size_t OFF_Z2   = OFF_Z + al(ZTSEG * NSEG * 2);
constexpr size_t OFF_PT   = OFF_Z2 + al((size_t)NSEG * NOWN * 8192);
constexpr size_t OFF_QK   = OFF_PT + al((size_t)NSEG * PTSEG * 2);
constexpr size_t OFF_WIN  = OFF_QK + (size_t)NSEG * H_ * 16384;
constexpr size_t OFF_END  = OFF_WIN + (size_t)6 * 1024 * 1024;
static_assert(OFF_END <= (size_t)256 * 1024 * 1024, "workspace");
constexpr unsigned O_FL = (unsigned)OFF_FL, O_ARM = O_FL - 256 * D_ * 2, O_ARM1 = O_FL + 2 * NCOL * GST, O_WFT = (unsigned)OFF_WFT, O_F = (unsigned)OFF_F, O_H = (unsigned)OFF_H, O_X1 = (unsigned)OFF_X1, O_Z = (unsigned)OFF_Z, O_Z2 = (unsigned)OFF_Z2, O_PT = (unsigned)OFF_PT, O_QK = (unsigned)OFF_QK;

constexpr double PI_ = 3.14159265358979323846;
constexpr double tay(double x, int s) {
    double t = s ? x : 1.0, r = t;
    for (int i = s + 1; i < 18; i += 2) { t *= -x * x / (double)(i * (i + 1)); r += t; }
    return r;
}
struct Cs { float c, s; };
constexpr Cs cis(int m, int n) {
    m %= n;
    const int q = (m * 8 / n + 1) / 2;
    const double x = 2.0 * PI_ * (double)(4 * m - q * n) / (double)(4 * n), c = tay(x, 0), s = tay(x, 1);
    return (q & 3) == 0 ? Cs{(float)c, (float)-s} : (q & 3) == 1 ? Cs{(float)-s, (float)-c} : (q & 3) == 2 ? Cs{(float)-c, (float)s} : Cs{(float)s, (float)c};
}
template <int NI> struct alignas(16) Tw7 { Cs v[4][NI][2]; };
template <int NI, int N, int KIND> constexpr Tw7<NI> mk_tw() {
    Tw7<NI> r{};
    for (int i = 0; i < NI; ++i)
        for (int q = 1; q <= 8; ++q) r.v[(q - 1) >> 1][i][(q - 1) & 1] = cis((KIND == 0 ? i : KIND == 1 ? 8 * (i >> 3) : (i >> 3) + 10 * (i & 7)) * q, N);
    return r;
}
struct TwCol { Tw7<256> a; Tw7<32> b; Tw7<4> c; };
static __device__ const TwCol TWC = {mk_tw<256, 2048, 0>(), mk_tw<32, 256, 0>(), mk_tw<4, 32, 0>()};
static __device__ const Tw7<80> TWR2 = mk_tw<80, 640, 1>();
static __device__ const Tw7<80> TWR3 = mk_tw<80, 640, 2>();

#define st128(v, r, vo, so, aux) do { u32x4 d_ = (v); asm volatile("" : "+v"(d_)); __builtin_amdgcn_raw_buffer_store_b128(d_, r, vo, so, aux); asm volatile("s_nop 1" :: "v"(d_)); } while (0)
__device__ __forceinline__ float bf2f(u16 v) { return __uint_as_float((unsigned)v << 16); }
__device__ __forceinline__ u16 f2bf(float f) { __bf16 b = (__bf16)f; return __builtin_bit_cast(u16, b); }
__device__ __forceinline__ float bfr(float f) { return (float)(__bf16)f; }
typedef __attribute__((ext_vector_type(2))) float f32x2;
typedef __attribute__((ext_vector_type(2))) __bf16 bf16x2;
__device__ __forceinline__ unsigned pack2(float a, float b) { return __builtin_bit_cast(unsigned, __builtin_convertvector((f32x2){a, b}, bf16x2)); }
__device__ __forceinline__ float lo2f(unsigned v) { return __uint_as_float(v << 16); }
__device__ __forceinline__ float hi2f(unsigned v) { return __uint_as_float(v & 0xFFFF0000u); }
__device__ __forceinline__ float fmx(float a, float b) { return __builtin_elementwise_maximum(a, b); }
__device__ __forceinline__ f32x4 mfma32(u32x4 a, u32x4 b, f32x4 c) {
    return __builtin_amdgcn_mfma_f32_16x16x32_bf16(__builtin_bit_cast(bf16x8, a), __builtin_bit_cast(bf16x8, b), c, 0, 0, 0);
}
__device__ __forceinline__ f32x4 mfma16(u32x2 a, u32x2 b, f32x4 c) {
    return __builtin_amdgcn_mfma_f32_16x16x16bf16_1k(__builtin_bit_cast(s16x4, a), __builtin_bit_cast(s16x4, b), c, 0, 0, 0);
}
__device__ __forceinline__ f32x4 smfmac(u32x4 a, u32x8 b, f32x4 c, int idx) {
    return __builtin_amdgcn_smfmac_f32_16x16x64_bf16(__builtin_bit_cast(bf16x8, a), __builtin_bit_cast(bf16x16, b), c, idx, 0, 0);
}
__device__ __forceinline__ int pi16(int k) { return ((k & 3) << 2) | ((k >> 1) & 2) | (k >> 3); }
__device__ __forceinline__ float addswap32(float a, float b) {
    auto r = __builtin_amdgcn_permlane32_swap(__float_as_uint(a), __float_as_uint(b), false, false);
    return __uint_as_float(r[0]) + __uint_as_float(r[1]);
}
__device__ __forceinline__ float xmax3216(float v) {
    auto r = __builtin_amdgcn_permlane32_swap(__float_as_uint(v), __float_as_uint(v), false, false);
    v = fmx(__uint_as_float(r[0]), __uint_as_float(r[1]));
    auto q = __builtin_amdgcn_permlane16_swap(__float_as_uint(v), __float_as_uint(v), false, false);
    return fmx(__uint_as_float(q[0]), __uint_as_float(q[1]));
}
__device__ __forceinline__ float xsum3216(float v) {
    auto r = __builtin_amdgcn_permlane32_swap(__float_as_uint(v), __float_as_uint(v), false, false);
    v = __uint_as_float(r[0]) + __uint_as_float(r[1]);
    auto q = __builtin_amdgcn_permlane16_swap(__float_as_uint(v), __float_as_uint(v), false, false);
    return __uint_as_float(q[0]) + __uint_as_float(q[1]);
}
template <int CTRL> __device__ __forceinline__ float dppf(float v) { return __uint_as_float((unsigned)__builtin_amdgcn_update_dpp(0, (int)__float_as_uint(v), CTRL, 0xF, 0xF, false)); }
__device__ __forceinline__ float wsum(float v) {
    v += dppf<0xB1>(v); v += dppf<0x4E>(v); v += dppf<0x141>(v); v += dppf<0x140>(v);
    return xsum3216(v);
}
__device__ __forceinline__ unsigned ror8(unsigned v) { return (unsigned)__builtin_amdgcn_update_dpp(0, (int)v, 0x128, 0xF, 0xF, false); }
__device__ __forceinline__ float ror8f(float v) { return __uint_as_float(ror8(__float_as_uint(v))); }
__device__ __forceinline__ unsigned nosent(unsigned v) {
    u16x2 lim = {0xFFFE, 0xFFFE};
    return __builtin_bit_cast(unsigned, __builtin_elementwise_min(__builtin_bit_cast(u16x2, v), lim));
}
__device__ __forceinline__ unsigned pkmax(unsigned a, unsigned b) {
    return __builtin_bit_cast(unsigned, __builtin_elementwise_max(__builtin_bit_cast(u16x2, a), __builtin_bit_cast(u16x2, b)));
}
__device__ __forceinline__ bool has_sent(u32x4 v) {
    unsigned m = pkmax(pkmax(v.x, v.y), pkmax(v.z, v.w));
    return (m & 0xFFFFu) == 0xFFFFu || (m >> 16) == 0xFFFFu;
}

__device__ __forceinline__ unsigned nosent1(unsigned v) { return min(v, 0xFFFFFFFEu); }
__device__ __forceinline__ unsigned smax(u32x4 v) { return max(v[1], v[3]); }
__device__ __forceinline__ u32x4 sent4() { unsigned x; asm volatile("v_mov_b32 %0, -1" : "=v"(x)); return (u32x4){x, x, x, x}; }
#define SENT sent4()
__device__ __forceinline__ f32x4 zero4() { f32x2 x, y; asm volatile("v_mov_b64 %0, 0\n\tv_mov_b64 %1, 0" : "=v"(x), "=v"(y)); return (f32x4){x[0], x[1], y[0], y[1]}; }
__device__ __forceinline__ u32x4 nosent4(u32x4 v) { return (u32x4){nosent1(v[0]), nosent1(v[1]), nosent1(v[2]), nosent1(v[3])}; }
#define SPIN_CAP 400000u
__device__ __forceinline__ void set_tmo(__amdgpu_buffer_rsrc_t rwin) { __builtin_amdgcn_raw_buffer_store_b32(0u, rwin, 0u, W_TMO, 16); }
__device__ __forceinline__ bool spin_fail(unsigned& spins, __amdgpu_buffer_rsrc_t rwin) {
    if (++spins > SPIN_CAP) { set_tmo(rwin); return true; }
    return (spins & 1023u) == 0u && __builtin_amdgcn_readfirstlane(__builtin_amdgcn_raw_buffer_load_b32(rwin, 0u, W_TMO, 16)) == 0u;
}
#define GATE 2
#define GATX 2
__device__ __forceinline__ void tell_at(__amdgpu_buffer_rsrc_t rwin, unsigned so, unsigned d) {
    __builtin_amdgcn_raw_buffer_store_b32(((unsigned)__builtin_amdgcn_s_memrealtime() + d) & 0xFFFFFFu, rwin, 0u, so, 16);
}
template <int N> __device__ __forceinline__ void wait_told(__amdgpu_buffer_rsrc_t rwin, const unsigned (&vo)[N], unsigned so, int slp) {
    unsigned v[N];
#pragma unroll
    for (int g = 0; g < N; ++g) v[g] = ~0u;
    for (unsigned spins = 0;;) {
        asm volatile("" ::: "memory");
        unsigned x[N], m = 0;
#pragma unroll
        for (int g = 0; g < N; ++g) x[g] = __builtin_amdgcn_raw_buffer_load_b32(rwin, v[g] == ~0u ? vo[g] : 0x40000000u, so, 16);
#pragma unroll
        for (int g = 0; g < N; ++g) { if (v[g] == ~0u) v[g] = x[g]; m = max(m, v[g]); }
        if (!__any(m == ~0u) || spin_fail(spins, rwin)) break;
        if (slp) __builtin_amdgcn_s_sleep(8); else __builtin_amdgcn_s_sleep(3);
    }
    for (int i = 0; i < 400; ++i) {
        const unsigned t = (unsigned)__builtin_amdgcn_s_memrealtime();
        bool e = false;
#pragma unroll
        for (int g = 0; g < N; ++g) e |= ((v[g] - t) << 8) - 1u < 0x7FFFFFFFu;
        if (!__any(e)) break;
    }
}
#define TG 250
#define TH 155
#define TH0 185
#define DVT0 950
#define A2P 0
#define TWG 1
#define TRK 4
#define X2W 2
#define PS1 3
#define PS2 3
#define PS3 2

typedef f32x2 c2;
__device__ __forceinline__ c2 swp(c2 a) { return __builtin_shufflevector(a, a, 1, 0); }
__device__ __forceinline__ c2 fm(c2 a, c2 b, c2 c) { return __builtin_elementwise_fma(a, b, c); }
__device__ __forceinline__ c2 bc(float x) { return (c2){x, x}; }
__device__ __forceinline__ c2 sgn() { c2 s = {-1.f, 1.f}; asm("" : "+s"(s)); return s; }
template <int SG> __device__ __forceinline__ c2 addi(c2 a, c2 b) { return fm(swp(b), SG > 0 ? sgn() : swp(sgn()), a); }
template <int SG> __device__ __forceinline__ c2 cfin(c2 a, c2 w, c2 t) {
    c2 d;
    if (SG < 0) asm("v_pk_fma_f32 %0, %1, %2, %3 op_sel:[1,1,0] op_sel_hi:[0,1,1] neg_lo:[1,0,0]" : "=v"(d) : "v"(a), "v"(w), "v"(t));
    else asm("v_pk_fma_f32 %0, %1, %2, %3 op_sel:[1,1,0] op_sel_hi:[0,1,1] neg_hi:[1,0,0]" : "=v"(d) : "v"(a), "v"(w), "v"(t));
    return d;
}
struct Tw { f32x4 v[4]; };
template <int NI> __device__ __forceinline__ void ldtw(Tw& t, const Tw7<NI>& tab, int i) {
#pragma unroll
    for (int j = 0; j < 4; ++j) t.v[j] = *(const f32x4*)tab.v[j][i];
}
template <int NI> __device__ __forceinline__ void ldtw(Tw& t, const LAS f32x4* p) {
#pragma unroll
    for (int j = 0; j < 4; ++j) t.v[j] = p[j * NI];
}
template <int SG> __device__ __forceinline__ void twid(c2 (&u)[8], const Tw& t) {
    c2 w[8], m[8];
#pragma unroll
    for (int r = 1; r < 8; ++r) {
        const f32x4 x = t.v[(r - 1) >> 1];
        w[r] = r & 1 ? (c2){x[0], x[1]} : (c2){x[2], x[3]};
        m[r] = u[r] * bc(w[r].x);
    }
    if (TWG) asm("" : "+v"(m[1]), "+v"(m[2]), "+v"(m[3]), "+v"(m[4]), "+v"(m[5]), "+v"(m[6]), "+v"(m[7]));
#pragma unroll
    for (int r = 1; r < 8; ++r) u[r] = cfin<SG>(u[r], w[r], m[r]);
}
template <int SG> __device__ __forceinline__ void dft4(c2& a, c2& b, c2& c, c2& d) {
    const c2 t0 = a + c, t1 = a - c, t2 = b + d, t3 = b - d;
    a = t0 + t2; c = t0 - t2; b = addi<SG>(t1, t3); d = addi<-SG>(t1, t3);
}
template <int SG> __device__ __forceinline__ void dft8(c2 (&u)[8]) {
    c2 e0 = u[0], e1 = u[2], e2 = u[4], e3 = u[6], o0 = u[1], o1 = u[3], o2 = u[5], o3 = u[7];
    dft4<SG>(e0, e1, e2, e3); dft4<SG>(o0, o1, o2, o3);
    const c2 r = bc(0.70710678118654752440f);
    o1 = addi<SG>(o1, o1); o3 = addi<-SG>(o3, o3);
    u[0] = e0 + o0; u[4] = e0 - o0;
    u[1] = fm(o1, r, e1); u[5] = fm(o1, -r, e1);
    u[2] = addi<SG>(e2, o2); u[6] = addi<-SG>(e2, o2);
    u[3] = fm(o3, -r, e3); u[7] = fm(o3, r, e3);
}
template <int SG> __device__ __forceinline__ void dft5(c2& x0, c2& x1, c2& x2, c2& x3, c2& x4) {
    const c2 c1 = bc(0.30901699437494742410f), c2_ = bc(-0.80901699437494742410f), s1 = bc(0.95105651629515357212f), s2 = bc(0.58778525229247312917f);
    const c2 t1 = x1 + x4, t2 = x2 + x3, t3 = x1 - x4, t4 = x2 - x3;
    const c2 a1 = fm(c2_, t2, fm(c1, t1, x0)), a2 = fm(c1, t2, fm(c2_, t1, x0));
    const c2 b1 = fm(s2, t4, s1 * t3), b2 = fm(-s1, t4, s2 * t3);
    x0 = x0 + t1 + t2;
    x1 = addi<SG>(a1, b1); x4 = addi<-SG>(a1, b1); x2 = addi<SG>(a2, b2); x3 = addi<-SG>(a2, b2);
}
template <int SG> __device__ __forceinline__ void dft10(c2 (&u)[10]) {
    c2 e0 = u[0], e1 = u[2], e2 = u[4], e3 = u[6], e4 = u[8], o0 = u[5], o1 = u[7], o2 = u[9], o3 = u[1], o4 = u[3];
    dft5<SG>(e0, e1, e2, e3, e4); dft5<SG>(o0, o1, o2, o3, o4);
    u[0] = e0 + o0; u[5] = e0 - o0; u[6] = e1 + o1; u[1] = e1 - o1; u[2] = e2 + o2; u[7] = e2 - o2; u[8] = e3 + o3; u[3] = e3 - o3; u[4] = e4 + o4; u[9] = e4 - o4;
}
__device__ __forceinline__ void swap32(c2& v) { auto r = __builtin_amdgcn_permlane32_swap(__float_as_uint(v.x), __float_as_uint(v.y), false, false); v = (c2){__uint_as_float(r[0]), __uint_as_float(r[1])}; }
template <int B> __device__ __forceinline__ void swapl(c2& a, c2& b) {
#pragma unroll
    for (int i = 0; i < 2; ++i) {
        auto r = B == 5 ? __builtin_amdgcn_permlane32_swap(__float_as_uint(a[i]), __float_as_uint(b[i]), false, false) : __builtin_amdgcn_permlane16_swap(__float_as_uint(a[i]), __float_as_uint(b[i]), false, false);
        a[i] = __uint_as_float(r[0]); b[i] = __uint_as_float(r[1]);
    }
}
__device__ __forceinline__ c2 unpk(unsigned v) { return (c2){lo2f(v), hi2f(v)}; }
__device__ __forceinline__ float hsum32(c2 s) {
    float v = s.x + s.y;
    v += dppf<0xB1>(v); v += dppf<0x4E>(v); v += dppf<0x141>(v); v += dppf<0x140>(v);
    auto q = __builtin_amdgcn_permlane16_swap(__float_as_uint(v), __float_as_uint(v), false, false);
    return __uint_as_float(q[0]) + __uint_as_float(q[1]);
}
__device__ __forceinline__ void ln_row(const c2 (&v)[10], unsigned (&o)[10]) {
    const c2 s = ((v[0] + v[1]) + (v[2] + v[3])) + ((v[4] + v[5]) + (v[6] + v[7])) + (v[8] + v[9]);
    const float mean = hsum32(s) * (1.f / 640.f);
    c2 q0 = bc(0.f), q1 = bc(0.f);
#pragma unroll
    for (int r = 0; r < 10; r += 2) { const c2 d = v[r] - bc(mean), e = v[r + 1] - bc(mean); q0 = fm(d, d, q0); q1 = fm(e, e, q1); }
    const float mb = bfr(mean);
    c2 d[10];
#pragma unroll
    for (int r = 0; r < 10; ++r) { const c2 x = v[r] - bc(mb); d[r] = unpk(pack2(x.x, x.y)); }
    const float var = hsum32(q0 + q1) * (1.f / 640.f);
    const float rs = __builtin_amdgcn_rcpf(bfr(sqrtf(bfr(bfr(var) + 1e-5f))));
#pragma unroll
    for (int r = 0; r < 10; ++r) { const c2 x = d[r] * bc(rs); o[r] = pack2(x.x, x.y); }
}

__device__ __forceinline__ __amdgpu_buffer_rsrc_t mkrsrc(const void* p, unsigned bytes) {
    return __builtin_amdgcn_make_buffer_rsrc((void*)p, 0, bytes, 0x00020000);
}
__device__ __forceinline__ unsigned xcc_id() { unsigned x; asm volatile("s_getreg_b32 %0, hwreg(HW_REG_XCC_ID)" : "=s"(x)); return x & 15u; }
#define VMWAIT() do { asm volatile("" ::: "memory"); __builtin_amdgcn_s_waitcnt(0x0F70); asm volatile("" ::: "memory"); } while (0)
#define BAR() asm volatile("s_waitcnt lgkmcnt(0)\n\ts_barrier" ::: "memory")
#define BAR0() asm volatile("s_barrier" ::: "memory")

#define FST(k) do {} while (0)
#define FSW(k) do {} while (0)
extern __shared__ __attribute__((aligned(16))) char smem[];
#define RST(k, lay, wv, sel) do {} while (0)
constexpr int R_C = 8 * 10 * 72 * 8;
constexpr float FSC = 0.5f / ((float)S_ * (float)D_);
__device__ __forceinline__ c2 ldv(const LAS void* p) { return *(const volatile LAS c2*)p; }
struct RowTw { Tw a, b; };
__device__ __forceinline__ void row_tw(RowTw& t, int tid) { const int i = tid % 80; ldtw(t.a, TWR2, i); ldtw(t.b, TWR3, i); }
template <int NF, bool LATE> __device__ __forceinline__ void a1_unit(__amdgpu_buffer_rsrc_t rx, int row0, __amdgpu_buffer_rsrc_t rws, __amdgpu_buffer_rsrc_t rwin, RowTw& tw, int tid, unsigned (&fs)[12]) {
    const int wave = __builtin_amdgcn_readfirstlane(tid >> 6), lane = tid & 63;
    LAS c2* A = (LAS c2*)smem; LAS c2* C = (LAS c2*)(smem + R_C);
    FST(0);
    if (wave < NF) {
        unsigned d[10];
        for (unsigned spins = 0;;) {
            asm volatile("" ::: "memory");
#pragma unroll
            for (int r = 0; r < 10; ++r) d[r] = __builtin_amdgcn_raw_buffer_load_b32(rx, (lane & 31) * 4u + (lane >> 5) * (D_ * 2u) + r * 128u, (unsigned)(row0 + 2 * wave) * (D_ * 2), 16);
            if (!LATE) break;
            unsigned m = d[0];
#pragma unroll
            for (int r = 1; r < 10; ++r) m = max(m, d[r]);
            if (!__any(m == ~0u) || spin_fail(spins, rwin)) break;
            __builtin_amdgcn_s_sleep(PS1);
        }
        c2 u[10];
#pragma unroll
        for (int r = 0; r < 10; ++r) u[r] = unpk(d[r]);
        FST(1);
        if (LATE) RST(16, 0, wave, 0);
        ln_row(u, d);
        FST(2);
#pragma unroll
        for (int r = 0; r < 10; ++r) { u[r] = unpk(d[r]); swap32(u[r]); }
        dft10<-1>(u);
#pragma unroll
        for (int r = 0; r < 10; ++r) A[(wave * 10 + r) * 72 + (lane & 31) + 36 * (lane >> 5)] = u[r];
        FST(3);
    }
    BAR();
    if (NF == 4 && !LATE) row_tw(tw, tid);
    FST(4);
#pragma unroll
    for (int c = 0; c < (NF > 4 ? 2 : 1); ++c) {
        if (tid < (NF > 4 ? (c ? 160 : 480) : 80 * NF)) {
            const int K = (tid >> 3) + 60 * c, m = tid & 7;
            LAS c2* a = A + K * 72;
            c2 u[8];
#pragma unroll
            for (int q = 0; q < 8; ++q) u[q] = ldv(a + (m >> 1) + 36 * (m & 1) + 4 * q);
            twid<-1>(u, tw.a);
            dft8<-1>(u);
#pragma unroll
            for (int p = 0; p < 8; ++p) a[9 * p + m] = u[p];
            asm volatile("" ::: "memory");
#pragma unroll
            for (int q = 0; q < 8; ++q) u[q] = ldv(a + 9 * m + q);
            twid<-1>(u, tw.b);
            dft8<-1>(u);
            LAS c2* o = C + (K / 10) * 638 + K + 10 * m;
#pragma unroll
            for (int p = 0; p < 8; ++p) o[80 * p] = u[p];
        }
    }
    FST(5);
    BAR();
    FST(6);
#pragma unroll
    for (int c = 0; c < (NF > 4 ? 5 : 3); ++c) {
        const int it = tid + 512 * c, kp = it / (2 * NF), row = it % (2 * NF);
        if (NF == 4 && !LATE && row0 < 1600) for (int i = 0; i < (c ? 8 : wave); ++i) __builtin_amdgcn_s_sleep(TRK);
        if (NF > 4 || c < 2 || tid < 256) {
            const LAS c2* z = C + (row >> 1) * 648;
            const f32x4 zk = *(const LAS f32x4*)(z + 2 * kp);
            const c2 n0 = ldv(z + (kp ? 640 - 2 * kp : 320)), n1 = ldv(z + 639 - 2 * kp);
            const bool odd = it & 1;
            f32x4 o = odd ? (f32x4){zk[1] + n0.y, n0.x - zk[0], zk[3] + n1.y, n1.x - zk[2]} : (f32x4){zk[0] + n0.x, zk[1] - n0.y, zk[2] + n1.x, zk[3] - n1.y};
            if (c == 0 && kp == 0) { o[0] = 2.f * (odd ? zk[1] : zk[0]); o[1] = 2.f * (odd ? n0.y : n0.x); }
            u32x4 ob = __builtin_bit_cast(u32x4, o * FSC);
            if (LATE) { ob[1] = nosent1(ob[1]); ob[3] = nosent1(ob[3]); }
            st128(ob, rws, kp * (LATE ? GST : FST_) + row * 16u, (LATE ? O_FL - (S_ - 256) * 16u : O_F) + (unsigned)row0 * 16u, 16);
        }
    }
    FST(7);
}
template <class FG> __device__ __forceinline__ void a3_unit(__amdgpu_buffer_rsrc_t rx, __amdgpu_buffer_rsrc_t ro, int row0, int layer, __amdgpu_buffer_rsrc_t rws, __amdgpu_buffer_rsrc_t rwin, const RowTw& tw, int tid, FG go, unsigned (&fs)[12]) {
    const int wave = __builtin_amdgcn_readfirstlane(tid >> 6), lane = tid & 63;
    LAS c2* A = (LAS c2*)smem; LAS c2* C = (LAS c2*)(smem + R_C);
    const unsigned n0 = (lane & 7) | ((lane & 24) << 1);
    c2 x[10];
    if (wave < 4) {
        const unsigned base = (unsigned)(row0 + 2 * wave) * (D_ * 2), vo = n0 * 2u + (lane >> 5) * (D_ * 2u);
#pragma unroll
        for (int r = 0; r < 10; ++r) x[r] = (c2){bf2f(__builtin_amdgcn_raw_buffer_load_b16(rx, vo + 128u * r, base, 16)), bf2f(__builtin_amdgcn_raw_buffer_load_b16(rx, vo + 16u + 128u * r, base, 16))};
    }
    const bool early = row0 < 256;
    if (!early) go();
    else if (TG) {
        if (wave == 7) { const unsigned vo[3] = {lane * 128u, lane * 128u + 8192u, min(lane, 31) * 128u + 16384u}; wait_told(rwin, vo, W_COL + (unsigned)layer * W_LAYER + 4u, 1); }
        BAR0();
    }
    FST(0);
    u32x4 fv[3];
    {
        const unsigned vo = (tid >> 3) * (early ? GST : FST_) + (tid & 7) * 16u, st = (early ? GST : FST_) * 64u, so = (early ? O_FL + (unsigned)(2 - layer) * (NCOL * GST) : O_F) + (unsigned)row0 * 16u;
#pragma unroll
        for (int c = 0; c < 3; ++c) fv[c] = __builtin_amdgcn_raw_buffer_load_b128(rws, vo, c < 2 || wave < 4 ? so + c * st : 0x40000000u, 16);
        if (early) for (unsigned spins = 0;;) {
            if (!__any(max(max(smax(fv[0]), smax(fv[1])), smax(fv[2])) == ~0u) || spin_fail(spins, rwin)) break;
            __builtin_amdgcn_s_sleep(PS3);
            asm volatile("" ::: "memory");
            u32x4 x[3];
#pragma unroll
            for (int c = 0; c < 3; ++c) x[c] = __builtin_amdgcn_raw_buffer_load_b128(rws, smax(fv[c]) == ~0u ? vo : 0x40000000u, so + c * st, 16);
#pragma unroll
            for (int c = 0; c < 3; ++c) if (smax(fv[c]) == ~0u) fv[c] = x[c];
        }
    }
#pragma unroll
    for (int c = 0; c < 3; ++c) {
        const int it = tid + 512 * c, kp = it >> 3;
        if (c < 2 || tid < 256) {
            const bool odd = it & 1;
            const f32x4 v = __builtin_bit_cast(f32x4, fv[c]), p = {dppf<0xB1>(v[0]), dppf<0xB1>(v[1]), dppf<0xB1>(v[2]), dppf<0xB1>(v[3])};
            c2 z0 = odd ? (c2){p[0] + v[1], v[0] - p[1]} : (c2){v[0] - p[1], p[0] + v[1]};
            const c2 z1 = odd ? (c2){p[2] + v[3], v[2] - p[3]} : (c2){v[2] - p[3], p[2] + v[3]};
            int i0 = odd ? 640 - 2 * kp : 2 * kp;
            if (c == 0 && kp == 0) { z0 = odd ? (c2){p[1], v[1]} : (c2){v[0], p[0]}; i0 >>= 1; }
            LAS c2* z = C + ((it >> 1) & 3) * 648;
            z[i0] = z0; z[odd ? 639 - 2 * kp : 2 * kp + 1] = z1;
        }
    }
    FST(1);
    BAR();
    if (early && TH && wave == 7) tell_at(rwin, W_H + (unsigned)layer * W_LAYER + (unsigned)((row0 >> 5) * 128 + 16 + ((row0 >> 3) & 3) * 4), layer ? TH : TH0);
    if (early) RST(3, layer, wave, 0);
    FST(2);
    if (tid < 320) {
        const int K = tid >> 3, m = tid & 7;
        LAS c2* a = A + K * 72;
        const LAS c2* o = C + (K / 10) * 638 + K + 10 * m;
        c2 u[8];
#pragma unroll
        for (int p = 0; p < 8; ++p) u[p] = ldv(o + 80 * p);
        dft8<1>(u);
        twid<1>(u, tw.b);
#pragma unroll
        for (int q = 0; q < 8; ++q) a[9 * q + m] = u[q];
        asm volatile("" ::: "memory");
#pragma unroll
        for (int p = 0; p < 8; ++p) u[p] = ldv(a + 9 * m + p);
        dft8<1>(u);
        twid<1>(u, tw.a);
#pragma unroll
        for (int q = 0; q < 8; ++q) a[m + 8 * (q >> 1) + 32 * (q & 1)] = u[q];
    }
    FST(3);
    BAR();
    FST(4);
    if (wave < 4) {
        c2 u[10];
#pragma unroll
        for (int r = 0; r < 10; ++r) u[r] = ldv(A + (wave * 10 + r) * 72 + lane);
        dft10<1>(u);
        unsigned x1[10], h[10];
#pragma unroll
        for (int r = 0; r < 10; ++r) {
            swap32(u[r]);
            const c2 y = x[r] + unpk(pack2(u[r].x, u[r].y));
            x1[r] = pack2(y.x, y.y);
            u[r] = unpk(x1[r]);
        }
        FST(5);
        ln_row(u, h);
        FST(6);
        LAS char* st = (LAS char*)C + wave * 5120;
        LAS char* hp = st + (lane >> 5) * 1280 + (n0 & 48) * 2 + pi16(n0 & 15) * 2;
#pragma unroll
        for (int r = 0; r < 10; ++r) *(LAS unsigned*)(hp + 128 * r) = nosent1(h[r]);
#pragma unroll
        for (int c = 0; c < 3; ++c) {
            if (c < 2 || lane < 32) {
                const int id = c * 64 + lane, rr = id >= 80, ch = id - 80 * rr, row = (row0 + 2 * wave + rr) & 31;
                st128(*(const LAS u32x4*)(st + id * 16), rws, (unsigned)((ch >> 3) * 4096 + row * 128 + (((ch & 7) ^ (row & 7)) << 4)), O_H + (unsigned)((row0 >> 5) - NHF * layer) * 40960u, 16);
            }
        }
        if (early) RST(4, layer, wave, 0);
        LAS char* xp = st + 2560 + (lane >> 5) * 1280 + n0 * 2;
#pragma unroll
        for (int r = 0; r < 10; ++r) { *(LAS u16*)(xp + 128 * r) = (u16)x1[r]; *(LAS u16*)(xp + 128 * r + 16) = (u16)(x1[r] >> 16); }
#pragma unroll
        for (int c = 0; c < 3; ++c)
            if (c < 2 || lane < 32) st128(*(const LAS u32x4*)(st + 2560 + (c * 64 + lane) * 16), rws, (c * 64 + lane) * 16u, O_X1 + (unsigned)(row0 + 2 * wave) * (D_ * 2), 16);
        FST(7);
    } else if (X2W > layer) {

#pragma unroll
        for (int c = 0; c < 3; ++c)
            if (c < 2 || lane < 32) st128(SENT, ro, (c * 64 + lane) * 16u, (unsigned)(row0 + 2 * (wave - 4)) * (D_ * 2), 16);
    }
}

constexpr int C_T = 8 * 544 * 8, C_B = C_T + (int)sizeof(TwCol), C_W = C_B + 32768, C_END = C_W + 8 * 516 * 4;
template <class FG> __device__ __forceinline__ void a2_unit(int kp, int late, __amdgpu_buffer_rsrc_t rws, __amdgpu_buffer_rsrc_t rwin, int tid, FG go, unsigned (&fs)[12]) {
    const int w = __builtin_amdgcn_readfirstlane(tid >> 6), l = tid & 63;
    const unsigned fb = O_F + (unsigned)kp * FST_, wb = O_WFT + (unsigned)kp * WST;
    const int b = (l >> 1) & 7;
    const int k0 = w + 8 * b + 128 * (l >> 4);
    c2 u[8];
    u32x4 wv[2];
    {
        f32x4 tv[3];
#pragma unroll
        for (int i = 0; i < 3; ++i) tv[i] = ((const f32x4*)&TWC)[min(tid + 512 * i, (int)sizeof(TwCol) / 16 - 1)];
        go(0);
#pragma unroll
        for (int i = 0; i < 3; ++i) ((LAS f32x4*)(smem + C_T))[min(tid + 512 * i, (int)sizeof(TwCol) / 16 - 1)] = tv[i];
        BAR();
        FST(0);
        u32x4 x[4];
#pragma unroll
        for (int r = 0; r < 4; ++r) x[r] = __builtin_amdgcn_raw_buffer_load_b128(rws, (tid >> 1) * 16u + (tid & 1) * 4096u, fb + r * 8192u, 16);
#pragma unroll
        for (int r = 0; r < 2; ++r) wv[r] = __builtin_amdgcn_raw_buffer_load_b128(rws, tid * 16u, wb + r * 8192u, 16);
#pragma unroll
        for (int r = 0; r < 4; ++r) {
            const f32x4 y = __builtin_bit_cast(f32x4, x[r]);
            const c2 z = {dppf<0xB1>(l & 1 ? y[0] : y[2]), dppf<0xB1>(l & 1 ? y[1] : y[3])};
            u[2 * r] = l & 1 ? z : (c2){y[0], y[1]};
            u[2 * r + 1] = l & 1 ? (c2){y[2], y[3]} : z;
        }
    }
    const LAS f32x4* t1 = (const LAS f32x4*)(smem + C_T) + (tid >> 1);
    const LAS f32x4* t2 = (const LAS f32x4*)(smem + C_T + sizeof(Tw7<256>)) + (l >> 1);
    const LAS f32x4* t3 = (const LAS f32x4*)(smem + C_T + sizeof(Tw7<256>) + sizeof(Tw7<32>)) + (l >> 4);
    LAS c2* e0 = (LAS c2*)smem + tid;
    LAS c2* e1 = (LAS c2*)smem + w * 544 + l;
    LAS c2* e2 = (LAS c2*)smem + w * 544 + 2 * (l >> 4) + (l & 1);
    Tw tw;
    ldtw<256>(tw, t1);
    if (late) {
        go(2);
        FST(1);
        for (unsigned spins = 0;;) {
            asm volatile("" ::: "memory");
            const u32x2 x = __builtin_amdgcn_raw_buffer_load_b64(rws, tid * 8u, O_FL + (unsigned)kp * GST, 16);
            u[7] = __builtin_bit_cast(c2, x);
            if (!__any(x[1] == ~0u) || spin_fail(spins, rwin)) break;
            if (w < 3) __builtin_amdgcn_s_sleep(16); else __builtin_amdgcn_s_sleep(PS2);
        }
    }
    FST(2);
    dft8<-1>(u);
    twid<-1>(u, tw);
    if (kp == 0) {
#pragma unroll
        for (int i = 0; i < 4; ++i)
            asm volatile("s_mov_b32 m0, %3\n\ts_nop 3\n\tbuffer_load_dwordx4 %0, %1, %2 offen sc1 lds" :: "v"(l * 16u), "s"(rws), "s"(O_WFT + NCOL * WST + (w * 4 + i) * 1024u), "s"(C_B + (w * 4 + i) * 1024u), "v"(u[7]) : "m0", "memory");
    }
#pragma unroll
    for (int r = 0; r < 8; ++r) e0[544 * r] = u[r];
    ldtw<32>(tw, t2);
    BAR();
    if (A2P == 1 && w < 4) __builtin_amdgcn_s_setprio(2);
    if (A2P == 2 && (w & 1)) __builtin_amdgcn_s_setprio(2);
    if (A2P == 3 && w >= 4) __builtin_amdgcn_s_sleep(4);
    if (TG && w == 0) tell_at(rwin, W_COL + (unsigned)late * W_LAYER + (unsigned)kp * 128u + 4u, kp ? TG : TG + 30);
#pragma unroll
    for (int r = 0; r < 8; ++r) u[r] = ldv(e1 + 64 * r);
    dft8<-1>(u);
    twid<-1>(u, tw);
#pragma unroll
    for (int r = 0; r < 8; ++r) e1[68 * r] = u[r];
    asm volatile("" ::: "memory");
#pragma unroll
    for (int r = 0; r < 8; ++r) u[r] = ldv(e2 + 68 * b + 8 * r);
    ldtw<4>(tw, t3);
    dft8<-1>(u);
    twid<-1>(u, tw);
    c2 f[8];
#pragma unroll
    for (int r = 0; r < 8; ++r) f[r] = unpk(wv[r >> 2][r & 3]);
    asm volatile("s_nop 1" : "+v"(u[1]), "+v"(u[2]), "+v"(u[3]), "+v"(u[4]), "+v"(u[5]), "+v"(u[6]), "+v"(u[7]));
#pragma unroll
    for (int r = 0; r < 4; ++r) swapl<5>(u[r], u[r + 4]);
#pragma unroll
    for (int r = 0; r < 4; ++r) swapl<4>(u[(r & 1) + 2 * (r & 2)], u[(r & 1) + 2 * (r & 2) + 2]);
    dft4<-1>(u[0], u[2], u[4], u[6]); dft4<-1>(u[1], u[3], u[5], u[7]);
    FST(3);
    if (kp == 0) {
        LAS c2* z = (LAS c2*)(smem + C_W);
#pragma unroll
        for (int r = 0; r < 8; ++r) if (!(l & 1)) z[w * 258 + b + 16 * (l >> 4) + 8 * (r & 1) + 64 * (r >> 1)] = u[r];
        VMWAIT();
        BAR();
#pragma unroll
        for (int r = 0; r < 8; ++r) {
            const int kn = (S_ - k0 - 64 * (r & 1) - 512 * (r >> 1)) & (S_ - 1);
            const f32x4 ab = ((const LAS f32x4*)(smem + C_B))[256 * r + (tid >> 1)];
            const c2 zn = z[(kn & 7) * 258 + (kn >> 3)], g = {ab[2], ab[3]}, h = l & 1 ? f[r] : (c2){ab[0], ab[1]};
            const c2 y = cfin<1>(g, zn, g * bc(zn.x));
            u[r] = cfin<-1>(u[r], h, fm(u[r], bc(h.x), l & 1 ? bc(0.f) : y));
        }
    } else {
#pragma unroll
        for (int r = 0; r < 8; ++r) u[r] = cfin<-1>(u[r], f[r], u[r] * bc(f[r].x));
    }
    FST(4);
    dft4<1>(u[0], u[2], u[4], u[6]); dft4<1>(u[1], u[3], u[5], u[7]);
#pragma unroll
    for (int r = 0; r < 4; ++r) swapl<4>(u[(r & 1) + 2 * (r & 2)], u[(r & 1) + 2 * (r & 2) + 2]);
#pragma unroll
    for (int r = 0; r < 4; ++r) swapl<5>(u[r], u[r + 4]);
    twid<1>(u, tw);
    dft8<1>(u);
#pragma unroll
    for (int r = 0; r < 8; ++r) e2[66 * b + 8 * r] = u[r];
    asm volatile("" ::: "memory");
    ldtw<32>(tw, t2);
#pragma unroll
    for (int r = 0; r < 8; ++r) u[r] = ldv(e1 + 66 * r);
    twid<1>(u, tw);
    dft8<1>(u);
#pragma unroll
    for (int r = 0; r < 8; ++r) e1[64 * r] = u[r];
    ldtw<256>(tw, t1);
    __builtin_amdgcn_s_setprio(0);
    FST(7);
    BAR();
    FST(10);
#pragma unroll
    for (int r = 0; r < 8; ++r) u[r] = ldv(e0 + 544 * r);
    twid<1>(u, tw);
    dft8<1>(u);
    FST(5);
    __builtin_amdgcn_raw_buffer_store_b64((u32x2){__float_as_uint(u[0].x), nosent1(__float_as_uint(u[0].y))}, rws, tid * 8u, O_FL + (unsigned)((2 - late) * NCOL + kp) * GST, 16);
#pragma unroll
    for (int r = 1; r < 8; ++r) __builtin_amdgcn_raw_buffer_store_b64(__builtin_bit_cast(u32x2, u[r]), rws, tid * 8u, fb + r * 4096u, 16);
    FST(6);
}

struct Args { char* ws; char* win; const u16 *x, *wr, *wi, *mem0, *wq, *wk, *wv, *wo; u16* out; };
__device__ __forceinline__ const u16* arg(int i) {
    const __attribute__((address_space(4))) u64* p = (const __attribute__((address_space(4))) u64*)__builtin_amdgcn_kernarg_segment_ptr();
    asm volatile("" : "+s"(p));
    return (const u16*)p[i];
}
enum { A_X = 2, A_WR, A_WI, A_MEM0, A_WQ, A_WK, A_WV, A_WO, A_OUT };
#define MXV 0x5556ull
#define MXQ 0x1554ull
#define NTAIL 10
static_assert(NTAIL >= 3, "the owners release segments up to NSEG - 4");
constexpr int TAILS = NSEG - NTAIL;
static_assert((MXV & 31) == 22 && (MXQ & 31) == 20, "segment sets");
constexpr int NREC = 96 + NOWN, NQT = NREC + NOWN, NXT = NQT + 32, NWRK = NXT + 32;
#define SST(k) do {} while (0)
#define LSEL 0
#define RT(k) do {} while (0)
#define RTS() do {} while (0)
#define B3ATTR
#define MTA(k)
#define MTRT() do {} while (0)
#define MT(k) do {} while (0)
#define MTS() do {} while (0)
#define FC(x) do {} while (0)
#define DQC 1200
#define DVC 1200
#define M1 44
#define M2 226
#define PEN 280
#define DZP 55
#define LT0 342
#define SAD 1
#define PEN1 230
#define PEN0 330
#define TSLP 1
#define OPR 2
#define BSPARSE 0
#define ZLAG 4
#define TKP 0
#define DPS 12
#define DXT 3000
#define LTM 12
#define XS0 10
#define XSU 8
#define XSM 32
#define LTU 4
#define LTS 3
#define XSS 3
#define TKS 3
#define LT1 450
#define FDL 2
#define ZLA 3
#define NORND 0
#define LTX 40
#define TKE 200
#define DVO 4000
#define ZPS 2
#define HLA 8
#define QB2 2
#define NB2 6
#define DZR 24
#define ML 150
#define NB1 3
#define DPT 1
#define DDMA 8
#define DZ2 800
#define QEARLY 2
#define OSLP 32
#define OSLP2 1
#define WSLP 64
#define DO0 4000
#define DW0 1200
#define DQT 2000
#define ZEARLY 6
#define HSLP 4
#define ASLP 4
#define CSLP 4
#define A1S 0
#define OSLP3 12
#define DVT 860
#define VSL 0
#define WARM 1
constexpr unsigned CODE_BYTES = 96000;
#define DTK 4
#define OSL4 10
#define OAUX 0
#define TSL0 6
#define SE 6
#define XE 20
#define PENS 20
#define CF 190
#define HSL2 16
#define DH1 10
#define DZ0 400
#define FZP 1
__device__ __forceinline__ unsigned fz_at(unsigned h, unsigned g) { return FZP ? g * 1024u + (h >> 2) * 128u + (h & 3u) * 4u : h * 128u + g * 4u; }
#define LKE 0
#define DW4 6
#define DBIG 580
#define PENH 280
#define ROT 0
#define VREV 0
#define DGV 0
#define VOFF0 "0"
#define VOFF1 "1024"
#define VOFF2 "2048"
#define VOFF3 "3072"
#define VOFF4 "4096"
#define VOFF5 "5120"
#define VOFF6 "6144"
#define VOFF7 "7168"
#define VOFF8 "8192"
#define VOFF9 "9216"
#define STK 1
#define STX 0
#define UFQ 14
#define DQ0 36
#define QSL 6
#define OST0 40
#define NPR 3
#define DQA 1300
#define DQA0 1250
#define TLBW 0
#define DIE 0
#define ALN ".p2align 6\n\t"
#define SB() __builtin_amdgcn_sched_barrier(0)
#define PIN1(a) asm volatile("" : "+v"(a) :: "memory")
#define PIN2(a, b) asm volatile("" : "+v"(a), "+v"(b) :: "memory")
__device__ __forceinline__ unsigned fresh_lane() { unsigned l; asm volatile("v_mbcnt_lo_u32_b32 %0, -1, 0\n\tv_mbcnt_hi_u32_b32 %0, -1, %0" : "=v"(l)); return l; }
#define LINE8 (fresh_lane() < 16u ? fresh_lane() * 8u : 0x40000000u)
#define LANE0 (min(fresh_lane(), 1u) << 30)
__device__ __forceinline__ void wait_until(unsigned long long t) { while ((long long)(__builtin_amdgcn_s_memtime() - t) < 0) __builtin_amdgcn_s_sleep(1); }
struct Clk { unsigned ta, ma, cpq, qpc; };
__device__ __forceinline__ int sx24(unsigned v) { return (int)(v << 8) >> 8; }
#define WAIT_FINE ALN \
        "s_memrealtime vcc\n\t" \
        "s_memtime s[100:101]\n\t" \
        "s_brev_b32 %[fi], -2\n\t" \
        "s_waitcnt lgkmcnt(0)\n\t" \
        "s_mov_b32 %[rp], vcc_lo\n\t" \
        "s_lshl_b32 vcc_hi, vcc_lo, 2\n\t" \
        "s_sub_u32 %[x], %[t], vcc_hi\n\t" \
        "s_bfe_i32 %[x], %[x], 0x180000\n\t" \
        "s_min_i32 %[x], %[x], 4000\n\t" \
        "s_max_i32 %[x], %[x], -4000\n\t" \
        "s_add_u32 %[t], vcc_hi, %[x]\n\t" \
        "s_mul_i32 %[x], %[x], %[cq]\n\t" \
        "s_ashr_i32 %[x], %[x], 8\n\t" \
        "s_sub_u32 %[tm], %[t], %[ta]\n\t" \
        "s_bfe_i32 %[tm], %[tm], 0x180000\n\t" \
        "s_min_i32 %[tm], %[tm], 1000000\n\t" \
        "s_max_i32 %[tm], %[tm], -1000000\n\t" \
        "s_mul_i32 %[tm], %[tm], %[cq]\n\t" \
        "s_ashr_i32 %[tm], %[tm], 8\n\t" \
        "s_add_u32 %[tm], %[tm], %[ma]\n\t" \
        "s_sub_u32 %[tm], %[tm], s100\n\t" \
        "s_lshr_b32 vcc_hi, %[cq], 4\n\t" \
        "s_sub_u32 vcc_hi, %[x], vcc_hi\n\t" \
        "s_max_i32 %[tm], %[tm], vcc_hi\n\t" \
        "s_min_i32 %[tm], %[tm], %[x]\n\t" \
        "s_add_u32 %[tm], %[tm], s100\n" \
        "1:\n\t" \
        "s_memrealtime vcc\n\t" \
        "s_memtime s[100:101]\n\t" \
        "s_waitcnt lgkmcnt(0)\n\t" \
        "s_cmp_eq_u32 vcc_lo, %[rp]\n\t" \
        "s_cbranch_scc1 2f\n\t" \
        "s_mov_b32 %[rp], vcc_lo\n\t" \
        "s_lshl_b32 %[x], vcc_lo, 2\n\t" \
        "s_sub_u32 %[x], %[t], %[x]\n\t" \
        "s_bfe_i32 %[x], %[x], 0x180000\n\t" \
        "s_mul_i32 %[x], %[x], %[cq]\n\t" \
        "s_ashr_i32 %[x], %[x], 8\n\t" \
        "s_add_u32 %[x], %[x], s100\n\t" \
        "s_sub_u32 s101, %[x], %[tm]\n\t" \
        "s_cmp_lt_i32 s101, %[fi]\n\t" \
        "s_cselect_b32 %[tm], %[x], %[tm]\n\t" \
        "s_mov_b32 %[fi], 0\n" \
        "2:\n\t" \
        "s_sub_u32 %[x], %[tm], s100\n\t" \
        "s_cmp_gt_i32 %[x], 260\n\t" \
        "s_cbranch_scc1 1b\n\t" \
        "s_cmp_eq_u32 %[fi], 0\n\t" \
        "s_cselect_b32 %[ta], %[t], %[ta]\n\t" \
        "s_cselect_b32 %[ma], %[tm], %[ma]\n" \
        "3:\n\t" \
        "s_memtime s[100:101]\n\t" \
        "s_waitcnt lgkmcnt(0)\n\t" \
        "s_sub_u32 %[x], %[tm], s100\n\t" \
        "s_cmp_gt_i32 %[x], 92\n\t" \
        "s_cbranch_scc1 3b\n\t" \
        "s_max_i32 %[x], %[x], 0\n\t" \
        "s_andn2_b32 %[x], %[x], 3\n\t" \
        "s_sub_u32 %[x], 104, %[x]\n\t" \
        "s_getpc_b64 vcc\n\t" \
        "s_add_u32 vcc_lo, vcc_lo, %[x]\n\t" \
        "s_addc_u32 vcc_hi, vcc_hi, 0\n\t" \
        "s_setpc_b64 vcc\n\t" \
        ".rept 23\n\t" \
        "s_nop 0\n\t" \
        ".endr\n\t"
#define WAIT_FINE_OUT [tm] "=&s"(tm), [rp] "=&s"(rp), [x] "=&s"(x), [fi] "=&s"(fi), [t] "+s"(t), [ta] "+s"(c.ta), [ma] "+s"(c.ma)
__device__ __forceinline__ void wait_fine(unsigned t, Clk& c) {
    unsigned tm, rp, x, fi;
    asm volatile(WAIT_FINE : WAIT_FINE_OUT : [cq] "s"(c.cpq) : "vcc", "scc", "s100", "s101", "memory");
}
__device__ __forceinline__ void wait_fine_bar(unsigned t, Clk& c) {
    unsigned tm, rp, x, fi;
    asm volatile(WAIT_FINE "s_barrier" : WAIT_FINE_OUT : [cq] "s"(c.cpq) : "vcc", "scc", "s100", "s101", "memory");
}
__device__ __forceinline__ void wait_cyc_bar(unsigned tm) {
    unsigned x;
    asm volatile(
        ALN
        "3:\n\t"
        "s_memtime s[100:101]\n\t"
        "s_waitcnt lgkmcnt(0)\n\t"
        "s_sub_u32 %0, %1, s100\n\t"
        "s_cmp_gt_i32 %0, 92\n\t"
        "s_cbranch_scc1 3b\n\t"
        "s_max_i32 %0, %0, 0\n\t"
        "s_andn2_b32 %0, %0, 3\n\t"
        "s_sub_u32 %0, 104, %0\n\t"
        "s_getpc_b64 vcc\n\t"
        "s_add_u32 vcc_lo, vcc_lo, %0\n\t"
        "s_addc_u32 vcc_hi, vcc_hi, 0\n\t"
        "s_setpc_b64 vcc\n\t"
        ".rept 23\n\t"
        "s_nop 0\n\t"
        ".endr\n\t"
        "s_barrier"
        : "=&s"(x) : "s"(tm) : "vcc", "scc", "s100", "s101", "memory");
}
__device__ __forceinline__ unsigned cyc_at(const Clk& c, int d) {
    d = min(d, 20000);
    asm volatile("" : "+s"(d));
    return c.ma + (unsigned)((max(d, -4000) * (int)c.cpq) >> 8);
}
__device__ __forceinline__ void clk_rate(Clk& c, unsigned ta0, unsigned ma0) {
    const int dq = sx24(c.ta - ta0);
    if (dq > 200) {
        const float r = fminf(fmaxf((float)(c.ma - ma0) * __builtin_amdgcn_rcpf((float)dq), 1.f), 8.f);
        asm volatile("s_nop 1\n\tv_readfirstlane_b32 %0, %2\n\tv_readfirstlane_b32 %1, %3\n\ts_nop 3" : "=s"(c.cpq), "=s"(c.qpc)
                     : "v"((unsigned)(256.f * r + 0.5f)), "v"((unsigned)(65536.f * __builtin_amdgcn_rcpf(r))));
    }
}
__device__ __forceinline__ void clk_init(Clk& c) {
    c.ta = 4u * (unsigned)__builtin_amdgcn_s_memrealtime(); c.ma = (unsigned)__builtin_amdgcn_s_memtime();
    for (int i = 0; i < 2; ++i) {
        const unsigned ta0 = c.ta, ma0 = c.ma;
        wait_fine(c.ta + (i ? 360u : 60u), c);
        clk_rate(c, ta0, ma0);
    }
}
__device__ __forceinline__ unsigned sched_lane(int s) { const unsigned l = fresh_lane(); return (l & 31u) * 128u + (l >> 5) * 4u; }
__device__ __forceinline__ int read_sched(__amdgpu_buffer_rsrc_t rwin, int s, unsigned vo, unsigned to, unsigned f, unsigned g0, int& lt, int& xs, unsigned& fc) {
    int d = (int)((f - g0) & 0xFFFFFFu);
    if (__builtin_expect(__any((f >> 24) != (unsigned)s) || s == 0, 0)) {
        for (unsigned spins = 0; __any((f >> 24) != (unsigned)s);) {
            FC(fc);
            if (spin_fail(spins, rwin)) break;
            if (s == 0) __builtin_amdgcn_s_sleep(TSL0); else __builtin_amdgcn_s_sleep(TSLP);
            asm volatile("" ::: "memory");
            if ((f >> 24) != (unsigned)s) f = __builtin_amdgcn_raw_buffer_load_b32(rwin, vo, to, 16);
        }
        const unsigned f0 = __builtin_amdgcn_readfirstlane(f) - 0x100000u;
        d = (int)((f - f0) & 0xFFFFFFu) + (int)((f0 - g0) & 0xFFFFFFu);
    }
    d = max(d, __builtin_amdgcn_update_dpp(0, d, 0xB1, 0xF, 0xF, true));
    d = max(d, __builtin_amdgcn_update_dpp(0, d, 0x4E, 0xF, 0xF, true));
    d = max(d, __builtin_amdgcn_update_dpp(0, d, 0x141, 0xF, 0xF, true));
    d = max(d, __builtin_amdgcn_update_dpp(0, d, 0x140, 0xF, 0xF, true));
    d = max(d, __builtin_amdgcn_update_dpp(0, d, 0x142, 0xA, 0xF, true));
    d = max(__builtin_amdgcn_readlane(d, 16), __builtin_amdgcn_readlane(d, 48));
    const int l0 = lt, x0 = xs;
    d -= 0x400000;
    const bool miss = d > l0 + min(x0 + LTM, LTX);
    if (s >= SAD) { lt = miss ? l0 : min(max(d, l0 - 1 - ((l0 - d) >> LTS)), l0 + LTU); xs = miss ? min(x0 + XSU, XSM) : max(x0 - 1 - (x0 >> XSS), 0); }
    if (s == 1) lt = LT0;
    if (s < SE) xs = max(xs, XE);
    return 0x400000 + (s < SAD ? d + (s ? PENS : PEN0) : miss ? d + (d < DBIG ? PENH : PEN) : l0 + x0) + M1 + M2 + x0;
}
constexpr int QS = 272;
constexpr int TQS = 1152;
constexpr int L_Q = 10240, L_K = L_Q + 2 * TQS, L_S = L_K + 2 * TQS, L_TOK = L_S + 256, L_H = L_TOK + 49152, L_END = L_H + 2 * 40960;
static_assert(C_END <= L_END && L_END == 146176, "LDS");
typedef __attribute__((ext_vector_type(4))) short v4i16_t;
__device__ __forceinline__ u32x2 ldtr(const char* p) { return __builtin_bit_cast(u32x2, __builtin_amdgcn_ds_read_tr16_b64_v4i16((LAS v4i16_t*)p)); }
__device__ __forceinline__ u32x2 ldtr1(const LAS char* p) { return __builtin_bit_cast(u32x2, __builtin_amdgcn_ds_read_tr16_b64_v4i16((LAS v4i16_t*)p)); }
__device__ __forceinline__ u32x4 ldtr2(const LAS char* p0, const LAS char* p1) {
    const u32x2 x = __builtin_bit_cast(u32x2, __builtin_amdgcn_ds_read_tr16_b64_v4i16((LAS v4i16_t*)p0)), y = __builtin_bit_cast(u32x2, __builtin_amdgcn_ds_read_tr16_b64_v4i16((LAS v4i16_t*)p1));
    return (u32x4){x[0], x[1], y[0], y[1]};
}
__device__ __forceinline__ u32x4 ldtr2(const char* p0, const char* p1) { const u32x2 x = ldtr(p0), y = ldtr(p1); return (u32x4){x[0], x[1], y[0], y[1]}; }

__device__ __forceinline__ unsigned poll_lim(int& budget) {
    --budget;
    unsigned lim = budget > 0 ? 0xFFFFFFFEu : 0xFFFFFFFFu;
    asm volatile("" : "+s"(lim));
    return lim;
}
#define GATHER \
        "buffer_load_dwordx4 %[vo], %[rw], %[s0] offen sc1 lds\n\t" \
        "buffer_load_dwordx4 %[vo], %[rw], %[s0] offen offset:1024 sc1 lds\n\t" \
        "buffer_load_dwordx4 %[vo], %[rw], %[s0] offen offset:2048 sc1 lds\n\t" \
        "buffer_load_dwordx4 %[vo], %[rw], %[s0] offen offset:3072 sc1 lds\n\t" \
        "s_movk_i32 m0, 0x1000\n\t" \
        "s_nop 0\n\t" \
        "buffer_load_dwordx4 %[vo], %[rw], %[s4] offen sc1 lds\n\t" \
        "buffer_load_dwordx4 %[vo], %[rw], %[s4] offen offset:1024 sc1 lds\n\t" \
        "buffer_load_dwordx4 %[vo], %[rw], %[s4] offen offset:2048 sc1 lds\n\t" \
        "buffer_load_dwordx4 %[vo], %[rw], %[s4] offen offset:3072 sc1 lds\n\t" \
        "s_movk_i32 m0, 0x2000\n\t" \
        "s_nop 0\n\t" \
        "buffer_load_dwordx4 %[vo], %[rw], %[s1] offen sc1 lds\n\t" \
        "buffer_load_dwordx4 %[vo], %[rw], %[s1] offen offset:1024 sc1 lds"
#define GATHER_R \
        "buffer_load_dwordx4 %[vo], %[rw], %[s1] offen offset:1024 sc1 lds\n\t" \
        "buffer_load_dwordx4 %[vo], %[rw], %[s1] offen sc1 lds\n\t" \
        "s_movk_i32 m0, 0x1000\n\t" \
        "s_nop 0\n\t" \
        "buffer_load_dwordx4 %[vo], %[rw], %[s4] offen offset:3072 sc1 lds\n\t" \
        "buffer_load_dwordx4 %[vo], %[rw], %[s4] offen offset:2048 sc1 lds\n\t" \
        "buffer_load_dwordx4 %[vo], %[rw], %[s4] offen offset:1024 sc1 lds\n\t" \
        "buffer_load_dwordx4 %[vo], %[rw], %[s4] offen sc1 lds\n\t" \
        "s_movk_i32 m0, 0\n\t" \
        "s_nop 0\n\t" \
        "buffer_load_dwordx4 %[vo], %[rw], %[s0] offen offset:3072 sc1 lds\n\t" \
        "buffer_load_dwordx4 %[vo], %[rw], %[s0] offen offset:2048 sc1 lds\n\t" \
        "buffer_load_dwordx4 %[vo], %[rw], %[s0] offen offset:1024 sc1 lds\n\t" \
        "buffer_load_dwordx4 %[vo], %[rw], %[s0] offen sc1 lds"
#define GATHER_IN [vo] "v"(vo), [rw] "s"(rwin), [s0] "s"(so0), [s4] "s"(so0 + 2u * PAIR), [s1] "s"(so0 + 4u * PAIR)
__device__ __forceinline__ void gather_dma(__amdgpu_buffer_rsrc_t rwin, unsigned vo, unsigned so0, bool rev) {
    if (rev) asm volatile("s_movk_i32 m0, 0x2000\n\ts_nop 0\n\t" GATHER_R :: GATHER_IN : "m0", "memory");
    else asm volatile("s_mov_b32 m0, 0\n\ts_nop 0\n\t" GATHER :: GATHER_IN : "m0", "memory");
}
__device__ __forceinline__ void gather_at(unsigned t, Clk& c, __amdgpu_buffer_rsrc_t rwin, unsigned vo, unsigned so0, bool rev) {
    unsigned tm, rp, x, fi;
    if (rev) asm volatile("s_movk_i32 m0, 0x2000\n\t" WAIT_FINE GATHER_R : WAIT_FINE_OUT : [cq] "s"(c.cpq), GATHER_IN : "vcc", "scc", "s100", "s101", "m0", "memory");
    else asm volatile("s_mov_b32 m0, 0\n\t" WAIT_FINE GATHER : WAIT_FINE_OUT : [cq] "s"(c.cpq), GATHER_IN : "vcc", "scc", "s100", "s101", "m0", "memory");
}
__device__ __forceinline__ void dma4k(__amdgpu_buffer_rsrc_t r, unsigned vo, unsigned so, unsigned lds) {
    asm volatile(
        "s_mov_b32 m0, %3\n\t"
        "s_nop 0\n\t"
        "buffer_load_dwordx4 %0, %1, %2 offen sc1 lds\n\t"
        "buffer_load_dwordx4 %0, %1, %2 offen offset:1024 sc1 lds\n\t"
        "buffer_load_dwordx4 %0, %1, %2 offen offset:2048 sc1 lds\n\t"
        "buffer_load_dwordx4 %0, %1, %2 offen offset:3072 sc1 lds"
        :: "v"(vo), "s"(r), "s"(so), "s"(lds) : "m0", "memory");
}
__device__ __forceinline__ int bs2(int c) { return (c & ~3) | ((c & 1) << 1) | ((c >> 1) & 1); }
__device__ __forceinline__ void gather_mem0(char* memL, int tid, int nt = 512) {
    const u16* mem0 = arg(A_MEM0);
    for (int c = tid; c < 640; c += nt) {
        const u32x2* src = (const u32x2*)(mem0 + ((c & 6) + ((c >> 3) & 1)) * D_ + (c >> 4) * 16 + (c & 1) * 4);
        const u32x2 x = src[0], y = src[2];
        *(u32x4*)(memL + c * 16) = (u32x4){(x[0] & 0xFFFFu) | (y[0] << 16), (x[0] >> 16) | (y[0] & 0xFFFF0000u), (x[1] & 0xFFFFu) | (y[1] << 16), (x[1] >> 16) | (y[1] & 0xFFFF0000u)};
    }
}
__device__ __forceinline__ f32x2 hadd(f32x4 c) { return (f32x2){c[0], c[1]} + (f32x2){c[2], c[3]}; }
__device__ __forceinline__ unsigned hpack(f32x4 c) { const f32x2 y = hadd(c); return pack2(y[0], y[1]); }
__device__ __forceinline__ u32x8 cat8(u32x4 lo, u32x4 hi) { return (u32x8){lo[0], lo[1], lo[2], lo[3], hi[0], hi[1], hi[2], hi[3]}; }
__device__ __forceinline__ u32x4 half8(const u32x8& v, int u) { return u ? (u32x4){v[4], v[5], v[6], v[7]} : (u32x4){v[0], v[1], v[2], v[3]}; }
__device__ __forceinline__ void dma1k(__amdgpu_buffer_rsrc_t r, unsigned vo, unsigned so, unsigned lds) {
    asm volatile("s_mov_b32 m0, %3\n\ts_nop 0\n\tbuffer_load_dwordx4 %0, %1, %2 offen sc1 lds" :: "v"(vo), "s"(r), "s"(so), "s"(lds) : "m0", "memory");
}
template <class FM> __device__ __forceinline__ void load_w(u32x8 (&wt)[2][10], const u16* src, unsigned rs, bool perm, unsigned b, unsigned c, int w, FM mid) {
    const __amdgpu_buffer_rsrc_t r = mkrsrc(src, 0x7FFFFFFFu);
    const unsigned l = fresh_lane(), q = l >> 4, lds = (unsigned)(w >> 2) * 65536u, v = w & 3;
    const unsigned vo0 = (perm ? ((q & 1u) << 3) | ((q & 2u) << 1) : q) * rs + ((l & 15u) ^ (q << 1)) * 16u, vo1 = vo0 ^ 128u, a = perm ? rs : 4u * rs;
    const LAS char* p0 = (const LAS char*)(size_t)(lds + q * 2048u + ((l >> 2) & 3u) * 256u + ((2u * v) ^ ((l >> 2) & 7u)) * 32u + (l & 3u) * 8u);
    const LAS char* p1 = (const LAS char*)((size_t)p0 ^ 32u);
    auto issue = [&](int g) {
#pragma unroll
        for (int i = 0; i < 4; ++i)
            asm volatile("s_mov_b32 m0, %3\n\ts_nop 3\n\tbuffer_load_dwordx4 %0, %1, %2 offen lds" :: "v"(i < 2 ? vo0 : vo1), "s"(r), "s"(i * a + (g & 1) * b + (g >> 1) * c), "s"(lds + (g & 3) * 16384u + v * 4096u + i * 1024u) : "m0", "memory");
    };
#pragma unroll
    for (int g = 0; g < 3; ++g) issue(g);
#pragma unroll
    for (int g = 0; g < 10; ++g) {
        asm volatile("s_waitcnt vmcnt(%0)\n\ts_barrier" :: "n"(g < 8 ? 8 : 4 * (9 - g)) : "memory");
        if (g + 3 < 10) issue(g + 3);
        const int o = (g & 3) * 16384;
        wt[0][g] = cat8(ldtr2(p0 + o, p0 + o + 1024), ldtr2(p0 + o + 8192, p0 + o + 9216));
        wt[1][g] = cat8(ldtr2(p1 + o, p1 + o + 1024), ldtr2(p1 + o + 8192, p1 + o + 9216));
        asm volatile("s_waitcnt lgkmcnt(0)" : "+v"(wt[0][g]), "+v"(wt[1][g]) :: "memory");
        if (g == 6) mid();
    }
    BAR0();
}
__device__ __forceinline__ int tile_of(int o) { return 8 * ((o % 10) >> 1) + 2 * (o / 10) + (o & 1); }
__device__ __forceinline__ void wait_tags(__amdgpu_buffer_rsrc_t rwin, int g0, int g1, unsigned n1, u64 val, bool boot) {
    const unsigned l = fresh_lane();
    unsigned o[4];
#pragma unroll
    for (int g = 0; g < 4; ++g) o[g] = g >= g0 && g < g1 && (g + 1 < g1 || l < n1) ? l * 128u + g * 8192u : 0x40000000u;
    for (unsigned spins = 0;;) {
        asm volatile("" ::: "memory");
        u32x2 v[4];
#pragma unroll
        for (int g = 0; g < 4; ++g) v[g] = __builtin_amdgcn_raw_buffer_load_b64(rwin, o[g], W_A1, 16);
#pragma unroll
        for (int g = 0; g < 4; ++g) if (__builtin_bit_cast(u64, v[g]) == val) o[g] = 0x40000000u;
        if (!__any((o[0] & o[1] & o[2] & o[3]) != 0x40000000u)) break;
        if (boot ? ++spins > SPIN_CAP : spin_fail(spins, rwin)) break;
        __builtin_amdgcn_s_sleep(ASLP);
    }
}
template <int N> __device__ __forceinline__ void stage_h(__amdgpu_buffer_rsrc_t rws, unsigned src, unsigned lds, unsigned st) {
    const unsigned l = fresh_lane() * 16u;
#pragma unroll
    for (int i = 0; i < N; ++i) dma1k(rws, l, src + i * st, lds + i * st);
}
template <int N> __device__ __forceinline__ void check_h(__amdgpu_buffer_rsrc_t rws, __amdgpu_buffer_rsrc_t rwin, unsigned src, unsigned lds, unsigned st) {
    const LAS char* p = (const LAS char*)smem + lds + fresh_lane() * 16u;
    for (unsigned spins = 0;;) {
        unsigned m = 0;
#pragma unroll
        for (int i = 0; i < N; ++i) { const u32x4 x = *(const volatile LAS u32x4*)(p + i * st); m = max(max(m, max(x[0], x[1])), max(x[2], x[3])); }
        if (__builtin_expect(!__any(m == ~0u), 1) || spin_fail(spins, rwin)) break;
        stage_h<N>(rws, src, lds, st);
        VMWAIT();
    }
}
__device__ __forceinline__ void wait_h(__amdgpu_buffer_rsrc_t rwin, unsigned fo, int seg, int& hrdy) {
    unsigned lim = 63u;
    if (__builtin_expect(hrdy <= seg, 0)) for (unsigned spins = 0;;) {
        asm volatile("" ::: "memory");
        const u32x4 f = __builtin_amdgcn_raw_buffer_load_b128(rwin, min(fresh_lane(), lim) * 128u, W_H + fo, 16);
        const unsigned long long miss = __ballot((f[0] | f[1] | f[2] | f[3]) != 0u);
        hrdy = miss ? __builtin_ctzll(miss) : (int)lim + 1;
        if (hrdy > seg) break;
        if (spin_fail(spins, rwin)) break;
        lim = (unsigned)min(seg + HLA, 63);
        if (seg < 8) __builtin_amdgcn_s_sleep(HSLP); else __builtin_amdgcn_s_sleep(HSL2);
    }
}
template <int CTRL> __device__ __forceinline__ unsigned dppu(unsigned v) { return (unsigned)__builtin_amdgcn_mov_dpp((int)v, CTRL, 0xF, 0xF, true); }
template <int CTRL> __device__ __forceinline__ u32x4 ror4(u32x4 v) { return (u32x4){dppu<CTRL>(v[0]), dppu<CTRL>(v[1]), dppu<CTRL>(v[2]), dppu<CTRL>(v[3])}; }
template <int N> struct IC { static constexpr int v = N; };
__device__ __forceinline__ float xmax16(float v) {
    auto q = __builtin_amdgcn_permlane16_swap(__float_as_uint(v), __float_as_uint(v), false, false);
    return fmx(__uint_as_float(q[0]), __uint_as_float(q[1]));
}
__device__ __forceinline__ float xsum16(float v) {
    auto q = __builtin_amdgcn_permlane16_swap(__float_as_uint(v), __float_as_uint(v), false, false);
    return __uint_as_float(q[0]) + __uint_as_float(q[1]);
}
__device__ __forceinline__ float addswap16(float a, float b) {
    auto r = __builtin_amdgcn_permlane16_swap(__float_as_uint(a), __float_as_uint(b), false, false);
    return __uint_as_float(r[0]) + __uint_as_float(r[1]);
}
#define LANES() \
    const int lane = (int)fresh_lane(), tid = w * 64 + lane, fr = lane & 15, fq = lane >> 4; \
    const int sidx = (lane & 2) ? 0xEEEE : 0x4444; \
    const int ma0 = fq * 256 + (fr & 1) * 128 + (fr >> 1) * 16; \
    const int prow = 16 * ((fr >> 2) & 1) + 4 * (fr >> 3) + (fr & 3), hoff = fr * 128; \
    (void)tid; (void)sidx; (void)ma0; (void)prow; (void)hoff

__device__ __forceinline__ int seg_after(unsigned long long m, int j) { m = j >= 63 ? 0ull : m >> (j + 1); return m ? j + 1 + __builtin_ctzll(m) : NSEG; }
__device__ __forceinline__ int seg_done(unsigned long long m, int f0, int f1, unsigned long long x = 0) { return min(seg_after(~m, f0), seg_after(m | x, f1)) - 1; }
constexpr unsigned long long MXA = 1ull << 40;
template <int OFF> __device__ __forceinline__ void ldfrag(u32x4& d, const LAS char* p) { asm volatile("ds_read_b128 %0, %1 offset:%2" : "=v"(d) : "v"(p), "n"(OFF) : "memory"); }
template <int U, class FM, class FV> __device__ __forceinline__ void project(const u32x8 (&wt)[2][10], f32x4 (&tc)[2][2], u32x4 (&bb)[8], const LAS char* c0, const LAS char* c1, const LAS char* n0, const LAS char* n1, FM mid, FV vm) {
    constexpr int t = U >> 2, k = U & 3, v = (U + 8) % 40;
    if (U == 32) mid();
    vm(IC<U>());
    const LAS char* p = U < 32 ? (k >> 1 ? c1 : c0) : (k >> 1 ? n1 : n0);
    if (U < 2) asm volatile("s_nop 0\n\ts_waitcnt lgkmcnt(7)\n\tv_mfma_f32_16x16x32_bf16 %0, %3, %2, 0\n\tv_mfma_f32_16x16x32_bf16 %1, %4, %2, 0\n\tds_read_b128 %2, %5 offset:%6"
                            : "=&v"(tc[0][k & 1]), "=&v"(tc[1][k & 1]), "+v"(bb[U & 7]) : "v"(half8(wt[0][t], k >> 1)), "v"(half8(wt[1][t], k >> 1)), "v"(p), "n"((v >> 2) * 4096 + (k & 1) * 2048) : "memory");
    else asm volatile(".rept %7\n\ts_nop 0\n\t.endr\n\ts_waitcnt lgkmcnt(7)\n\tv_mfma_f32_16x16x32_bf16 %0, %3, %2, %0\n\tv_mfma_f32_16x16x32_bf16 %1, %4, %2, %1\n\tds_read_b128 %2, %5 offset:%6"
                      : "+v"(tc[0][k & 1]), "+v"(tc[1][k & 1]), "+v"(bb[U & 7]) : "v"(half8(wt[0][t], k >> 1)), "v"(half8(wt[1][t], k >> 1)), "v"(p), "n"((v >> 2) * 4096 + (k & 1) * 2048), "n"(U < 8 ? 1 : 0) : "memory");
    if constexpr (U < 39) project<U + 1>(wt, tc, bb, c0, c1, n0, n1, mid, vm);
    else asm volatile("s_nop 7" : "+v"(tc[0][0]), "+v"(tc[0][1]), "+v"(tc[1][0]), "+v"(tc[1][1]));
}
__device__ __forceinline__ void project_end(u32x4 (&bb)[8]) {
    asm volatile("s_waitcnt lgkmcnt(0)" : "+v"(bb[0]), "+v"(bb[1]), "+v"(bb[2]), "+v"(bb[3]), "+v"(bb[4]), "+v"(bb[5]), "+v"(bb[6]), "+v"(bb[7]) :: "memory");
}
__device__ __forceinline__ void project0(u32x4 (&bb)[8], const LAS char* c0, const LAS char* c1) {
    ldfrag<0>(bb[0], c0); ldfrag<2048>(bb[1], c0); ldfrag<0>(bb[2], c1); ldfrag<2048>(bb[3], c1);
    ldfrag<4096>(bb[4], c0); ldfrag<6144>(bb[5], c0); ldfrag<4096>(bb[6], c1); ldfrag<6144>(bb[7], c1);
    project_end(bb);
}

__global__ __launch_bounds__(512, 2) B3ATTR void mega_kernel(Args a) {
    char* memL = smem;
    char* tokL = smem + L_TOK;
    char* hL = smem + L_H;
    int w = __builtin_amdgcn_readfirstlane(threadIdx.x >> 6);
    __amdgpu_buffer_rsrc_t rwin = mkrsrc(a.win, W_END);
    char* ws = a.ws;
    __amdgpu_buffer_rsrc_t rws = mkrsrc(ws, (unsigned)OFF_WIN);
    auto fresh_rsrc = [&]() INL { ws = (char*)arg(0); rws = mkrsrc(ws, (unsigned)OFF_WIN); rwin = mkrsrc(arg(1), W_END); };
    const char* pc;
    asm volatile("s_getpc_b64 %0" : "=s"(pc));
    unsigned fs[12] = {};
    const u32x2 tg0 = __builtin_amdgcn_raw_buffer_load_b64(rws, 0u, 0u, 16);
    u64 tag;
    u32x2 rot0;
    {
        const int tid = threadIdx.x;
        if (blockIdx.x < 64) {
            const unsigned j = blockIdx.x;
            if (j == 0 && w == 0) __builtin_amdgcn_raw_buffer_store_b64(__builtin_bit_cast(u32x2, (__builtin_bit_cast(u64, tg0) & ~15ull) + 16ull + (xcc_id() & 7u)), rws, LINE8, 128u, 16);
            const unsigned g0 = (j * 512u + tid) * 16u;
#pragma unroll
            for (int i = 0; i < 3; ++i) st128(SENT, mkrsrc(ws + O_ARM1, O_H + NHF * 40960u - O_ARM1), g0 + i * 524288u, 0, 16);
#pragma unroll
            for (int i = 0; i < 7; ++i) {
                const unsigned g = g0 + i * 524288u;
                st128(SENT, mkrsrc(a.win, W_END + 2 * PAIR), g + (g < W_ARM - W_LAYER ? 0u : W_ZM - (W_ARM - W_LAYER)), 0, 16);
            }
#pragma unroll
            for (int i = 0; i < 3; ++i) st128(SENT, mkrsrc(ws + O_Z, ZTSEG * 2), g0 + i * 524288u, 0, 16);
            static_assert(W_ARM - W_LAYER + W_END + 2 * PAIR - W_ZM <= 7u * 524288u && O_ARM1 + 3u * 524288u >= O_H + NHF * 40960u && ZTSEG * 2 <= 3u * 524288u, "arming");
        } else if (blockIdx.x >= 128) {
            RowTw tw;
            row_tw(tw, tid);
            a1_unit<8, false>(mkrsrc(a.x, S_ * D_ * 2), (blockIdx.x - 128) * 16, rws, rwin, tw, tid, fs);
        } else {
            const unsigned j = blockIdx.x - 64;
            FST(0);
            auto row = [&](unsigned i) { return (j & 7u) + 8u * (i & 15u) + 128u * ((j >> 3) & 3u) + 512u * (((j >> 4) & 2u) + ((i >> 4) & 1u)); };
            u32x4 v[6];
#pragma unroll
            for (int i = 0; i < 6; ++i) {
                const unsigned id = min(tid + 512 * i, 64 * 41 - 1), r = id / 41u;
                v[i] = ((const u32x4*)(r < 32u ? a.wr : a.wi))[row(r) * (DF * 2) / 16u + id - r * 41u];
            }
            u16 n[4];
            if (w == 7) {
                const size_t kn = (size_t)((S_ - row(tid)) & (S_ - 1)) * DF;
                n[0] = a.wr[kn]; n[1] = a.wi[kn]; n[2] = a.wr[kn + 320]; n[3] = a.wi[kn + 320];
            }
            const unsigned wo = WARM ? (w * 8u + (j >> 3)) * 8192u : 0x40000000u;
            const unsigned x0 = __builtin_amdgcn_raw_buffer_load_b32(mkrsrc(pc, CODE_BYTES), (tid & 63) * 128u, wo, 0), x1 = __builtin_amdgcn_raw_buffer_load_b32(mkrsrc(&TWC, sizeof(TwCol)), (tid & 63) * 128u, wo, 0);
#pragma unroll
            for (int i = 0; i < 6; ++i) ((u32x4*)smem)[min(tid + 512 * i, 64 * 41 - 1)] = v[i];
            FST(1);
            BAR();
            FST(2);
            const u16* q0 = (const u16*)smem + (j & 7u);
            u32x4 o[5];
#pragma unroll
            for (int i = 0; i < 5; ++i) {
                const u16* q = q0 + ((tid >> 1) & 7) * 328 + (tid >> 4) * 2 + (tid & 1) + 64 * i;
#pragma unroll
                for (int e = 0; e < 4; ++e) o[i][e] = (unsigned)q[e * 8 * 328] | ((unsigned)q[(32 + e * 8) * 328] << 16);
            }
#pragma unroll
            for (int i = 0; i < 5; ++i) st128(o[i], rws, (tid >> 4) * WST + (tid & 15) * 16u, O_WFT + i * 32u * WST + (j >> 5) * 8192u + (j & 7u) * 1024u + ((j >> 3) & 3u) * 256u, 16);
            if (w == 7) {
                const u16* q = q0 + (tid & 31) * 328;
                const unsigned k = row(tid);
                const c2 h0 = (c2){bf2f(q[0]) + bf2f(n[0]), bf2f(q[32 * 328]) - bf2f(n[1])} * bc(0.25f), h1 = (c2){bf2f(q[320]) + bf2f(n[2]), bf2f(q[32 * 328 + 320]) - bf2f(n[3])} * bc(0.25f);
                st128(__builtin_bit_cast(u32x4, (f32x4){h0.x + h1.x, h0.y + h1.y, h0.x - h1.x, h0.y - h1.y}), rws, (((k >> 8) & 6u) * 256u + (k & 64u) * 4u + (k & 7u) * 32u + ((k >> 4) & 24u) + ((k >> 3) & 7u)) * 16u, O_WFT + NCOL * WST, 16);
            }
            FST(3);
            asm volatile("" :: "v"(x0), "v"(x1));
            FST(4);
        }
        rot0 = __builtin_amdgcn_raw_buffer_load_b64(rws, 0u, 128u, 16);
        VMWAIT();
        FST(8);
        BAR0();
        FST(9);
    }
    tag = (((u64)__builtin_amdgcn_readfirstlane(tg0[1]) << 32) | (__builtin_amdgcn_readfirstlane(tg0[0]) & ~15u)) + 16ull;
    if (w == 0) __builtin_amdgcn_raw_buffer_store_b64(__builtin_bit_cast(u32x2, tag + 8ull), rwin, LINE8, W_A1 + blockIdx.x * 128u, 16);
    if (blockIdx.x >= 128) FSW(0); else if (blockIdx.x >= 64) FSW(6);
    unsigned vx;
    int bid;
    auto set_role = [&](unsigned x0) INL {
        vx = x0;
        const int j = blockIdx.x >> 3, x = vx & 3;
        if (!(vx & 4)) bid = j < 8 ? 8 * x + j : j < 16 ? 24 + 8 * x + j : j < 26 ? 80 + 10 * x + j : j < 30 ? NWRK + 4 * (j - 26) + x : NREC - 22 + 10 * x + j;
        else bid = j < 8 ? 64 + 8 * x + j : j < 16 ? NQT - 8 + 8 * x + j : j < 24 ? NREC - 16 + 10 * x + j : NXT - 24 + 8 * x + j;
    };
    auto get_rot = [&]() INL {
        unsigned r = 0;
        for (unsigned spins = 0; spins < SPIN_CAP; ++spins) {
            const u64 x = __builtin_bit_cast(u64, rot0) - tag;
            r = __builtin_amdgcn_readfirstlane((unsigned)x);
            if (__all(x < 8ull)) break;
            __builtin_amdgcn_s_sleep(2);
            asm volatile("" ::: "memory");
            rot0 = __builtin_amdgcn_raw_buffer_load_b64(rws, 0u, 128u, 16);
        }
        return (r + blockIdx.x + 4u * DIE) & 7u;
    };
    set_role(blockIdx.x < 64 ? (xcc_id() ^ (4u * DIE)) & 7u : get_rot());
    unsigned long long tref = 0;
    int layer = 0;
    unsigned fo = 0;
    unsigned nh = 0, nz = 0, mc = 0, qz = 0, mo = 0, tro = 0;
    auto near_init = [&]() INL {
        unsigned f;
        for (unsigned spins = 0;;) {
            asm volatile("" ::: "memory");
            f = __builtin_amdgcn_raw_buffer_load_b32(rwin, (fresh_lane() & 15u) * 4u, W_NEAR, 16);
            if (!__any(f == ~0u) || spin_fail(spins, rwin)) break;
            __builtin_amdgcn_s_sleep(8);
        }
        const unsigned h = (unsigned)__ballot(f & 1u), q = (unsigned)__ballot(f & 2u) ^ 0xAAAAu;
        auto most = [&](unsigned m) { return (__builtin_popcount(m & 0x0F0Fu) + 3u) >> 3; };
        nh = most(h) * HALF; nz = most(h >> 4) * HALF;
        mc = (vx & 3u) ^ most(q) ^ (STK ? 2u * most((unsigned)__ballot(f & 4u) ^ 0x3333u) : STX);
        qz = most(q >> 4);
        mo = W_MEM + (mc & 1u) * PAIR + nh + (mc >> 1) * 4096u;
        tro = mo - W_MEM + W_T;
    };
    auto zpair = [&](unsigned x, unsigned k) { return (((x >> 1) * 10u + k) * 2u + ((x & 1u) ^ qz)) * PAIR + nz; };
    auto two = [&]() { const unsigned l = fresh_lane(); return l < 4 ? (l >> 1) * PAIR + (l & 1) * 4096u + nh : 0x40000000u; };
    const f32x4 z4 = {0.f, 0.f, 0.f, 0.f};
    int hrdy = 0, budget = (int)SPIN_CAP;
    unsigned fc0 = 0, fc1 = 0, fc2 = 0;
    Clk ck = {0u, 0u, 1600u, 10486u};
    SST(5);
#define xt (bid >= NXT && bid < NWRK)
#define qx (bid >= NQT && bid < NWRK)
#define xq (bid >= NREC && bid < NQT && (bid - NREC) % 10 < 8)
    unsigned tpp = 0;
    int lt = LT1, xs = XS0;
    struct Tk { unsigned vo, tw, sl; const LAS char* sp; };
    auto tk_init = [&]() {
        const unsigned l = fresh_lane();
        Tk k = {l * 16u, two(), (l & 31u) * 128u + 8u - (l >> 5) * 4u, (const LAS char*)(memL + l * 64 + 4 + ((l >> 1) & 7) * 8)};
        PIN2(k.vo, k.tw); PIN2(k.sl, k.sp);
        return k;
    };
    auto sched2 = [&](Tk& k, int s, unsigned f) INL {
        const unsigned g0 = tpp - 0x400000u;
        tpp = g0 + (unsigned)read_sched(rwin, s, k.sl, tro + (unsigned)(s & 3) * 2u * PAIR, f, g0, lt, xs, fc1);
        k.sl &= ~12u;
        RT(6);
    };
    auto sched = [&](Tk& k, int s) INL { sched2(k, s, __builtin_amdgcn_raw_buffer_load_b32(rwin, k.sl, tro + (unsigned)(s & 3) * 2u * PAIR, 16)); };
    unsigned it = 0;
    auto fetch = [&](const Tk& k, int s, bool first, unsigned lim, unsigned dc, unsigned fo, bool tell) INL {
        const unsigned sb = (unsigned)(s & 3) * 6u * PAIR, ta0 = ck.ta, mm0 = ck.ma;
        if (__builtin_expect(s > 0, 1)) {
            if (__builtin_expect(first, 1)) { const unsigned te = cyc_at(ck, sx24(tpp - TKE - ck.ta)); while ((int)((unsigned)__builtin_amdgcn_s_memtime() - te) < 0) __builtin_amdgcn_s_sleep(1); }
            if (__builtin_expect(first, 1)) gather_at(tpp + (tell ? 0 : DGV), ck, rwin, k.vo, mo + sb, VREV && !tell); else { const unsigned c = (mc + 1u + (it & 1u)) & 3u; gather_dma(rwin, k.vo, W_MEM + (c & 1u) * PAIR + (c >> 1) * 4096u + nh + sb, VREV && !tell); }
        }
        __builtin_amdgcn_s_setprio(0);
        RT(1);
        clk_rate(ck, ta0, mm0);
        unsigned tb = ck.ma - dc, tg = (unsigned)s << 24, so = W_T + (unsigned)(s & 3) * 2u * PAIR + fo;
        asm volatile("" : "+s"(tb), "+s"(tg), "+s"(so));
        asm volatile(ALN "s_waitcnt vmcnt(%1) lgkmcnt(0)\n\ts_barrier\n\ts_waitcnt vmcnt(%2)\n\ts_barrier\n\ts_waitcnt vmcnt(0)\n\ts_memtime %0\n\ts_barrier"
                     : "=s"(tref) : "n"(10 - NB1), "n"(10 - NB2) : "memory");
        MT(1);
        if (!tell) { RT(3); return; }
        unsigned x0 = *(const LAS unsigned*)k.sp, x1 = *(const LAS unsigned*)(k.sp + 4096), x2 = *(const LAS unsigned*)(k.sp + 6144);
        __builtin_amdgcn_s_waitcnt(0xC07F);
        asm volatile("" : "+s"(tref) :: "memory");
        const unsigned fw = ((ck.ta + ((((unsigned)tref - tb) * ck.qpc) >> 16)) & 0xFFFFFFu) | tg;
        __builtin_amdgcn_raw_buffer_store_b32(fw, rwin, k.tw, __any(max(max(x0, x1), x2) > lim) ? 0x40000000u : so, 16);
        RT(3);
    };
    auto rout = [&]() { return mkrsrc(layer ? arg(A_OUT) : (const u16*)(ws + OFF_X2), S_ * D_ * 2); };
    auto rec_end = [&](unsigned g) INL {
        if (g) for (unsigned spins = 0;;) {
            asm volatile("" ::: "memory");
            if (__builtin_amdgcn_readfirstlane(__builtin_amdgcn_raw_buffer_load_b32(rws, 0u, g == 3 ? O_FL + (unsigned)(bid & 127) * GST + 4092u : O_FL - g * SEG * D_ * 2u + (unsigned)bid * 128u, 16)) != -1 || spin_fail(spins, rwin)) break;
            __builtin_amdgcn_s_sleep(16);
        }
    };
    auto mytid = [&]() { return w * 64 + (int)fresh_lane(); };
    auto new_layer = [&]() { fo = (unsigned)layer * W_LAYER; asm volatile("" : "+s"(fo), "+s"(w), "+s"(bid), "+s"(nh), "+s"(nz), "+s"(mc), "+s"(qz), "+s"(tag)); hrdy = 0; tpp = 0; lt = LT1; xs = XS0; };
    auto load_qv = [&](u32x8 (&wt)[2][10], int hh, bool v, auto mid) INL {
        const bool o = v && w >= 4;
        load_w(wt, arg(A_WQ + 2 * v + (w >> 2)) + (o ? (hh * 128 + (w & 3) * 32) * D_ : hh * 128 + (w & 3) * 16 * HD), o ? D_ * 2 : HD * 2, !o, o ? 16 * D_ * 2 : 64 * HD * 2, o ? 256 : 128 * HD * 2, w, mid);
    };
    auto hseg = [&](int j) { return O_H + (unsigned)(j - NHF * layer) * 40960u; };
    auto stage2 = [&](int j0, int j1, bool vt) INL {
        if (w == 0) {
            if (j1 >= NHF) wait_h(rwin, fo, j1, hrdy);
            else if (TH) { const unsigned vo[1] = {(fresh_lane() & 3u) * 4u}; wait_told(rwin, vo, W_H + fo + (unsigned)j0 * 128u + 16u, 1); }
            else for (unsigned spins = 0;;) {
                asm volatile("" ::: "memory");
                const unsigned l = fresh_lane();
                if (!__any(__builtin_amdgcn_raw_buffer_load_b32(rws, (l & 31u) * 128u + (l >> 5) * (unsigned)(j1 - j0) * 40960u, hseg(j0) + 36864u, 16) == ~0u) || spin_fail(spins, rwin)) break;
                __builtin_amdgcn_s_sleep(HSLP);
            }
            *(volatile LAS int*)(smem + L_S + 32) = hrdy;
        }
        BAR();
        hrdy = __builtin_amdgcn_readfirstlane(*(volatile LAS int*)(smem + L_S + 32));
        SST(2);
        if (j0 == 1 && vt) __builtin_amdgcn_s_sleep(DH1);
        stage_h<5>(rws, hseg(j0) + w * 1024u, L_H + w * 1024u, 8192u);
        if (!vt) {
            stage_h<5>(rws, hseg(j1) + w * 1024u, L_H + 40960u + w * 1024u, 8192u);
            asm volatile("s_waitcnt vmcnt(5)" ::: "memory");
        } else if (w >= 4 && j1 != j0) {
            stage_h<10>(rws, hseg(j1) + (w - 4) * 1024u, L_H + 40960u + (w - 4) * 1024u, 4096u);
            asm volatile("s_waitcnt vmcnt(10)" ::: "memory");
        } else VMWAIT();
        check_h<5>(rws, rwin, hseg(j0) + w * 1024u, L_H + w * 1024u, 8192u);
        BAR0();
        SST(3);
        if (vt && w >= 4 && j1 != j0) {
            VMWAIT();
            check_h<10>(rws, rwin, hseg(j1) + (w - 4) * 1024u, L_H + 40960u + (w - 4) * 1024u, 4096u);
        }
    };
#define WO(eb, nt) wt[(nt) & 1][((nt) & ~1) + (eb)]
    unsigned zb = W_ZM;
    auto probe = [&]() INL {
        {
            unsigned* best = (unsigned*)smem;
            const int tid = mytid();
            const __amdgpu_buffer_rsrc_t rw = mkrsrc(a.win + (bid & 4 ? W_END : W_PROBE), 2 * PAIR);
            const unsigned base = (unsigned)(bid & 8) * 64u + w * 1024u;
            if (tid < 8) best[tid] = 100000u;
            if (w == 0) wait_tags(rwin, 0, 1, 64u, tag + 8ull, true);
            BAR();
            unsigned mn[4] = {~0u, ~0u, ~0u, ~0u};
            for (int r = 0; r < NPR; ++r) {
#pragma unroll
                for (int i = 0; i < 4; ++i) {
                    const unsigned long long t1 = __builtin_amdgcn_s_memtime();
                    asm volatile("s_waitcnt lgkmcnt(0)" ::: "memory");
                    unsigned v = __builtin_amdgcn_raw_buffer_load_b32(rw, base + r * 128u + i * HALF, 0, 16);
                    asm volatile("s_waitcnt vmcnt(0)" : "+v"(v) :: "memory");
                    const unsigned long long t2 = __builtin_amdgcn_s_memtime();
                    mn[i] = min(mn[i], (unsigned)(t2 - t1));
                }
            }
            if ((tid & 63) == 0) {
#pragma unroll
                for (int i = 0; i < 4; ++i) atomicMin(&best[i + (w & 4)], mn[i]);
            }
            BAR();
            const unsigned h = min(min(best[1], best[3]), min(best[5], best[7])) < min(min(best[0], best[2]), min(best[4], best[6])), q = best[h + 2] + best[h + 6] < best[h] + best[h + 4], k = best[h + 4] + best[h + 6] < best[h] + best[h + 2];
            if (w == 0) __builtin_amdgcn_raw_buffer_store_b32(h + 2u * q + 4u * k, rwin, LANE0, W_NEAR + (unsigned)(bid & 15) * 4u, 16);
            BAR();
        }
    };
    auto vo_role = [&](const u32x8 (&wt)[2][10]) INL {
        const int hh = bid & 31;
        auto steps = [&](auto cc) INL {
            constexpr int C = decltype(cc)::v & 1;
            constexpr bool TK = decltype(cc)::v == 3;
            LANES();
            Tk tk = {0u, 0u, 0x40000000u, nullptr};
            if (TK) { tk = tk_init(); __builtin_amdgcn_s_setprio(TKP); }
            const unsigned zi = (unsigned)(hh * 128 + fr * 8) * 2u;
            unsigned zo0 = zpair(w & 3, fq) + zi, zo1 = zo0 + 8 * PAIR, zo2 = zpair(w & 3, 8 + (fq >> 1)) + zi + (fq & 1) * 8;
            const LAS char* va = (const LAS char*)(smem + L_Q + (fr & 1) * 544 + ((fr >> 1) & 1) * 1152 + (16 * fq + 2 * (fr >> 2)) * 4);
            LAS char* vw = (LAS char*)(smem + L_Q + (fq & 1) * 544 + (fq >> 1) * 1152 + (16 * w + bs2(fr)) * 4);
            const LAS char* mp0 = (const LAS char*)(memL + ma0);
            PIN2(zo0, zo1); PIN1(zo2);
            if (C == 0) { PIN2(mp0, vw); } else PIN1(va);
            u32x4 za = SENT, zc = SENT;
            u32x2 ze = {0u, 0u};
            auto put_z2 = [&](int s) {
                const unsigned z2b = s < NSEG ? O_Z2 + (unsigned)s * (NOWN * 8192u) + (unsigned)(10 * (w & 3) * 8192 + hh * 256) : 0x40000000u, l2 = fresh_lane();
                st128(za, rws, (l2 >> 4) * 8192u + (l2 & 15u) * 16u, z2b, 16);
                st128(zc, rws, (l2 >> 4) * 8192u + (l2 & 15u) * 16u, z2b + 4u * 8192u, 16);
                __builtin_amdgcn_raw_buffer_store_b64(ze, rws, (l2 >> 5) * 8192u + (l2 & 15u) * 16u + ((l2 >> 4) & 1u) * 8u, z2b + 8u * 8192u, 16);
            };
            for (int s = 0; s < NSEG; ++s) {
                RT(0);
                bool redo = false;
                unsigned sf = 0;
                do {
                    ++it;
                    if (C == 0) {
                        const unsigned lim = s > 0 ? poll_lim(budget) : ~0u;
                        asm volatile(
                            "v_mov_b64 v[200:201], 0\n\t"
                            "v_mov_b64 v[202:203], 0\n\t"
                            "v_mov_b64 v[204:205], 0\n\t"
                            "v_mov_b64 v[206:207], 0\n\t"
                            "v_mov_b32 v208, 0\n\t"
                            "s_setprio 2\n\t"
                            ALN
                            "s_waitcnt lgkmcnt(0)\n\t"
                            "s_barrier\n\t"
                            MTA(0)
                            "ds_read_b128 v[212:215], %[mp] offset:" VOFF0 "\n\t"
                            "ds_read_b128 v[216:219], %[mp] offset:" VOFF1 "\n\t"
                            "ds_read_b128 v[220:223], %[mp] offset:" VOFF2 "\n\t"
                            "s_barrier\n\t"
                            "ds_read_b128 v[224:227], %[mp] offset:" VOFF3 "\n\t"
                            "ds_read_b128 v[228:231], %[mp] offset:" VOFF4 "\n\t"
                            "ds_read_b128 v[232:235], %[mp] offset:" VOFF5 "\n\t"
                            "s_waitcnt lgkmcnt(5)\n\t"
                            "v_smfmac_f32_16x16x64_bf16 v[200:203], v[212:215], %[w00], %[ix]\n\t"
                            "s_waitcnt lgkmcnt(4)\n\t"
                            "v_smfmac_f32_16x16x64_bf16 v[200:203], v[216:219], %[w01], %[ix]\n\t"
                            "s_barrier\n\t"
                            MTA(1)
                            "ds_read_b128 v[236:239], %[mp] offset:" VOFF6 "\n\t"
                            "s_waitcnt lgkmcnt(4)\n\t"
                            "v_smfmac_f32_16x16x64_bf16 v[200:203], v[220:223], %[w02], %[ix]\n\t"
                            "ds_read_b128 v[240:243], %[mp] offset:" VOFF7 "\n\t"
                            "s_waitcnt lgkmcnt(4)\n\t"
                            "v_smfmac_f32_16x16x64_bf16 v[200:203], v[224:227], %[w03], %[ix]\n\t"
                            "ds_read_b128 v[244:247], %[mp] offset:" VOFF8 "\n\t"
                            "s_waitcnt lgkmcnt(4)\n\t"
                            "v_smfmac_f32_16x16x64_bf16 v[200:203], v[228:231], %[w04], %[ix]\n\t"
                            "ds_read_b128 v[248:251], %[mp] offset:" VOFF9 "\n\t"
                            "s_waitcnt lgkmcnt(4)\n\t"
                            "v_smfmac_f32_16x16x64_bf16 v[200:203], v[232:235], %[w05], %[ix]\n\t"
                            "s_waitcnt lgkmcnt(3)\n\t"
                            "v_smfmac_f32_16x16x64_bf16 v[200:203], v[236:239], %[w06], %[ix]\n\t"
                            "s_waitcnt lgkmcnt(2)\n\t"
                            "v_smfmac_f32_16x16x64_bf16 v[200:203], v[240:243], %[w07], %[ix]\n\t"
                            "s_waitcnt lgkmcnt(1)\n\t"
                            "v_smfmac_f32_16x16x64_bf16 v[200:203], v[244:247], %[w08], %[ix]\n\t"
                            "s_waitcnt lgkmcnt(0)\n\t"
                            "v_smfmac_f32_16x16x64_bf16 v[200:203], v[248:251], %[w09], %[ix]\n\t"
                            "s_nop 2\n\t"
                            "v_smfmac_f32_16x16x64_bf16 v[204:207], v[212:215], %[w10], %[ix]\n\t"
                            "s_nop 2\n\t"
                            "v_smfmac_f32_16x16x64_bf16 v[204:207], v[216:219], %[w11], %[ix]\n\t"
                            "v_add_f32 v200, v200, v202\n\t"
                            "v_add_f32 v201, v201, v203\n\t"
                            "v_cvt_pk_bf16_f32 v200, v200, v201\n\t"
                            "ds_write_b32 %[vw], v200\n\t"
                            MTA(2)
                            "v_smfmac_f32_16x16x64_bf16 v[204:207], v[220:223], %[w12], %[ix]\n\t"
                            "v_max3_u32 v208, v208, v212, v213\n\t"
                            "v_max3_u32 v208, v208, v214, v215\n\t"
                            "v_smfmac_f32_16x16x64_bf16 v[204:207], v[224:227], %[w13], %[ix]\n\t"
                            "v_max3_u32 v208, v208, v216, v217\n\t"
                            "v_max3_u32 v208, v208, v218, v219\n\t"
                            "v_smfmac_f32_16x16x64_bf16 v[204:207], v[228:231], %[w14], %[ix]\n\t"
                            "v_max3_u32 v208, v208, v220, v221\n\t"
                            "v_max3_u32 v208, v208, v222, v223\n\t"
                            "v_smfmac_f32_16x16x64_bf16 v[204:207], v[232:235], %[w15], %[ix]\n\t"
                            "v_max3_u32 v208, v208, v224, v225\n\t"
                            "v_max3_u32 v208, v208, v226, v227\n\t"
                            "v_smfmac_f32_16x16x64_bf16 v[204:207], v[236:239], %[w16], %[ix]\n\t"
                            "v_max3_u32 v208, v208, v228, v229\n\t"
                            "v_max3_u32 v208, v208, v230, v231\n\t"
                            "v_smfmac_f32_16x16x64_bf16 v[204:207], v[240:243], %[w17], %[ix]\n\t"
                            "s_waitcnt lgkmcnt(0)\n\t"
                            "s_barrier\n\t"
                            MTA(5)
                            "v_smfmac_f32_16x16x64_bf16 v[204:207], v[244:247], %[w18], %[ix]\n\t"
                            "v_max3_u32 v208, v208, v232, v233\n\t"
                            "v_max3_u32 v208, v208, v234, v235\n\t"
                            "v_smfmac_f32_16x16x64_bf16 v[204:207], v[248:251], %[w19], %[ix]\n\t"
                            "v_max3_u32 v208, v208, v236, v237\n\t"
                            "v_max3_u32 v208, v208, v238, v239\n\t"
                            "v_max3_u32 v208, v208, v240, v241\n\t"
                            "v_max3_u32 v208, v208, v242, v243\n\t"
                            "v_max3_u32 v208, v208, v244, v245\n\t"
                            "v_max3_u32 v208, v208, v246, v247\n\t"
                            "v_max3_u32 v208, v208, v248, v249\n\t"
                            "v_max3_u32 v208, v208, v250, v251\n\t"
                            "v_cmp_lt_u32 vcc, %[lim], v208\n\t"
                            "v_add_f32 v204, v204, v206\n\t"
                            "v_add_f32 v205, v205, v207\n\t"
                            "v_cvt_pk_bf16_f32 v204, v204, v205\n\t"
                            "ds_write_b32 %[vw], v204 offset:256\n\t"
                            MTA(3)
                            "s_cbranch_vccz 1f\n\t"
                            "v_mov_b32 v208, %[it]\n\t"
                            "v_mov_b32 v209, 0\n\t"
                            "ds_write_b32 v209, v208 offset:%[ls]\n\t"
                            "1:\n\t"
                            "s_waitcnt vmcnt(0) lgkmcnt(0)\n\t"
                            "s_barrier\n\t"
                            "s_setprio 0\n\t"
                            :: [mp] "v"(mp0), [vw] "v"(vw), [ix] "v"(sidx), [lim] "s"(lim), [it] "s"(it), [ls] "n"(L_S),
                              [w00] "v"(wt[0][VREV ? 9 - 0 : 0]), [w01] "v"(wt[0][VREV ? 9 - 1 : 1]), [w02] "v"(wt[0][VREV ? 9 - 2 : 2]), [w03] "v"(wt[0][VREV ? 9 - 3 : 3]), [w04] "v"(wt[0][VREV ? 9 - 4 : 4]), [w05] "v"(wt[0][VREV ? 9 - 5 : 5]), [w06] "v"(wt[0][VREV ? 9 - 6 : 6]), [w07] "v"(wt[0][VREV ? 9 - 7 : 7]), [w08] "v"(wt[0][VREV ? 9 - 8 : 8]), [w09] "v"(wt[0][VREV ? 9 - 9 : 9]), [w10] "v"(wt[1][VREV ? 9 - 0 : 0]), [w11] "v"(wt[1][VREV ? 9 - 1 : 1]), [w12] "v"(wt[1][VREV ? 9 - 2 : 2]), [w13] "v"(wt[1][VREV ? 9 - 3 : 3]), [w14] "v"(wt[1][VREV ? 9 - 4 : 4]), [w15] "v"(wt[1][VREV ? 9 - 5 : 5]), [w16] "v"(wt[1][VREV ? 9 - 6 : 6]), [w17] "v"(wt[1][VREV ? 9 - 7 : 7]), [w18] "v"(wt[1][VREV ? 9 - 8 : 8]), [w19] "v"(wt[1][VREV ? 9 - 9 : 9])
                            : "v200", "v201", "v202", "v203", "v204", "v205", "v206", "v207", "v208", "v209", "v212", "v213", "v214", "v215", "v216", "v217", "v218", "v219", "v220", "v221", "v222", "v223", "v224", "v225", "v226", "v227", "v228", "v229", "v230", "v231", "v232", "v233", "v234", "v235", "v236", "v237", "v238", "v239", "v240", "v241", "v242", "v243", "v244", "v245", "v246", "v247", "v248", "v249", "v250", "v251", "vcc", "memory");
                        MT(4);
                        redo = __builtin_amdgcn_readfirstlane(*(volatile LAS unsigned*)(smem + L_S)) == it;
                        if (redo) FC(fc0);
                    } else {
                        f32x16 ca, cb;
                        f32x8 cc;
                        asm volatile("v_mov_b64 v[212:213], 0\n\tv_mov_b64 v[214:215], 0\n\tv_mov_b64 v[216:217], 0\n\tv_mov_b64 v[218:219], 0\n\tv_mov_b64 v[220:221], 0\n\tv_mov_b64 v[222:223], 0\n\tv_mov_b64 v[224:225], 0\n\tv_mov_b64 v[226:227], 0\n\tv_mov_b64 v[228:229], 0\n\tv_mov_b64 v[230:231], 0\n\tv_mov_b64 v[232:233], 0\n\tv_mov_b64 v[234:235], 0\n\tv_mov_b64 v[236:237], 0\n\tv_mov_b64 v[238:239], 0\n\tv_mov_b64 v[240:241], 0\n\tv_mov_b64 v[242:243], 0\n\tv_mov_b64 v[244:245], 0\n\tv_mov_b64 v[246:247], 0\n\tv_mov_b64 v[248:249], 0\n\tv_mov_b64 v[250:251], 0\n\t" : "={v[212:227]}"(ca), "={v[228:243]}"(cb), "={v[244:251]}"(cc));
                        unsigned sr = tro + (unsigned)(s & 3) * 2u * PAIR;
                        asm volatile("" : "+s"(sr));
                        if (TK) fetch(tk, s, !redo, s > 0 ? poll_lim(budget) : ~0u, 0u, 0u, false);
                        else asm volatile(ALN "s_waitcnt lgkmcnt(0)\n\ts_barrier\n\ts_barrier\n\ts_barrier" ::: "memory");
                        unsigned vd, vv, zs = zb;
                        u32x4 af1;
                        asm volatile(
                            "s_setprio 1\n\t"
                            "s_waitcnt lgkmcnt(0)\n\t"
                            "s_barrier\n\t"
                            "ds_read_b64_tr_b16 v[192:193], %[va]\n\t"
                            "ds_read_b64_tr_b16 v[194:195], %[va] offset:32\n\t"
                            "s_waitcnt lgkmcnt(0)\n\t"
                            "v_smfmac_f32_16x16x64_bf16 v[212:215], v[192:195], %[w0], %[ix]\n\t"
                            "v_smfmac_f32_16x16x64_bf16 v[220:223], v[192:195], %[w2], %[ix]\n\t"
                            "v_smfmac_f32_16x16x64_bf16 v[216:219], v[192:195], %[w1], %[ix]\n\t"
                            "v_smfmac_f32_16x16x64_bf16 v[224:227], v[192:195], %[w3], %[ix]\n\t"
                            "s_barrier\n\t"
                            MTA(4)
                            "ds_read_b64_tr_b16 v[196:197], %[va] offset:256\n\t"
                            "v_smfmac_f32_16x16x64_bf16 v[228:231], v[192:195], %[w4], %[ix]\n\t"
                            "ds_read_b64_tr_b16 v[198:199], %[va] offset:288\n\t"
                            "v_smfmac_f32_16x16x64_bf16 v[236:239], v[192:195], %[w6], %[ix]\n\t"
                            "ds_read_b32 v210, %[vz] offset:%[ls]\n\t"
                            "v_smfmac_f32_16x16x64_bf16 v[232:235], v[192:195], %[w5], %[ix]\n\t"
                            "v_smfmac_f32_16x16x64_bf16 v[240:243], v[192:195], %[w7], %[ix]\n\t"
                            "v_smfmac_f32_16x16x64_bf16 v[244:247], v[192:195], %[w8], %[ix]\n\t"
                            "v_smfmac_f32_16x16x64_bf16 v[248:251], v[192:195], %[w9], %[ix]\n\t"
                            "buffer_load_dword %[sf], %[sl], %[rw], %[sr] offen sc1\n\t"
                            : [sf] "=&v"(sf), "+{v[212:227]}"(ca), "+{v[228:243]}"(cb), "+{v[244:251]}"(cc), "=&{v[196:199]}"(af1), "=&{v210}"(vv)
                            : [va] "v"(va), [vz] "v"(0u), [ls] "n"(L_S), [ix] "v"(sidx), [sl] "v"(tk.sl), [sr] "s"(sr), [rw] "s"(rwin),
                              [w0] "v"(WO(0, 0)), [w1] "v"(WO(0, 1)), [w2] "v"(WO(0, 2)), [w3] "v"(WO(0, 3)), [w4] "v"(WO(0, 4)), [w5] "v"(WO(0, 5)), [w6] "v"(WO(0, 6)), [w7] "v"(WO(0, 7)), [w8] "v"(WO(0, 8)), [w9] "v"(WO(0, 9))
                            : "v192", "v193", "v194", "v195", "memory");
                        asm volatile(
                            "s_waitcnt lgkmcnt(0)\n\t"
                            "v_smfmac_f32_16x16x64_bf16 v[212:215], v[196:199], %[w0], %[ix]\n\t"
                            "v_readfirstlane_b32 %[vd], v210\n\t"
                            "v_smfmac_f32_16x16x64_bf16 v[220:223], v[196:199], %[w2], %[ix]\n\t"
                            "s_cmp_eq_u32 %[vd], %[it]\n\t"
                            "s_cselect_b32 %[zs], 2.0, %[zs]\n\t"
                            "v_smfmac_f32_16x16x64_bf16 v[216:219], v[196:199], %[w1], %[ix]\n\t"
                            "s_nop 1\n\t"
                            "v_smfmac_f32_16x16x64_bf16 v[224:227], v[196:199], %[w3], %[ix]\n\t"
                            "v_add_f32 v212, v212, v214\n\t"
                            "v_smfmac_f32_16x16x64_bf16 v[228:231], v[196:199], %[w4], %[ix]\n\t"
                            "v_add_f32 v213, v213, v215\n\t"
                            "v_smfmac_f32_16x16x64_bf16 v[236:239], v[196:199], %[w6], %[ix]\n\t"
                            "v_add_f32 v220, v220, v222\n\t"
                            "v_smfmac_f32_16x16x64_bf16 v[232:235], v[196:199], %[w5], %[ix]\n\t"
                            "v_add_f32 v221, v221, v223\n\t"
                            "v_smfmac_f32_16x16x64_bf16 v[240:243], v[196:199], %[w7], %[ix]\n\t"
                            "v_cvt_pk_bf16_f32 v200, v212, v213\n\t"
                            "v_smfmac_f32_16x16x64_bf16 v[244:247], v[196:199], %[w8], %[ix]\n\t"
                            "v_cvt_pk_bf16_f32 v201, v220, v221\n\t"
                            "v_smfmac_f32_16x16x64_bf16 v[248:251], v[196:199], %[w9], %[ix]\n\t"
                            "v_add_f32 v216, v216, v218\n\t"
                            "v_add_f32 v217, v217, v219\n\t"
                            "v_add_f32 v224, v224, v226\n\t"
                            "v_add_f32 v225, v225, v227\n\t"
                            "v_cvt_pk_bf16_f32 v202, v216, v217\n\t"
                            "v_cvt_pk_bf16_f32 v203, v224, v225\n\t"
                            "v_permlane32_swap_b32 v200, v201\n\t"
                            "v_pk_add_f32 v[228:229], v[228:229], v[230:231]\n\t"
                            "v_permlane32_swap_b32 v202, v203\n\t"
                            "v_min_u32 v201, -2, v201\n\t"
                            "v_pk_add_f32 v[236:237], v[236:237], v[238:239]\n\t"
                            "v_min_u32 v203, -2, v203\n\t"
                            "v_permlane16_swap_b32 v200, v202\n\t"
                            "v_pk_add_f32 v[232:233], v[232:233], v[234:235]\n\t"
                            "v_permlane16_swap_b32 v201, v203\n\t"
                            "v_pk_add_f32 v[240:241], v[240:241], v[242:243]\n\t"
                            MTA(5)
                            "buffer_store_dwordx4 v[200:203], %[z0], %[rw], %[zs] offen sc1\n\t"
                            "v_cvt_pk_bf16_f32 v204, v228, v229\n\t"
                            "v_cvt_pk_bf16_f32 v205, v236, v237\n\t"
                            "v_cvt_pk_bf16_f32 v206, v232, v233\n\t"
                            "v_cvt_pk_bf16_f32 v207, v240, v241\n\t"
                            "v_permlane32_swap_b32 v204, v205\n\t"
                            "v_pk_add_f32 v[244:245], v[244:245], v[246:247]\n\t"
                            "v_permlane32_swap_b32 v206, v207\n\t"
                            "v_min_u32 v205, -2, v205\n\t"
                            "v_pk_add_f32 v[248:249], v[248:249], v[250:251]\n\t"
                            "v_min_u32 v207, -2, v207\n\t"
                            "v_permlane16_swap_b32 v204, v206\n\t"
                            "v_cvt_pk_bf16_f32 v208, v244, v245\n\t"
                            "v_permlane16_swap_b32 v205, v207\n\t"
                            "v_cvt_pk_bf16_f32 v209, v248, v249\n\t"
                            "buffer_store_dwordx4 v[204:207], %[z1], %[rw], %[zs] offen sc1\n\t"
                            "s_nop 0\n\t"
                            "v_permlane32_swap_b32 v208, v209\n\t"
                            "v_min_u32 v209, -2, v209\n\t"
                            "buffer_store_dwordx2 v[208:209], %[z2], %[rw], %[zs] offen sc1\n\t"
                            : [vd] "=&s"(vd), [zs] "+s"(zs), "=&{v[200:203]}"(za), "=&{v[204:207]}"(zc), "=&{v[208:209]}"(ze), "+{v[212:227]}"(ca), "+{v[228:243]}"(cb), "+{v[244:251]}"(cc), "+v"(sf)
                            : [ix] "v"(sidx), [it] "s"(it), [rw] "s"(rwin), [z0] "v"(zo0), [z1] "v"(zo1), [z2] "v"(zo2), "{v[196:199]}"(af1), "{v210}"(vv),
                              [w0] "v"(WO(1, 0)), [w1] "v"(WO(1, 1)), [w2] "v"(WO(1, 2)), [w3] "v"(WO(1, 3)), [w4] "v"(WO(1, 4)), [w5] "v"(WO(1, 5)), [w6] "v"(WO(1, 6)), [w7] "v"(WO(1, 7)), [w8] "v"(WO(1, 8)), [w9] "v"(WO(1, 9))
                            : "scc", "memory");
                        redo = vd == it;
                        RT(4);
                        MT(6);
                        tref = __builtin_amdgcn_s_memtime();
                        __builtin_amdgcn_s_setprio(TKP);
                        asm volatile("s_waitcnt vmcnt(3)" : "+v"(sf) :: "memory");
                    }
                } while (__builtin_expect(redo, 0));
                if (s < 8) SST(8 + s);
                if (C == 0) {
                    if (s > 0 && s + 1 < NSEG) {
                        __builtin_amdgcn_s_sleep(DZR);
                        const unsigned zq = zb == W_ZM ? W_ZM + 2u * NOWN * PAIR : zb - NOWN * PAIR;
                        unsigned s1;
                        asm volatile("v_mov_b32 %0, -1" : "=v"(s1));
                        st128(((u32x4){s1, s1, s1, s1}), rwin, zo0, zq, 16);
                        st128(((u32x4){s1, s1, s1, s1}), rwin, zo1, zq, 16);
                        __builtin_amdgcn_raw_buffer_store_b64((u32x2){s1, s1}, rwin, zo2, zq, 16);
                    }
                } else {
                    if (s > TAILS) __builtin_amdgcn_raw_buffer_store_b32((unsigned)s - 1u, rwin, 0u, W_FZ2 + fo + (unsigned)(hh * 128 + (w & 3) * 4), 16);
                    if (TK && __builtin_expect(s == 0, 0)) { wait_h(rwin, fo, 5, hrdy); clk_init(ck); }
                    if (TK && s + 1 < NSEG) sched2(tk, s, sf);
                    if (!TK) wait_until(tref + DZ2);
                    if (s + 2 < NSEG) put_z2(s);
                }
                if (budget <= 0) set_tmo(rwin);
                zb = zb == W_ZM + 2u * NOWN * PAIR ? W_ZM : zb + NOWN * PAIR;
                RT(5);
                RTS();
                MT(7);
                MTS();
            }
        };
        const int tid = mytid();
        gather_mem0(memL, tid);
        if (tid < 8) *(unsigned*)(smem + L_S + tid * 4) = 0u;
        if (w < 4) steps(IC<0>()); else if (w < 7) steps(IC<1>()); else steps(IC<3>());
    };
    auto vt_role = [&](const u32x8 (&wt)[2][10], int hh, unsigned long long mv) INL {
        constexpr bool vt = true;
        const int jl = 63 - __builtin_clzll(mv);
        auto nextv = [&](int j) { return min(seg_after(mv, j), jl); };
        const int jv1 = nextv(-1), jv2 = nextv(jv1);
        if (w == 0) *(unsigned*)(smem + L_S + (fresh_lane() & 7u) * 4) = 0u;
        stage2(jv1, jv2, true);
        {
            LANES();
            const int ni = __builtin_popcountll(mv) + 1;
            if (w < 4) {
                const LAS char* c0 = (const LAS char*)(hL + hoff + ((fq ^ (fr & 7)) << 4));
                const LAS char* c1 = (const LAS char*)(hL + hoff + (((4 + fq) ^ (fr & 7)) << 4));
                const LAS char* n0 = c0 + 40960; const LAS char* n1 = c1 + 40960;
                u32x4 bb[8];
                project0(bb, c0, c1);
                for (int i = 1; i < ni; ++i) {
                    if (vt) MT(0);
                    f32x4 tc[2][2];
                    project<0>(wt, tc, bb, c0, c1, n0, n1,
                        [&]() {
                            MT(1);
                            if (i == 1) SST(16);
                            asm volatile("s_waitcnt vmcnt(0) lgkmcnt(0)\n\ts_barrier" ::: "memory");
                            if (i == 1) SST(17);
                            MT(2);
                        }, [](auto) {});
                    { const LAS char* x = c0; c0 = n0; n0 = x; x = c1; c1 = n1; n1 = x; }
                    char* tb = tokL + (i & 1) * 32 * QS + (16 * w + 4 * fq) * 2;
#pragma unroll
                    for (int g = 0; g < 2; ++g)
#pragma unroll
                        for (int tt = 0; tt < 2; ++tt) *(u32x2*)(tb + (prow + 8 * tt) * QS + g * 128) = (u32x2){pack2(tc[g][tt][0], tc[g][tt][1]), pack2(tc[g][tt][2], tc[g][tt][3])};
                    asm volatile("" ::: "memory");
                    *(volatile LAS int*)(smem + L_S + 16 + w * 4) = i;
                    if (i == 1) SST(18);
                    if (__builtin_expect(i == 1, 0)) __builtin_amdgcn_s_sleep(VSL);
                    if (vt) { MT(3); const int s = i & 63; MTRT(); MTS(); }
                }
                project_end(bb);
            } else {
                __builtin_amdgcn_s_setprio(OPR);
                const unsigned hv = lane * 16u, fz_ = W_FZ + fo + fz_at(hh, w - 4) + (jv1 ? 4096u : 0u);
                unsigned fp = ~0u;
                unsigned fzp = 0x40000000u;
                int jo = jv1, jh = jv2;
                for (int i = 1; i < ni; ++i, jo = nextv(jo)) {
                    const int b = i + (i >= ZLAG);
                    if (__builtin_expect(i == 1 || i == ZLAG, 0)) {
                        VMWAIT();
                        if (i == ZLAG) {
                            jh = nextv(jh);
                            wait_h(rwin, fo, jh, hrdy);
                            BAR0();
                            for (int t = 0; t < 10; ++t) dma1k(rws, hv, hseg(jh) + (unsigned)((w - 4) * 1024 + t * 4096), (unsigned)(L_H + ((i + 1) & 1) * 40960 + (w - 4) * 1024 + t * 4096));
                            VMWAIT();
                        }
                    }
                    jh = nextv(jh);
                    if (b < ni) BAR0();
                    if (__builtin_expect(jo + layer == 0 && w == 7, 0)) near_init();
                    wait_h(rwin, fo, jh, hrdy);
                    if (i == 1) SST(19);
                    if (vt) MT(1);
                    for (;;) {
                        const u32x4 f = *(volatile LAS u32x4*)(smem + L_S + 16);
                        if (__builtin_amdgcn_readfirstlane(min(min(f[0], f[1]), min(f[2], f[3]))) >= (unsigned)i) break;
                        __builtin_amdgcn_s_sleep(1);
                    }
                    asm volatile("" ::: "memory");
                    if (i == 1) SST(20);
                    if (__builtin_expect(jo == 0 && w == 7, 0)) __builtin_amdgcn_raw_buffer_store_b32((4u * (unsigned)__builtin_amdgcn_s_memrealtime() + (layer ? DVT : DVT0)) & 0xFFFFFFu, rwin, two(), W_T + (unsigned)hh * 128u + 4u, 16);
                    if (vt) MT(0);
                    u32x4 vf[2][4];
#pragma unroll
                    for (int t = 0; t < 2; ++t)
#pragma unroll
                        for (int ks = 0; ks < 4; ++ks) vf[t][ks] = *(const u32x4*)(tokL + (i & 1) * 32 * QS + (16 * t + fr) * QS + ks * 64 + fq * 16);
                    const unsigned ztb = O_Z + (unsigned)((jo - layer) * (int)ZTSEG) * 2u + (unsigned)(10 * (w - 4) * H_ + hh) * 1024u;
                    f32x4 c[2][2];
                    u32x4 r;
                    unsigned hs = hseg(jh) + (unsigned)((w - 4) * 1024), hd = (unsigned)(L_H + ((b + 1) & 1) * 40960 + (w - 4) * 1024);
                    asm volatile("" : "+s"(hs), "+s"(hd));
#pragma unroll
                    for (int nt = 0; nt < 11; ++nt) {
                        auto& cc = c[nt & 1]; auto& cp = c[(nt + 1) & 1];
#pragma unroll
                        for (int ks = 0; ks < 4; ++ks) {
                            if (nt < 10) { cc[0] = mfma32(vf[0][ks], half8(WO(ks >> 1, nt < 10 ? nt : 0), ks & 1), ks ? cc[0] : z4); PIN1(cc[0]); }
                            if (nt > 0 && ks >= 1 && ks < 3) { r[2 * ks - 2] = pack2(cp[ks - 1][0], cp[ks - 1][1]); PIN1(r); }
                            if (nt < 10) { cc[1] = mfma32(vf[1][ks], half8(WO(ks >> 1, nt < 10 ? nt : 0), ks & 1), ks ? cc[1] : z4); PIN1(cc[1]); }
                            if (nt > 0 && ks >= 1 && ks < 3) { r[2 * ks - 1] = nosent1(pack2(cp[ks - 1][2], cp[ks - 1][3])); PIN1(r); }
                            if (nt > 0 && ks == 2) st128(r, rws, lane * 16u, ztb + (unsigned)((nt - 1) * H_) * 1024u, 16);
                            if (nt < 5 && !(ks & 1)) dma1k(rws, hv, hs + (2 * nt + (ks >> 1)) * 4096u, hd + (2 * nt + (ks >> 1)) * 4096u);
                            if (nt == 5 && ks == 0) {
                                asm volatile("s_waitcnt vmcnt(14)" ::: "memory");
                                __builtin_amdgcn_raw_buffer_store_b32(fp, rwin, 0u, fzp, 16);
                            }
                            SB();
                        }
                    }
                    if (vt) MT(2);
                    if (i == 1) SST(21);
                    fp = (unsigned)jo;
                    fzp = fz_;
                    if (__builtin_expect(jo <= ZEARLY || i + 1 == ni, 0)) {
                        VMWAIT();
                        __builtin_amdgcn_raw_buffer_store_b32(fp, rwin, 0u, fz_, 16);
                    } else asm volatile("s_waitcnt vmcnt(7)" ::: "memory");
                    if (jo < 8) SST(8 + jo);
                    if (vt && (!BSPARSE || jo % 6 == 2)) { const int s = jo; RT(0); RTS(); }
                    if (vt) { MT(3); const int s = i & 63; MTRT(); MTS(); }
                }
            }
            __builtin_amdgcn_s_setprio(0);
            VMWAIT();
            BAR();
        }
    };
    auto qt_role = [&](const u32x8 (&wt)[2][10], int hh, auto qq) INL {
        constexpr bool qt = decltype(qq)::v;
        const unsigned long long mq = bid < NQT ? MXQ : ~MXQ;
        const int jl = 63 - __builtin_clzll(mq);
        auto nextq = [&](int j) { return min(seg_after(mq, j), jl); };
        const int jq0 = qt ? nextq(0) : 0, jq1 = qt ? nextq(jq0) : 0;
        if (qt && jq0 + layer == 1 && w == 7) near_init();
        stage2(jq0, jq1, !qt);
        if (qt && jq0 == 1 && w == 7) __builtin_amdgcn_raw_buffer_store_b32((4u * (unsigned)__builtin_amdgcn_s_memrealtime() + (layer ? DQA : DQA0)) & 0xFFFFFFu, rwin, two(), W_T + (unsigned)hh * 128u + 8u, 16);
        auto check2 = [&]() INL { VMWAIT(); check_h<5>(rws, rwin, hseg(jq1) + w * 1024u, L_H + 40960u + w * 1024u, 8192u); };
        {
            LANES();
            const unsigned to0 = (unsigned)((w ^ 4) >> 2) * 8192u + (unsigned)((w & 3) * 64 + (lane ^ ((fq & 1) * (w < 4 ? 8 : 5)))) * 16u, to1 = to0 + 4096u, hv = lane * 16u;
            const LAS char* c0 = (const LAS char*)(hL + hoff + ((fq ^ (fr & 7)) << 4));
            const LAS char* c1 = (const LAS char*)(hL + hoff + (((4 + fq) ^ (fr & 7)) << 4));
            u32x4 bb[8], g0 = SENT, g1 = SENT;
            f32x4 tc[2][2];
            auto pack = [&]() {
                g0 = (u32x4){pack2(tc[0][0][0], tc[0][0][1]), pack2(tc[0][0][2], tc[0][0][3]), pack2(tc[1][0][0], tc[1][0][1]), pack2(tc[1][0][2], tc[1][0][3])};
                g1 = (u32x4){pack2(tc[0][1][0], tc[0][1][1]), pack2(tc[0][1][2], tc[0][1][3]), pack2(tc[1][1][0], tc[1][1][1]), pack2(tc[1][1][2], tc[1][1][3])};
            };
            const LAS char* n0 = c0 + 40960; const LAS char* n1 = c1 + 40960;
            if (!qt) {
                project0(bb, c0, c1);
                project<0>(wt, tc, bb, c0, c1, c0, c1, []() {}, [](auto) {});
                pack();
                *(u32x4*)(tokL + to0) = g0; *(u32x4*)(tokL + to1) = g1;
                project_end(bb);
            } else {
                project0(bb, c0, c1);
                unsigned hs = hseg(jq1) + (unsigned)(w * 1024), hd = (unsigned)(L_H + 40960 + w * 1024), fv = lane == 0 ? 0u : 0x40000000u, fj = 0;
                const unsigned fq_ = W_FQ + fo + (unsigned)(hh * 128 + (jq0 > 1 ? 4096 : 0) + w * 4);
                for (int j = jq0, jn = jq1;; j = jn, jn = nextq(jn)) {
                    MT(0);
                    project<0>(wt, tc, bb, c0, c1, n0, n1,
                        [&]() {
                            if (__builtin_expect(j == jq0, 0)) check2();
                            wait_h(rwin, fo, nextq(jn), hrdy);
                            hs = hseg(nextq(jn)) + (unsigned)(w * 1024); hd = (unsigned)(2 * L_H + 40960 + 2 * w * 1024) - hd;
                            asm volatile("" : "+s"(hs), "+s"(hd));
                            MT(1);
                            VMWAIT();
                            BAR();
                            MT(2);
                        },
                        [&](auto uu) {
                            constexpr int u = decltype(uu)::v;
                            if (u == 34 || u == 38 || u == 2 || u == 6 || u == 10) { constexpr int t = ((u + 6) % 40) / 4; dma1k(rws, hv, hs + t * 8192u, hd + t * 8192u); }
                            if (u == UFQ) { asm volatile("s_waitcnt vmcnt(3)" ::: "memory"); __builtin_amdgcn_raw_buffer_store_b32(fj, rwin, fv, fq_, 16); asm volatile("" ::: "memory"); }
                        });
                    pack();
                    const unsigned qs = O_QK + (unsigned)(j * H_ + hh) * 16384u;
                    st128(g0, rws, to0, qs, 16);
                    st128(g1, rws, to1, qs, 16);
                    { const LAS char* x = c0; c0 = n0; n0 = x; x = c1; c1 = n1; n1 = x; }
                    fj = (unsigned)j;
                    PIN1(fj);
                    if (__builtin_expect(j <= QEARLY || j == jl, 0)) {
                        VMWAIT();
                        __builtin_amdgcn_raw_buffer_store_b32(fj, rwin, fv, fq_, 16);
                        if (j == jl) break;
                    }
                    MT(3);
                    if (j < 8) SST(8 + j);
                    if (!BSPARSE || j % 6 == 2) { const int s = j; RT(0); RTS(); MTRT(); MTS(); }
                }
                project_end(bb);
                VMWAIT();
                BAR0();
            }
        }
        SST(4);
    };
    auto qk_role = [&](const u32x8 (&wt)[2][10]) INL {
        const int hh = bid;
        int qrdy = 0;
        auto steps = [&](auto cc) INL {
        constexpr int C = decltype(cc)::v >> 1, R = decltype(cc)::v & 1;
        LANES();
        const LAS char* ka = (const LAS char*)(smem + L_K + (fr & 1) * TQS + (4 * fq + (fr >> 2)) * 8);
        const LAS char* qa = (const LAS char*)(smem + L_Q + (fr & 1) * 544 + ((fr >> 1) & 1) * 1152 + (16 * fq + 2 * (fr >> 2)) * 4);
        LAS char* qw = (LAS char*)(C < 2 ? smem + L_Q + (fq & 1) * 544 + (fq >> 1) * 1152 + (32 * w + 8 * (fr >> 2) + bs2(fr & 3)) * 4
                                         : smem + L_K + (fq >> 1) * TQS + (32 * (w & 3) + fr) * 8 + (fq & 1) * 4);
        const LAS char* mp0 = (const LAS char*)(memL + ma0);
        const int z = 16 * (fr >> 3) + 4 * (fr & 3) + 2 * ((fr >> 2) & 1);
        const int ko = ((z >> 4) * 256 + fq * 16 + ((z & 15) ^ ((fq & 1) * 5))) * 16;
        unsigned pa0 = 2u * (hh >> 3) * PAIR + (unsigned)(C * 4096 + (hh & 7) * 512 + R * 256 + lane * 4);
        unsigned pa2 = ((8u + (fr >> 3)) * PAIR + C * 4096u + (unsigned)((hh >> 1) * 256 + R * 128 + (hh & 1) * 64 + fq * 16 + (fr & 6) * 2)) | (fr & 1) << 30;
        PIN2(ka, qw); PIN1(mp0);
        constexpr bool TK = C == 2 && R == 1;
        Tk tk = {0u, 0u, 0x40000000u, nullptr};
        if (TK) { tk = tk_init(); __builtin_amdgcn_s_setprio(TKP); }
        if (C < 2) { PIN2(qa, pa0); PIN1(pa2); }
        int qf_ = 0;
        int tbo = 0;
        for (int s = 0; s < NSEG; ++s) {
            RT(0);
            const char* tb = tokL + tbo;
            unsigned pb = __builtin_amdgcn_readfirstlane(s + 1 < NSEG ? p_slot(s) + nh : 0x40000000u);
            asm volatile("" : "+s"(pb));
            bool redo = false;
            unsigned sf = 0;
            if (s <= 1) BAR();
            u32x8 kx[2][2];
            if (C < 2) {
#pragma unroll
                for (int u = 0; u < 4; ++u) kx[u >> 1][u & 1] = cat8(*(const u32x4*)(tb + (ko ^ ((u >> 1) * 16)) + (u & 1) * 2048), *(const u32x4*)(tb + (ko ^ ((u >> 1) * 16)) + (u & 1) * 2048 + 1024));
                PIN2(kx[0][0], kx[0][1]); PIN2(kx[1][0], kx[1][1]);
            }
            do {
                ++it;
                f32x4 a0 = zero4(), a1 = zero4();
                PIN2(a0, a1);
                u32x4 m[10];
                if (C < 2) {
                    __builtin_amdgcn_s_setprio(2);
                    asm volatile(ALN ::: "memory");
                    BAR();
                    MT(0);
#pragma unroll
                    for (int t = 0; t < NB1; ++t) { m[t] = *(const LAS u32x4*)(mp0 + t * 1024); SB(); }
                    BAR0();
#pragma unroll
                    for (int t = 0; t < 10; ++t) {
                        if (t == QB2) { BAR0(); MT(1); }
                        if (t + NB1 < 10) m[t + NB1] = *(const LAS u32x4*)(mp0 + (t + NB1) * 1024);
                        SB();
                        a0 = smfmac(m[t], wt[0][t], a0, sidx); a1 = smfmac(m[t], wt[1][t], a1, sidx);
                        PIN2(a0, a1);
                    }
                } else {
                    unsigned mx = 0;
                    asm volatile("" : "+{v[200:203]}"(a0), "+{v[204:207]}"(a1), "+{v208}"(mx));
                    unsigned lim = s > 0 ? poll_lim(budget) : ~0u, sr = tro + (unsigned)(s & 3) * 2u * PAIR;
                    asm volatile("" : "+s"(lim), "+s"(sr));
                    if (TK) fetch(tk, s, !redo, lim, DQC, (unsigned)hh * 128u, true);
                    else asm volatile(ALN "s_waitcnt lgkmcnt(0)\n\ts_barrier\n\ts_barrier\n\ts_barrier" ::: "memory");
                    asm volatile(
                        "ds_read_b128 v[212:215], %[mp] offset:0\n\t"
                        "ds_read_b128 v[216:219], %[mp] offset:1024\n\t"
                        "ds_read_b128 v[220:223], %[mp] offset:2048\n\t"
                        "ds_read_b128 v[224:227], %[mp] offset:3072\n\t"
                        "ds_read_b128 v[228:231], %[mp] offset:4096\n\t"
                        "ds_read_b128 v[232:235], %[mp] offset:5120\n\t"
                        "ds_read_b128 v[236:239], %[mp] offset:6144\n\t"
                        "ds_read_b128 v[240:243], %[mp] offset:7168\n\t"
                        "ds_read_b128 v[244:247], %[mp] offset:8192\n\t"
                        "ds_read_b128 v[248:251], %[mp] offset:9216\n\t"
                        MTA(4)
                        "s_waitcnt lgkmcnt(9)\n\t"
                        "v_smfmac_f32_16x16x64_bf16 v[200:203], v[212:215], %[w00], %[ix]\n\t"
                        "v_max3_u32 v208, v208, v212, v213\n\t"
                        "v_smfmac_f32_16x16x64_bf16 v[204:207], v[212:215], %[w10], %[ix]\n\t"
                        "s_waitcnt lgkmcnt(8)\n\t"
                        "v_max3_u32 v208, v208, v214, v215\n\t"
                        "v_smfmac_f32_16x16x64_bf16 v[200:203], v[216:219], %[w01], %[ix]\n\t"
                        "v_max3_u32 v208, v208, v216, v217\n\t"
                        "v_smfmac_f32_16x16x64_bf16 v[204:207], v[216:219], %[w11], %[ix]\n\t"
                        "s_waitcnt lgkmcnt(7)\n\t"
                        "v_max3_u32 v208, v208, v218, v219\n\t"
                        "v_smfmac_f32_16x16x64_bf16 v[200:203], v[220:223], %[w02], %[ix]\n\t"
                        "v_max3_u32 v208, v208, v220, v221\n\t"
                        "v_smfmac_f32_16x16x64_bf16 v[204:207], v[220:223], %[w12], %[ix]\n\t"
                        "s_waitcnt lgkmcnt(6)\n\t"
                        "v_max3_u32 v208, v208, v222, v223\n\t"
                        "v_smfmac_f32_16x16x64_bf16 v[200:203], v[224:227], %[w03], %[ix]\n\t"
                        "v_max3_u32 v208, v208, v224, v225\n\t"
                        "v_smfmac_f32_16x16x64_bf16 v[204:207], v[224:227], %[w13], %[ix]\n\t"
                        "s_waitcnt lgkmcnt(0)\n\t"
                        "v_max3_u32 v208, v208, v226, v227\n\t"
                        "v_smfmac_f32_16x16x64_bf16 v[200:203], v[228:231], %[w04], %[ix]\n\t"
                        "v_max3_u32 v208, v208, v228, v229\n\t"
                        "v_smfmac_f32_16x16x64_bf16 v[204:207], v[228:231], %[w14], %[ix]\n\t"
                        "v_max3_u32 v208, v208, v230, v231\n\t"
                        "v_smfmac_f32_16x16x64_bf16 v[200:203], v[232:235], %[w05], %[ix]\n\t"
                        "v_max3_u32 v208, v208, v232, v233\n\t"
                        "v_smfmac_f32_16x16x64_bf16 v[204:207], v[232:235], %[w15], %[ix]\n\t"
                        "v_max3_u32 v208, v208, v234, v235\n\t"
                        "v_smfmac_f32_16x16x64_bf16 v[200:203], v[236:239], %[w06], %[ix]\n\t"
                        "v_max3_u32 v208, v208, v236, v237\n\t"
                        "v_smfmac_f32_16x16x64_bf16 v[204:207], v[236:239], %[w16], %[ix]\n\t"
                        "v_max3_u32 v208, v208, v238, v239\n\t"
                        "v_smfmac_f32_16x16x64_bf16 v[200:203], v[240:243], %[w07], %[ix]\n\t"
                        "v_max3_u32 v208, v208, v240, v241\n\t"
                        "v_smfmac_f32_16x16x64_bf16 v[204:207], v[240:243], %[w17], %[ix]\n\t"
                        "v_max3_u32 v208, v208, v242, v243\n\t"
                        "v_smfmac_f32_16x16x64_bf16 v[200:203], v[244:247], %[w08], %[ix]\n\t"
                        "v_max3_u32 v208, v208, v244, v245\n\t"
                        "v_smfmac_f32_16x16x64_bf16 v[204:207], v[244:247], %[w18], %[ix]\n\t"
                        "v_max3_u32 v208, v208, v246, v247\n\t"
                        "v_smfmac_f32_16x16x64_bf16 v[200:203], v[248:251], %[w09], %[ix]\n\t"
                        "v_max3_u32 v208, v208, v248, v249\n\t"
                        "v_smfmac_f32_16x16x64_bf16 v[204:207], v[248:251], %[w19], %[ix]\n\t"
                        "v_max3_u32 v208, v208, v250, v251\n\t"
                        "buffer_load_dword %[sf], %[sl], %[rw], %[sr] offen sc1\n\t"
                        "v_cmp_lt_u32 vcc, %[lim], v208\n\t"
                        "s_nop 3\n\t"
                        "v_pk_add_f32 v[200:201], v[200:201], v[202:203]\n\t"
                        "v_pk_add_f32 v[204:205], v[204:205], v[206:207]\n\t"
                        "v_cvt_pk_bf16_f32 v200, v200, v201\n\t"
                        "v_cvt_pk_bf16_f32 v204, v204, v205\n\t"
                        "ds_write2_b32 %[qw], v200, v204 offset1:32\n\t"
                        MTA(2)
                        "s_cbranch_vccz 1f\n\t"
                        "v_mov_b32 v208, %[it]\n\t"
                        "v_mov_b32 v209, 0\n\t"
                        "ds_write_b32 v209, v208 offset:%[ls]\n\t"
                        "1:\n\t"
                        "s_waitcnt vmcnt(%[nv]) lgkmcnt(0)\n\t"
                        "s_barrier\n\t"
                        "s_waitcnt vmcnt(0)\n\t"
                        : [sf] "=&v"(sf), "+v"(qf_), "+{v[200:203]}"(a0), "+{v[204:207]}"(a1), "+{v208}"(mx)
                        : [mp] "v"(mp0), [qw] "v"(qw), [ix] "v"(sidx), [lim] "s"(lim), [it] "s"(it), [ls] "n"(L_S), [sl] "v"(tk.sl), [sr] "s"(sr), [rw] "s"(rwin), [nv] "n"(TK ? 2 : 1),
                          [w00] "v"(wt[0][0]), [w01] "v"(wt[0][1]), [w02] "v"(wt[0][2]), [w03] "v"(wt[0][3]), [w04] "v"(wt[0][4]), [w05] "v"(wt[0][5]), [w06] "v"(wt[0][6]), [w07] "v"(wt[0][7]), [w08] "v"(wt[0][8]), [w09] "v"(wt[0][9]), [w10] "v"(wt[1][0]), [w11] "v"(wt[1][1]), [w12] "v"(wt[1][2]), [w13] "v"(wt[1][3]), [w14] "v"(wt[1][4]), [w15] "v"(wt[1][5]), [w16] "v"(wt[1][6]), [w17] "v"(wt[1][7]), [w18] "v"(wt[1][8]), [w19] "v"(wt[1][9])
                        : "v209", "v212", "v213", "v214", "v215", "v216", "v217", "v218", "v219", "v220", "v221", "v222", "v223", "v224", "v225", "v226", "v227", "v228", "v229", "v230", "v231", "v232", "v233", "v234", "v235", "v236", "v237", "v238", "v239", "v240", "v241", "v242", "v243", "v244", "v245", "v246", "v247", "v248", "v249", "v250", "v251", "vcc", "memory");
                }
                if (C < 2) {
                    *(LAS unsigned*)qw = hpack(a0); *(LAS unsigned*)(qw + 16) = hpack(a1);
                    SB();
                    MT(2);
                    unsigned vd;
#define SC0 "v[212:215]"
#define SC1 "v[216:219]"
#define SC2 "v[220:223]"
#define DPP(op, d, ctl) op " " d ", " d ", " d " " ctl " row_mask:0xf bank_mask:0xf\n\t"
#define TAIL(A0, A2, B0, B2, C0, C2) \
                        "v_mov_b64 v[212:213], 0\n\tv_mov_b64 v[214:215], 0\n\tv_mov_b64 v[216:217], 0\n\tv_mov_b64 v[218:219], 0\n\tv_mov_b64 v[220:221], 0\n\tv_mov_b64 v[222:223], 0\n\t" \
                        "s_setprio 3\n\t" \
                        "s_waitcnt lgkmcnt(0)\n\t" \
                        "s_barrier\n\t" MTA(3) \
                        "ds_read_b64_tr_b16 v[224:225], %[qa]\n\t" \
                        "ds_read_b64_tr_b16 v[226:227], %[qa] offset:32\n\t" \
                        "ds_read_b64_tr_b16 v[228:229], %[qa] offset:256\n\t" \
                        "ds_read_b64_tr_b16 v[230:231], %[qa] offset:288\n\t" \
                        "ds_read_b64_tr_b16 v[232:233], %[ka]\n\t" \
                        "ds_read_b64_tr_b16 v[234:235], %[ka] offset:128\n\t" \
                        "ds_read_b64_tr_b16 v[236:237], %[ka] offset:256\n\t" \
                        "ds_read_b64_tr_b16 v[238:239], %[ka] offset:384\n\t" \
                        "ds_read_b64_tr_b16 v[240:241], %[ka] offset:512\n\t" \
                        "ds_read_b64_tr_b16 v[242:243], %[ka] offset:640\n\t" \
                        "s_waitcnt lgkmcnt(8)\n\t" \
                        "v_smfmac_f32_16x16x64_bf16 " SC0 ", v[224:227], %[k00], %[ix]\n\t" \
                        "ds_read_b64_tr_b16 v[244:245], %[ka] offset:768\n\t" \
                        "v_smfmac_f32_16x16x64_bf16 " SC1 ", v[224:227], %[k10], %[ix]\n\t" \
                        "ds_read_b64_tr_b16 v[246:247], %[ka] offset:896\n\t" \
                        "s_waitcnt lgkmcnt(8)\n\t" \
                        "v_smfmac_f32_16x16x64_bf16 " SC0 ", v[228:231], %[k01], %[ix]\n\t" \
                        "ds_read_b32 v248, %[vz] offset:%[ls]\n\t" \
                        "v_smfmac_f32_16x16x64_bf16 " SC1 ", v[228:231], %[k11], %[ix]\n\t" \
                        "s_waitcnt lgkmcnt(5)\n\t" \
                        "v_smfmac_f32_16x16x64_bf16 " SC2 ", v[224:227], v[232:239], %[ix]\n\t" \
                        "s_waitcnt lgkmcnt(1)\n\t" \
                        "v_smfmac_f32_16x16x64_bf16 " SC2 ", v[228:231], v[240:247], %[ix]\n\t" \
                        "s_waitcnt lgkmcnt(0)\n\t" \
                        MTA(4) "v_readfirstlane_b32 %[vd], v248\n\t" \
                        "v_add_f32 v232, " A0 ", " A2 "\n\t" \
                        "s_nop 0\n\t" \
                        "v_add_f32 v233, " B0 ", " B2 "\n\t" \
                        "v_max_f32 v235, v232, v233\n\t" \
                        "s_cmp_eq_u32 %[vd], %[it]\n\t" \
                        "s_cselect_b32 %[p0], 2.0, %[p0]\n\t" \
                        DPP("v_max_f32_dpp", "v235", "row_ror:8") \
                        "v_add_f32 v234, " C0 ", " C2 "\n\t" \
                        "v_max_f32 v235, v235, v234\n\t" \
                        "s_add_u32 %[p1], %[p0], 0x4000\n\t" \
                        "s_nop 0\n\t" DPP("v_max_f32_dpp", "v235", "row_ror:4") \
                        "s_nop 1\n\t" DPP("v_max_f32_dpp", "v235", "row_ror:2") \
                        "s_nop 1\n\t" DPP("v_max_f32_dpp", "v235", "row_ror:1") \
                        "v_mul_f32 v235, 0xbe0293ee, v235\n\t" \
                        "v_fmamk_f32 v232, v232, 0x3e0293ee, v235\n\t" \
                        "v_fmamk_f32 v233, v233, 0x3e0293ee, v235\n\t" \
                        "v_exp_f32 v232, v232\n\t" \
                        "v_exp_f32 v233, v233\n\t" \
                        "v_fmamk_f32 v234, v234, 0x3e0293ee, v235\n\t" \
                        "v_add_f32 v235, v232, v233\n\t" \
                        "v_exp_f32 v234, v234\n\t" \
                        "s_nop 0\n\t" DPP("v_add_f32_dpp", "v235", "row_ror:8") \
                        "v_add_f32 v235, v235, v234\n\t" \
                        "v_mov_b32_dpp v236, v234 quad_perm:[1,1,3,3] row_mask:0xf bank_mask:0xf\n\t" \
                        "s_nop 0\n\t" DPP("v_add_f32_dpp", "v235", "row_ror:4") \
                        "s_nop 1\n\t" DPP("v_add_f32_dpp", "v235", "row_ror:2") \
                        "s_nop 1\n\t" DPP("v_add_f32_dpp", "v235", "row_ror:1") \
                        "v_rcp_f32 v235, v235\n\t" \
                        "s_nop 0\n\t" \
                        "v_mul_f32_e64 v232, v232, v235 clamp\n\t" \
                        "v_mul_f32_e64 v233, v233, v235 clamp\n\t" \
                        "v_cvt_pk_bf16_f32 v232, v232, v233\n\t" \
                        MTA(5) "buffer_store_dword v232, %[pa], %[rw], %[p0] offen sc1\n\t" \
                        "v_mul_f32_e64 v234, v234, v235 clamp\n\t" \
                        "v_mul_f32_e64 v236, v236, v235 clamp\n\t" \
                        "buffer_store_dword v232, %[pa], %[rw], %[p1] offen sc1\n\t" \
                        "v_cvt_pk_bf16_f32 v234, v234, v236\n\t" \
                        "buffer_store_dword v234, %[pm], %[rw], %[p0] offen sc1\n\t" \
                        "s_setprio 0"
#define TAILIO : [vd] "=&s"(vd), [p0] "+s"(ps), [p1] "=&s"(ps1) \
                        : [qa] "v"(qa), [ka] "v"(ka), [vz] "v"(0u), [ls] "n"(L_S), [ix] "v"(sidx), [it] "s"(it), [pa] "v"(pa0), [pm] "v"(pa2), [rw] "s"(rwin), \
                          [k00] "v"(kx[0][0]), [k01] "v"(kx[0][1]), [k10] "v"(kx[1][0]), [k11] "v"(kx[1][1]) \
                        : "v212", "v213", "v214", "v215", "v216", "v217", "v218", "v219", "v220", "v221", "v222", "v223", "v224", "v225", "v226", "v227", "v228", "v229", "v230", "v231", "v232", \
                          "v233", "v234", "v235", "v236", "v237", "v238", "v239", "v240", "v241", "v242", "v243", "v244", "v245", "v246", "v247", "v248", "scc", "memory"
                    unsigned ps = pb, ps1;
                    if (R == 0) asm volatile(TAIL("v212", "v214", "v216", "v218", "v220", "v222") TAILIO);
                    else asm volatile(TAIL("v213", "v215", "v217", "v219", "v221", "v223") TAILIO);
                    redo = vd == it;
                    RT(4);
                    MT(6);
                } else {
                    MT(3);
                    if (TK) { __builtin_amdgcn_s_setprio(TKP); if (TKS) __builtin_amdgcn_s_sleep(TKS); }
                    redo = __builtin_amdgcn_readfirstlane(*(volatile LAS unsigned*)(smem + L_S)) == it;
                    if (redo) FC(fc0);
                }
            } while (__builtin_expect(redo, 0));
            if (s < 8) SST(8 + s);
            if (s >= 56) SST(s - 40);
            if (C + R == 1) {
                if (s + layer > 0) {
                    __builtin_amdgcn_s_sleep(DDMA);
                    unsigned l2 = fresh_lane(), s1;
                    asm volatile("v_mov_b32 %0, -1" : "=v"(s1));
                    const unsigned base = p_slot(s - 1) + nh;
                    asm volatile("s_waitcnt vmcnt(3)" ::: "memory");
                    st128(((u32x4){s1, s1, s1, s1}), rwin, (l2 >> 5) * PAIR + (l2 & 31u) * 16u, base + (unsigned)(2 * (hh >> 3)) * PAIR + (unsigned)(R * 4096 + (hh & 7) * 512), 16);
                    if (R) __builtin_amdgcn_raw_buffer_store_b64((u32x2){s1, s1}, rwin, (l2 >> 5) * PAIR + (l2 & 16u) * 256u + (l2 & 8u) * 16u + (l2 & 7u) * 8u, base + 8u * PAIR + (unsigned)((hh >> 1) * 256 + (hh & 1) * 64), 16);
                    else st128(((u32x4){s1, s1, s1, s1}), rwin, two(), W_T + (unsigned)((s + 2) & 3) * 2u * PAIR + (unsigned)hh * 128u, 16);
                }
            } else if (TK) {
                if (s + 1 < NSEG) sched2(tk, s, sf);
            } else if (C == 2 && w == 4) {
                if (s + 2 < NSEG) {
                    auto done = [&](int f) INL {
                        f = min(f, __builtin_amdgcn_update_dpp(f, f, 0xB1, 0xF, 0xF, false)); f = min(f, __builtin_amdgcn_update_dpp(f, f, 0x4E, 0xF, 0xF, false));
                        f = min(f, __builtin_amdgcn_update_dpp(f, f, 0x141, 0xF, 0xF, false));
                        return seg_done(MXQ, __builtin_amdgcn_readlane(f, 0), __builtin_amdgcn_readlane(f, 8));
                    };
                    qrdy = max(qrdy, done(qf_));
                    const int sq = s + 2;
                    if (__builtin_expect(s == 0, 0)) __builtin_amdgcn_s_sleep(DQ0);
                    if (__builtin_expect(qrdy < sq, 0)) for (unsigned spins = 0;;) {
                        asm volatile("" ::: "memory");
                        const int f = (int)__builtin_amdgcn_raw_buffer_load_b32(rwin, (unsigned)((lane & 8) * 512 + (lane & 7) * 4), W_FQ + fo + (unsigned)hh * 128u, 16);
                        qrdy = done(f);
                        if (qrdy >= sq) break;
                        FC(fc2);
                        if (spin_fail(spins, rwin)) break;
                        __builtin_amdgcn_s_sleep(QSL);
                    }
                    const unsigned so = O_QK + (unsigned)(((s + 2) * H_ + hh) * 16384), lo = (unsigned)(L_TOK + (tbo ? tbo - 16384 : 32768)), l2 = fresh_lane();
                    if (__builtin_expect(s == 0, 0)) {
                        for (int i = 0; i < 4; ++i) dma4k(rws, l2 * 16u, so - H_ * 16384u + i * 4096u, lo - 16384u + i * 4096u);
                    }
#pragma unroll
                    for (int i = 0; i < 4; ++i) dma4k(rws, l2 * 16u, so + i * 4096u, lo + i * 4096u);
                    if (__builtin_expect(s == 0, 0)) asm volatile("s_waitcnt vmcnt(16)" ::: "memory");
                    if (qrdy < min(s + 5, NSEG - 1))
                        asm volatile("buffer_load_dword %0, %1, %2, %3 offen sc1" : "+v"(qf_) : "v"((l2 & 8u) * 512u + (l2 & 7u) * 4u), "s"(rwin), "s"(W_FQ + fo + (unsigned)hh * 128u) : "memory");
                }
            } else {
                asm volatile("s_waitcnt vmcnt(3)" ::: "memory");
                if (s > TAILS) __builtin_amdgcn_raw_buffer_store_b32((unsigned)s - 1u, rwin, 0u, W_FPT + fo + (unsigned)(hh * 128 + (w & 3) * 4), 16);
                if (C == 2 && s + 1 < NSEG) __builtin_amdgcn_s_sleep(DPT);
                const float scale = 0.08838834764831845f, l2e = 1.44269504088896340736f * (NORND ? scale : 1.f);
                const int tq = (w >> 1) & 1, qh = w & 1;
                f32x4 sc[3] = {z4, z4, z4};
                u32x4 kt[2][4], qf[4], kf[4];
                const unsigned l2 = fresh_lane(), kto = ((l2 & 8u) * 512u + (l2 & 51u) * 16u + (l2 & 4u) * 32u) ^ ((l2 & 16u) * 5u), qto = ((l2 & 55u) * 16u + qh * 128u) ^ ((l2 & 16u) * 8u);
#pragma unroll
                for (int ks = 0; ks < 4; ++ks) {
                    qf[ks] = *(const u32x4*)(tb + 8192 + (tq * 4 + ks) * 1024 + qto);
                    kt[0][ks] = *(const u32x4*)(tb + ks * 1024 + kto);
                    kt[1][ks] = *(const u32x4*)(tb + ks * 1024 + (kto ^ 64u));
                    SB();
                }
#pragma unroll
                for (int ks = 0; ks < 4; ++ks) {
                    sc[0] = mfma32(kt[0][ks], qf[ks], sc[0]); PIN1(sc[0]);
                    kf[ks] = ldtr2(ka + ks * 256, ka + ks * 256 + 128);
                    SB();
                    sc[1] = mfma32(kt[1][ks], qf[ks], sc[1]); PIN1(sc[1]);
                }
#pragma unroll
                for (int ks = 0; ks < 4; ++ks) sc[2] = mfma32(kf[ks], qf[ks], sc[2]);
                const bool hi = (l2 & 8u) != 0u;
                float v[6];
#pragma unroll
                for (int r = 0; r < 4; ++r) v[r] = hi ? sc[1][r] : sc[0][r];
                v[4] = hi ? sc[2][2] : sc[2][0]; v[5] = hi ? sc[2][3] : sc[2][1];
#pragma unroll
                for (int r = 0; r < 6; r += 2) {
                    if (NORND) break;
                    const unsigned b0 = pack2(v[r], v[r + 1]);
                    const unsigned b1 = pack2(lo2f(b0) * scale, hi2f(b0) * scale);
                    v[r] = lo2f(b1); v[r + 1] = hi2f(b1);
                }
                const int lim = 8 * (w & 3) + (int)(l2 & 7u) - 8 * (int)(l2 >> 4) - (int)((l2 & 8u) >> 1);
#pragma unroll
                for (int r = 0; r < 4; ++r) if (r > lim) v[r] = NORND ? -1000000000.0f / scale : bfr(-1000000000.0f);
                float mx = fmx(fmx(fmx(fmx(fmx(v[0], v[1]), v[2]), v[3]), v[4]), v[5]);
                mx = xmax3216(fmx(mx, ror8f(mx)));
                const float mb = -mx * l2e;
#pragma unroll
                for (int r = 0; r < 6; ++r) v[r] = __builtin_amdgcn_exp2f(fmaf(v[r], l2e, mb));
                float sum = fmaf(v[4] + v[5], 0.5f, (v[0] + v[1]) + (v[2] + v[3]));
                sum = xsum3216(sum + ror8f(sum));
                const float inv = __builtin_amdgcn_rcpf(sum);
                const unsigned p0 = pack2(v[0] * inv, v[1] * inv), p1 = nosent1(pack2(v[2] * inv, v[3] * inv)), p2 = nosent1(pack2(v[4] * inv, v[5] * inv));
                const unsigned base = (s + 2 < NSEG ? O_PT : W_PL + fo - (NSEG - 2) * PTSEG * 2u) + (unsigned)(s * PTSEG) * 2u;
                const __amdgpu_buffer_rsrc_t rp = s + 2 < NSEG ? rws : rwin;
                const unsigned oob = (l2 & 8u) << 27;
                st128(((u32x4){p0, p1, dppu<0x128>(p0), dppu<0x128>(p1)}), rp, (l2 * 16u) | oob, base + (unsigned)((hh * 2 + tq) * 1024 + qh * 128), 16);
                __builtin_amdgcn_raw_buffer_store_b64((u32x2){p2, dppu<0x128>(p2)}, rp, ((l2 & 7u) * 16u + (l2 >> 4) * 8u) | oob | (l2 & 32u) << 25, base + 65536u + (unsigned)((tq * 32 + hh) * 256 + qh * 128), 16);
            }
            tbo = tbo == 32768 ? 0 : tbo + 16384;
            if (C == 2 && __builtin_expect(budget <= 0, 0)) set_tmo(rwin);
            RT(5);
            RTS();
            MT(7);
            MTS();
        }
        };
        if (w) gather_mem0(memL, mytid() - 64, 448);
        else *(unsigned*)(smem + L_S) = 0u;
        if (layer == 0) near_init();
        qt_role(wt, hh, IC<0>());
        if (w == 7) { ck.ta = 4u * (unsigned)__builtin_amdgcn_s_memrealtime() + 8u; ck.ma = (unsigned)__builtin_amdgcn_s_memtime(); }
        if (w == 0) steps(IC<0>()); else if (w == 1) steps(IC<1>()); else if (w == 2) steps(IC<2>()); else if (w == 3) steps(IC<3>()); else if (w < 7) steps(IC<4>()); else steps(IC<5>());
    };
    auto own_role = [&]() INL {
        const unsigned long long tstart = __builtin_amdgcn_s_memtime();
        if (layer == 0) near_init();
        LANES();
        const int o = bid - 96, hg = (w + ROT * o) & 3, ot = tile_of(o);
        const unsigned ztoff = (unsigned)((o * H_ + 8 * hg) * 64 + lane) * 16u;
        const unsigned zoff = zpair(o / 10, o % 10) + (unsigned)(lane * 16);
        const unsigned poff = (2u * hg + (mc & 1u)) * PAIR + nh + (mc >> 1) * 4096u + (unsigned)((fq >> 1) * 512 + (fr >> 2) * 128 + (fr & 1) * 64 + (fq & 1) * 32 + (fr & 2) * 8);
        const unsigned pmoff = (8u + (mc & 1u)) * PAIR + nh + (mc >> 1) * 4096u + (unsigned)(fq * 256 + (fr >> 3) * 128 + (fr & 2) * 32 + (fr & 4) * 8 + (fr & 1) * 16);
        const unsigned moff = (unsigned)(ot >> 4) * 2u * PAIR + nh + (unsigned)((ot & 15) * 256 + lane * 4);
        constexpr unsigned mc2 = 4096u;
        char* op = smem + 8192;
        if (w == 4 && lane < 2) *(volatile LAS int*)(smem + 4096 + lane * 4) = -1;
        BAR();
        SST(2);
        if (w == 7) clk_init(ck);
        wait_until(tstart + DO0);
        if (w < 4) {
            u32x8 zf[4];
            int zr = -1;
            unsigned zb = W_ZM + (unsigned)layer * (NOWN * PAIR), zso = O_Z - (unsigned)layer * (unsigned)(ZTSEG * 2);
            const unsigned ra = (unsigned)((ot >> 4) * 2 + (fq & 1)) * PAIR + nh + (unsigned)((fq >> 1) * 4096 + (ot & 15) * 256 + fr * 16);
            int np = 0;
            auto piece = [&]() INL {
                asm volatile("s_mov_b32 m0, %3\n\ts_nop 3\n\tbuffer_load_dwordx4 %0, %1, %2 offen sc1 lds\n\tbuffer_load_dwordx4 %0, %1, %2 offen offset:1024 sc1 lds"
                             :: "v"(ztoff), "s"(rws), "s"(O_Z + (unsigned)((NSEG - 1 - layer) * (int)ZTSEG) * 2u + np * 2048u), "s"(32768 + hg * 8192 + np * 2048) : "m0", "memory");
                ++np;
            };
            for (int s = 0; s + 1 < NSEG; ++s) {
                const unsigned pb = p_slot(s);
                const int sz = s;
                while (__builtin_expect(zr < sz, 0)) {
                    __builtin_amdgcn_s_sleep(1);
                    zr = __builtin_amdgcn_readfirstlane(*(volatile LAS int*)(smem + 4096));
                }
                if (s == 0) SST(1);
                asm volatile(
                    "1:\n\t"
                    "buffer_load_dwordx4 v[160:163], %[zt], %[rs], %[so] offen sc1\n\t"
                    "buffer_load_dwordx4 v[164:167], %[zt], %[rs], %[so] offen offset:1024 sc1\n\t"
                    "buffer_load_dwordx4 v[168:171], %[zt], %[rs], %[so] offen offset:2048 sc1\n\t"
                    "buffer_load_dwordx4 v[172:175], %[zt], %[rs], %[so] offen offset:3072 sc1\n\t"
                    "buffer_load_dwordx4 v[176:179], %[zt], %[rs], %[s4] offen sc1\n\t"
                    "buffer_load_dwordx4 v[180:183], %[zt], %[rs], %[s4] offen offset:1024 sc1\n\t"
                    "buffer_load_dwordx4 v[184:187], %[zt], %[rs], %[s4] offen offset:2048 sc1\n\t"
                    "buffer_load_dwordx4 v[188:191], %[zt], %[rs], %[s4] offen offset:3072 sc1\n\t"
                    "s_cmp_lg_u32 %[s], 0\n\t"
                    "s_cbranch_scc1 2f\n\t"
                    "s_waitcnt vmcnt(0)\n\t"
                    "v_max3_u32 v100, v161, v163, v165\n\t"
                    "v_max3_u32 v100, v100, v167, v169\n\t"
                    "v_max3_u32 v100, v100, v171, v173\n\t"
                    "v_max3_u32 v100, v100, v175, v177\n\t"
                    "v_max3_u32 v100, v100, v179, v181\n\t"
                    "v_max3_u32 v100, v100, v183, v185\n\t"
                    "v_max3_u32 v100, v100, v187, v189\n\t"
                    "v_max_u32 v100, v100, v191\n\t"
                    "v_cmp_eq_u32 vcc, -1, v100\n\t"
                    "s_sub_i32 %[tr], %[tr], 1\n\t"
                    "s_cbranch_vccz 2f\n\t"
                    "s_cmp_gt_i32 %[tr], 0\n\t"
                    "s_cbranch_scc1 1b\n\t"
                    "2:"
                    : "=&{v[160:167]}"(zf[0]), "=&{v[168:175]}"(zf[1]), "=&{v[176:183]}"(zf[2]), "=&{v[184:191]}"(zf[3]), [tr] "+s"(budget)
                    : [zt] "v"(ztoff), [rs] "s"(rws), [so] "s"(zso), [s4] "s"(zso + 4096u), [s] "s"(s) : "v100", "vcc", "scc", "memory");
                if (s == 0) SST(3);
                zso += (unsigned)(ZTSEG * 2);
                if (zr == NSEG - 1 && np < 4) piece();
                if (__builtin_expect(budget <= 0, 0)) set_tmo(rwin);
                RT(6);
                const unsigned spa = (unsigned)((s & 1) * 2048 + (w < 3 ? w * 512 : 0) + lane * 8);
#define OST(r)
                unsigned long long t0_, t1_, t2_, t3_, t4_ = 0, bad;
                int tries = budget;
                const unsigned sw = (unsigned)(((s & 3) + 1) & 4) * 1024u;
                unsigned po = poff ^ sw, pm = pmoff ^ sw, x0 = 4096u, x1 = 4096u;
                const unsigned mb = W_MEM + (unsigned)((s + 1) & 3) * 6u * PAIR;
                if (w < 3) asm volatile(
                    ALN
                    "s_barrier\n\t"
                    OST("%[t0]")
                    "v_mov_b64 v[96:97], 0\n\t"
                    "v_mov_b64 v[98:99], 0\n\t"
                    "v_mov_b32 v100, 0\n\t"
                    "s_barrier\n\t"
                    "1:\n\t"
                    "buffer_load_dwordx4 v[208:211], %[po], %[rw], %[pb] offen sc1\n\t"
                    "buffer_load_dwordx4 v[212:215], %[po], %[rw], %[pb] offen offset:1024 sc1\n\t"
                    "buffer_load_dwordx4 v[216:219], %[po], %[rw], %[pb] offen offset:2048 sc1\n\t"
                    "buffer_load_dwordx4 v[220:223], %[po], %[rw], %[pb] offen offset:3072 sc1\n\t"
                    OST("%[t1]")
                    "s_waitcnt vmcnt(3)\n\t"
                    OST("%[t3]")
                    "v_smfmac_f32_16x16x64_bf16 v[96:99], v[208:211], v[160:167], %[ix]\n\t"
                    "v_max3_u32 v100, v100, v208, v209\n\t"
                    "v_max3_u32 v100, v100, v210, v211\n\t"
                    "s_waitcnt vmcnt(2)\n\t"
                    "v_smfmac_f32_16x16x64_bf16 v[96:99], v[212:215], v[168:175], %[ix]\n\t"
                    "v_max3_u32 v100, v100, v212, v213\n\t"
                    "v_max3_u32 v100, v100, v214, v215\n\t"
                    "s_waitcnt vmcnt(1)\n\t"
                    "v_smfmac_f32_16x16x64_bf16 v[96:99], v[216:219], v[176:183], %[ix]\n\t"
                    "v_max3_u32 v100, v100, v216, v217\n\t"
                    "v_max3_u32 v100, v100, v218, v219\n\t"
                    "s_waitcnt vmcnt(0)\n\t"
                    "v_smfmac_f32_16x16x64_bf16 v[96:99], v[220:223], v[184:191], %[ix]\n\t"
                    "v_max3_u32 v100, v100, v220, v221\n\t"
                    "v_max3_u32 v100, v100, v222, v223\n\t"
                    "v_cmp_eq_u32 vcc, -1, v100\n\t"
                    OST("%[t2]")
                    "s_nop 4\n\t"
                    "v_pk_add_f32 v[96:97], v[96:97], v[98:99]\n\t"
                    "ds_write_b64 %[sp], v[96:97]\n\t"
                    "s_cbranch_vccz 2f\n\t"
                    "s_sub_i32 %[tr], %[tr], 1\n\t"
                    "s_cmp_lt_i32 %[tr], 1\n\t"
                    "s_cbranch_scc1 2f\n\t"
                    "v_xor_b32 %[po], %[x0], %[po]\n\t"
                    "s_xor_b32 %[x0], %[x0], 0x5000\n\t"
                    "s_waitcnt lgkmcnt(0)\n\t"
                    "v_mov_b64 v[96:97], 0\n\t"
                    "v_mov_b64 v[98:99], 0\n\t"
                    "v_mov_b32 v100, 0\n\t"
                    "s_branch 1b\n\t"
                    "2:\n\t"
                    "s_waitcnt lgkmcnt(0)\n\t"
                    "s_barrier\n\t"
                    "s_sleep 3\n\t"
                    : [tr] "+s"(tries), [po] "+v"(po), [x0] "+s"(x0), [t0] "=&s"(t0_), [t1] "=&s"(t1_), [t2] "=&s"(t2_), [t3] "=&s"(t3_)
                    : [sp] "v"(spa), [ix] "v"(sidx), [rw] "s"(rwin), [pb] "s"(pb), "{v[160:167]}"(zf[0]), "{v[168:175]}"(zf[1]), "{v[176:183]}"(zf[2]), "{v[184:191]}"(zf[3])
                    : "v96", "v97", "v98", "v99", "v100", "v208", "v209", "v210", "v211", "v212", "v213", "v214", "v215", "v216", "v217", "v218", "v219", "v220", "v221", "v222", "v223", "vcc", "scc", "memory");
                else asm volatile(
                    ALN
                    "s_barrier\n\t"
                    "buffer_load_dwordx4 v[128:131], %[zo], %[rw], %[zb] offen sc1\n\t"
                    "buffer_load_dwordx4 v[132:135], %[zo], %[rw], %[zb] offen offset:1024 sc1\n\t"
                    "buffer_load_dwordx4 v[136:139], %[zo], %[rw], %[zb] offen offset:2048 sc1\n\t"
                    "buffer_load_dwordx4 v[140:143], %[zo], %[rw], %[zb] offen offset:3072 sc1\n\t"
                    "buffer_load_dwordx4 v[144:147], %[zo], %[rw], %[z4] offen sc1\n\t"
                    "buffer_load_dwordx4 v[148:151], %[zo], %[rw], %[z4] offen offset:1024 sc1\n\t"
                    "buffer_load_dwordx4 v[152:155], %[zo], %[rw], %[z4] offen offset:2048 sc1\n\t"
                    "buffer_load_dwordx4 v[156:159], %[zo], %[rw], %[z4] offen offset:3072 sc1\n\t"
                    OST("%[t0]")
                    "v_mov_b64 v[96:97], 0\n\t"
                    "v_mov_b64 v[98:99], 0\n\t"
                    "v_mov_b32 v100, 0\n\t"
                    "s_barrier\n\t"
                    "s_sleep %[fd]\n\t"
                    "buffer_load_dwordx4 v[208:211], %[po], %[rw], %[pb] offen sc1\n\t"
                    "buffer_load_dwordx4 v[212:215], %[po], %[rw], %[pb] offen offset:1024 sc1\n\t"
                    "buffer_load_dwordx4 v[216:219], %[po], %[rw], %[pb] offen offset:2048 sc1\n\t"
                    "buffer_load_dwordx4 v[220:223], %[po], %[rw], %[pb] offen offset:3072 sc1\n\t"
                    "buffer_load_dwordx4 v[224:227], %[pm], %[rw], %[pb] offen sc1\n\t"
                    "buffer_load_dwordx4 v[228:231], %[pm], %[rw], %[pb] offen offset:1024 sc1\n\t"
                    "buffer_load_dwordx4 v[232:235], %[pm], %[rw], %[pb] offen offset:2048 sc1\n\t"
                    "buffer_load_dwordx4 v[236:239], %[pm], %[rw], %[pb] offen offset:3072 sc1\n\t"
                    OST("%[t1]")
                    "s_waitcnt vmcnt(8)\n\t"
                    "v_max3_u32 v101, v129, v131, v133\n\t"
                    "v_max3_u32 v101, v101, v135, v137\n\t"
                    "v_max3_u32 v101, v101, v139, v141\n\t"
                    "v_max3_u32 v101, v101, v143, v145\n\t"
                    "v_max3_u32 v101, v101, v147, v149\n\t"
                    "v_max3_u32 v101, v101, v151, v153\n\t"
                    "v_max3_u32 v101, v101, v155, v157\n\t"
                    "v_max_u32 v101, v101, v159\n\t"
                    "v_cmp_eq_u32 vcc, -1, v101\n\t"
                    "s_cbranch_vccz 3f\n\t"
                    "buffer_load_dwordx4 v[128:131], %[zo], %[rw], %[zb] offen sc1\n\t"
                    "buffer_load_dwordx4 v[132:135], %[zo], %[rw], %[zb] offen offset:1024 sc1\n\t"
                    "buffer_load_dwordx4 v[136:139], %[zo], %[rw], %[zb] offen offset:2048 sc1\n\t"
                    "buffer_load_dwordx4 v[140:143], %[zo], %[rw], %[zb] offen offset:3072 sc1\n\t"
                    "buffer_load_dwordx4 v[144:147], %[zo], %[rw], %[z4] offen sc1\n\t"
                    "buffer_load_dwordx4 v[148:151], %[zo], %[rw], %[z4] offen offset:1024 sc1\n\t"
                    "buffer_load_dwordx4 v[152:155], %[zo], %[rw], %[z4] offen offset:2048 sc1\n\t"
                    "buffer_load_dwordx4 v[156:159], %[zo], %[rw], %[z4] offen offset:3072 sc1\n\t"
                    "s_waitcnt vmcnt(0)\n\t"
                    "v_max3_u32 v101, v129, v131, v133\n\t"
                    "v_max3_u32 v101, v101, v135, v137\n\t"
                    "v_max3_u32 v101, v101, v139, v141\n\t"
                    "v_max3_u32 v101, v101, v143, v145\n\t"
                    "v_max3_u32 v101, v101, v147, v149\n\t"
                    "v_max3_u32 v101, v101, v151, v153\n\t"
                    "v_max3_u32 v101, v101, v155, v157\n\t"
                    "v_max_u32 v101, v101, v159\n\t"
                    "v_mov_b32 v100, v101\n\t"
                    "3:\n\t"
                    "v_permlane16_swap_b32 v128, v129\n\t"
                    "v_permlane16_swap_b32 v130, v131\n\t"
                    "v_permlane16_swap_b32 v132, v133\n\t"
                    "v_permlane16_swap_b32 v134, v135\n\t"
                    "v_permlane16_swap_b32 v136, v137\n\t"
                    "v_permlane16_swap_b32 v138, v139\n\t"
                    "v_permlane16_swap_b32 v140, v141\n\t"
                    "v_permlane16_swap_b32 v142, v143\n\t"
                    "v_permlane16_swap_b32 v144, v145\n\t"
                    "v_permlane16_swap_b32 v146, v147\n\t"
                    "v_permlane16_swap_b32 v148, v149\n\t"
                    "v_permlane16_swap_b32 v150, v151\n\t"
                    "v_permlane16_swap_b32 v152, v153\n\t"
                    "v_permlane16_swap_b32 v154, v155\n\t"
                    "v_permlane16_swap_b32 v156, v157\n\t"
                    "v_permlane16_swap_b32 v158, v159\n\t"
                    "s_mov_b32 vcc_lo, 0x00ff00ff\n\t"
                    "s_mov_b32 vcc_hi, 0x00ff00ff\n\t"
                    "s_waitcnt vmcnt(7)\n\t"
                    OST("%[t3]")
                    "v_smfmac_f32_16x16x64_bf16 v[96:99], v[208:211], v[160:167], %[ix]\n\t"
                    "v_max3_u32 v100, v100, v208, v209\n\t"
                    "v_max3_u32 v100, v100, v210, v211\n\t"
                    "s_waitcnt vmcnt(6)\n\t"
                    "v_smfmac_f32_16x16x64_bf16 v[96:99], v[212:215], v[168:175], %[ix]\n\t"
                    "v_max3_u32 v100, v100, v212, v213\n\t"
                    "v_max3_u32 v100, v100, v214, v215\n\t"
                    "s_waitcnt vmcnt(5)\n\t"
                    "v_smfmac_f32_16x16x64_bf16 v[96:99], v[216:219], v[176:183], %[ix]\n\t"
                    "v_max3_u32 v100, v100, v216, v217\n\t"
                    "v_max3_u32 v100, v100, v218, v219\n\t"
                    "s_waitcnt vmcnt(4)\n\t"
                    "v_smfmac_f32_16x16x64_bf16 v[96:99], v[220:223], v[184:191], %[ix]\n\t"
                    "v_max3_u32 v100, v100, v220, v221\n\t"
                    "v_max3_u32 v100, v100, v222, v223\n\t"
                    "s_waitcnt vmcnt(3)\n\t"
                    "v_smfmac_f32_16x16x64_bf16 v[96:99], v[224:227], v[128:135], %[ix]\n\t"
                    "v_max3_u32 v100, v100, v224, v225\n\t"
                    "v_max3_u32 v100, v100, v226, v227\n\t"
                    "s_waitcnt vmcnt(2)\n\t"
                    "v_smfmac_f32_16x16x64_bf16 v[96:99], v[228:231], v[136:143], %[ix]\n\t"
                    "s_barrier\n\t"
                    "ds_read_b64 v[102:103], %[sp]\n\t"
                    "ds_read_b64 v[104:105], %[sp] offset:512\n\t"
                    "ds_read_b64 v[106:107], %[sp] offset:1024\n\t"
                    "v_max3_u32 v100, v100, v228, v229\n\t"
                    "v_max3_u32 v100, v100, v230, v231\n\t"
                    "s_waitcnt vmcnt(1)\n\t"
                    "v_smfmac_f32_16x16x64_bf16 v[96:99], v[232:235], v[144:151], %[ix]\n\t"
                    "v_max3_u32 v100, v100, v232, v233\n\t"
                    "v_max3_u32 v100, v100, v234, v235\n\t"
                    "s_waitcnt vmcnt(0)\n\t"
                    "v_smfmac_f32_16x16x64_bf16 v[96:99], v[236:239], v[152:159], %[ix]\n\t"
                    "v_max3_u32 v100, v100, v236, v237\n\t"
                    "v_max3_u32 v100, v100, v238, v239\n\t"
                    OST("%[t2]")
                    "v_cmp_eq_u32 %[bad], -1, v100\n\t"
                    "s_waitcnt lgkmcnt(0)\n\t"
                    "v_add_f32 v102, v102, v104\n\t"
                    "v_add_f32 v103, v103, v105\n\t"
                    "v_add_f32 v102, v102, v106\n\t"
                    "v_add_f32 v103, v103, v107\n\t"
                    "v_add_f32 v96, v96, v102\n\t"
                    "v_add_f32 v97, v97, v103\n\t"
                    "v_add_f32 v96, v96, v98\n\t"
                    "v_add_f32 v97, v97, v99\n\t"
                    "s_cmp_lg_u64 %[bad], 0\n\t"
                    "s_cbranch_scc1 4f\n\t"
                    "2:\n\t"
                    "v_cndmask_b32_dpp v98, v97, v96, vcc row_ror:8 row_mask:0xf bank_mask:0xf\n\t"
                    "s_not_b64 vcc, vcc\n\t"
                    "v_cndmask_b32_dpp v99, v96, v97, vcc row_ror:8 row_mask:0xf bank_mask:0xf\n\t"
                    "v_cvt_pk_bf16_f32 v98, v98, v99\n\t"
                    "v_min_u32 v98, -2, v98\n\t"
                    "buffer_store_dword v98, %[mo], %[rw], %[m0] offen sc1\n\t"
                    "buffer_store_dword v98, %[mo], %[rw], %[m1] offen sc1\n\t"
                    "buffer_store_dword v98, %[mo], %[rw], %[m2] offen sc1\n\t"
                    "buffer_store_dword v98, %[mo], %[rw], %[m3] offen sc1\n\t"
                    OST("%[t4]")
                    "s_branch 5f\n\t"
                    "4:\n\t"
                    "s_sub_i32 %[tr], %[tr], 1\n\t"
                    "s_cmp_lt_i32 %[tr], 1\n\t"
                    "s_cbranch_scc1 2b\n\t"
                    "v_xor_b32 %[po], %[x0], %[po]\n\t"
                    "s_xor_b32 %[x0], %[x0], 0x5000\n\t"
                    "v_xor_b32 %[pm], %[x1], %[pm]\n\t"
                    "s_xor_b32 %[x1], %[x1], 0x5000\n\t"
                    "buffer_load_dwordx4 v[128:131], %[zo], %[rw], %[zb] offen sc1\n\t"
                    "buffer_load_dwordx4 v[132:135], %[zo], %[rw], %[zb] offen offset:1024 sc1\n\t"
                    "buffer_load_dwordx4 v[136:139], %[zo], %[rw], %[zb] offen offset:2048 sc1\n\t"
                    "buffer_load_dwordx4 v[140:143], %[zo], %[rw], %[zb] offen offset:3072 sc1\n\t"
                    "buffer_load_dwordx4 v[144:147], %[zo], %[rw], %[z4] offen sc1\n\t"
                    "buffer_load_dwordx4 v[148:151], %[zo], %[rw], %[z4] offen offset:1024 sc1\n\t"
                    "buffer_load_dwordx4 v[152:155], %[zo], %[rw], %[z4] offen offset:2048 sc1\n\t"
                    "buffer_load_dwordx4 v[156:159], %[zo], %[rw], %[z4] offen offset:3072 sc1\n\t"
                    "buffer_load_dwordx4 v[208:211], %[po], %[rw], %[pb] offen sc1\n\t"
                    "buffer_load_dwordx4 v[212:215], %[po], %[rw], %[pb] offen offset:1024 sc1\n\t"
                    "buffer_load_dwordx4 v[216:219], %[po], %[rw], %[pb] offen offset:2048 sc1\n\t"
                    "buffer_load_dwordx4 v[220:223], %[po], %[rw], %[pb] offen offset:3072 sc1\n\t"
                    "buffer_load_dwordx4 v[224:227], %[pm], %[rw], %[pb] offen sc1\n\t"
                    "buffer_load_dwordx4 v[228:231], %[pm], %[rw], %[pb] offen offset:1024 sc1\n\t"
                    "buffer_load_dwordx4 v[232:235], %[pm], %[rw], %[pb] offen offset:2048 sc1\n\t"
                    "buffer_load_dwordx4 v[236:239], %[pm], %[rw], %[pb] offen offset:3072 sc1\n\t"
                    "v_mov_b64 v[96:97], 0\n\t"
                    "v_mov_b64 v[98:99], 0\n\t"
                    "v_mov_b32 v100, 0\n\t"
                    "s_waitcnt vmcnt(0)\n\t"
                    "v_max3_u32 v101, v129, v131, v133\n\t"
                    "v_max3_u32 v101, v101, v135, v137\n\t"
                    "v_max3_u32 v101, v101, v139, v141\n\t"
                    "v_max3_u32 v101, v101, v143, v145\n\t"
                    "v_max3_u32 v101, v101, v147, v149\n\t"
                    "v_max3_u32 v101, v101, v151, v153\n\t"
                    "v_max3_u32 v101, v101, v155, v157\n\t"
                    "v_max_u32 v101, v101, v159\n\t"
                    "v_mov_b32 v100, v101\n\t"
                    "v_permlane16_swap_b32 v128, v129\n\t"
                    "v_permlane16_swap_b32 v130, v131\n\t"
                    "v_permlane16_swap_b32 v132, v133\n\t"
                    "v_permlane16_swap_b32 v134, v135\n\t"
                    "v_permlane16_swap_b32 v136, v137\n\t"
                    "v_permlane16_swap_b32 v138, v139\n\t"
                    "v_permlane16_swap_b32 v140, v141\n\t"
                    "v_permlane16_swap_b32 v142, v143\n\t"
                    "v_permlane16_swap_b32 v144, v145\n\t"
                    "v_permlane16_swap_b32 v146, v147\n\t"
                    "v_permlane16_swap_b32 v148, v149\n\t"
                    "v_permlane16_swap_b32 v150, v151\n\t"
                    "v_permlane16_swap_b32 v152, v153\n\t"
                    "v_permlane16_swap_b32 v154, v155\n\t"
                    "v_permlane16_swap_b32 v156, v157\n\t"
                    "v_permlane16_swap_b32 v158, v159\n\t"
                    "s_nop 1\n\t"
                    "v_smfmac_f32_16x16x64_bf16 v[96:99], v[208:211], v[160:167], %[ix]\n\t"
                    "v_max3_u32 v100, v100, v208, v209\n\t"
                    "v_max3_u32 v100, v100, v210, v211\n\t"
                    "v_smfmac_f32_16x16x64_bf16 v[96:99], v[212:215], v[168:175], %[ix]\n\t"
                    "v_max3_u32 v100, v100, v212, v213\n\t"
                    "v_max3_u32 v100, v100, v214, v215\n\t"
                    "v_smfmac_f32_16x16x64_bf16 v[96:99], v[216:219], v[176:183], %[ix]\n\t"
                    "v_max3_u32 v100, v100, v216, v217\n\t"
                    "v_max3_u32 v100, v100, v218, v219\n\t"
                    "v_smfmac_f32_16x16x64_bf16 v[96:99], v[220:223], v[184:191], %[ix]\n\t"
                    "v_max3_u32 v100, v100, v220, v221\n\t"
                    "v_max3_u32 v100, v100, v222, v223\n\t"
                    "v_smfmac_f32_16x16x64_bf16 v[96:99], v[224:227], v[128:135], %[ix]\n\t"
                    "v_max3_u32 v100, v100, v224, v225\n\t"
                    "v_max3_u32 v100, v100, v226, v227\n\t"
                    "v_smfmac_f32_16x16x64_bf16 v[96:99], v[228:231], v[136:143], %[ix]\n\t"
                    "v_max3_u32 v100, v100, v228, v229\n\t"
                    "v_max3_u32 v100, v100, v230, v231\n\t"
                    "v_smfmac_f32_16x16x64_bf16 v[96:99], v[232:235], v[144:151], %[ix]\n\t"
                    "v_max3_u32 v100, v100, v232, v233\n\t"
                    "v_max3_u32 v100, v100, v234, v235\n\t"
                    "v_smfmac_f32_16x16x64_bf16 v[96:99], v[236:239], v[152:159], %[ix]\n\t"
                    "v_max3_u32 v100, v100, v236, v237\n\t"
                    "v_max3_u32 v100, v100, v238, v239\n\t"
                    "v_cmp_eq_u32 %[bad], -1, v100\n\t"
                    "s_nop 5\n\t"
                    "v_add_f32 v96, v96, v102\n\t"
                    "v_add_f32 v97, v97, v103\n\t"
                    "v_add_f32 v96, v96, v98\n\t"
                    "v_add_f32 v97, v97, v99\n\t"
                    "s_cmp_lg_u64 %[bad], 0\n\t"
                    "s_cbranch_scc1 4b\n\t"
                    "s_branch 2b\n\t"
                    "5:\n\t"
                    : [tr] "+s"(tries), [po] "+v"(po), [pm] "+v"(pm), [x0] "+s"(x0), [x1] "+s"(x1), [bad] "=&s"(bad), [t0] "=&s"(t0_), [t1] "=&s"(t1_), [t2] "=&s"(t2_), [t3] "=&s"(t3_), [t4] "=&s"(t4_)
                    : [zo] "v"(zoff), [sp] "v"(spa), [ix] "v"(sidx), [rw] "s"(rwin), [zb] "s"(zb), [z4] "s"(zb + 4096u), [pb] "s"(pb), [mo] "v"(moff), [fd] "n"(FDL),
                      [m0] "s"(mb), [m1] "s"(mb + PAIR), [m2] "s"(mb + mc2), [m3] "s"(mb + mc2 + PAIR), "{v[160:167]}"(zf[0]), "{v[168:175]}"(zf[1]), "{v[176:183]}"(zf[2]), "{v[184:191]}"(zf[3])
                    : "v96", "v97", "v98", "v99", "v100", "v101", "v102", "v103", "v104", "v105", "v106", "v107", "v128", "v129", "v130", "v131", "v132", "v133", "v134", "v135", "v136", "v137", "v138", "v139", "v140", "v141", "v142", "v143", "v144", "v145", "v146", "v147", "v148", "v149", "v150", "v151", "v152", "v153", "v154", "v155", "v156", "v157", "v158", "v159", "v208", "v209", "v210", "v211", "v212", "v213", "v214", "v215", "v216", "v217", "v218", "v219", "v220", "v221", "v222", "v223", "v224", "v225", "v226", "v227", "v228", "v229", "v230", "v231", "v232", "v233", "v234", "v235", "v236", "v237", "v238", "v239", "vcc", "scc", "memory");
                budget = tries;
                if (w == 3) *(volatile LAS int*)(smem + 4100) = s;
                __builtin_amdgcn_s_sleep(DPS - 4);
                if (w == 3) {
                    st128(SENT, rwin, ra, W_MEM + (unsigned)(s & 3) * 6u * PAIR, 16);
                    if (lane == 0 && s > 1) __builtin_amdgcn_raw_buffer_store_b32((unsigned)s - 2u, rwin, 0u, W_GO + fo + (unsigned)o * 128u, 16);
                }
                for (;;) {
                    const u32x2 x = *(volatile LAS u32x2*)(smem + 4096);
                    zr = __builtin_amdgcn_readfirstlane(x[0]);
                    if (__builtin_amdgcn_readfirstlane(x[1]) == s) break;
                    __builtin_amdgcn_s_sleep(2);
                }
                zb = zb == W_ZM + 2u * NOWN * PAIR ? W_ZM : zb + NOWN * PAIR;
            }
            const unsigned zq = zb == W_ZM ? W_ZM + 2u * NOWN * PAIR : zb - NOWN * PAIR;
            while (np < 4) {
                while (zr < NSEG - 1) { __builtin_amdgcn_s_sleep(1); zr = __builtin_amdgcn_readfirstlane(*(volatile LAS int*)(smem + 4096)); }
                piece();
            }
            __builtin_amdgcn_s_sleep(DTK);
            for (int e = 0; e < 2; ++e) {
                f32x4 c[2];
                SST(16 + 3 * e);
                for (unsigned spins = 0;;) {
                    asm volatile("" ::: "memory");
                    SST(17 + 3 * e);
                    u32x4 pt[16], ptm[2][2], zm[2];
                    const unsigned pl = W_PL + fo + (unsigned)e * (PTSEG * 2u);
#pragma unroll
                    for (int q = 0; q < 2; ++q) zm[q] = __builtin_amdgcn_raw_buffer_load_b128(rwin, zoff + hg * 2048u + q * 1024u, e ? zb : zq, 16);
#pragma unroll
                    for (int q = 0; q < 16; ++q) pt[q] = __builtin_amdgcn_raw_buffer_load_b128(rwin, ((unsigned)(16 * hg) * 1024u + lane * 16u) | (q & 1 ? 0u : (lane & 32u) << 25), pl + q * 1024u, 16);
#pragma unroll
                    for (int q = 0; q < 4; ++q) ptm[q >> 1][q & 1] = __builtin_amdgcn_raw_buffer_load_b128(rwin, (unsigned)(hg * 2048 + lane * 16), pl + 65536u + (q >> 1) * 8192u + (q & 1) * 1024u, 16);
                    unsigned m = max(smax(zm[0]), smax(zm[1]));
#pragma unroll
                    for (int t = 0; t < 2; ++t) {
                        c[t] = z4;
#pragma unroll
                        for (int q = 0; q < 8; ++q) { c[t] = mfma32(half8(zf[q >> 1], q & 1), pt[2 * q + t], c[t]); m = max(m, smax(pt[2 * q + t])); }
#pragma unroll
                        for (int q = 0; q < 2; ++q) { c[t] = mfma32((u32x4){zm[q][0], zm[q][2], zm[q][1], zm[q][3]}, ptm[t][q], c[t]); m = max(m, smax(ptm[t][q])); }
                    }
                    if (!__any(m == 0xFFFFFFFFu)) break;
                    if (spin_fail(spins, rwin)) break;
                }
                SST(18 + 3 * e);
#pragma unroll
                for (int t = 0; t < 2; ++t) *(f32x4*)(op + e * 8192 + hg * 2048 + (16 * t + fr) * 64 + fq * 16) = c[t];
                BAR();
                VMWAIT();
#pragma unroll
                for (int q = 0; q < 4; ++q) zf[q] = cat8(*(const u32x4*)(smem + 32768 + hg * 8192 + q * 2048 + lane * 16), *(const u32x4*)(smem + 33792 + hg * 8192 + q * 2048 + lane * 16));
                if (e == 0) BAR0();
            }
            if (layer == 0) {
#pragma unroll
                for (int q = 0; q < 4; ++q) st128(SENT, rwin, zoff + hg * 2048u + (q & 1) * 1024u, q < 2 ? zb : zq, 16);
                if (w == 3) st128(SENT, rwin, ra, W_MEM + 3u * 6u * PAIR, 16);
            }
        } else if (w == 4) {
            const unsigned fl = fz_at(lane & 31, o / 10) + (lane & 32) * 128u;
            int zk = -1;
            auto note = [&](int f) INL {
                f = min(f, __builtin_amdgcn_update_dpp(f, f, 0xB1, 0xF, 0xF, false)); f = min(f, __builtin_amdgcn_update_dpp(f, f, 0x4E, 0xF, 0xF, false));
                f = min(f, __builtin_amdgcn_update_dpp(f, f, 0x141, 0xF, 0xF, false)); f = min(f, __builtin_amdgcn_update_dpp(f, f, 0x140, 0xF, 0xF, false));
                zk = seg_done(MXV, min(__builtin_amdgcn_readlane(f, 0), __builtin_amdgcn_readlane(f, 16)), min(__builtin_amdgcn_readlane(f, 32), __builtin_amdgcn_readlane(f, 48)), MXA);
                *(volatile LAS int*)(smem + 4096) = zk;
            };
            for (int s = 0; s < NSEG; ++s) {
                for (unsigned spins = 0; __builtin_expect(zk < s && s > 0, 0);) {
                    if (spins == 0) __builtin_amdgcn_s_sleep(DW4);
                    asm volatile("" ::: "memory");
                    note((int)__builtin_amdgcn_raw_buffer_load_b32(rwin, fl, W_FZ + fo, 16));
                    FC(fc2);
                    if (spin_fail(spins, rwin)) note(NSEG);
                    if (zk < s) __builtin_amdgcn_s_sleep(ZPS);
                }
                if (LKE && s > 0 && zk < (s + 8 >= NSEG ? NSEG - 1 : s + ZLA)) { __builtin_amdgcn_s_sleep(LKE); note((int)__builtin_amdgcn_raw_buffer_load_b32(rwin, fl, W_FZ + fo, 16)); }
                asm volatile("s_waitcnt lgkmcnt(0)\n\ts_barrier\n\ts_barrier" ::: "memory");
                int f = 0;
                const bool look = !LKE && zk < (s + 8 >= NSEG ? NSEG - 1 : s + ZLA);
                if (look) { __builtin_amdgcn_s_sleep(6); f = (int)__builtin_amdgcn_raw_buffer_load_b32(rwin, fl, W_FZ + fo, 16); }
                SB();
                BAR0();
                if (look) note(f);
            }
        } else if (w < 7) {
            const unsigned ooff = (unsigned)((S_ - (7 - w) * SEG + (lane >> 1)) * D_ + ot * 16 + (lane & 1) * 8) * 2u;
            const __amdgpu_buffer_rsrc_t ro = rout();
            for (int s = 0; s < 3 * NSEG - 3 + (w - 5); ++s) BAR0();
            if (w == 5) __builtin_amdgcn_s_sleep(DPS);
            const u32x4 xr = __builtin_amdgcn_raw_buffer_load_b128(rws, ooff, O_X1, 16);
            BAR0();
            BAR0();
            const char* src = op + (w - 5) * 8192 + lane * 32;
            f32x4 x = *(const f32x4*)src, y = *(const f32x4*)(src + 16);
#pragma unroll
            for (int ww = 1; ww < 4; ++ww) { x += *(const f32x4*)(src + ww * 2048); y += *(const f32x4*)(src + ww * 2048 + 16); }
            const u32x4 r = {pack2(lo2f(xr[0]) + bfr(x[0]), hi2f(xr[0]) + bfr(x[1])), pack2(lo2f(xr[1]) + bfr(x[2]), hi2f(xr[1]) + bfr(x[3])),
                             pack2(lo2f(xr[2]) + bfr(y[0]), hi2f(xr[2]) + bfr(y[1])), pack2(lo2f(xr[3]) + bfr(y[2]), hi2f(xr[3]) + bfr(y[3]))};
            st128(nosent4(r), ro, ooff, 0u, 16);
            RST(19 + w, layer, w, w & 3);
            if (w == 5) BAR0();
        } else {
            Tk tk = tk_init();
            __builtin_amdgcn_s_setprio(3);
            for (int s = 0; s < NSEG; ++s) {
                const unsigned ta0 = ck.ta, mm0 = ck.ma;
                unsigned tp = tpp + lt + xs + M1;
                if (s < SAD) {
                    if (s) wait_fine(tpp + CF, ck);
                    tp = M2 + xs + (s ? 0 : PEN1);
                    sched(tk, s);
                    tp = tpp - tp;
                    if (s == 0) { wait_fine(tp - DZ0, ck); *(volatile LAS int*)(smem + 4096) = 0; }
                }
                wait_fine_bar(tp - DZP, ck);
                wait_cyc_bar(cyc_at(ck, sx24(tp + (s + 1 < NSEG ? 0 : ML) - ck.ta)));
                clk_rate(ck, ta0, mm0);
                asm volatile("s_barrier ; 7" ::: "memory");
                if (s >= SAD && s + 1 < NSEG) sched(tk, s);
                RTS();
            }
        }
        __builtin_amdgcn_s_setprio(0);
        SST(23);
    };
    static_assert(NSEG % 3 == 1, "ring");
    auto a1_units = [&](auto late, int& u, int u1) INL {
        constexpr bool LATE = decltype(late)::v;
        int tid = mytid();
        RowTw tw;
        if (LATE) row_tw(tw, tid);
        for (; u < u1; u += !LATE ? 24 : u >= 232 && u < 240 ? 8 : 256) {
            if (!LATE) tid = mytid();
            if (w == 4) {
                for (unsigned spins = 0;;) {
                    asm volatile("" ::: "memory");
                    if (LATE) {
                        const int go = __builtin_amdgcn_readfirstlane(__builtin_amdgcn_raw_buffer_load_b32(rwin, 0u, W_GO + (unsigned)(u & 31) * 128u, 16)) - min((u >> 2) - 2, NSEG - 4);
                        if (go >= 0) break;
                        if (go < -3) __builtin_amdgcn_s_sleep(127);
                    } else if (!__any(__builtin_amdgcn_raw_buffer_load_b32(rwin, min(fresh_lane(), (unsigned)NOWN - 1u) * 4u, W_OUT + (unsigned)(u >> 2) * 256u, 16) != 0u)) break;
                    if (spin_fail(spins, rwin)) break;
                    if (u < 200) __builtin_amdgcn_s_sleep(WSLP); else __builtin_amdgcn_s_sleep(16);
                }
            }
            BAR();
            SST(24);
            a1_unit<4, LATE>(mkrsrc(ws + OFF_X2, S_ * D_ * 2), u * 8, rws, rwin, tw, tid, fs);
            if (!LATE) VMWAIT();
            FST(8);
            BAR0();
            FST(9);
            if (!LATE && w == 0) __builtin_amdgcn_raw_buffer_store_b64(__builtin_bit_cast(u32x2, tag + 9ull), rwin, LANE0, W_A1 + (unsigned)u * 128u, 16);
            SST(25);
            FSW(7);
            if (!LATE && u < 176) __builtin_amdgcn_s_sleep(A1S);
        }
    };
    auto out_role = [&](auto nt, int t0, int s0, auto between) INL {
        constexpr int NT = decltype(nt)::v, W0 = 8 - NT;
        const int o = s0 * 10 + (t0 >> 2), send = NSEG - 2;
        int rdy = -1, go = -1, zrdy = -1;
        for (int i = 0; i < s0; ++i) __builtin_amdgcn_s_sleep(OST0);
        for (int s = s0; s < send; s += 4) {
        LANES();
        const unsigned ztoff = (unsigned)((t0 * H_ + 4 * w) * 64 + lane) * 16u;
        const unsigned ptoff = (unsigned)(8 * w * 64 + lane) * 16u, ptmoff = 65536u + (unsigned)(4 * w * 16 + lane) * 16u;
        const unsigned ptoff0 = ptoff | (lane & 32u) << 25;
        const unsigned ooff = (unsigned)((lane >> 1) * D_ + tile_of(t0 + w - W0) * 16 + (lane & 1) * 8) * 2u;
        const unsigned fg = fo + (unsigned)((4 * w + (lane & 3)) * 128), gw = (unsigned)(t0 / 10) * 4u;
        const unsigned tfoff = fg + (lane < 16 ? W_FPT + (lane >> 2) * 4u : W_FZ2 + gw), zfoff = fo + W_FZ + fz_at(4 * w + (lane & 3), gw / 4u) + (lane & 32u) * 128u;
        auto wait_rdy = [&](int s) INL {
            for (unsigned spins = 0; rdy < s;) {
                asm volatile("" ::: "memory");
                if (s < TAILS || go < s - 2) {
                    go = (int)__builtin_amdgcn_readfirstlane(__builtin_amdgcn_raw_buffer_load_b32(rwin, 0u, W_GO + fo + (unsigned)o * 128u, 16));
                    rdy = max(rdy, go);
                    if (rdy < s) { if (s < TAILS || go < s - 3) __builtin_amdgcn_s_sleep(OSLP); else __builtin_amdgcn_s_sleep(OSLP3); }
                } else {
                    if (!__any((int)__builtin_amdgcn_raw_buffer_load_b32(rwin, tfoff, 0u, 16) < s)) rdy = s;
                    else __builtin_amdgcn_s_sleep(OSLP2);
                }
                if (rdy < s && spin_fail(spins, rwin)) break;
            }
        };
        const __amdgpu_buffer_rsrc_t ro = rout();
        u32x4 zf[NT][4];
        auto fetch_z = [&](int s) INL {
            for (unsigned spins = 0; zrdy < s;) {
                asm volatile("" ::: "memory");
                int f = (int)__builtin_amdgcn_raw_buffer_load_b32(rwin, zfoff, 0u, 16);
                f = min(f, __builtin_amdgcn_update_dpp(f, f, 0xB1, 0xF, 0xF, false)); f = min(f, __builtin_amdgcn_update_dpp(f, f, 0x4E, 0xF, 0xF, false));
                f = min(f, __builtin_amdgcn_update_dpp(f, f, 0x141, 0xF, 0xF, false));
                zrdy = seg_done(MXV, __builtin_amdgcn_readlane(f, 0), __builtin_amdgcn_readlane(f, 32));
                if (zrdy >= s || spin_fail(spins, rwin)) break;
                __builtin_amdgcn_s_sleep(OSLP);
            }
#pragma unroll
            for (int q = 0; q < 4 * NT; ++q) zf[q >> 2][q & 3] = __builtin_amdgcn_raw_buffer_load_b128(rws, ztoff + (q & 3) * 1024u, O_Z + (unsigned)((s - layer) * (int)ZTSEG) * 2u + (q >> 2) * (H_ * 1024u), 16);
        };
            u32x4 xr, zx[NT], pt[8], ptm[2];
            RT(0);
            fetch_z(s);
            if (NT > 1 && w >= W0 && layer == 0 && s >= 4 && s < 60) {
                asm volatile("s_waitcnt vmcnt(%0)" :: "n"(4 * NT) : "memory");
                __builtin_amdgcn_raw_buffer_store_b32(0u, rwin, LANE0, W_OUT + (unsigned)((s - 4) * 64 + t0 + w - W0) * 4u, 16);
            }
            if (w >= W0) xr = __builtin_amdgcn_raw_buffer_load_b128(rws, ooff, O_X1 + (unsigned)(s * SEG * D_ * 2), 16);
            if (s == 61) SST(27);
            wait_rdy(s);
            if (s == 61) SST(28);
            RT(1);
            const unsigned ptb = O_PT + (unsigned)(s * PTSEG) * 2u;
#pragma unroll
            for (int q = 0; q < 8; ++q) pt[q] = __builtin_amdgcn_raw_buffer_load_b128(rws, (q & 1 ? ptoff : ptoff0) + q * 1024u, ptb, 16);
#pragma unroll
            for (int q = 0; q < 2; ++q) ptm[q] = __builtin_amdgcn_raw_buffer_load_b128(rws, ptmoff + q * 8192u, ptb, 16);
#pragma unroll
            for (int q = 0; q < NT; ++q) zx[q] = __builtin_amdgcn_raw_buffer_load_b128(rws, tid * 16u, O_Z2 + (unsigned)(s * NOWN + t0 + q) * 8192u, 16);
            BAR0();
            f32x4 c[2 * NT];
#pragma unroll
            for (int q = 0; q < 4; ++q) {
#pragma unroll
                for (int u = 0; u < 2 * NT; ++u) c[u] = mfma32(zf[u >> 1][q], pt[2 * q + (u & 1)], q ? c[u] : z4);
                SB();
            }
#pragma unroll
            for (int u = 0; u < 2 * NT; ++u) {
                const int tq = u & 1, t = u >> 1;
                c[u] = mfma32((u32x4){zx[t][0], zx[t][2], zx[t][1], zx[t][3]}, ptm[tq], c[u]);
                *(f32x4*)(smem + w * (NT * 2048) + t * 2048 + (16 * tq + fr) * 64 + ((fq ^ ((fr >> 1) & 3)) << 4)) = c[u];
            }
            BAR();
            if (s == 61) SST(30);
            if (w >= W0) {
                const char* p0 = smem + (w - W0) * 2048 + (lane >> 1) * 64 + (((2 * (lane & 1)) ^ ((lane >> 2) & 3)) << 4);
                const char* p1 = smem + (w - W0) * 2048 + (lane >> 1) * 64 + (((2 * (lane & 1) + 1) ^ ((lane >> 2) & 3)) << 4);
                f32x4 x = *(const f32x4*)p0, y = *(const f32x4*)p1;
#pragma unroll
                for (int ww = 1; ww < 8; ++ww) { x += *(const f32x4*)(p0 + ww * (NT * 2048)); y += *(const f32x4*)(p1 + ww * (NT * 2048)); }
                const u32x4 r = {pack2(lo2f(xr[0]) + bfr(x[0]), hi2f(xr[0]) + bfr(x[1])), pack2(lo2f(xr[1]) + bfr(x[2]), hi2f(xr[1]) + bfr(x[3])),
                                 pack2(lo2f(xr[2]) + bfr(y[0]), hi2f(xr[2]) + bfr(y[1])), pack2(lo2f(xr[3]) + bfr(y[2]), hi2f(xr[3]) + bfr(y[3]))};
                if (layer) st128(r, ro, ooff, (unsigned)(s * SEG * D_ * 2), OAUX);
                else {
                    st128(nosent4(r), ro, ooff, (unsigned)(s * SEG * D_ * 2), 16);
                    if (NT == 1 && s < 56) { VMWAIT(); __builtin_amdgcn_raw_buffer_store_b32(0u, rwin, LANE0, W_OUT + (unsigned)(s * 64 + t0) * 4u, 16); }
                }
            } else __builtin_amdgcn_s_sleep(OSL4);
            if (s < 8 && NT > 1) SST(8 + s);
            if (s >= 56) SST(s - 40);
            RT(2);
            RTS();
            between(s);
        }
    };
    u32x8 wt[2][10];
#define PINW() asm volatile("" : "+{v[0:7]}"(wt[0][0]), "+{v[8:15]}"(wt[1][0]), "+{v[16:23]}"(wt[0][1]), "+{v[24:31]}"(wt[1][1]), "+{v[32:39]}"(wt[0][2]), "+{v[40:47]}"(wt[1][2]), "+{v[48:55]}"(wt[0][3]), "+{v[56:63]}"(wt[1][3]), "+{v[64:71]}"(wt[0][4]), "+{v[72:79]}"(wt[1][4]), "+{v[80:87]}"(wt[0][5]), "+{v[88:95]}"(wt[1][5]), "+{v[96:103]}"(wt[0][6]), "+{v[104:111]}"(wt[1][6]), "+{v[112:119]}"(wt[0][7]), "+{v[120:127]}"(wt[1][7]), "+{v[128:135]}"(wt[0][8]), "+{v[136:143]}"(wt[1][8]), "+{v[144:151]}"(wt[0][9]), "+{v[152:159]}"(wt[1][9]))
#define KILLW() asm volatile("" : "={v[0:7]}"(wt[0][0]), "={v[8:15]}"(wt[1][0]), "={v[16:23]}"(wt[0][1]), "={v[24:31]}"(wt[1][1]), "={v[32:39]}"(wt[0][2]), "={v[40:47]}"(wt[1][2]), "={v[48:55]}"(wt[0][3]), "={v[56:63]}"(wt[1][3]), "={v[64:71]}"(wt[0][4]), "={v[72:79]}"(wt[1][4]), "={v[80:87]}"(wt[0][5]), "={v[88:95]}"(wt[1][5]), "={v[96:103]}"(wt[0][6]), "={v[104:111]}"(wt[1][6]), "={v[112:119]}"(wt[0][7]), "={v[120:127]}"(wt[1][7]), "={v[128:135]}"(wt[0][8]), "={v[136:143]}"(wt[1][8]), "={v[144:151]}"(wt[0][9]), "={v[152:159]}"(wt[1][9]))
    if (blockIdx.x < 64) {
        for (;;) {
            load_qv(wt, bid & 31, bid >= 32, [&]() INL {
                rot0 = __builtin_amdgcn_raw_buffer_load_b64(rws, 0u, 128u, 16);
                if (w == 0) wait_tags(rwin, 0, 1, 64u, tag + 8ull, true);
                if (w == 7) clk_init(ck);
                if (w == 1) { const unsigned x = __builtin_amdgcn_raw_buffer_load_b32(mkrsrc(arg(A_MEM0), NM * D_ * 2), fresh_lane() * 160u, 0, 0); asm volatile("" :: "v"(x)); }
            });
            const unsigned x = get_rot();
            if (__builtin_expect(x == vx, 1)) break;
            set_role(x);
        }
        SST(1);
        if (TLBW) {
            const unsigned x = __builtin_amdgcn_raw_buffer_load_b32(rws, fresh_lane() << TLBW, ((blockIdx.x >> 3) + w * 8u) << (TLBW + 6) & 0x7FFFFFFFu, 0);
            asm volatile("" :: "v"(x));
        }
        PINW();
    } else {
        if (bid >= NWRK) probe();
        KILLW();
    }
    for (; layer < 2; ++layer) {
        new_layer();
        PINW();
        BAR();
        if (qx || (layer ? bid < 96 : (bid >= 32 && bid < 64) || (bid >= 104 && bid < NREC) || xq)) {
            const int ci = qx ? bid - 80 : bid < 96 ? bid : bid < NREC ? bid - 104 : 64 + (bid - NREC) / 10 * 8 + (bid - NREC) % 10;
            SST(26);
            a2_unit(ci >= 100 ? ci - 100 : ci + 60, layer, rws, rwin, mytid(), [&](int k) INL {
                if (k == 2) SST(28);
                else if (w == 0) {
                    if (layer && bid >= 64) for (unsigned spins = 0;;) {
                        asm volatile("" ::: "memory");
                        if (__all(__builtin_bit_cast(u64, __builtin_amdgcn_raw_buffer_load_b64(rwin, 0u, W_A1 + (unsigned)(200 + (bid & 15)) * 128u, 16)) == tag + 9ull) || spin_fail(spins, rwin)) break;
                        __builtin_amdgcn_s_sleep(100);
                    }
                    if (layer && bid >= 64) rec_end(GATE);
                    wait_tags(rwin, 0, 4, layer ? 32u : 64u, tag + 8ull + layer, !layer);
                    SST(27);
                }
            }, fs);
            SST(29);
            VMWAIT();
            FST(8);
            BAR0();
            FST(9);
            if (w == 0) {
                __builtin_amdgcn_raw_buffer_store_b32(0u, rwin, LANE0, W_COL + fo + (unsigned)ci * 128u, 16);
                if (bid == NQT && layer == 1) __builtin_amdgcn_raw_buffer_store_b64(__builtin_bit_cast(u32x2, tag), rws, LANE0, 0u, 16);
            }
            SST(30);
            FSW(1 + layer);
        }
#define NEXTB() do { PINW(); asm volatile("" : "+s"(bid), "+s"(w)); } while (0)
        NEXTB();
        if (bid < 32) { fresh_rsrc(); qk_role(wt); PINW(); }
        NEXTB();
        const int hh = bid < 96 ? bid & 31 : bid < NQT ? (bid - NREC) / 10 * 8 + (bid - NREC) % 10 : (bid - NQT) & 31;
        if (((qx || (bid >= 32 && bid < 64)) && layer == 0) || xq) { if (layer) { if (w == 0) rec_end(GATX); BAR0(); } load_qv(wt, hh, xt || bid < 64, []() {}); SST(1); PINW(); }
        NEXTB();
        if (bid >= 32 && bid < 64) { if (layer == 0) near_init(); fresh_rsrc(); vo_role(wt); PINW(); }
        NEXTB();
        if ((bid >= 64 && bid < 96) || xt) {
            fresh_rsrc();
            vt_role(wt, hh, xt ? MXV : ~MXV);
            if (xt) {
                out_role(IC<1>(), 5 * (hh & 7) + (hh & 1 ? 0 : 4), hh >> 3, [&](int s) INL {
                    if (s < 28) {
                        if (layer == 0) for (unsigned i = (s >> 2) * 2; i < (s >> 2) * 2 + 2; ++i)
                            st128(SENT, i < 7 ? mkrsrc(ws + O_ARM, O_ARM1 - O_ARM) : i < 12 ? mkrsrc(ws + O_Z - ZTSEG * 2, ZTSEG * 2) : mkrsrc(arg(1) + (W_ARM - W_LAYER) / 2, W_LAYER),
                                  (unsigned)(hh * 512 + mytid()) * 16u, (i < 7 ? i : i < 12 ? i - 7 : i - 12) * 262144u, 16);
                    } else if (s < 32) {
                        VMWAIT();
                        BAR0();
                        if (w >= 4) __builtin_amdgcn_raw_buffer_store_b32(NSEG - 1, rwin, LANE0, W_FZ + fo + fz_at(hh, w - 4) + 4096u, 16);
                    }
                });
                static_assert(O_ARM + 7u * 262144u >= O_ARM1 && ZTSEG * 2 <= 5u * 262144u && W_LAYER <= 262144u && !(MXV >> 40), "arming");
            }
            PINW();
        }
        NEXTB();
        if ((qx && !xt) || xq) { fresh_rsrc(); qt_role(wt, hh, IC<1>()); PINW(); }
        NEXTB();
        if (bid >= 96 && !qx) {
            const int fi = xq ? 256 : bid >= NWRK ? 255 - bid : bid >= NREC ? 8 + (bid - NREC) / 10 * 2 + (bid - NREC) % 10 : bid - 72, tid = mytid();
            RowTw tw;
            row_tw(tw, tid);
            if (layer == 0 && fi >= 16 && fi < 32) { if (w == 0) wait_tags(rwin, 0, 1, 64u, tag + 8ull, true); BAR0(); }
            if (layer && fi < 64) {
                for (unsigned spins = 0;;) {
                    asm volatile("" ::: "memory");
                    if (__all(__builtin_bit_cast(u64, __builtin_amdgcn_raw_buffer_load_b64(rwin, 0u, W_A1 + (unsigned)fi * 128u, 16)) == tag + 9ull) || spin_fail(spins, rwin)) break;
                    __builtin_amdgcn_s_sleep(8);
                }
            }
            bool cols = false;
            for (int u = fi; u < 256; u += 24) {
                a3_unit(mkrsrc(layer ? (const u16*)(ws + OFF_X2) : arg(A_X), S_ * D_ * 2), rout(), u * 8, layer, rws, rwin, tw, tid, [&]() INL {
                    if (cols) return;
                    cols = true;
                    if (w == 7) {
                        const unsigned l = fresh_lane();
                        unsigned o[3] = {l * 128u, l * 128u + 8192u, l < 32u ? l * 128u + 16384u : 0x40000000u};
                        for (unsigned spins = 0;;) {
                            asm volatile("" ::: "memory");
                            unsigned v[3];
#pragma unroll
                            for (int g = 0; g < 3; ++g) v[g] = __builtin_amdgcn_raw_buffer_load_b32(rwin, o[g], W_COL + fo, 16);
#pragma unroll
                            for (int g = 0; g < 3; ++g) if (v[g] == 0u) o[g] = 0x40000000u;
                            if (!__any((v[0] | v[1] | v[2]) != 0u) || spin_fail(spins, rwin)) break;
                            __builtin_amdgcn_s_sleep(CSLP);
                        }
                    }
                    BAR0();
                    SST(6);
                }, fs);
                VMWAIT();
                FST(8);
                BAR0();
                FST(9);
                if (w == 0) __builtin_amdgcn_raw_buffer_store_b32(0u, rwin, LANE0, W_H + fo + (unsigned)((u >> 2) * 128 + (u & 3) * 4), 16);
                if (u < 64) SST(7);
                FSW(u < 64 ? 3 + layer : 5);
                if (fi >= 24) break;
                if (u < 64) u += 40;
            }
            if (bid < NREC) { fresh_rsrc(); own_role(); }
            else if (xq) out_role(IC<4>(), 5 * (hh & 7) + (hh & 1), hh >> 3, [](int) {});
            else if (layer == 0) { int u = fi; a1_units(IC<0>(), u, 224); u = bid < NQT ? 192 + (bid - NREC) / 10 + 4 * ((bid - NREC) % 10) : bid < 248 ? bid - 8 : bid; a1_units(IC<1>(), u, 256); }
            KILLW();
        }
        NEXTB();
        SST(31);
    }
}

constexpr int L_DBG = 0;
extern "C" size_t dbg_offset(int i) {
    const size_t o[] = {OFF_WIN, W_TMO, W_NEAR, OFF_F, OFF_X1, OFF_X2, OFF_H, OFF_WFT};
    return o[i];
}

extern "C" void kernel_launch(void* const* d_in, const int* in_sizes, int n_in, void* d_out, int out_size, void* d_ws, size_t ws_size,
                              hipStream_t stream) {
    char* ws = (char*)d_ws;
    hipFuncSetAttribute((const void*)mega_kernel, hipFuncAttributeMaxDynamicSharedMemorySize, L_END + L_DBG);
    char* win = (char*)(((uintptr_t)(ws + OFF_WIN) + 0x1FFFFFu) & ~(uintptr_t)0x1FFFFFu);
    Args a{ws, win, (const u16*)d_in[0], (const u16*)d_in[1], (const u16*)d_in[2], (const u16*)d_in[4], (const u16*)d_in[5], (const u16*)d_in[6], (const u16*)d_in[7], (const u16*)d_in[8], (u16*)d_out};
    mega_kernel<<<256, 512, L_END + L_DBG, stream>>>(a);
}
```

```cpp
#include <hip/hip_runtime.h>
#include <hip/hip_bf16.h>
#include <stdint.h>

typedef unsigned short u16;
typedef unsigned long long u64;
typedef __attribute__((ext_vector_type(8))) __bf16 bf16x8;
typedef __attribute__((ext_vector_type(4))) short s16x4;
typedef __attribute__((ext_vector_type(4))) float f32x4;
typedef __attribute__((ext_vector_type(8))) float f32x8;
typedef __attribute__((ext_vector_type(16))) float f32x16;
typedef __attribute__((ext_vector_type(4))) unsigned u32x4;
typedef __attribute__((ext_vector_type(2))) unsigned u32x2;
typedef __attribute__((ext_vector_type(8))) unsigned u32x8;
typedef __attribute__((ext_vector_type(16))) __bf16 bf16x16;
typedef __attribute__((ext_vector_type(2))) unsigned short u16x2;
#define LAS __attribute__((address_space(3)))
#define INL __attribute__((always_inline))

constexpr int S_ = 2048, D_ = 640, DF = 321, H_ = 32, DH = 128, HD = 4096, NM = 8, SEG = 32, NSEG = 64;
constexpr int NOWN = 40;
constexpr int NCOL = 160;
constexpr size_t ZTSEG = (size_t)NOWN * H_ * 512;
constexpr unsigned PAIR = 16384, HALF = 8192;
constexpr unsigned W_MEM = 0, W_T = W_MEM + 24 * PAIR, W_P = 33 * PAIR, W_PROBE = 74 * PAIR, W_GO = W_PROBE + 2 * PAIR, W_FZ = W_GO + 5120, W_FPT = W_FZ + 8192, W_FZ2 = W_FPT + 4096,
                   W_FQ = W_FZ2 + 4096, W_H = W_FQ + 8192, W_COL = W_H + HALF, W_PL = W_COL + 20480, W_LAYER = W_PL + 2 * 81920 - W_GO, W_OUT = 32 * PAIR, W_MISC = W_OUT + 14336,
                   W_ARM = W_GO + 2 * W_LAYER, W_A1 = 106 * PAIR, W_ZM = 2u * 1024 * 1024, W_END = W_ZM + 120 * PAIR;
static_assert(W_ARM <= W_A1 && W_ARM % 1024 == 0, "window");
#define ZFX 1
__device__ __forceinline__ unsigned zfix(unsigned o, unsigned slot) { return ZFX ? o ^ (~(slot + o) >> 5 & PAIR) : o; }
__device__ __forceinline__ unsigned p_slot(int s) { return W_P + (unsigned)((s & 3) * 10 + (((s & 3) + 1) >> 2)) * PAIR; }
constexpr unsigned W_TMO = W_MISC, W_NEAR = W_MISC + 128;
constexpr int PTSEG = 32768 + 8192;

#define FPAD 256
constexpr unsigned FST_ = S_ * 16 + FPAD, GST = 4096 + FPAD, WST = S_ * 8 + FPAD;
constexpr size_t al(size_t x) { return (x + 255) & ~(size_t)255; }
constexpr size_t OFF_CTL  = 0;
constexpr size_t OFF_WFT  = 4096 + 4 * 1024 * 1024;
constexpr size_t OFF_F    = OFF_WFT + al((size_t)(NCOL + 2) * WST);
constexpr size_t OFF_X1   = OFF_F + al((size_t)NCOL * FST_);
constexpr size_t OFF_X2   = OFF_X1 + al((size_t)S_ * D_ * 2);
constexpr size_t OFF_FL   = OFF_X2 + al((size_t)S_ * D_ * 2);
constexpr int NHF = 5;
constexpr size_t OFF_H    = OFF_FL + 3 * (size_t)NCOL * GST + NHF * 40960;
#define ZPAD 0
constexpr size_t OFF_Z    = OFF_H + al((size_t)S_ * D_ * 2) + ZTSEG * 2 + ZPAD;
constexpr size_t OFF_Z2   = OFF_Z + al(ZTSEG * NSEG * 2);
constexpr size_t OFF_PT   = OFF_Z2 + al((size_t)NSEG * NOWN * 8192);
constexpr size_t OFF_QK   = OFF_PT + al((size_t)NSEG * PTSEG * 2);
constexpr size_t OFF_WIN  = OFF_QK + (size_t)NSEG * H_ * 16384;
constexpr size_t OFF_END  = OFF_WIN + (size_t)6 * 1024 * 1024;
static_assert(OFF_END <= (size_t)256 * 1024 * 1024, "workspace");
constexpr unsigned O_FL = (unsigned)OFF_FL, O_ARM = O_FL - 256 * D_ * 2, O_ARM1 = O_FL + 2 * NCOL * GST, O_WFT = (unsigned)OFF_WFT, O_F = (unsigned)OFF_F, O_H = (unsigned)OFF_H, O_X1 = (unsigned)OFF_X1, O_Z = (unsigned)OFF_Z, O_Z2 = (unsigned)OFF_Z2, O_PT = (unsigned)OFF_PT, O_QK = (unsigned)OFF_QK;

constexpr double PI_ = 3.14159265358979323846;
constexpr double tay(double x, int s) {
    double t = s ? x : 1.0, r = t;
    for (int i = s + 1; i < 18; i += 2) { t *= -x * x / (double)(i * (i + 1)); r += t; }
    return r;
}
struct Cs { float c, s; };
constexpr Cs cis(int m, int n) {
    m %= n;
    const int q = (m * 8 / n + 1) / 2;
    const double x = 2.0 * PI_ * (double)(4 * m - q * n) / (double)(4 * n), c = tay(x, 0), s = tay(x, 1);
    return (q & 3) == 0 ? Cs{(float)c, (float)-s} : (q & 3) == 1 ? Cs{(float)-s, (float)-c} : (q & 3) == 2 ? Cs{(float)-c, (float)s} : Cs{(float)s, (float)c};
}
template <int NI> struct alignas(16) Tw7 { Cs v[4][NI][2]; };
template <int NI, int N, int KIND> constexpr Tw7<NI> mk_tw() {
    Tw7<NI> r{};
    for (int i = 0; i < NI; ++i)
        for (int q = 1; q <= 8; ++q) r.v[(q - 1) >> 1][i][(q - 1) & 1] = cis((KIND == 0 ? i : KIND == 1 ? 8 * (i >> 3) : (i >> 3) + 10 * (i & 7)) * q, N);
    return r;
}
struct TwCol { Tw7<256> a; Tw7<32> b; Tw7<4> c; };
static __device__ const TwCol TWC = {mk_tw<256, 2048, 0>(), mk_tw<32, 256, 0>(), mk_tw<4, 32, 0>()};
static __device__ const Tw7<80> TWR2 = mk_tw<80, 640, 1>();
static __device__ const Tw7<80> TWR3 = mk_tw<80, 640, 2>();

#define st128(v, r, vo, so, aux) do { u32x4 d_ = (v); asm volatile("" : "+v"(d_)); __builtin_amdgcn_raw_buffer_store_b128(d_, r, vo, so, aux); asm volatile("s_nop 1" :: "v"(d_)); } while (0)
__device__ __forceinline__ float bf2f(u16 v) { return __uint_as_float((unsigned)v << 16); }
__device__ __forceinline__ u16 f2bf(float f) { __bf16 b = (__bf16)f; return __builtin_bit_cast(u16, b); }
__device__ __forceinline__ float bfr(float f) { return (float)(__bf16)f; }
typedef __attribute__((ext_vector_type(2))) float f32x2;
typedef __attribute__((ext_vector_type(2))) __bf16 bf16x2;
__device__ __forceinline__ unsigned pack2(float a, float b) { return __builtin_bit_cast(unsigned, __builtin_convertvector((f32x2){a, b}, bf16x2)); }
__device__ __forceinline__ float lo2f(unsigned v) { return __uint_as_float(v << 16); }
__device__ __forceinline__ float hi2f(unsigned v) { return __uint_as_float(v & 0xFFFF0000u); }
__device__ __forceinline__ float fmx(float a, float b) { return __builtin_elementwise_maximum(a, b); }
__device__ __forceinline__ f32x4 mfma32(u32x4 a, u32x4 b, f32x4 c) {
    return __builtin_amdgcn_mfma_f32_16x16x32_bf16(__builtin_bit_cast(bf16x8, a), __builtin_bit_cast(bf16x8, b), c, 0, 0, 0);
}
__device__ __forceinline__ f32x4 mfma16(u32x2 a, u32x2 b, f32x4 c) {
    return __builtin_amdgcn_mfma_f32_16x16x16bf16_1k(__builtin_bit_cast(s16x4, a), __builtin_bit_cast(s16x4, b), c, 0, 0, 0);
}
__device__ __forceinline__ f32x4 smfmac(u32x4 a, u32x8 b, f32x4 c, int idx) {
    return __builtin_amdgcn_smfmac_f32_16x16x64_bf16(__builtin_bit_cast(bf16x8, a), __builtin_bit_cast(bf16x16, b), c, idx, 0, 0);
}
__device__ __forceinline__ int pi16(int k) { return ((k & 3) << 2) | ((k >> 1) & 2) | (k >> 3); }
__device__ __forceinline__ float addswap32(float a, float b) {
    auto r = __builtin_amdgcn_permlane32_swap(__float_as_uint(a), __float_as_uint(b), false, false);
    return __uint_as_float(r[0]) + __uint_as_float(r[1]);
}
__device__ __forceinline__ float xmax3216(float v) {
    auto r = __builtin_amdgcn_permlane32_swap(__float_as_uint(v), __float_as_uint(v), false, false);
    v = fmx(__uint_as_float(r[0]), __uint_as_float(r[1]));
    auto q = __builtin_amdgcn_permlane16_swap(__float_as_uint(v), __float_as_uint(v), false, false);
    return fmx(__uint_as_float(q[0]), __uint_as_float(q[1]));
}
__device__ __forceinline__ float xsum3216(float v) {
    auto r = __builtin_amdgcn_permlane32_swap(__float_as_uint(v), __float_as_uint(v), false, false);
    v = __uint_as_float(r[0]) + __uint_as_float(r[1]);
    auto q = __builtin_amdgcn_permlane16_swap(__float_as_uint(v), __float_as_uint(v), false, false);
    return __uint_as_float(q[0]) + __uint_as_float(q[1]);
}
template <int CTRL> __device__ __forceinline__ float dppf(float v) { return __uint_as_float((unsigned)__builtin_amdgcn_update_dpp(0, (int)__float_as_uint(v), CTRL, 0xF, 0xF, false)); }
__device__ __forceinline__ float wsum(float v) {
    v += dppf<0xB1>(v); v += dppf<0x4E>(v); v += dppf<0x141>(v); v += dppf<0x140>(v);
    return xsum3216(v);
}
__device__ __forceinline__ unsigned ror8(unsigned v) { return (unsigned)__builtin_amdgcn_update_dpp(0, (int)v, 0x128, 0xF, 0xF, false); }
__device__ __forceinline__ float ror8f(float v) { return __uint_as_float(ror8(__float_as_uint(v))); }
__device__ __forceinline__ unsigned nosent(unsigned v) {
    u16x2 lim = {0xFFFE, 0xFFFE};
    return __builtin_bit_cast(unsigned, __builtin_elementwise_min(__builtin_bit_cast(u16x2, v), lim));
}
__device__ __forceinline__ unsigned pkmax(unsigned a, unsigned b) {
    return __builtin_bit_cast(unsigned, __builtin_elementwise_max(__builtin_bit_cast(u16x2, a), __builtin_bit_cast(u16x2, b)));
}
__device__ __forceinline__ bool has_sent(u32x4 v) {
    unsigned m = pkmax(pkmax(v.x, v.y), pkmax(v.z, v.w));
    return (m & 0xFFFFu) == 0xFFFFu || (m >> 16) == 0xFFFFu;
}

__device__ __forceinline__ unsigned nosent1(unsigned v) { return min(v, 0xFFFFFFFEu); }
__device__ __forceinline__ unsigned smax(u32x4 v) { return max(v[1], v[3]); }
__device__ __forceinline__ u32x4 sent4() { unsigned x; asm volatile("v_mov_b32 %0, -1" : "=v"(x)); return (u32x4){x, x, x, x}; }
#define SENT sent4()
__device__ __forceinline__ f32x4 zero4() { f32x2 x, y; asm volatile("v_mov_b64 %0, 0\n\tv_mov_b64 %1, 0" : "=v"(x), "=v"(y)); return (f32x4){x[0], x[1], y[0], y[1]}; }
__device__ __forceinline__ u32x4 nosent4(u32x4 v) { return (u32x4){nosent1(v[0]), nosent1(v[1]), nosent1(v[2]), nosent1(v[3])}; }
#define SPIN_CAP 400000u
__device__ __forceinline__ void set_tmo(__amdgpu_buffer_rsrc_t rwin) { __builtin_amdgcn_raw_buffer_store_b32(0u, rwin, 0u, W_TMO, 16); }
__device__ __forceinline__ bool spin_fail(unsigned& spins, __amdgpu_buffer_rsrc_t rwin) {
    if (++spins > SPIN_CAP) { set_tmo(rwin); return true; }
    return (spins & 1023u) == 0u && __builtin_amdgcn_readfirstlane(__builtin_amdgcn_raw_buffer_load_b32(rwin, 0u, W_TMO, 16)) == 0u;
}
#define GATE 2
#define GATX 2
__device__ __forceinline__ void tell_at(__amdgpu_buffer_rsrc_t rwin, unsigned so, unsigned d) {
    __builtin_amdgcn_raw_buffer_store_b32(((unsigned)__builtin_amdgcn_s_memrealtime() + d) & 0xFFFFFFu, rwin, 0u, so, 16);
}
template <int N> __device__ __forceinline__ void wait_told(__amdgpu_buffer_rsrc_t rwin, const unsigned (&vo)[N], unsigned so, int slp) {
    unsigned v[N];
#pragma unroll
    for (int g = 0; g < N; ++g) v[g] = ~0u;
    for (unsigned spins = 0;;) {
        asm volatile("" ::: "memory");
        unsigned x[N], m = 0;
#pragma unroll
        for (int g = 0; g < N; ++g) x[g] = __builtin_amdgcn_raw_buffer_load_b32(rwin, v[g] == ~0u ? vo[g] : 0x40000000u, so, 16);
#pragma unroll
        for (int g = 0; g < N; ++g) { if (v[g] == ~0u) v[g] = x[g]; m = max(m, v[g]); }
        if (!__any(m == ~0u) || spin_fail(spins, rwin)) break;
        if (slp) __builtin_amdgcn_s_sleep(8); else __builtin_amdgcn_s_sleep(3);
    }
    for (int i = 0; i < 400; ++i) {
        const unsigned t = (unsigned)__builtin_amdgcn_s_memrealtime();
        bool e = false;
#pragma unroll
        for (int g = 0; g < N; ++g) e |= ((v[g] - t) << 8) - 1u < 0x7FFFFFFFu;
        if (!__any(e)) break;
    }
}
#define TG 250
#define TH 155
#define TH0 185
#define DVT0 950
#define A2P 0
#define TWG 1
#define TRK 4
#define X2W 2
#define PS1 3
#define PS2 3
#define PS3 2

typedef f32x2 c2;
__device__ __forceinline__ c2 swp(c2 a) { return __builtin_shufflevector(a, a, 1, 0); }
__device__ __forceinline__ c2 fm(c2 a, c2 b, c2 c) { return __builtin_elementwise_fma(a, b, c); }
__device__ __forceinline__ c2 bc(float x) { return (c2){x, x}; }
__device__ __forceinline__ c2 sgn() { c2 s = {-1.f, 1.f}; asm("" : "+s"(s)); return s; }
template <int SG> __device__ __forceinline__ c2 addi(c2 a, c2 b) { return fm(swp(b), SG > 0 ? sgn() : swp(sgn()), a); }
template <int SG> __device__ __forceinline__ c2 cfin(c2 a, c2 w, c2 t) {
    c2 d;
    if (SG < 0) asm("v_pk_fma_f32 %0, %1, %2, %3 op_sel:[1,1,0] op_sel_hi:[0,1,1] neg_lo:[1,0,0]" : "=v"(d) : "v"(a), "v"(w), "v"(t));
    else asm("v_pk_fma_f32 %0, %1, %2, %3 op_sel:[1,1,0] op_sel_hi:[0,1,1] neg_hi:[1,0,0]" : "=v"(d) : "v"(a), "v"(w), "v"(t));
    return d;
}
struct Tw { f32x4 v[4]; };
template <int NI> __device__ __forceinline__ void ldtw(Tw& t, const Tw7<NI>& tab, int i) {
#pragma unroll
    for (int j = 0; j < 4; ++j) t.v[j] = *(const f32x4*)tab.v[j][i];
}
template <int NI> __device__ __forceinline__ void ldtw(Tw& t, const LAS f32x4* p) {
#pragma unroll
    for (int j = 0; j < 4; ++j) t.v[j] = p[j * NI];
}
template <int SG> __device__ __forceinline__ void twid(c2 (&u)[8], const Tw& t) {
    c2 w[8], m[8];
#pragma unroll
    for (int r = 1; r < 8; ++r) {
        const f32x4 x = t.v[(r - 1) >> 1];
        w[r] = r & 1 ? (c2){x[0], x[1]} : (c2){x[2], x[3]};
        m[r] = u[r] * bc(w[r].x);
    }
    if (TWG) asm("" : "+v"(m[1]), "+v"(m[2]), "+v"(m[3]), "+v"(m[4]), "+v"(m[5]), "+v"(m[6]), "+v"(m[7]));
#pragma unroll
    for (int r = 1; r < 8; ++r) u[r] = cfin<SG>(u[r], w[r], m[r]);
}
template <int SG> __device__ __forceinline__ void dft4(c2& a, c2& b, c2& c, c2& d) {
    const c2 t0 = a + c, t1 = a - c, t2 = b + d, t3 = b - d;
    a = t0 + t2; c = t0 - t2; b = addi<SG>(t1, t3); d = addi<-SG>(t1, t3);
}
template <int SG> __device__ __forceinline__ void dft8(c2 (&u)[8]) {
    c2 e0 = u[0], e1 = u[2], e2 = u[4], e3 = u[6], o0 = u[1], o1 = u[3], o2 = u[5], o3 = u[7];
    dft4<SG>(e0, e1, e2, e3); dft4<SG>(o0, o1, o2, o3);
    const c2 r = bc(0.70710678118654752440f);
    o1 = addi<SG>(o1, o1); o3 = addi<-SG>(o3, o3);
    u[0] = e0 + o0; u[4] = e0 - o0;
    u[1] = fm(o1, r, e1); u[5] = fm(o1, -r, e1);
    u[2] = addi<SG>(e2, o2); u[6] = addi<-SG>(e2, o2);
    u[3] = fm(o3, -r, e3); u[7] = fm(o3, r, e3);
}
template <int SG> __device__ __forceinline__ void dft5(c2& x0, c2& x1, c2& x2, c2& x3, c2& x4) {
    const c2 c1 = bc(0.30901699437494742410f), c2_ = bc(-0.80901699437494742410f), s1 = bc(0.95105651629515357212f), s2 = bc(0.58778525229247312917f);
    const c2 t1 = x1 + x4, t2 = x2 + x3, t3 = x1 - x4, t4 = x2 - x3;
    const c2 a1 = fm(c2_, t2, fm(c1, t1, x0)), a2 = fm(c1, t2, fm(c2_, t1, x0));
    const c2 b1 = fm(s2, t4, s1 * t3), b2 = fm(-s1, t4, s2 * t3);
    x0 = x0 + t1 + t2;
    x1 = addi<SG>(a1, b1); x4 = addi<-SG>(a1, b1); x2 = addi<SG>(a2, b2); x3 = addi<-SG>(a2, b2);
}
template <int SG> __device__ __forceinline__ void dft10(c2 (&u)[10]) {
    c2 e0 = u[0], e1 = u[2], e2 = u[4], e3 = u[6], e4 = u[8], o0 = u[5], o1 = u[7], o2 = u[9], o3 = u[1], o4 = u[3];
    dft5<SG>(e0, e1, e2, e3, e4); dft5<SG>(o0, o1, o2, o3, o4);
    u[0] = e0 + o0; u[5] = e0 - o0; u[6] = e1 + o1; u[1] = e1 - o1; u[2] = e2 + o2; u[7] = e2 - o2; u[8] = e3 + o3; u[3] = e3 - o3; u[4] = e4 + o4; u[9] = e4 - o4;
}
__device__ __forceinline__ void swap32(c2& v) { auto r = __builtin_amdgcn_permlane32_swap(__float_as_uint(v.x), __float_as_uint(v.y), false, false); v = (c2){__uint_as_float(r[0]), __uint_as_float(r[1])}; }
template <int B> __device__ __forceinline__ void swapl(c2& a, c2& b) {
#pragma unroll
    for (int i = 0; i < 2; ++i) {
        auto r = B == 5 ? __builtin_amdgcn_permlane32_swap(__float_as_uint(a[i]), __float_as_uint(b[i]), false, false) : __builtin_amdgcn_permlane16_swap(__float_as_uint(a[i]), __float_as_uint(b[i]), false, false);
        a[i] = __uint_as_float(r[0]); b[i] = __uint_as_float(r[1]);
    }
}
__device__ __forceinline__ c2 unpk(unsigned v) { return (c2){lo2f(v), hi2f(v)}; }
__device__ __forceinline__ float hsum32(c2 s) {
    float v = s.x + s.y;
    v += dppf<0xB1>(v); v += dppf<0x4E>(v); v += dppf<0x141>(v); v += dppf<0x140>(v);
    auto q = __builtin_amdgcn_permlane16_swap(__float_as_uint(v), __float_as_uint(v), false, false);
    return __uint_as_float(q[0]) + __uint_as_float(q[1]);
}
__device__ __forceinline__ void ln_row(const c2 (&v)[10], unsigned (&o)[10]) {
    const c2 s = ((v[0] + v[1]) + (v[2] + v[3])) + ((v[4] + v[5]) + (v[6] + v[7])) + (v[8] + v[9]);
    const float mean = hsum32(s) * (1.f / 640.f);
    c2 q0 = bc(0.f), q1 = bc(0.f);
#pragma unroll
    for (int r = 0; r < 10; r += 2) { const c2 d = v[r] - bc(mean), e = v[r + 1] - bc(mean); q0 = fm(d, d, q0); q1 = fm(e, e, q1); }
    const float mb = bfr(mean);
    c2 d[10];
#pragma unroll
    for (int r = 0; r < 10; ++r) { const c2 x = v[r] - bc(mb); d[r] = unpk(pack2(x.x, x.y)); }
    const float var = hsum32(q0 + q1) * (1.f / 640.f);
    const float rs = __builtin_amdgcn_rcpf(bfr(sqrtf(bfr(bfr(var) + 1e-5f))));
#pragma unroll
    for (int r = 0; r < 10; ++r) { const c2 x = d[r] * bc(rs); o[r] = pack2(x.x, x.y); }
}

__device__ __forceinline__ __amdgpu_buffer_rsrc_t mkrsrc(const void* p, unsigned bytes) {
    return __builtin_amdgcn_make_buffer_rsrc((void*)p, 0, bytes, 0x00020000);
}
__device__ __forceinline__ unsigned xcc_id() { unsigned x; asm volatile("s_getreg_b32 %0, hwreg(HW_REG_XCC_ID)" : "=s"(x)); return x & 15u; }
#define VMWAIT() do { asm volatile("" ::: "memory"); __builtin_amdgcn_s_waitcnt(0x0F70); asm volatile("" ::: "memory"); } while (0)
#define BAR() asm volatile("s_waitcnt lgkmcnt(0)\n\ts_barrier" ::: "memory")
#define BAR0() asm volatile("s_barrier" ::: "memory")

#define FST(k) do {} while (0)
#define FSW(k) do {} while (0)
extern __shared__ __attribute__((aligned(16))) char smem[];
#define RST(k, lay, wv, sel) do {} while (0)
constexpr int R_C = 8 * 10 * 72 * 8;
constexpr float FSC = 0.5f / ((float)S_ * (float)D_);
__device__ __forceinline__ c2 ldv(const LAS void* p) { return *(const volatile LAS c2*)p; }
struct RowTw { Tw a, b; };
__device__ __forceinline__ void row_tw(RowTw& t, int tid) { const int i = tid % 80; ldtw(t.a, TWR2, i); ldtw(t.b, TWR3, i); }
template <int NF, bool LATE> __device__ __forceinline__ void a1_unit(__amdgpu_buffer_rsrc_t rx, int row0, __amdgpu_buffer_rsrc_t rws, __amdgpu_buffer_rsrc_t rwin, RowTw& tw, int tid, unsigned (&fs)[12]) {
    const int wave = __builtin_amdgcn_readfirstlane(tid >> 6), lane = tid & 63;
    LAS c2* A = (LAS c2*)smem; LAS c2* C = (LAS c2*)(smem + R_C);
    FST(0);
    if (wave < NF) {
        unsigned d[10];
        for (unsigned spins = 0;;) {
            asm volatile("" ::: "memory");
#pragma unroll
            for (int r = 0; r < 10; ++r) d[r] = __builtin_amdgcn_raw_buffer_load_b32(rx, (lane & 31) * 4u + (lane >> 5) * (D_ * 2u) + r * 128u, (unsigned)(row0 + 2 * wave) * (D_ * 2), 16);
            if (!LATE) break;
            unsigned m = d[0];
#pragma unroll
            for (int r = 1; r < 10; ++r) m = max(m, d[r]);
            if (!__any(m == ~0u) || spin_fail(spins, rwin)) break;
            __builtin_amdgcn_s_sleep(PS1);
        }
        c2 u[10];
#pragma unroll
        for (int r = 0; r < 10; ++r) u[r] = unpk(d[r]);
        FST(1);
        if (LATE) RST(16, 0, wave, 0);
        ln_row(u, d);
        FST(2);
#pragma unroll
        for (int r = 0; r < 10; ++r) { u[r] = unpk(d[r]); swap32(u[r]); }
        dft10<-1>(u);
#pragma unroll
        for (int r = 0; r < 10; ++r) A[(wave * 10 + r) * 72 + (lane & 31) + 36 * (lane >> 5)] = u[r];
        FST(3);
    }
    BAR();
    if (NF == 4 && !LATE) row_tw(tw, tid);
    FST(4);
#pragma unroll
    for (int c = 0; c < (NF > 4 ? 2 : 1); ++c) {
        if (tid < (NF > 4 ? (c ? 160 : 480) : 80 * NF)) {
            const int K = (tid >> 3) + 60 * c, m = tid & 7;
            LAS c2* a = A + K * 72;
            c2 u[8];
#pragma unroll
            for (int q = 0; q < 8; ++q) u[q] = ldv(a + (m >> 1) + 36 * (m & 1) + 4 * q);
            twid<-1>(u, tw.a);
            dft8<-1>(u);
#pragma unroll
            for (int p = 0; p < 8; ++p) a[9 * p + m] = u[p];
            asm volatile("" ::: "memory");
#pragma unroll
            for (int q = 0; q < 8; ++q) u[q] = ldv(a + 9 * m + q);
            twid<-1>(u, tw.b);
            dft8<-1>(u);
            LAS c2* o = C + (K / 10) * 638 + K + 10 * m;
#pragma unroll
            for (int p = 0; p < 8; ++p) o[80 * p] = u[p];
        }
    }
    FST(5);
    BAR();
    FST(6);
#pragma unroll
    for (int c = 0; c < (NF > 4 ? 5 : 3); ++c) {
        const int it = tid + 512 * c, kp = it / (2 * NF), row = it % (2 * NF);
        if (NF == 4 && !LATE && row0 < 1600) for (int i = 0; i < (c ? 8 : wave); ++i) __builtin_amdgcn_s_sleep(TRK);
        if (NF > 4 || c < 2 || tid < 256) {
            const LAS c2* z = C + (row >> 1) * 648;
            const f32x4 zk = *(const LAS f32x4*)(z + 2 * kp);
            const c2 n0 = ldv(z + (kp ? 640 - 2 * kp : 320)), n1 = ldv(z + 639 - 2 * kp);
            const bool odd = it & 1;
            f32x4 o = odd ? (f32x4){zk[1] + n0.y, n0.x - zk[0], zk[3] + n1.y, n1.x - zk[2]} : (f32x4){zk[0] + n0.x, zk[1] - n0.y, zk[2] + n1.x, zk[3] - n1.y};
            if (c == 0 && kp == 0) { o[0] = 2.f * (odd ? zk[1] : zk[0]); o[1] = 2.f * (odd ? n0.y : n0.x); }
            u32x4 ob = __builtin_bit_cast(u32x4, o * FSC);
            if (LATE) { ob[1] = nosent1(ob[1]); ob[3] = nosent1(ob[3]); }
            st128(ob, rws, kp * (LATE ? GST : FST_) + row * 16u, (LATE ? O_FL - (S_ - 256) * 16u : O_F) + (unsigned)row0 * 16u, 16);
        }
    }
    FST(7);
}
template <class FG> __device__ __forceinline__ void a3_unit(__amdgpu_buffer_rsrc_t rx, __amdgpu_buffer_rsrc_t ro, int row0, int layer, __amdgpu_buffer_rsrc_t rws, __amdgpu_buffer_rsrc_t rwin, const RowTw& tw, int tid, FG go, unsigned (&fs)[12]) {
    const int wave = __builtin_amdgcn_readfirstlane(tid >> 6), lane = tid & 63;
    LAS c2* A = (LAS c2*)smem; LAS c2* C = (LAS c2*)(smem + R_C);
    const unsigned n0 = (lane & 7) | ((lane & 24) << 1);
    c2 x[10];
    if (wave < 4) {
        const unsigned base = (unsigned)(row0 + 2 * wave) * (D_ * 2), vo = n0 * 2u + (lane >> 5) * (D_ * 2u);
#pragma unroll
        for (int r = 0; r < 10; ++r) x[r] = (c2){bf2f(__builtin_amdgcn_raw_buffer_load_b16(rx, vo + 128u * r, base, 16)), bf2f(__builtin_amdgcn_raw_buffer_load_b16(rx, vo + 16u + 128u * r, base, 16))};
    }
    const bool early = row0 < 256;
    if (!early) go();
    else if (TG) {
        if (wave == 7) { const unsigned vo[3] = {lane * 128u, lane * 128u + 8192u, min(lane, 31) * 128u + 16384u}; wait_told(rwin, vo, W_COL + (unsigned)layer * W_LAYER + 4u, 1); }
        BAR0();
    }
    FST(0);
    u32x4 fv[3];
    {
        const unsigned vo = (tid >> 3) * (early ? GST : FST_) + (tid & 7) * 16u, st = (early ? GST : FST_) * 64u, so = (early ? O_FL + (unsigned)(2 - layer) * (NCOL * GST) : O_F) + (unsigned)row0 * 16u;
#pragma unroll
        for (int c = 0; c < 3; ++c) fv[c] = __builtin_amdgcn_raw_buffer_load_b128(rws, vo, c < 2 || wave < 4 ? so + c * st : 0x40000000u, 16);
        if (early) for (unsigned spins = 0;;) {
            if (!__any(max(max(smax(fv[0]), smax(fv[1])), smax(fv[2])) == ~0u) || spin_fail(spins, rwin)) break;
            __builtin_amdgcn_s_sleep(PS3);
            asm volatile("" ::: "memory");
            u32x4 x[3];
#pragma unroll
            for (int c = 0; c < 3; ++c) x[c] = __builtin_amdgcn_raw_buffer_load_b128(rws, smax(fv[c]) == ~0u ? vo : 0x40000000u, so + c * st, 16);
#pragma unroll
            for (int c = 0; c < 3; ++c) if (smax(fv[c]) == ~0u) fv[c] = x[c];
        }
    }
#pragma unroll
    for (int c = 0; c < 3; ++c) {
        const int it = tid + 512 * c, kp = it >> 3;
        if (c < 2 || tid < 256) {
            const bool odd = it & 1;
            const f32x4 v = __builtin_bit_cast(f32x4, fv[c]), p = {dppf<0xB1>(v[0]), dppf<0xB1>(v[1]), dppf<0xB1>(v[2]), dppf<0xB1>(v[3])};
            c2 z0 = odd ? (c2){p[0] + v[1], v[0] - p[1]} : (c2){v[0] - p[1], p[0] + v[1]};
            const c2 z1 = odd ? (c2){p[2] + v[3], v[2] - p[3]} : (c2){v[2] - p[3], p[2] + v[3]};
            int i0 = odd ? 640 - 2 * kp : 2 * kp;
            if (c == 0 && kp == 0) { z0 = odd ? (c2){p[1], v[1]} : (c2){v[0], p[0]}; i0 >>= 1; }
            LAS c2* z = C + ((it >> 1) & 3) * 648;
            z[i0] = z0; z[odd ? 639 - 2 * kp : 2 * kp + 1] = z1;
        }
    }
    FST(1);
    BAR();
    if (early && TH && wave == 7) tell_at(rwin, W_H + (unsigned)layer * W_LAYER + (unsigned)((row0 >> 5) * 128 + 16 + ((row0 >> 3) & 3) * 4), layer ? TH : TH0);
    if (early) RST(3, layer, wave, 0);
    FST(2);
    if (tid < 320) {
        const int K = tid >> 3, m = tid & 7;
        LAS c2* a = A + K * 72;
        const LAS c2* o = C + (K / 10) * 638 + K + 10 * m;
        c2 u[8];
#pragma unroll
        for (int p = 0; p < 8; ++p) u[p] = ldv(o + 80 * p);
        dft8<1>(u);
        twid<1>(u, tw.b);
#pragma unroll
        for (int q = 0; q < 8; ++q) a[9 * q + m] = u[q];
        asm volatile("" ::: "memory");
#pragma unroll
        for (int p = 0; p < 8; ++p) u[p] = ldv(a + 9 * m + p);
        dft8<1>(u);
        twid<1>(u, tw.a);
#pragma unroll
        for (int q = 0; q < 8; ++q) a[m + 8 * (q >> 1) + 32 * (q & 1)] = u[q];
    }
    FST(3);
    BAR();
    FST(4);
    if (wave < 4) {
        c2 u[10];
#pragma unroll
        for (int r = 0; r < 10; ++r) u[r] = ldv(A + (wave * 10 + r) * 72 + lane);
        dft10<1>(u);
        unsigned x1[10], h[10];
#pragma unroll
        for (int r = 0; r < 10; ++r) {
            swap32(u[r]);
            const c2 y = x[r] + unpk(pack2(u[r].x, u[r].y));
            x1[r] = pack2(y.x, y.y);
            u[r] = unpk(x1[r]);
        }
        FST(5);
        ln_row(u, h);
        FST(6);
        LAS char* st = (LAS char*)C + wave * 5120;
        LAS char* hp = st + (lane >> 5) * 1280 + (n0 & 48) * 2 + pi16(n0 & 15) * 2;
#pragma unroll
        for (int r = 0; r < 10; ++r) *(LAS unsigned*)(hp + 128 * r) = nosent1(h[r]);
#pragma unroll
        for (int c = 0; c < 3; ++c) {
            if (c < 2 || lane < 32) {
                const int id = c * 64 + lane, rr = id >= 80, ch = id - 80 * rr, row = (row0 + 2 * wave + rr) & 31;
                st128(*(const LAS u32x4*)(st + id * 16), rws, (unsigned)((ch >> 3) * 4096 + row * 128 + (((ch & 7) ^ (row & 7)) << 4)), O_H + (unsigned)((row0 >> 5) - NHF * layer) * 40960u, 16);
            }
        }
        if (early) RST(4, layer, wave, 0);
        LAS char* xp = st + 2560 + (lane >> 5) * 1280 + n0 * 2;
#pragma unroll
        for (int r = 0; r < 10; ++r) { *(LAS u16*)(xp + 128 * r) = (u16)x1[r]; *(LAS u16*)(xp + 128 * r + 16) = (u16)(x1[r] >> 16); }
#pragma unroll
        for (int c = 0; c < 3; ++c)
            if (c < 2 || lane < 32) st128(*(const LAS u32x4*)(st + 2560 + (c * 64 + lane) * 16), rws, (c * 64 + lane) * 16u, O_X1 + (unsigned)(row0 + 2 * wave) * (D_ * 2), 16);
        FST(7);
    } else if (X2W > layer) {

#pragma unroll
        for (int c = 0; c < 3; ++c)
            if (c < 2 || lane < 32) st128(SENT, ro, (c * 64 + lane) * 16u, (unsigned)(row0 + 2 * (wave - 4)) * (D_ * 2), 16);
    }
}

constexpr int C_T = 8 * 544 * 8, C_B = C_T + (int)sizeof(TwCol), C_W = C_B + 32768, C_END = C_W + 8 * 516 * 4;
template <class FG> __device__ __forceinline__ void a2_unit(int kp, int late, __amdgpu_buffer_rsrc_t rws, __amdgpu_buffer_rsrc_t rwin, int tid, FG go, unsigned (&fs)[12]) {
    const int w = __builtin_amdgcn_readfirstlane(tid >> 6), l = tid & 63;
    const unsigned fb = O_F + (unsigned)kp * FST_, wb = O_WFT + (unsigned)kp * WST;
    const int b = (l >> 1) & 7;
    const int k0 = w + 8 * b + 128 * (l >> 4);
    c2 u[8];
    u32x4 wv[2];
    {
        f32x4 tv[3];
#pragma unroll
        for (int i = 0; i < 3; ++i) tv[i] = ((const f32x4*)&TWC)[min(tid + 512 * i, (int)sizeof(TwCol) / 16 - 1)];
        go(0);
#pragma unroll
        for (int i = 0; i < 3; ++i) ((LAS f32x4*)(smem + C_T))[min(tid + 512 * i, (int)sizeof(TwCol) / 16 - 1)] = tv[i];
        BAR();
        FST(0);
        u32x4 x[4];
#pragma unroll
        for (int r = 0; r < 4; ++r) x[r] = __builtin_amdgcn_raw_buffer_load_b128(rws, (tid >> 1) * 16u + (tid & 1) * 4096u, fb + r * 8192u, 16);
#pragma unroll
        for (int r = 0; r < 2; ++r) wv[r] = __builtin_amdgcn_raw_buffer_load_b128(rws, tid * 16u, wb + r * 8192u, 16);
#pragma unroll
        for (int r = 0; r < 4; ++r) {
            const f32x4 y = __builtin_bit_cast(f32x4, x[r]);
            const c2 z = {dppf<0xB1>(l & 1 ? y[0] : y[2]), dppf<0xB1>(l & 1 ? y[1] : y[3])};
            u[2 * r] = l & 1 ? z : (c2){y[0], y[1]};
            u[2 * r + 1] = l & 1 ? (c2){y[2], y[3]} : z;
        }
    }
    const LAS f32x4* t1 = (const LAS f32x4*)(smem + C_T) + (tid >> 1);
    const LAS f32x4* t2 = (const LAS f32x4*)(smem + C_T + sizeof(Tw7<256>)) + (l >> 1);
    const LAS f32x4* t3 = (const LAS f32x4*)(smem + C_T + sizeof(Tw7<256>) + sizeof(Tw7<32>)) + (l >> 4);
    LAS c2* e0 = (LAS c2*)smem + tid;
    LAS c2* e1 = (LAS c2*)smem + w * 544 + l;
    LAS c2* e2 = (LAS c2*)smem + w * 544 + 2 * (l >> 4) + (l & 1);
    Tw tw;
    ldtw<256>(tw, t1);
    if (late) {
        go(2);
        FST(1);
        for (unsigned spins = 0;;) {
            asm volatile("" ::: "memory");
            const u32x2 x = __builtin_amdgcn_raw_buffer_load_b64(rws, tid * 8u, O_FL + (unsigned)kp * GST, 16);
            u[7] = __builtin_bit_cast(c2, x);
            if (!__any(x[1] == ~0u) || spin_fail(spins, rwin)) break;
            if (w < 3) __builtin_amdgcn_s_sleep(16); else __builtin_amdgcn_s_sleep(PS2);
        }
    }
    FST(2);
    dft8<-1>(u);
    twid<-1>(u, tw);
    if (kp == 0) {
#pragma unroll
        for (int i = 0; i < 4; ++i)
            asm volatile("s_mov_b32 m0, %3\n\ts_nop 3\n\tbuffer_load_dwordx4 %0, %1, %2 offen sc1 lds" :: "v"(l * 16u), "s"(rws), "s"(O_WFT + NCOL * WST + (w * 4 + i) * 1024u), "s"(C_B + (w * 4 + i) * 1024u), "v"(u[7]) : "m0", "memory");
    }
#pragma unroll
    for (int r = 0; r < 8; ++r) e0[544 * r] = u[r];
    ldtw<32>(tw, t2);
    BAR();
    if (A2P == 1 && w < 4) __builtin_amdgcn_s_setprio(2);
    if (A2P == 2 && (w & 1)) __builtin_amdgcn_s_setprio(2);
    if (A2P == 3 && w >= 4) __builtin_amdgcn_s_sleep(4);
    if (TG && w == 0) tell_at(rwin, W_COL + (unsigned)late * W_LAYER + (unsigned)kp * 128u + 4u, kp ? TG : TG + 30);
#pragma unroll
    for (int r = 0; r < 8; ++r) u[r] = ldv(e1 + 64 * r);
    dft8<-1>(u);
    twid<-1>(u, tw);
#pragma unroll
    for (int r = 0; r < 8; ++r) e1[68 * r] = u[r];
    asm volatile("" ::: "memory");
#pragma unroll
    for (int r = 0; r < 8; ++r) u[r] = ldv(e2 + 68 * b + 8 * r);
    ldtw<4>(tw, t3);
    dft8<-1>(u);
    twid<-1>(u, tw);
    c2 f[8];
#pragma unroll
    for (int r = 0; r < 8; ++r) f[r] = unpk(wv[r >> 2][r & 3]);
    asm volatile("s_nop 1" : "+v"(u[1]), "+v"(u[2]), "+v"(u[3]), "+v"(u[4]), "+v"(u[5]), "+v"(u[6]), "+v"(u[7]));
#pragma unroll
    for (int r = 0; r < 4; ++r) swapl<5>(u[r], u[r + 4]);
#pragma unroll
    for (int r = 0; r < 4; ++r) swapl<4>(u[(r & 1) + 2 * (r & 2)], u[(r & 1) + 2 * (r & 2) + 2]);
    dft4<-1>(u[0], u[2], u[4], u[6]); dft4<-1>(u[1], u[3], u[5], u[7]);
    FST(3);
    if (kp == 0) {
        LAS c2* z = (LAS c2*)(smem + C_W);
#pragma unroll
        for (int r = 0; r < 8; ++r) if (!(l & 1)) z[w * 258 + b + 16 * (l >> 4) + 8 * (r & 1) + 64 * (r >> 1)] = u[r];
        VMWAIT();
        BAR();
#pragma unroll
        for (int r = 0; r < 8; ++r) {
            const int kn = (S_ - k0 - 64 * (r & 1) - 512 * (r >> 1)) & (S_ - 1);
            const f32x4 ab = ((const LAS f32x4*)(smem + C_B))[256 * r + (tid >> 1)];
            const c2 zn = z[(kn & 7) * 258 + (kn >> 3)], g = {ab[2], ab[3]}, h = l & 1 ? f[r] : (c2){ab[0], ab[1]};
            const c2 y = cfin<1>(g, zn, g * bc(zn.x));
            u[r] = cfin<-1>(u[r], h, fm(u[r], bc(h.x), l & 1 ? bc(0.f) : y));
        }
    } else {
#pragma unroll
        for (int r = 0; r < 8; ++r) u[r] = cfin<-1>(u[r], f[r], u[r] * bc(f[r].x));
    }
    FST(4);
    dft4<1>(u[0], u[2], u[4], u[6]); dft4<1>(u[1], u[3], u[5], u[7]);
#pragma unroll
    for (int r = 0; r < 4; ++r) swapl<4>(u[(r & 1) + 2 * (r & 2)], u[(r & 1) + 2 * (r & 2) + 2]);
#pragma unroll
    for (int r = 0; r < 4; ++r) swapl<5>(u[r], u[r + 4]);
    twid<1>(u, tw);
    dft8<1>(u);
#pragma unroll
    for (int r = 0; r < 8; ++r) e2[66 * b + 8 * r] = u[r];
    asm volatile("" ::: "memory");
    ldtw<32>(tw, t2);
#pragma unroll
    for (int r = 0; r < 8; ++r) u[r] = ldv(e1 + 66 * r);
    twid<1>(u, tw);
    dft8<1>(u);
#pragma unroll
    for (int r = 0; r < 8; ++r) e1[64 * r] = u[r];
    ldtw<256>(tw, t1);
    __builtin_amdgcn_s_setprio(0);
    FST(7);
    BAR();
    FST(10);
#pragma unroll
    for (int r = 0; r < 8; ++r) u[r] = ldv(e0 + 544 * r);
    twid<1>(u, tw);
    dft8<1>(u);
    FST(5);
    __builtin_amdgcn_raw_buffer_store_b64((u32x2){__float_as_uint(u[0].x), nosent1(__float_as_uint(u[0].y))}, rws, tid * 8u, O_FL + (unsigned)((2 - late) * NCOL + kp) * GST, 16);
#pragma unroll
    for (int r = 1; r < 8; ++r) __builtin_amdgcn_raw_buffer_store_b64(__builtin_bit_cast(u32x2, u[r]), rws, tid * 8u, fb + r * 4096u, 16);
    FST(6);
}

struct Args { char* ws; char* win; const u16 *x, *wr, *wi, *mem0, *wq, *wk, *wv, *wo; u16* out; };
__device__ __forceinline__ const u16* arg(int i) {
    const __attribute__((address_space(4))) u64* p = (const __attribute__((address_space(4))) u64*)__builtin_amdgcn_kernarg_segment_ptr();
    asm volatile("" : "+s"(p));
    return (const u16*)p[i];
}
enum { A_X = 2, A_WR, A_WI, A_MEM0, A_WQ, A_WK, A_WV, A_WO, A_OUT };
#define MXV 0x5556ull
#define MXQ 0x1554ull
#define NTAIL 10
static_assert(NTAIL >= 3, "the owners release segments up to NSEG - 4");
constexpr int TAILS = NSEG - NTAIL;
static_assert((MXV & 31) == 22 && (MXQ & 31) == 20, "segment sets");
constexpr int NREC = 96 + NOWN, NQT = NREC + NOWN, NXT = NQT + 32, NWRK = NXT + 32;
#define SST(k) do {} while (0)
#define LSEL 0
#define RT(k) do {} while (0)
#define RTS() do {} while (0)
#define B3ATTR
#define MTA(k)
#define MTRT() do {} while (0)
#define MT(k) do {} while (0)
#define MTS() do {} while (0)
#define FC(x) do {} while (0)
#define DQC 1200
#define DVC 1200
#define M1 44
#define M2 224
#define PEN 280
#define DZP 46
#define LT0 342
#define SAD 1
#define PEN1 230
#define PEN0 330
#define TSLP 1
#define OPR 2
#define BSPARSE 0
#define ZLAG 4
#define TKP 0
#define DPS 12
#define DXT 3000
#define LTM 12
#define XS0 10
#define XSU 8
#define XSM 32
#define LTU 4
#define LTS 3
#define XSS 3
#define TKS 3
#define LT1 450
#define FDL 2
#define ZLA 3
#define NORND 0
#define LTX 40
#define TKE 200
#define DVO 4000
#define ZPS 2
#define HLA 8
#define QB2 2
#define NB2 6
#define DZR 24
#define ML 150
#define NB1 3
#define DPT 1
#define DDMA 8
#define DZ2 800
#define QEARLY 2
#define OSLP 32
#define OSLP2 1
#define WSLP 64
#define DO0 4000
#define DW0 1200
#define DQT 2000
#define ZEARLY 6
#define HSLP 4
#define ASLP 4
#define CSLP 4
#define A1S 0
#define OSLP3 12
#define DVT 860
#define R1F 1
#define WARM 1
constexpr unsigned CODE_BYTES = 96000;
#define DTK 4
#define OSL4 10
#define OAUX 0
#define TSL0 6
#define SE 6
#define XE 20
#define PENS 20
#define CF 190
#define HSL2 16
#define DH1 10
#define DZ0 400
#define FZP 1
__device__ __forceinline__ unsigned fz_at(unsigned h, unsigned g) { return FZP ? g * 1024u + (h >> 2) * 128u + (h & 3u) * 4u : h * 128u + g * 4u; }
#define LKE 0
#define DW4 6
#define DBIG 580
#define PENH 280
#define ROT 0
#define VREV 0
#define DGV 0
#define VOFF0 "0"
#define VOFF1 "1024"
#define VOFF2 "2048"
#define VOFF3 "3072"
#define VOFF4 "4096"
#define VOFF5 "5120"
#define VOFF6 "6144"
#define VOFF7 "7168"
#define VOFF8 "8192"
#define VOFF9 "9216"
#define STK 1
#define STX 0
#define UFQ 14
#define DQ0 36
#define QSL 6
#define OST0 40
#define NPR 3
#define DQA 1300
#define DQA0 1400
#define TLBW 0
#define JFW 12
#define M2D 3
#define LA1 0
#define DIE 0
#define ALN ".p2align 6\n\t"
#define SB() __builtin_amdgcn_sched_barrier(0)
#define PIN1(a) asm volatile("" : "+v"(a) :: "memory")
#define PIN2(a, b) asm volatile("" : "+v"(a), "+v"(b) :: "memory")
__device__ __forceinline__ unsigned fresh_lane() { unsigned l; asm volatile("v_mbcnt_lo_u32_b32 %0, -1, 0\n\tv_mbcnt_hi_u32_b32 %0, -1, %0" : "=v"(l)); return l; }
#define LINE8 (fresh_lane() < 16u ? fresh_lane() * 8u : 0x40000000u)
#define LANE0 (min(fresh_lane(), 1u) << 30)
__device__ __forceinline__ void wait_until(unsigned long long t) { while ((long long)(__builtin_amdgcn_s_memtime() - t) < 0) __builtin_amdgcn_s_sleep(1); }
struct Clk { unsigned ta, ma, cpq, qpc; };
__device__ __forceinline__ int sx24(unsigned v) { return (int)(v << 8) >> 8; }
#define WAIT_FINE ALN \
        "s_memrealtime vcc\n\t" \
        "s_memtime s[100:101]\n\t" \
        "s_brev_b32 %[fi], -2\n\t" \
        "s_waitcnt lgkmcnt(0)\n\t" \
        "s_mov_b32 %[rp], vcc_lo\n\t" \
        "s_lshl_b32 vcc_hi, vcc_lo, 2\n\t" \
        "s_sub_u32 %[x], %[t], vcc_hi\n\t" \
        "s_bfe_i32 %[x], %[x], 0x180000\n\t" \
        "s_min_i32 %[x], %[x], 4000\n\t" \
        "s_max_i32 %[x], %[x], -4000\n\t" \
        "s_add_u32 %[t], vcc_hi, %[x]\n\t" \
        "s_mul_i32 %[x], %[x], %[cq]\n\t" \
        "s_ashr_i32 %[x], %[x], 8\n\t" \
        "s_sub_u32 %[tm], %[t], %[ta]\n\t" \
        "s_bfe_i32 %[tm], %[tm], 0x180000\n\t" \
        "s_min_i32 %[tm], %[tm], 1000000\n\t" \
        "s_max_i32 %[tm], %[tm], -1000000\n\t" \
        "s_mul_i32 %[tm], %[tm], %[cq]\n\t" \
        "s_ashr_i32 %[tm], %[tm], 8\n\t" \
        "s_add_u32 %[tm], %[tm], %[ma]\n\t" \
        "s_sub_u32 %[tm], %[tm], s100\n\t" \
        "s_lshr_b32 vcc_hi, %[cq], 4\n\t" \
        "s_sub_u32 vcc_hi, %[x], vcc_hi\n\t" \
        "s_max_i32 %[tm], %[tm], vcc_hi\n\t" \
        "s_min_i32 %[tm], %[tm], %[x]\n\t" \
        "s_add_u32 %[tm], %[tm], s100\n" \
        "1:\n\t" \
        "s_memrealtime vcc\n\t" \
        "s_memtime s[100:101]\n\t" \
        "s_waitcnt lgkmcnt(0)\n\t" \
        "s_cmp_eq_u32 vcc_lo, %[rp]\n\t" \
        "s_cbranch_scc1 2f\n\t" \
        "s_mov_b32 %[rp], vcc_lo\n\t" \
        "s_lshl_b32 %[x], vcc_lo, 2\n\t" \
        "s_sub_u32 %[x], %[t], %[x]\n\t" \
        "s_bfe_i32 %[x], %[x], 0x180000\n\t" \
        "s_mul_i32 %[x], %[x], %[cq]\n\t" \
        "s_ashr_i32 %[x], %[x], 8\n\t" \
        "s_add_u32 %[x], %[x], s100\n\t" \
        "s_sub_u32 s101, %[x], %[tm]\n\t" \
        "s_cmp_lt_i32 s101, %[fi]\n\t" \
        "s_cselect_b32 %[tm], %[x], %[tm]\n\t" \
        "s_mov_b32 %[fi], 0\n" \
        "2:\n\t" \
        "s_sub_u32 %[x], %[tm], s100\n\t" \
        "s_cmp_gt_i32 %[x], 260\n\t" \
        "s_cbranch_scc1 1b\n\t" \
        "s_cmp_eq_u32 %[fi], 0\n\t" \
        "s_cselect_b32 %[ta], %[t], %[ta]\n\t" \
        "s_cselect_b32 %[ma], %[tm], %[ma]\n" \
        "3:\n\t" \
        "s_memtime s[100:101]\n\t" \
        "s_waitcnt lgkmcnt(0)\n\t" \
        "s_sub_u32 %[x], %[tm], s100\n\t" \
        "s_cmp_gt_i32 %[x], 92\n\t" \
        "s_cbranch_scc1 3b\n\t" \
        "s_max_i32 %[x], %[x], 0\n\t" \
        "s_andn2_b32 %[x], %[x], 3\n\t" \
        "s_sub_u32 %[x], 104, %[x]\n\t" \
        "s_getpc_b64 vcc\n\t" \
        "s_add_u32 vcc_lo, vcc_lo, %[x]\n\t" \
        "s_addc_u32 vcc_hi, vcc_hi, 0\n\t" \
        "s_setpc_b64 vcc\n\t" \
        ".rept 23\n\t" \
        "s_nop 0\n\t" \
        ".endr\n\t"
#define WAIT_FINE_OUT [tm] "=&s"(tm), [rp] "=&s"(rp), [x] "=&s"(x), [fi] "=&s"(fi), [t] "+s"(t), [ta] "+s"(c.ta), [ma] "+s"(c.ma)
__device__ __forceinline__ void wait_fine(unsigned t, Clk& c) {
    unsigned tm, rp, x, fi;
    asm volatile(WAIT_FINE : WAIT_FINE_OUT : [cq] "s"(c.cpq) : "vcc", "scc", "s100", "s101", "memory");
}
__device__ __forceinline__ void wait_fine_bar(unsigned t, Clk& c) {
    unsigned tm, rp, x, fi;
    asm volatile(WAIT_FINE "s_barrier" : WAIT_FINE_OUT : [cq] "s"(c.cpq) : "vcc", "scc", "s100", "s101", "memory");
}
__device__ __forceinline__ void wait_cyc_bar(unsigned tm) {
    unsigned x;
    asm volatile(
        ALN
        "3:\n\t"
        "s_memtime s[100:101]\n\t"
        "s_waitcnt lgkmcnt(0)\n\t"
        "s_sub_u32 %0, %1, s100\n\t"
        "s_cmp_gt_i32 %0, 92\n\t"
        "s_cbranch_scc1 3b\n\t"
        "s_max_i32 %0, %0, 0\n\t"
        "s_andn2_b32 %0, %0, 3\n\t"
        "s_sub_u32 %0, 104, %0\n\t"
        "s_getpc_b64 vcc\n\t"
        "s_add_u32 vcc_lo, vcc_lo, %0\n\t"
        "s_addc_u32 vcc_hi, vcc_hi, 0\n\t"
        "s_setpc_b64 vcc\n\t"
        ".rept 23\n\t"
        "s_nop 0\n\t"
        ".endr\n\t"
        "s_barrier"
        : "=&s"(x) : "s"(tm) : "vcc", "scc", "s100", "s101", "memory");
}
__device__ __forceinline__ unsigned cyc_at(const Clk& c, int d) {
    d = min(d, 20000);
    asm volatile("" : "+s"(d));
    return c.ma + (unsigned)((max(d, -4000) * (int)c.cpq) >> 8);
}
__device__ __forceinline__ void clk_rate(Clk& c, unsigned ta0, unsigned ma0) {
    const int dq = sx24(c.ta - ta0);
    if (dq > 200) {
        const float r = fminf(fmaxf((float)(c.ma - ma0) * __builtin_amdgcn_rcpf((float)dq), 1.f), 8.f);
        asm volatile("s_nop 1\n\tv_readfirstlane_b32 %0, %2\n\tv_readfirstlane_b32 %1, %3\n\ts_nop 3" : "=s"(c.cpq), "=s"(c.qpc)
                     : "v"((unsigned)(256.f * r + 0.5f)), "v"((unsigned)(65536.f * __builtin_amdgcn_rcpf(r))));
    }
}
__device__ __forceinline__ void clk_init(Clk& c) {
    c.ta = 4u * (unsigned)__builtin_amdgcn_s_memrealtime(); c.ma = (unsigned)__builtin_amdgcn_s_memtime();
    for (int i = 0; i < 2; ++i) {
        const unsigned ta0 = c.ta, ma0 = c.ma;
        wait_fine(c.ta + (i ? 360u : 60u), c);
        clk_rate(c, ta0, ma0);
    }
}
__device__ __forceinline__ unsigned sched_lane(int s) { const unsigned l = fresh_lane(); return (l & 31u) * 128u + (l >> 5) * 4u; }
__device__ __forceinline__ int read_sched(__amdgpu_buffer_rsrc_t rwin, int s, unsigned vo, unsigned to, unsigned f, unsigned g0, int& lt, int& xs, unsigned& fc) {
    int d = (int)((f - g0) & 0xFFFFFFu);
    if (__builtin_expect(__any((f >> 24) != (unsigned)s) || s == 0, 0)) {
        for (unsigned spins = 0; __any((f >> 24) != (unsigned)s);) {
            FC(fc);
            if (spin_fail(spins, rwin)) break;
            if (s == 0) __builtin_amdgcn_s_sleep(TSL0); else __builtin_amdgcn_s_sleep(TSLP);
            asm volatile("" ::: "memory");
            if ((f >> 24) != (unsigned)s) f = __builtin_amdgcn_raw_buffer_load_b32(rwin, vo, to, 16);
        }
        const unsigned f0 = __builtin_amdgcn_readfirstlane(f) - 0x100000u;
        d = (int)((f - f0) & 0xFFFFFFu) + (int)((f0 - g0) & 0xFFFFFFu);
    }
    d = max(d, __builtin_amdgcn_update_dpp(0, d, 0xB1, 0xF, 0xF, true));
    d = max(d, __builtin_amdgcn_update_dpp(0, d, 0x4E, 0xF, 0xF, true));
    d = max(d, __builtin_amdgcn_update_dpp(0, d, 0x141, 0xF, 0xF, true));
    d = max(d, __builtin_amdgcn_update_dpp(0, d, 0x140, 0xF, 0xF, true));
    d = max(d, __builtin_amdgcn_update_dpp(0, d, 0x142, 0xA, 0xF, true));
    d = max(__builtin_amdgcn_readlane(d, 16), __builtin_amdgcn_readlane(d, 48));
    const int l0 = lt, x0 = xs;
    d -= 0x400000;
    const bool miss = d > l0 + min(x0 + LTM, LTX);
    if (s >= SAD) { lt = miss ? l0 : min(max(d, l0 - 1 - ((l0 - d) >> LTS)), l0 + LTU); xs = miss ? min(x0 + XSU, XSM) : max(x0 - 1 - (x0 >> XSS), 0); }
    if (s == 1) lt = LT0;
    if (s < SE) xs = max(xs, XE);
    return 0x400000 + (s < SAD ? d + (s ? PENS : PEN0) : miss ? d + (d < DBIG ? PENH : PEN) : l0 + x0) + M1 + M2 + x0;
}
constexpr int QS = 272;
constexpr int TQS = 1152;
constexpr int L_Q = 10240, L_K = L_Q + 2 * TQS, L_S = L_K + 2 * TQS, L_TOK = L_S + 256, L_H = L_TOK + 49152, L_END = L_H + 2 * 40960;
static_assert(C_END <= L_END && L_END == 146176, "LDS");
typedef __attribute__((ext_vector_type(4))) short v4i16_t;
__device__ __forceinline__ u32x2 ldtr(const char* p) { return __builtin_bit_cast(u32x2, __builtin_amdgcn_ds_read_tr16_b64_v4i16((LAS v4i16_t*)p)); }
__device__ __forceinline__ u32x2 ldtr1(const LAS char* p) { return __builtin_bit_cast(u32x2, __builtin_amdgcn_ds_read_tr16_b64_v4i16((LAS v4i16_t*)p)); }
__device__ __forceinline__ u32x4 ldtr2(const LAS char* p0, const LAS char* p1) {
    const u32x2 x = __builtin_bit_cast(u32x2, __builtin_amdgcn_ds_read_tr16_b64_v4i16((LAS v4i16_t*)p0)), y = __builtin_bit_cast(u32x2, __builtin_amdgcn_ds_read_tr16_b64_v4i16((LAS v4i16_t*)p1));
    return (u32x4){x[0], x[1], y[0], y[1]};
}
__device__ __forceinline__ u32x4 ldtr2(const char* p0, const char* p1) { const u32x2 x = ldtr(p0), y = ldtr(p1); return (u32x4){x[0], x[1], y[0], y[1]}; }

__device__ __forceinline__ unsigned poll_lim(int& budget) {
    --budget;
    unsigned lim = budget > 0 ? 0xFFFFFFFEu : 0xFFFFFFFFu;
    asm volatile("" : "+s"(lim));
    return lim;
}
#define GATHER \
        "buffer_load_dwordx4 %[vo], %[rw], %[s0] offen sc1 lds\n\t" \
        "buffer_load_dwordx4 %[vo], %[rw], %[s0] offen offset:1024 sc1 lds\n\t" \
        "buffer_load_dwordx4 %[vo], %[rw], %[s0] offen offset:2048 sc1 lds\n\t" \
        "buffer_load_dwordx4 %[vo], %[rw], %[s0] offen offset:3072 sc1 lds\n\t" \
        "s_movk_i32 m0, 0x1000\n\t" \
        "s_nop 0\n\t" \
        "buffer_load_dwordx4 %[vo], %[rw], %[s4] offen sc1 lds\n\t" \
        "buffer_load_dwordx4 %[vo], %[rw], %[s4] offen offset:1024 sc1 lds\n\t" \
        "buffer_load_dwordx4 %[vo], %[rw], %[s4] offen offset:2048 sc1 lds\n\t" \
        "buffer_load_dwordx4 %[vo], %[rw], %[s4] offen offset:3072 sc1 lds\n\t" \
        "s_movk_i32 m0, 0x2000\n\t" \
        "s_nop 0\n\t" \
        "buffer_load_dwordx4 %[vo], %[rw], %[s1] offen sc1 lds\n\t" \
        "buffer_load_dwordx4 %[vo], %[rw], %[s1] offen offset:1024 sc1 lds"
#define GATHER_R \
        "buffer_load_dwordx4 %[vo], %[rw], %[s1] offen offset:1024 sc1 lds\n\t" \
        "buffer_load_dwordx4 %[vo], %[rw], %[s1] offen sc1 lds\n\t" \
        "s_movk_i32 m0, 0x1000\n\t" \
        "s_nop 0\n\t" \
        "buffer_load_dwordx4 %[vo], %[rw], %[s4] offen offset:3072 sc1 lds\n\t" \
        "buffer_load_dwordx4 %[vo], %[rw], %[s4] offen offset:2048 sc1 lds\n\t" \
        "buffer_load_dwordx4 %[vo], %[rw], %[s4] offen offset:1024 sc1 lds\n\t" \
        "buffer_load_dwordx4 %[vo], %[rw], %[s4] offen sc1 lds\n\t" \
        "s_movk_i32 m0, 0\n\t" \
        "s_nop 0\n\t" \
        "buffer_load_dwordx4 %[vo], %[rw], %[s0] offen offset:3072 sc1 lds\n\t" \
        "buffer_load_dwordx4 %[vo], %[rw], %[s0] offen offset:2048 sc1 lds\n\t" \
        "buffer_load_dwordx4 %[vo], %[rw], %[s0] offen offset:1024 sc1 lds\n\t" \
        "buffer_load_dwordx4 %[vo], %[rw], %[s0] offen sc1 lds"
#define GATHER_IN [vo] "v"(vo), [rw] "s"(rwin), [s0] "s"(so0), [s4] "s"(so0 + 2u * PAIR), [s1] "s"(so0 + 4u * PAIR)
__device__ __forceinline__ void gather_dma(__amdgpu_buffer_rsrc_t rwin, unsigned vo, unsigned so0, bool rev) {
    if (rev) asm volatile("s_movk_i32 m0, 0x2000\n\ts_nop 0\n\t" GATHER_R :: GATHER_IN : "m0", "memory");
    else asm volatile("s_mov_b32 m0, 0\n\ts_nop 0\n\t" GATHER :: GATHER_IN : "m0", "memory");
}
__device__ __forceinline__ void gather_at(unsigned t, Clk& c, __amdgpu_buffer_rsrc_t rwin, unsigned vo, unsigned so0, bool rev) {
    unsigned tm, rp, x, fi;
    if (rev) asm volatile("s_movk_i32 m0, 0x2000\n\t" WAIT_FINE GATHER_R : WAIT_FINE_OUT : [cq] "s"(c.cpq), GATHER_IN : "vcc", "scc", "s100", "s101", "m0", "memory");
    else asm volatile("s_mov_b32 m0, 0\n\t" WAIT_FINE GATHER : WAIT_FINE_OUT : [cq] "s"(c.cpq), GATHER_IN : "vcc", "scc", "s100", "s101", "m0", "memory");
}
__device__ __forceinline__ void dma4k(__amdgpu_buffer_rsrc_t r, unsigned vo, unsigned so, unsigned lds) {
    asm volatile(
        "s_mov_b32 m0, %3\n\t"
        "s_nop 0\n\t"
        "buffer_load_dwordx4 %0, %1, %2 offen sc1 lds\n\t"
        "buffer_load_dwordx4 %0, %1, %2 offen offset:1024 sc1 lds\n\t"
        "buffer_load_dwordx4 %0, %1, %2 offen offset:2048 sc1 lds\n\t"
        "buffer_load_dwordx4 %0, %1, %2 offen offset:3072 sc1 lds"
        :: "v"(vo), "s"(r), "s"(so), "s"(lds) : "m0", "memory");
}
__device__ __forceinline__ int bs2(int c) { return (c & ~3) | ((c & 1) << 1) | ((c >> 1) & 1); }
__device__ __forceinline__ void gather_mem0(char* memL, int tid, int nt = 512) {
    const u16* mem0 = arg(A_MEM0);
    for (int c = tid; c < 640; c += nt) {
        const u32x2* src = (const u32x2*)(mem0 + ((c & 6) + ((c >> 3) & 1)) * D_ + (c >> 4) * 16 + (c & 1) * 4);
        const u32x2 x = src[0], y = src[2];
        *(u32x4*)(memL + c * 16) = (u32x4){(x[0] & 0xFFFFu) | (y[0] << 16), (x[0] >> 16) | (y[0] & 0xFFFF0000u), (x[1] & 0xFFFFu) | (y[1] << 16), (x[1] >> 16) | (y[1] & 0xFFFF0000u)};
    }
}
__device__ __forceinline__ f32x2 hadd(f32x4 c) { return (f32x2){c[0], c[1]} + (f32x2){c[2], c[3]}; }
__device__ __forceinline__ unsigned hpack(f32x4 c) { const f32x2 y = hadd(c); return pack2(y[0], y[1]); }
__device__ __forceinline__ u32x8 cat8(u32x4 lo, u32x4 hi) { return (u32x8){lo[0], lo[1], lo[2], lo[3], hi[0], hi[1], hi[2], hi[3]}; }
__device__ __forceinline__ u32x4 half8(const u32x8& v, int u) { return u ? (u32x4){v[4], v[5], v[6], v[7]} : (u32x4){v[0], v[1], v[2], v[3]}; }
__device__ __forceinline__ void dma1k(__amdgpu_buffer_rsrc_t r, unsigned vo, unsigned so, unsigned lds) {
    asm volatile("s_mov_b32 m0, %3\n\ts_nop 0\n\tbuffer_load_dwordx4 %0, %1, %2 offen sc1 lds" :: "v"(vo), "s"(r), "s"(so), "s"(lds) : "m0", "memory");
}
template <class FM> __device__ __forceinline__ void load_w(u32x8 (&wt)[2][10], const u16* src, unsigned rs, bool perm, unsigned b, unsigned c, int w, FM mid) {
    const __amdgpu_buffer_rsrc_t r = mkrsrc(src, 0x7FFFFFFFu);
    const unsigned l = fresh_lane(), q = l >> 4, lds = (unsigned)(w >> 2) * 65536u, v = w & 3;
    const unsigned vo0 = (perm ? ((q & 1u) << 3) | ((q & 2u) << 1) : q) * rs + ((l & 15u) ^ (q << 1)) * 16u, vo1 = vo0 ^ 128u, a = perm ? rs : 4u * rs;
    const LAS char* p0 = (const LAS char*)(size_t)(lds + q * 2048u + ((l >> 2) & 3u) * 256u + ((2u * v) ^ ((l >> 2) & 7u)) * 32u + (l & 3u) * 8u);
    const LAS char* p1 = (const LAS char*)((size_t)p0 ^ 32u);
    auto issue = [&](int g) {
#pragma unroll
        for (int i = 0; i < 4; ++i)
            asm volatile("s_mov_b32 m0, %3\n\ts_nop 3\n\tbuffer_load_dwordx4 %0, %1, %2 offen lds" :: "v"(i < 2 ? vo0 : vo1), "s"(r), "s"(i * a + (g & 1) * b + (g >> 1) * c), "s"(lds + (g & 3) * 16384u + v * 4096u + i * 1024u) : "m0", "memory");
    };
#pragma unroll
    for (int g = 0; g < 3; ++g) issue(g);
#pragma unroll
    for (int g = 0; g < 10; ++g) {
        asm volatile("s_waitcnt vmcnt(%0)\n\ts_barrier" :: "n"(g < 8 ? 8 : 4 * (9 - g)) : "memory");
        if (g + 3 < 10) issue(g + 3);
        const int o = (g & 3) * 16384;
        wt[0][g] = cat8(ldtr2(p0 + o, p0 + o + 1024), ldtr2(p0 + o + 8192, p0 + o + 9216));
        wt[1][g] = cat8(ldtr2(p1 + o, p1 + o + 1024), ldtr2(p1 + o + 8192, p1 + o + 9216));
        asm volatile("s_waitcnt lgkmcnt(0)" : "+v"(wt[0][g]), "+v"(wt[1][g]) :: "memory");
        if (g == 6) mid();
    }
    BAR0();
}
__device__ __forceinline__ int tile_of(int o) { return 8 * ((o % 10) >> 1) + 2 * (o / 10) + (o & 1); }
__device__ __forceinline__ void wait_tags(__amdgpu_buffer_rsrc_t rwin, int g0, int g1, unsigned n1, u64 val, bool boot) {
    const unsigned l = fresh_lane();
    unsigned o[4];
#pragma unroll
    for (int g = 0; g < 4; ++g) o[g] = g >= g0 && g < g1 && (g + 1 < g1 || l < n1) ? l * 128u + g * 8192u : 0x40000000u;
    for (unsigned spins = 0;;) {
        asm volatile("" ::: "memory");
        u32x2 v[4];
#pragma unroll
        for (int g = 0; g < 4; ++g) v[g] = __builtin_amdgcn_raw_buffer_load_b64(rwin, o[g], W_A1, 16);
#pragma unroll
        for (int g = 0; g < 4; ++g) if (__builtin_bit_cast(u64, v[g]) == val) o[g] = 0x40000000u;
        if (!__any((o[0] & o[1] & o[2] & o[3]) != 0x40000000u)) break;
        if (boot ? ++spins > SPIN_CAP : spin_fail(spins, rwin)) break;
        __builtin_amdgcn_s_sleep(ASLP);
    }
}
template <int N> __device__ __forceinline__ void stage_h(__amdgpu_buffer_rsrc_t rws, unsigned src, unsigned lds, unsigned st) {
    const unsigned l = fresh_lane() * 16u;
#pragma unroll
    for (int i = 0; i < N; ++i) dma1k(rws, l, src + i * st, lds + i * st);
}
template <int N> __device__ __forceinline__ void check_h(__amdgpu_buffer_rsrc_t rws, __amdgpu_buffer_rsrc_t rwin, unsigned src, unsigned lds, unsigned st) {
    const LAS char* p = (const LAS char*)smem + lds + fresh_lane() * 16u;
    for (unsigned spins = 0;;) {
        unsigned m = 0;
#pragma unroll
        for (int i = 0; i < N; ++i) { const u32x4 x = *(const volatile LAS u32x4*)(p + i * st); m = max(max(m, max(x[0], x[1])), max(x[2], x[3])); }
        if (__builtin_expect(!__any(m == ~0u), 1) || spin_fail(spins, rwin)) break;
        stage_h<N>(rws, src, lds, st);
        VMWAIT();
    }
}
__device__ __forceinline__ void wait_h(__amdgpu_buffer_rsrc_t rwin, unsigned fo, int seg, int& hrdy) {
    unsigned lim = 63u;
    if (__builtin_expect(hrdy <= seg, 0)) for (unsigned spins = 0;;) {
        asm volatile("" ::: "memory");
        const u32x4 f = __builtin_amdgcn_raw_buffer_load_b128(rwin, min(fresh_lane(), lim) * 128u, W_H + fo, 16);
        const unsigned long long miss = __ballot((f[0] | f[1] | f[2] | f[3]) != 0u);
        hrdy = miss ? __builtin_ctzll(miss) : (int)lim + 1;
        if (hrdy > seg) break;
        if (spin_fail(spins, rwin)) break;
        lim = (unsigned)min(seg + HLA, 63);
        if (seg < 8) __builtin_amdgcn_s_sleep(HSLP); else __builtin_amdgcn_s_sleep(HSL2);
    }
}
template <int CTRL> __device__ __forceinline__ unsigned dppu(unsigned v) { return (unsigned)__builtin_amdgcn_mov_dpp((int)v, CTRL, 0xF, 0xF, true); }
template <int CTRL> __device__ __forceinline__ u32x4 ror4(u32x4 v) { return (u32x4){dppu<CTRL>(v[0]), dppu<CTRL>(v[1]), dppu<CTRL>(v[2]), dppu<CTRL>(v[3])}; }
template <int N> struct IC { static constexpr int v = N; };
__device__ __forceinline__ float xmax16(float v) {
    auto q = __builtin_amdgcn_permlane16_swap(__float_as_uint(v), __float_as_uint(v), false, false);
    return fmx(__uint_as_float(q[0]), __uint_as_float(q[1]));
}
__device__ __forceinline__ float xsum16(float v) {
    auto q = __builtin_amdgcn_permlane16_swap(__float_as_uint(v), __float_as_uint(v), false, false);
    return __uint_as_float(q[0]) + __uint_as_float(q[1]);
}
__device__ __forceinline__ float addswap16(float a, float b) {
    auto r = __builtin_amdgcn_permlane16_swap(__float_as_uint(a), __float_as_uint(b), false, false);
    return __uint_as_float(r[0]) + __uint_as_float(r[1]);
}
#define LANES() \
    const int lane = (int)fresh_lane(), tid = w * 64 + lane, fr = lane & 15, fq = lane >> 4; \
    const int sidx = (lane & 2) ? 0xEEEE : 0x4444; \
    const int ma0 = fq * 256 + (fr & 1) * 128 + (fr >> 1) * 16; \
    const int prow = 16 * ((fr >> 2) & 1) + 4 * (fr >> 3) + (fr & 3), hoff = fr * 128; \
    (void)tid; (void)sidx; (void)ma0; (void)prow; (void)hoff

__device__ __forceinline__ int seg_after(unsigned long long m, int j) { m = j >= 63 ? 0ull : m >> (j + 1); return m ? j + 1 + __builtin_ctzll(m) : NSEG; }
__device__ __forceinline__ int seg_done(unsigned long long m, int f0, int f1, unsigned long long x = 0) { return min(seg_after(~m, f0), seg_after(m | x, f1)) - 1; }
constexpr unsigned long long MXA = 1ull << 40;
template <int OFF> __device__ __forceinline__ void ldfrag(u32x4& d, const LAS char* p) { asm volatile("ds_read_b128 %0, %1 offset:%2" : "=v"(d) : "v"(p), "n"(OFF) : "memory"); }
template <int U, class FM, class FV> __device__ __forceinline__ void project(const u32x8 (&wt)[2][10], f32x4 (&tc)[2][2], u32x4 (&bb)[8], const LAS char* c0, const LAS char* c1, const LAS char* n0, const LAS char* n1, FM mid, FV vm) {
    constexpr int t = U >> 2, k = U & 3, v = (U + 8) % 40;
    if (U == 32) mid();
    vm(IC<U>());
    const LAS char* p = U < 32 ? (k >> 1 ? c1 : c0) : (k >> 1 ? n1 : n0);
    if (U < 2) asm volatile("s_nop 0\n\ts_waitcnt lgkmcnt(7)\n\tv_mfma_f32_16x16x32_bf16 %0, %3, %2, 0\n\tv_mfma_f32_16x16x32_bf16 %1, %4, %2, 0\n\tds_read_b128 %2, %5 offset:%6"
                            : "=&v"(tc[0][k & 1]), "=&v"(tc[1][k & 1]), "+v"(bb[U & 7]) : "v"(half8(wt[0][t], k >> 1)), "v"(half8(wt[1][t], k >> 1)), "v"(p), "n"((v >> 2) * 4096 + (k & 1) * 2048) : "memory");
    else asm volatile(".rept %7\n\ts_nop 0\n\t.endr\n\ts_waitcnt lgkmcnt(7)\n\tv_mfma_f32_16x16x32_bf16 %0, %3, %2, %0\n\tv_mfma_f32_16x16x32_bf16 %1, %4, %2, %1\n\tds_read_b128 %2, %5 offset:%6"
                      : "+v"(tc[0][k & 1]), "+v"(tc[1][k & 1]), "+v"(bb[U & 7]) : "v"(half8(wt[0][t], k >> 1)), "v"(half8(wt[1][t], k >> 1)), "v"(p), "n"((v >> 2) * 4096 + (k & 1) * 2048), "n"(U < 8 ? 1 : 0) : "memory");
    if constexpr (U < 39) project<U + 1>(wt, tc, bb, c0, c1, n0, n1, mid, vm);
    else asm volatile("s_nop 7" : "+v"(tc[0][0]), "+v"(tc[0][1]), "+v"(tc[1][0]), "+v"(tc[1][1]));
}
__device__ __forceinline__ void project_end(u32x4 (&bb)[8]) {
    asm volatile("s_waitcnt lgkmcnt(0)" : "+v"(bb[0]), "+v"(bb[1]), "+v"(bb[2]), "+v"(bb[3]), "+v"(bb[4]), "+v"(bb[5]), "+v"(bb[6]), "+v"(bb[7]) :: "memory");
}
__device__ __forceinline__ void project0(u32x4 (&bb)[8], const LAS char* c0, const LAS char* c1) {
    ldfrag<0>(bb[0], c0); ldfrag<2048>(bb[1], c0); ldfrag<0>(bb[2], c1); ldfrag<2048>(bb[3], c1);
    ldfrag<4096>(bb[4], c0); ldfrag<6144>(bb[5], c0); ldfrag<4096>(bb[6], c1); ldfrag<6144>(bb[7], c1);
    project_end(bb);
}

__global__ __launch_bounds__(512, 2) B3ATTR void mega_kernel(Args a) {
    char* memL = smem;
    char* tokL = smem + L_TOK;
    char* hL = smem + L_H;
    int w = __builtin_amdgcn_readfirstlane(threadIdx.x >> 6);
    __amdgpu_buffer_rsrc_t rwin = mkrsrc(a.win, W_END);
    char* ws = a.ws;
    __amdgpu_buffer_rsrc_t rws = mkrsrc(ws, (unsigned)OFF_WIN);
    auto fresh_rsrc = [&]() INL { ws = (char*)arg(0); rws = mkrsrc(ws, (unsigned)OFF_WIN); rwin = mkrsrc(arg(1), W_END); };
    const char* pc;
    asm volatile("s_getpc_b64 %0" : "=s"(pc));
    unsigned fs[12] = {};
    const u32x2 tg0 = __builtin_amdgcn_raw_buffer_load_b64(rws, 0u, 0u, 16);
    u64 tag;
    u32x2 rot0;
    {
        const int tid = threadIdx.x;
        if (blockIdx.x < 64) {
            const unsigned j = blockIdx.x;
            if (j == 0 && w == 0) __builtin_amdgcn_raw_buffer_store_b64(__builtin_bit_cast(u32x2, (__builtin_bit_cast(u64, tg0) & ~15ull) + 16ull + (xcc_id() & 7u)), rws, LINE8, 128u, 16);
            const unsigned g0 = (j * 512u + tid) * 16u;
#pragma unroll
            for (int i = 0; i < 3; ++i) st128(SENT, mkrsrc(ws + O_ARM1, O_H + NHF * 40960u - O_ARM1), g0 + i * 524288u, 0, 16);
#pragma unroll
            for (int i = 0; i < 7; ++i) {
                const unsigned g = g0 + i * 524288u;
                st128(SENT, mkrsrc(a.win, W_END + 2 * PAIR), g + (g < W_ARM - W_LAYER ? 0u : W_ZM - (W_ARM - W_LAYER)), 0, 16);
            }
#pragma unroll
            for (int i = 0; i < 3; ++i) st128(SENT, mkrsrc(ws + O_Z, ZTSEG * 2), g0 + i * 524288u, 0, 16);
            static_assert(W_ARM - W_LAYER + W_END + 2 * PAIR - W_ZM <= 7u * 524288u && O_ARM1 + 3u * 524288u >= O_H + NHF * 40960u && ZTSEG * 2 <= 3u * 524288u, "arming");
        } else if (blockIdx.x >= 128) {
            RowTw tw;
            row_tw(tw, tid);
            a1_unit<8, false>(mkrsrc(a.x, S_ * D_ * 2), (blockIdx.x - 128) * 16, rws, rwin, tw, tid, fs);
        } else {
            const unsigned j = blockIdx.x - 64;
            FST(0);
            auto row = [&](unsigned i) { return (j & 7u) + 8u * (i & 15u) + 128u * ((j >> 3) & 3u) + 512u * (((j >> 4) & 2u) + ((i >> 4) & 1u)); };
            u32x4 v[6];
#pragma unroll
            for (int i = 0; i < 6; ++i) {
                const unsigned id = min(tid + 512 * i, 64 * 41 - 1), r = id / 41u;
                v[i] = ((const u32x4*)(r < 32u ? a.wr : a.wi))[row(r) * (DF * 2) / 16u + id - r * 41u];
            }
            u16 n[4];
            if (w == 7) {
                const size_t kn = (size_t)((S_ - row(tid)) & (S_ - 1)) * DF;
                n[0] = a.wr[kn]; n[1] = a.wi[kn]; n[2] = a.wr[kn + 320]; n[3] = a.wi[kn + 320];
            }
            const unsigned wo = WARM ? (w * 8u + (j >> 3)) * 8192u : 0x40000000u;
            const unsigned x0 = __builtin_amdgcn_raw_buffer_load_b32(mkrsrc(pc, CODE_BYTES), (tid & 63) * 128u, wo, 0), x1 = __builtin_amdgcn_raw_buffer_load_b32(mkrsrc(&TWC, sizeof(TwCol)), (tid & 63) * 128u, wo, 0);
#pragma unroll
            for (int i = 0; i < 6; ++i) ((u32x4*)smem)[min(tid + 512 * i, 64 * 41 - 1)] = v[i];
            FST(1);
            BAR();
            FST(2);
            const u16* q0 = (const u16*)smem + (j & 7u);
            u32x4 o[5];
#pragma unroll
            for (int i = 0; i < 5; ++i) {
                const u16* q = q0 + ((tid >> 1) & 7) * 328 + (tid >> 4) * 2 + (tid & 1) + 64 * i;
#pragma unroll
                for (int e = 0; e < 4; ++e) o[i][e] = (unsigned)q[e * 8 * 328] | ((unsigned)q[(32 + e * 8) * 328] << 16);
            }
#pragma unroll
            for (int i = 0; i < 5; ++i) st128(o[i], rws, (tid >> 4) * WST + (tid & 15) * 16u, O_WFT + i * 32u * WST + (j >> 5) * 8192u + (j & 7u) * 1024u + ((j >> 3) & 3u) * 256u, 16);
            if (w == 7) {
                const u16* q = q0 + (tid & 31) * 328;
                const unsigned k = row(tid);
                const c2 h0 = (c2){bf2f(q[0]) + bf2f(n[0]), bf2f(q[32 * 328]) - bf2f(n[1])} * bc(0.25f), h1 = (c2){bf2f(q[320]) + bf2f(n[2]), bf2f(q[32 * 328 + 320]) - bf2f(n[3])} * bc(0.25f);
                st128(__builtin_bit_cast(u32x4, (f32x4){h0.x + h1.x, h0.y + h1.y, h0.x - h1.x, h0.y - h1.y}), rws, (((k >> 8) & 6u) * 256u + (k & 64u) * 4u + (k & 7u) * 32u + ((k >> 4) & 24u) + ((k >> 3) & 7u)) * 16u, O_WFT + NCOL * WST, 16);
            }
            FST(3);
            asm volatile("" :: "v"(x0), "v"(x1));
            FST(4);
        }
        rot0 = __builtin_amdgcn_raw_buffer_load_b64(rws, 0u, 128u, 16);
        VMWAIT();
        FST(8);
        BAR0();
        FST(9);
    }
    tag = (((u64)__builtin_amdgcn_readfirstlane(tg0[1]) << 32) | (__builtin_amdgcn_readfirstlane(tg0[0]) & ~15u)) + 16ull;
    if (w == 0) __builtin_amdgcn_raw_buffer_store_b64(__builtin_bit_cast(u32x2, tag + 8ull), rwin, LINE8, W_A1 + blockIdx.x * 128u, 16);
    if (blockIdx.x >= 128) FSW(0); else if (blockIdx.x >= 64) FSW(6);
    unsigned vx;
    int bid;
    auto set_role = [&](unsigned x0) INL {
        vx = x0;
        const int j = blockIdx.x >> 3, x = vx & 3;
        if (!(vx & 4)) bid = j < 8 ? 8 * x + j : j < 16 ? 24 + 8 * x + j : j < 26 ? 80 + 10 * x + j : j < 30 ? NWRK + 4 * (j - 26) + x : NREC - 22 + 10 * x + j;
        else bid = j < 8 ? 64 + 8 * x + j : j < 16 ? NQT - 8 + 8 * x + j : j < 24 ? NREC - 16 + 10 * x + j : NXT - 24 + 8 * x + j;
    };
    auto get_rot = [&]() INL {
        unsigned r = 0;
        for (unsigned spins = 0; spins < SPIN_CAP; ++spins) {
            const u64 x = __builtin_bit_cast(u64, rot0) - tag;
            r = __builtin_amdgcn_readfirstlane((unsigned)x);
            if (__all(x < 8ull)) break;
            __builtin_amdgcn_s_sleep(2);
            asm volatile("" ::: "memory");
            rot0 = __builtin_amdgcn_raw_buffer_load_b64(rws, 0u, 128u, 16);
        }
        return (r + blockIdx.x + 4u * DIE) & 7u;
    };
    set_role(blockIdx.x < 64 ? (xcc_id() ^ (4u * DIE)) & 7u : get_rot());
    unsigned long long tref = 0;
    int layer = 0;
    unsigned fo = 0;
    unsigned nh = 0, nz = 0, mc = 0, qz = 0, mo = 0, tro = 0;
    auto near_init = [&]() INL {
        unsigned f;
        for (unsigned spins = 0;;) {
            asm volatile("" ::: "memory");
            f = __builtin_amdgcn_raw_buffer_load_b32(rwin, (fresh_lane() & 15u) * 4u, W_NEAR, 16);
            if (!__any(f == ~0u) || spin_fail(spins, rwin)) break;
            __builtin_amdgcn_s_sleep(8);
        }
        const unsigned h = (unsigned)__ballot(f & 1u), q = (unsigned)__ballot(f & 2u) ^ 0xAAAAu;
        auto most = [&](unsigned m) { return (__builtin_popcount(m & 0x0F0Fu) + 3u) >> 3; };
        nh = most(h) * HALF; nz = most(h >> 4) * HALF;
        mc = (vx & 3u) ^ most(q) ^ (STK ? 2u * most((unsigned)__ballot(f & 4u) ^ 0x3333u) : STX);
        qz = most(q >> 4);
        mo = W_MEM + (mc & 1u) * PAIR + nh + (mc >> 1) * 4096u;
        tro = mo - W_MEM + W_T;
    };
    auto zpair = [&](unsigned x, unsigned k) { return (((x >> 1) * 10u + k) * 2u + ((x & 1u) ^ qz)) * PAIR + nz; };
    auto two = [&]() { const unsigned l = fresh_lane(); return l < 4 ? (l >> 1) * PAIR + (l & 1) * 4096u + nh : 0x40000000u; };
    const f32x4 z4 = {0.f, 0.f, 0.f, 0.f};
    int hrdy = 0, budget = (int)SPIN_CAP;
    unsigned fc0 = 0, fc1 = 0, fc2 = 0;
    Clk ck = {0u, 0u, 1600u, 10486u};
    SST(5);
#define xt (bid >= NXT && bid < NWRK)
#define qx (bid >= NQT && bid < NWRK)
#define xq (bid >= NREC && bid < NQT && (bid - NREC) % 10 < 8)
    unsigned tpp = 0;
    int lt = LT1, xs = XS0;
    struct Tk { unsigned vo, tw, sl; const LAS char* sp; };
    auto tk_init = [&]() {
        const unsigned l = fresh_lane();
        Tk k = {l * 16u, two(), (l & 31u) * 128u + 8u - (l >> 5) * 4u, (const LAS char*)(memL + l * 64 + 4 + ((l >> 1) & 7) * 8)};
        PIN2(k.vo, k.tw); PIN2(k.sl, k.sp);
        return k;
    };
    auto sched2 = [&](Tk& k, int s, unsigned f) INL {
        const unsigned g0 = tpp - 0x400000u;
        tpp = g0 + (unsigned)read_sched(rwin, s, k.sl, tro + (unsigned)(s & 3) * 2u * PAIR, f, g0, lt, xs, fc1) - M2D * layer;
        k.sl &= ~12u;
        RT(6);
    };
    auto sched = [&](Tk& k, int s) INL { sched2(k, s, __builtin_amdgcn_raw_buffer_load_b32(rwin, k.sl, tro + (unsigned)(s & 3) * 2u * PAIR, 16)); };
    unsigned it = 0;
    auto fetch = [&](const Tk& k, int s, bool first, unsigned lim, unsigned dc, unsigned fo, bool tell) INL {
        const unsigned sb = (unsigned)(s & 3) * 6u * PAIR, ta0 = ck.ta, mm0 = ck.ma;
        if (__builtin_expect(s > 0, 1)) {
            if (__builtin_expect(first, 1)) { const unsigned te = cyc_at(ck, sx24(tpp - TKE - ck.ta)); while ((int)((unsigned)__builtin_amdgcn_s_memtime() - te) < 0) __builtin_amdgcn_s_sleep(1); }
            if (__builtin_expect(first, 1)) gather_at(tpp + (tell ? 0 : DGV), ck, rwin, k.vo, mo + sb, VREV && !tell); else { const unsigned c = (mc + 1u + (it & 1u)) & 3u; gather_dma(rwin, k.vo, W_MEM + (c & 1u) * PAIR + (c >> 1) * 4096u + nh + sb, VREV && !tell); }
        }
        __builtin_amdgcn_s_setprio(0);
        RT(1);
        clk_rate(ck, ta0, mm0);
        unsigned tb = ck.ma - dc, tg = (unsigned)s << 24, so = W_T + (unsigned)(s & 3) * 2u * PAIR + fo;
        asm volatile("" : "+s"(tb), "+s"(tg), "+s"(so));
        asm volatile(ALN "s_waitcnt vmcnt(%1) lgkmcnt(0)\n\ts_barrier\n\ts_waitcnt vmcnt(%2)\n\ts_barrier\n\ts_waitcnt vmcnt(0)\n\ts_memtime %0\n\ts_barrier"
                     : "=s"(tref) : "n"(10 - NB1), "n"(10 - NB2) : "memory");
        MT(1);
        if (!tell) { RT(3); return; }
        unsigned x0 = *(const LAS unsigned*)k.sp, x1 = *(const LAS unsigned*)(k.sp + 4096), x2 = *(const LAS unsigned*)(k.sp + 6144);
        __builtin_amdgcn_s_waitcnt(0xC07F);
        asm volatile("" : "+s"(tref) :: "memory");
        const unsigned fw = ((ck.ta + ((((unsigned)tref - tb) * ck.qpc) >> 16)) & 0xFFFFFFu) | tg;
        __builtin_amdgcn_raw_buffer_store_b32(fw, rwin, k.tw, __any(max(max(x0, x1), x2) > lim) ? 0x40000000u : so, 16);
        RT(3);
    };
    auto rout = [&]() { return mkrsrc(layer ? arg(A_OUT) : (const u16*)(ws + OFF_X2), S_ * D_ * 2); };
    auto rec_end = [&](unsigned g) INL {
        if (g) for (unsigned spins = 0;;) {
            asm volatile("" ::: "memory");
            if (__builtin_amdgcn_readfirstlane(__builtin_amdgcn_raw_buffer_load_b32(rws, 0u, g == 3 ? O_FL + (unsigned)(bid & 127) * GST + 4092u : O_FL - g * SEG * D_ * 2u + (unsigned)bid * 128u, 16)) != -1 || spin_fail(spins, rwin)) break;
            __builtin_amdgcn_s_sleep(16);
        }
    };
    auto mytid = [&]() { return w * 64 + (int)fresh_lane(); };
    auto new_layer = [&]() { fo = (unsigned)layer * W_LAYER; asm volatile("" : "+s"(fo), "+s"(w), "+s"(bid), "+s"(nh), "+s"(nz), "+s"(mc), "+s"(qz), "+s"(tag)); hrdy = 0; tpp = 0; lt = LT1; xs = XS0; };
    auto load_qv = [&](u32x8 (&wt)[2][10], int hh, bool v, auto mid) INL {
        const bool o = v && w >= 4;
        load_w(wt, arg(A_WQ + 2 * v + (w >> 2)) + (o ? (hh * 128 + (w & 3) * 32) * D_ : hh * 128 + (w & 3) * 16 * HD), o ? D_ * 2 : HD * 2, !o, o ? 16 * D_ * 2 : 64 * HD * 2, o ? 256 : 128 * HD * 2, w, mid);
    };
    auto hseg = [&](int j) { return O_H + (unsigned)(j - NHF * layer) * 40960u; };
    auto stage2 = [&](int j0, int j1, bool vt) INL {
        if (w == 0) {
            if (j1 >= NHF) wait_h(rwin, fo, j1, hrdy);
            else if (TH) { const unsigned vo[1] = {(fresh_lane() & 3u) * 4u}; wait_told(rwin, vo, W_H + fo + (unsigned)j0 * 128u + 16u, 1); }
            else for (unsigned spins = 0;;) {
                asm volatile("" ::: "memory");
                const unsigned l = fresh_lane();
                if (!__any(__builtin_amdgcn_raw_buffer_load_b32(rws, (l & 31u) * 128u + (l >> 5) * (unsigned)(j1 - j0) * 40960u, hseg(j0) + 36864u, 16) == ~0u) || spin_fail(spins, rwin)) break;
                __builtin_amdgcn_s_sleep(HSLP);
            }
            *(volatile LAS int*)(smem + L_S + 32) = hrdy;
        }
        BAR();
        hrdy = __builtin_amdgcn_readfirstlane(*(volatile LAS int*)(smem + L_S + 32));
        SST(2);
        if (j0 == 1 && vt) __builtin_amdgcn_s_sleep(DH1);
        stage_h<5>(rws, hseg(j0) + w * 1024u, L_H + w * 1024u, 8192u);
        if (!vt) {
            stage_h<5>(rws, hseg(j1) + w * 1024u, L_H + 40960u + w * 1024u, 8192u);
            asm volatile("s_waitcnt vmcnt(5)" ::: "memory");
        } else if (w >= 4 && j1 != j0) {
            stage_h<10>(rws, hseg(j1) + (w - 4) * 1024u, L_H + 40960u + (w - 4) * 1024u, 4096u);
            asm volatile("s_waitcnt vmcnt(10)" ::: "memory");
        } else VMWAIT();
        check_h<5>(rws, rwin, hseg(j0) + w * 1024u, L_H + w * 1024u, 8192u);
        BAR0();
        SST(3);
        if (vt && w >= 4 && j1 != j0) {
            VMWAIT();
            check_h<10>(rws, rwin, hseg(j1) + (w - 4) * 1024u, L_H + 40960u + (w - 4) * 1024u, 4096u);
        }
    };
#define WO(eb, nt) wt[(nt) & 1][((nt) & ~1) + (eb)]
    unsigned zb = W_ZM;
    auto probe = [&]() INL {
        {
            unsigned* best = (unsigned*)smem;
            const int tid = mytid();
            const __amdgpu_buffer_rsrc_t rw = mkrsrc(a.win + (bid & 4 ? W_END : W_PROBE), 2 * PAIR);
            const unsigned base = (unsigned)(bid & 8) * 64u + w * 1024u;
            if (tid < 8) best[tid] = 100000u;
            if (w == 0) wait_tags(rwin, 0, 1, 64u, tag + 8ull, true);
            BAR();
            unsigned mn[4] = {~0u, ~0u, ~0u, ~0u};
            for (int r = 0; r < NPR; ++r) {
#pragma unroll
                for (int i = 0; i < 4; ++i) {
                    const unsigned long long t1 = __builtin_amdgcn_s_memtime();
                    asm volatile("s_waitcnt lgkmcnt(0)" ::: "memory");
                    unsigned v = __builtin_amdgcn_raw_buffer_load_b32(rw, base + r * 128u + i * HALF, 0, 16);
                    asm volatile("s_waitcnt vmcnt(0)" : "+v"(v) :: "memory");
                    const unsigned long long t2 = __builtin_amdgcn_s_memtime();
                    mn[i] = min(mn[i], (unsigned)(t2 - t1));
                }
            }
            if ((tid & 63) == 0) {
#pragma unroll
                for (int i = 0; i < 4; ++i) atomicMin(&best[i + (w & 4)], mn[i]);
            }
            BAR();
            const unsigned h = min(min(best[1], best[3]), min(best[5], best[7])) < min(min(best[0], best[2]), min(best[4], best[6])), q = best[h + 2] + best[h + 6] < best[h] + best[h + 4], k = best[h + 4] + best[h + 6] < best[h] + best[h + 2];
            if (w == 0) __builtin_amdgcn_raw_buffer_store_b32(h + 2u * q + 4u * k, rwin, LANE0, W_NEAR + (unsigned)(bid & 15) * 4u, 16);
            BAR();
        }
    };
    auto vo_role = [&](const u32x8 (&wt)[2][10]) INL {
        const int hh = bid & 31;
        auto steps = [&](auto cc) INL {
            constexpr int C = decltype(cc)::v & 1;
            constexpr bool TK = decltype(cc)::v == 3;
            LANES();
            Tk tk = {0u, 0u, 0x40000000u, nullptr};
            if (TK) { tk = tk_init(); __builtin_amdgcn_s_setprio(TKP); }
            const unsigned zi = (unsigned)(hh * 128 + fr * 8) * 2u;
            unsigned zo0 = zpair(w & 3, fq) + zi, zo1 = zo0 + 8 * PAIR, zo2 = zpair(w & 3, 8 + (fq >> 1)) + zi + (fq & 1) * 8;
            const LAS char* va = (const LAS char*)(smem + L_Q + (fr & 1) * 544 + ((fr >> 1) & 1) * 1152 + (16 * fq + 2 * (fr >> 2)) * 4);
            LAS char* vw = (LAS char*)(smem + L_Q + (fq & 1) * 544 + (fq >> 1) * 1152 + (16 * w + bs2(fr)) * 4);
            const LAS char* mp0 = (const LAS char*)(memL + ma0);
            PIN2(zo0, zo1); PIN1(zo2);
            if (C == 0) { PIN2(mp0, vw); } else PIN1(va);
            u32x4 za = SENT, zc = SENT;
            u32x2 ze = {0u, 0u};
            auto put_z2 = [&](int s) {
                const unsigned z2b = s < NSEG ? O_Z2 + (unsigned)s * (NOWN * 8192u) + (unsigned)(10 * (w & 3) * 8192 + hh * 256) : 0x40000000u, l2 = fresh_lane();
                st128(za, rws, (l2 >> 4) * 8192u + (l2 & 15u) * 16u, z2b, 16);
                st128(zc, rws, (l2 >> 4) * 8192u + (l2 & 15u) * 16u, z2b + 4u * 8192u, 16);
                __builtin_amdgcn_raw_buffer_store_b64(ze, rws, (l2 >> 5) * 8192u + (l2 & 15u) * 16u + ((l2 >> 4) & 1u) * 8u, z2b + 8u * 8192u, 16);
            };
            for (int s = 0; s < NSEG; ++s) {
                RT(0);
                bool redo = false;
                unsigned sf = 0;
                do {
                    ++it;
                    if (C == 0) {
                        const unsigned lim = s > 0 ? poll_lim(budget) : ~0u;
                        asm volatile(
                            "v_mov_b64 v[200:201], 0\n\t"
                            "v_mov_b64 v[202:203], 0\n\t"
                            "v_mov_b64 v[204:205], 0\n\t"
                            "v_mov_b64 v[206:207], 0\n\t"
                            "v_mov_b32 v208, 0\n\t"
                            "s_setprio 2\n\t"
                            ALN
                            "s_waitcnt lgkmcnt(0)\n\t"
                            "s_barrier\n\t"
                            MTA(0)
                            "ds_read_b128 v[212:215], %[mp] offset:" VOFF0 "\n\t"
                            "ds_read_b128 v[216:219], %[mp] offset:" VOFF1 "\n\t"
                            "ds_read_b128 v[220:223], %[mp] offset:" VOFF2 "\n\t"
                            "s_barrier\n\t"
                            "ds_read_b128 v[224:227], %[mp] offset:" VOFF3 "\n\t"
                            "ds_read_b128 v[228:231], %[mp] offset:" VOFF4 "\n\t"
                            "ds_read_b128 v[232:235], %[mp] offset:" VOFF5 "\n\t"
                            "s_waitcnt lgkmcnt(5)\n\t"
                            "v_smfmac_f32_16x16x64_bf16 v[200:203], v[212:215], %[w00], %[ix]\n\t"
                            "s_waitcnt lgkmcnt(4)\n\t"
                            "v_smfmac_f32_16x16x64_bf16 v[200:203], v[216:219], %[w01], %[ix]\n\t"
                            "s_barrier\n\t"
                            MTA(1)
                            "ds_read_b128 v[236:239], %[mp] offset:" VOFF6 "\n\t"
                            "s_waitcnt lgkmcnt(4)\n\t"
                            "v_smfmac_f32_16x16x64_bf16 v[200:203], v[220:223], %[w02], %[ix]\n\t"
                            "ds_read_b128 v[240:243], %[mp] offset:" VOFF7 "\n\t"
                            "s_waitcnt lgkmcnt(4)\n\t"
                            "v_smfmac_f32_16x16x64_bf16 v[200:203], v[224:227], %[w03], %[ix]\n\t"
                            "ds_read_b128 v[244:247], %[mp] offset:" VOFF8 "\n\t"
                            "s_waitcnt lgkmcnt(4)\n\t"
                            "v_smfmac_f32_16x16x64_bf16 v[200:203], v[228:231], %[w04], %[ix]\n\t"
                            "ds_read_b128 v[248:251], %[mp] offset:" VOFF9 "\n\t"
                            "s_waitcnt lgkmcnt(4)\n\t"
                            "v_smfmac_f32_16x16x64_bf16 v[200:203], v[232:235], %[w05], %[ix]\n\t"
                            "s_waitcnt lgkmcnt(3)\n\t"
                            "v_smfmac_f32_16x16x64_bf16 v[200:203], v[236:239], %[w06], %[ix]\n\t"
                            "s_waitcnt lgkmcnt(2)\n\t"
                            "v_smfmac_f32_16x16x64_bf16 v[200:203], v[240:243], %[w07], %[ix]\n\t"
                            "s_waitcnt lgkmcnt(1)\n\t"
                            "v_smfmac_f32_16x16x64_bf16 v[200:203], v[244:247], %[w08], %[ix]\n\t"
                            "s_waitcnt lgkmcnt(0)\n\t"
                            "v_smfmac_f32_16x16x64_bf16 v[200:203], v[248:251], %[w09], %[ix]\n\t"
                            "s_nop 2\n\t"
                            "v_smfmac_f32_16x16x64_bf16 v[204:207], v[212:215], %[w10], %[ix]\n\t"
                            "s_nop 2\n\t"
                            "v_smfmac_f32_16x16x64_bf16 v[204:207], v[216:219], %[w11], %[ix]\n\t"
                            "v_add_f32 v200, v200, v202\n\t"
                            "v_add_f32 v201, v201, v203\n\t"
                            "v_cvt_pk_bf16_f32 v200, v200, v201\n\t"
                            "ds_write_b32 %[vw], v200\n\t"
                            MTA(2)
                            "v_smfmac_f32_16x16x64_bf16 v[204:207], v[220:223], %[w12], %[ix]\n\t"
                            "v_max3_u32 v208, v208, v212, v213\n\t"
                            "v_max3_u32 v208, v208, v214, v215\n\t"
                            "v_smfmac_f32_16x16x64_bf16 v[204:207], v[224:227], %[w13], %[ix]\n\t"
                            "v_max3_u32 v208, v208, v216, v217\n\t"
                            "v_max3_u32 v208, v208, v218, v219\n\t"
                            "v_smfmac_f32_16x16x64_bf16 v[204:207], v[228:231], %[w14], %[ix]\n\t"
                            "v_max3_u32 v208, v208, v220, v221\n\t"
                            "v_max3_u32 v208, v208, v222, v223\n\t"
                            "v_smfmac_f32_16x16x64_bf16 v[204:207], v[232:235], %[w15], %[ix]\n\t"
                            "v_max3_u32 v208, v208, v224, v225\n\t"
                            "v_max3_u32 v208, v208, v226, v227\n\t"
                            "v_smfmac_f32_16x16x64_bf16 v[204:207], v[236:239], %[w16], %[ix]\n\t"
                            "v_max3_u32 v208, v208, v228, v229\n\t"
                            "v_max3_u32 v208, v208, v230, v231\n\t"
                            "v_smfmac_f32_16x16x64_bf16 v[204:207], v[240:243], %[w17], %[ix]\n\t"
                            "s_waitcnt lgkmcnt(0)\n\t"
                            "s_barrier\n\t"
                            MTA(5)
                            "v_smfmac_f32_16x16x64_bf16 v[204:207], v[244:247], %[w18], %[ix]\n\t"
                            "v_max3_u32 v208, v208, v232, v233\n\t"
                            "v_max3_u32 v208, v208, v234, v235\n\t"
                            "v_smfmac_f32_16x16x64_bf16 v[204:207], v[248:251], %[w19], %[ix]\n\t"
                            "v_max3_u32 v208, v208, v236, v237\n\t"
                            "v_max3_u32 v208, v208, v238, v239\n\t"
                            "v_max3_u32 v208, v208, v240, v241\n\t"
                            "v_max3_u32 v208, v208, v242, v243\n\t"
                            "v_max3_u32 v208, v208, v244, v245\n\t"
                            "v_max3_u32 v208, v208, v246, v247\n\t"
                            "v_max3_u32 v208, v208, v248, v249\n\t"
                            "v_max3_u32 v208, v208, v250, v251\n\t"
                            "v_cmp_lt_u32 vcc, %[lim], v208\n\t"
                            "v_add_f32 v204, v204, v206\n\t"
                            "v_add_f32 v205, v205, v207\n\t"
                            "v_cvt_pk_bf16_f32 v204, v204, v205\n\t"
                            "ds_write_b32 %[vw], v204 offset:256\n\t"
                            MTA(3)
                            "s_cbranch_vccz 1f\n\t"
                            "v_mov_b32 v208, %[it]\n\t"
                            "v_mov_b32 v209, 0\n\t"
                            "ds_write_b32 v209, v208 offset:%[ls]\n\t"
                            "1:\n\t"
                            "s_waitcnt vmcnt(0) lgkmcnt(0)\n\t"
                            "s_barrier\n\t"
                            "s_setprio 0\n\t"
                            :: [mp] "v"(mp0), [vw] "v"(vw), [ix] "v"(sidx), [lim] "s"(lim), [it] "s"(it), [ls] "n"(L_S),
                              [w00] "v"(wt[0][VREV ? 9 - 0 : 0]), [w01] "v"(wt[0][VREV ? 9 - 1 : 1]), [w02] "v"(wt[0][VREV ? 9 - 2 : 2]), [w03] "v"(wt[0][VREV ? 9 - 3 : 3]), [w04] "v"(wt[0][VREV ? 9 - 4 : 4]), [w05] "v"(wt[0][VREV ? 9 - 5 : 5]), [w06] "v"(wt[0][VREV ? 9 - 6 : 6]), [w07] "v"(wt[0][VREV ? 9 - 7 : 7]), [w08] "v"(wt[0][VREV ? 9 - 8 : 8]), [w09] "v"(wt[0][VREV ? 9 - 9 : 9]), [w10] "v"(wt[1][VREV ? 9 - 0 : 0]), [w11] "v"(wt[1][VREV ? 9 - 1 : 1]), [w12] "v"(wt[1][VREV ? 9 - 2 : 2]), [w13] "v"(wt[1][VREV ? 9 - 3 : 3]), [w14] "v"(wt[1][VREV ? 9 - 4 : 4]), [w15] "v"(wt[1][VREV ? 9 - 5 : 5]), [w16] "v"(wt[1][VREV ? 9 - 6 : 6]), [w17] "v"(wt[1][VREV ? 9 - 7 : 7]), [w18] "v"(wt[1][VREV ? 9 - 8 : 8]), [w19] "v"(wt[1][VREV ? 9 - 9 : 9])
                            : "v200", "v201", "v202", "v203", "v204", "v205", "v206", "v207", "v208", "v209", "v212", "v213", "v214", "v215", "v216", "v217", "v218", "v219", "v220", "v221", "v222", "v223", "v224", "v225", "v226", "v227", "v228", "v229", "v230", "v231", "v232", "v233", "v234", "v235", "v236", "v237", "v238", "v239", "v240", "v241", "v242", "v243", "v244", "v245", "v246", "v247", "v248", "v249", "v250", "v251", "vcc", "memory");
                        MT(4);
                        redo = __builtin_amdgcn_readfirstlane(*(volatile LAS unsigned*)(smem + L_S)) == it;
                        if (redo) FC(fc0);
                    } else {
                        f32x16 ca, cb;
                        f32x8 cc;
                        asm volatile("v_mov_b64 v[212:213], 0\n\tv_mov_b64 v[214:215], 0\n\tv_mov_b64 v[216:217], 0\n\tv_mov_b64 v[218:219], 0\n\tv_mov_b64 v[220:221], 0\n\tv_mov_b64 v[222:223], 0\n\tv_mov_b64 v[224:225], 0\n\tv_mov_b64 v[226:227], 0\n\tv_mov_b64 v[228:229], 0\n\tv_mov_b64 v[230:231], 0\n\tv_mov_b64 v[232:233], 0\n\tv_mov_b64 v[234:235], 0\n\tv_mov_b64 v[236:237], 0\n\tv_mov_b64 v[238:239], 0\n\tv_mov_b64 v[240:241], 0\n\tv_mov_b64 v[242:243], 0\n\tv_mov_b64 v[244:245], 0\n\tv_mov_b64 v[246:247], 0\n\tv_mov_b64 v[248:249], 0\n\tv_mov_b64 v[250:251], 0\n\t" : "={v[212:227]}"(ca), "={v[228:243]}"(cb), "={v[244:251]}"(cc));
                        unsigned sr = tro + (unsigned)(s & 3) * 2u * PAIR, y0 = zfix(zo0, zb), y1 = zfix(zo1, zb), y2 = zfix(zo2, zb);
                        asm volatile("" : "+s"(sr), "+v"(y0), "+v"(y1), "+v"(y2));
                        if (TK) fetch(tk, s, !redo, s > 0 ? poll_lim(budget) : ~0u, 0u, 0u, false);
                        else asm volatile(ALN "s_waitcnt lgkmcnt(0)\n\ts_barrier\n\ts_barrier\n\ts_barrier" ::: "memory");
                        unsigned vd, vv, zs = zb;
                        u32x4 af1;
                        asm volatile(
                            "s_setprio 1\n\t"
                            "s_waitcnt lgkmcnt(0)\n\t"
                            "s_barrier\n\t"
                            "ds_read_b64_tr_b16 v[192:193], %[va]\n\t"
                            "ds_read_b64_tr_b16 v[194:195], %[va] offset:32\n\t"
                            "s_waitcnt lgkmcnt(0)\n\t"
                            "v_smfmac_f32_16x16x64_bf16 v[212:215], v[192:195], %[w0], %[ix]\n\t"
                            "v_smfmac_f32_16x16x64_bf16 v[220:223], v[192:195], %[w2], %[ix]\n\t"
                            "v_smfmac_f32_16x16x64_bf16 v[216:219], v[192:195], %[w1], %[ix]\n\t"
                            "v_smfmac_f32_16x16x64_bf16 v[224:227], v[192:195], %[w3], %[ix]\n\t"
                            "s_barrier\n\t"
                            MTA(4)
                            "ds_read_b64_tr_b16 v[196:197], %[va] offset:256\n\t"
                            "v_smfmac_f32_16x16x64_bf16 v[228:231], v[192:195], %[w4], %[ix]\n\t"
                            "ds_read_b64_tr_b16 v[198:199], %[va] offset:288\n\t"
                            "v_smfmac_f32_16x16x64_bf16 v[236:239], v[192:195], %[w6], %[ix]\n\t"
                            "ds_read_b32 v210, %[vz] offset:%[ls]\n\t"
                            "v_smfmac_f32_16x16x64_bf16 v[232:235], v[192:195], %[w5], %[ix]\n\t"
                            "v_smfmac_f32_16x16x64_bf16 v[240:243], v[192:195], %[w7], %[ix]\n\t"
                            "v_smfmac_f32_16x16x64_bf16 v[244:247], v[192:195], %[w8], %[ix]\n\t"
                            "v_smfmac_f32_16x16x64_bf16 v[248:251], v[192:195], %[w9], %[ix]\n\t"
                            "buffer_load_dword %[sf], %[sl], %[rw], %[sr] offen sc1\n\t"
                            : [sf] "=&v"(sf), "+{v[212:227]}"(ca), "+{v[228:243]}"(cb), "+{v[244:251]}"(cc), "=&{v[196:199]}"(af1), "=&{v210}"(vv)
                            : [va] "v"(va), [vz] "v"(0u), [ls] "n"(L_S), [ix] "v"(sidx), [sl] "v"(tk.sl), [sr] "s"(sr), [rw] "s"(rwin),
                              [w0] "v"(WO(0, 0)), [w1] "v"(WO(0, 1)), [w2] "v"(WO(0, 2)), [w3] "v"(WO(0, 3)), [w4] "v"(WO(0, 4)), [w5] "v"(WO(0, 5)), [w6] "v"(WO(0, 6)), [w7] "v"(WO(0, 7)), [w8] "v"(WO(0, 8)), [w9] "v"(WO(0, 9))
                            : "v192", "v193", "v194", "v195", "memory");
                        asm volatile(
                            "s_waitcnt lgkmcnt(0)\n\t"
                            "v_smfmac_f32_16x16x64_bf16 v[212:215], v[196:199], %[w0], %[ix]\n\t"
                            "v_readfirstlane_b32 %[vd], v210\n\t"
                            "v_smfmac_f32_16x16x64_bf16 v[220:223], v[196:199], %[w2], %[ix]\n\t"
                            "s_cmp_eq_u32 %[vd], %[it]\n\t"
                            "s_cselect_b32 %[zs], 2.0, %[zs]\n\t"
                            "v_smfmac_f32_16x16x64_bf16 v[216:219], v[196:199], %[w1], %[ix]\n\t"
                            "s_nop 1\n\t"
                            "v_smfmac_f32_16x16x64_bf16 v[224:227], v[196:199], %[w3], %[ix]\n\t"
                            "v_add_f32 v212, v212, v214\n\t"
                            "v_smfmac_f32_16x16x64_bf16 v[228:231], v[196:199], %[w4], %[ix]\n\t"
                            "v_add_f32 v213, v213, v215\n\t"
                            "v_smfmac_f32_16x16x64_bf16 v[236:239], v[196:199], %[w6], %[ix]\n\t"
                            "v_add_f32 v220, v220, v222\n\t"
                            "v_smfmac_f32_16x16x64_bf16 v[232:235], v[196:199], %[w5], %[ix]\n\t"
                            "v_add_f32 v221, v221, v223\n\t"
                            "v_smfmac_f32_16x16x64_bf16 v[240:243], v[196:199], %[w7], %[ix]\n\t"
                            "v_cvt_pk_bf16_f32 v200, v212, v213\n\t"
                            "v_smfmac_f32_16x16x64_bf16 v[244:247], v[196:199], %[w8], %[ix]\n\t"
                            "v_cvt_pk_bf16_f32 v201, v220, v221\n\t"
                            "v_smfmac_f32_16x16x64_bf16 v[248:251], v[196:199], %[w9], %[ix]\n\t"
                            "v_add_f32 v216, v216, v218\n\t"
                            "v_add_f32 v217, v217, v219\n\t"
                            "v_add_f32 v224, v224, v226\n\t"
                            "v_add_f32 v225, v225, v227\n\t"
                            "v_cvt_pk_bf16_f32 v202, v216, v217\n\t"
                            "v_cvt_pk_bf16_f32 v203, v224, v225\n\t"
                            "v_permlane32_swap_b32 v200, v201\n\t"
                            "v_pk_add_f32 v[228:229], v[228:229], v[230:231]\n\t"
                            "v_permlane32_swap_b32 v202, v203\n\t"
                            "v_min_u32 v201, -2, v201\n\t"
                            "v_pk_add_f32 v[236:237], v[236:237], v[238:239]\n\t"
                            "v_min_u32 v203, -2, v203\n\t"
                            "v_permlane16_swap_b32 v200, v202\n\t"
                            "v_pk_add_f32 v[232:233], v[232:233], v[234:235]\n\t"
                            "v_permlane16_swap_b32 v201, v203\n\t"
                            "v_pk_add_f32 v[240:241], v[240:241], v[242:243]\n\t"
                            MTA(5)
                            "buffer_store_dwordx4 v[200:203], %[z0], %[rw], %[zs] offen sc1\n\t"
                            "v_cvt_pk_bf16_f32 v204, v228, v229\n\t"
                            "v_cvt_pk_bf16_f32 v205, v236, v237\n\t"
                            "v_cvt_pk_bf16_f32 v206, v232, v233\n\t"
                            "v_cvt_pk_bf16_f32 v207, v240, v241\n\t"
                            "v_permlane32_swap_b32 v204, v205\n\t"
                            "v_pk_add_f32 v[244:245], v[244:245], v[246:247]\n\t"
                            "v_permlane32_swap_b32 v206, v207\n\t"
                            "v_min_u32 v205, -2, v205\n\t"
                            "v_pk_add_f32 v[248:249], v[248:249], v[250:251]\n\t"
                            "v_min_u32 v207, -2, v207\n\t"
                            "v_permlane16_swap_b32 v204, v206\n\t"
                            "v_cvt_pk_bf16_f32 v208, v244, v245\n\t"
                            "v_permlane16_swap_b32 v205, v207\n\t"
                            "v_cvt_pk_bf16_f32 v209, v248, v249\n\t"
                            "buffer_store_dwordx4 v[204:207], %[z1], %[rw], %[zs] offen sc1\n\t"
                            "s_nop 0\n\t"
                            "v_permlane32_swap_b32 v208, v209\n\t"
                            "v_min_u32 v209, -2, v209\n\t"
                            "buffer_store_dwordx2 v[208:209], %[z2], %[rw], %[zs] offen sc1\n\t"
                            : [vd] "=&s"(vd), [zs] "+s"(zs), "=&{v[200:203]}"(za), "=&{v[204:207]}"(zc), "=&{v[208:209]}"(ze), "+{v[212:227]}"(ca), "+{v[228:243]}"(cb), "+{v[244:251]}"(cc), "+v"(sf)
                            : [ix] "v"(sidx), [it] "s"(it), [rw] "s"(rwin), [z0] "v"(y0), [z1] "v"(y1), [z2] "v"(y2), "{v[196:199]}"(af1), "{v210}"(vv),
                              [w0] "v"(WO(1, 0)), [w1] "v"(WO(1, 1)), [w2] "v"(WO(1, 2)), [w3] "v"(WO(1, 3)), [w4] "v"(WO(1, 4)), [w5] "v"(WO(1, 5)), [w6] "v"(WO(1, 6)), [w7] "v"(WO(1, 7)), [w8] "v"(WO(1, 8)), [w9] "v"(WO(1, 9))
                            : "scc", "memory");
                        redo = vd == it;
                        RT(4);
                        MT(6);
                        tref = __builtin_amdgcn_s_memtime();
                        __builtin_amdgcn_s_setprio(TKP);
                        asm volatile("s_waitcnt vmcnt(3)" : "+v"(sf) :: "memory");
                    }
                } while (__builtin_expect(redo, 0));
                if (s < 8) SST(8 + s);
                if (C == 0) {
                    if (s > 0 && s + 1 < NSEG) {
                        __builtin_amdgcn_s_sleep(DZR);
                        const unsigned zq = zb == W_ZM ? W_ZM + 2u * NOWN * PAIR : zb - NOWN * PAIR;
                        unsigned s1;
                        asm volatile("v_mov_b32 %0, -1" : "=v"(s1));
                        st128(((u32x4){s1, s1, s1, s1}), rwin, zfix(zo0, zq), zq, 16);
                        st128(((u32x4){s1, s1, s1, s1}), rwin, zfix(zo1, zq), zq, 16);
                        __builtin_amdgcn_raw_buffer_store_b64((u32x2){s1, s1}, rwin, zfix(zo2, zq), zq, 16);
                    }
                } else {
                    if (s > TAILS) __builtin_amdgcn_raw_buffer_store_b32((unsigned)s - 1u, rwin, 0u, W_FZ2 + fo + (unsigned)(hh * 128 + (w & 3) * 4), 16);
                    if (TK && __builtin_expect(s == 0, 0)) { wait_h(rwin, fo, 5, hrdy); clk_init(ck); }
                    if (TK && s + 1 < NSEG) sched2(tk, s, sf);
                    if (!TK) wait_until(tref + DZ2);
                    if (s + 2 < NSEG) put_z2(s);
                }
                if (budget <= 0) set_tmo(rwin);
                zb = zb == W_ZM + 2u * NOWN * PAIR ? W_ZM : zb + NOWN * PAIR;
                RT(5);
                RTS();
                MT(7);
                MTS();
            }
        };
        const int tid = mytid();
        gather_mem0(memL, tid);
        if (tid < 8) *(unsigned*)(smem + L_S + tid * 4) = 0u;
        if (w < 4) steps(IC<0>()); else if (w < 7) steps(IC<1>()); else steps(IC<3>());
    };
    auto vt_role = [&](const u32x8 (&wt)[2][10], int hh, unsigned long long mv) INL {
        constexpr bool vt = true;
        const int jl = 63 - __builtin_clzll(mv);
        auto nextv = [&](int j) { return min(seg_after(mv, j), jl); };
        const int jv1 = nextv(-1), jv2 = nextv(jv1);
        if (w == 0) *(unsigned*)(smem + L_S + (fresh_lane() & 7u) * 4) = 0u;
        stage2(jv1, jv2, true);
        {
            LANES();
            const int ni = __builtin_popcountll(mv) + 1;
            if (w < 4) {
                const LAS char* c0 = (const LAS char*)(hL + hoff + ((fq ^ (fr & 7)) << 4));
                const LAS char* c1 = (const LAS char*)(hL + hoff + (((4 + fq) ^ (fr & 7)) << 4));
                const LAS char* n0 = c0 + 40960; const LAS char* n1 = c1 + 40960;
                u32x4 bb[8];
                project0(bb, c0, c1);
                for (int i = 1; i < ni; ++i) {
                    if (vt) MT(0);
                    f32x4 tc[2][2];
                    project<0>(wt, tc, bb, c0, c1, n0, n1,
                        [&]() {
                            MT(1);
                            if (i == 1) SST(16);
                            if (__builtin_expect(!R1F || i > 1, 1)) asm volatile("s_waitcnt vmcnt(0) lgkmcnt(0)\n\ts_barrier" ::: "memory");
                            if (i == 1) SST(17);
                            MT(2);
                        }, [](auto) {});
                    { const LAS char* x = c0; c0 = n0; n0 = x; x = c1; c1 = n1; n1 = x; }
                    char* tb = tokL + (i & 1) * 32 * QS + (16 * w + 4 * fq) * 2;
#pragma unroll
                    for (int g = 0; g < 2; ++g)
#pragma unroll
                        for (int tt = 0; tt < 2; ++tt) *(u32x2*)(tb + (prow + 8 * tt) * QS + g * 128) = (u32x2){pack2(tc[g][tt][0], tc[g][tt][1]), pack2(tc[g][tt][2], tc[g][tt][3])};
                    asm volatile("" ::: "memory");
                    *(volatile LAS int*)(smem + L_S + 16 + w * 4) = i;
                    if (i == 1) SST(18);
                    if (__builtin_expect(R1F && i == 1, 0)) { project_end(bb); BAR0(); project0(bb, c0, c1); }
                    if (vt) { MT(3); const int s = i & 63; MTRT(); MTS(); }
                }
                project_end(bb);
            } else {
                __builtin_amdgcn_s_setprio(OPR);
                const unsigned hv = lane * 16u, fz_ = W_FZ + fo + fz_at(hh, w - 4) + (jv1 ? 4096u : 0u);
                unsigned fp = ~0u;
                unsigned fzp = 0x40000000u;
                int jo = jv1, jh = jv2;
                for (int i = 1; i < ni; ++i, jo = nextv(jo)) {
                    const int b = i + (i >= ZLAG);
                    if (__builtin_expect(i == 1 || i == ZLAG, 0)) {
                        VMWAIT();
                        if (i == ZLAG) {
                            jh = nextv(jh);
                            wait_h(rwin, fo, jh, hrdy);
                            BAR0();
                            for (int t = 0; t < 10; ++t) dma1k(rws, hv, hseg(jh) + (unsigned)((w - 4) * 1024 + t * 4096), (unsigned)(L_H + ((i + 1) & 1) * 40960 + (w - 4) * 1024 + t * 4096));
                            VMWAIT();
                        }
                    }
                    jh = nextv(jh);
                    if (__builtin_expect(R1F && i == 1, 0)) wait_h(rwin, fo, jh, hrdy);
                    if (b < ni) BAR0();
                    if (__builtin_expect(jo + layer == 0 && w == 7, 0)) near_init();
                    wait_h(rwin, fo, jh, hrdy);
                    if (i == 1) SST(19);
                    if (vt) MT(1);
                    for (;;) {
                        const u32x4 f = *(volatile LAS u32x4*)(smem + L_S + 16);
                        if (__builtin_amdgcn_readfirstlane(min(min(f[0], f[1]), min(f[2], f[3]))) >= (unsigned)i) break;
                        __builtin_amdgcn_s_sleep(1);
                    }
                    asm volatile("" ::: "memory");
                    if (i == 1) SST(20);
                    if (__builtin_expect(jo == 0 && w == 7, 0)) __builtin_amdgcn_raw_buffer_store_b32((4u * (unsigned)__builtin_amdgcn_s_memrealtime() + (layer ? DVT : DVT0)) & 0xFFFFFFu, rwin, two(), W_T + (unsigned)hh * 128u + 4u, 16);
                    if (vt) MT(0);
                    u32x4 vf[2][4];
#pragma unroll
                    for (int t = 0; t < 2; ++t)
#pragma unroll
                        for (int ks = 0; ks < 4; ++ks) vf[t][ks] = *(const u32x4*)(tokL + (i & 1) * 32 * QS + (16 * t + fr) * QS + ks * 64 + fq * 16);
                    const unsigned ztb = O_Z + (unsigned)((jo - layer) * (int)ZTSEG) * 2u + (unsigned)(10 * (w - 4) * H_ + hh) * 1024u;
                    f32x4 c[2][2];
                    u32x4 r;
                    unsigned hs = hseg(jh) + (unsigned)((w - 4) * 1024), hd = (unsigned)(L_H + ((b + 1) & 1) * 40960 + (w - 4) * 1024);
                    asm volatile("" : "+s"(hs), "+s"(hd));
#pragma unroll
                    for (int nt = 0; nt < 11; ++nt) {
                        auto& cc = c[nt & 1]; auto& cp = c[(nt + 1) & 1];
#pragma unroll
                        for (int ks = 0; ks < 4; ++ks) {
                            if (nt < 10) { cc[0] = mfma32(vf[0][ks], half8(WO(ks >> 1, nt < 10 ? nt : 0), ks & 1), ks ? cc[0] : z4); PIN1(cc[0]); }
                            if (nt > 0 && ks >= 1 && ks < 3) { r[2 * ks - 2] = pack2(cp[ks - 1][0], cp[ks - 1][1]); PIN1(r); }
                            if (nt < 10) { cc[1] = mfma32(vf[1][ks], half8(WO(ks >> 1, nt < 10 ? nt : 0), ks & 1), ks ? cc[1] : z4); PIN1(cc[1]); }
                            if (nt > 0 && ks >= 1 && ks < 3) { r[2 * ks - 1] = nosent1(pack2(cp[ks - 1][2], cp[ks - 1][3])); PIN1(r); }
                            if (nt > 0 && ks == 2) st128(r, rws, lane * 16u, ztb + (unsigned)((nt - 1) * H_) * 1024u, 16);
                            if (nt < 5 && !(ks & 1)) dma1k(rws, hv, hs + (2 * nt + (ks >> 1)) * 4096u, hd + (2 * nt + (ks >> 1)) * 4096u);
                            if (nt == 5 && ks == 0) {
                                asm volatile("s_waitcnt vmcnt(14)" ::: "memory");
                                __builtin_amdgcn_raw_buffer_store_b32(fp, rwin, 0u, fzp, 16);
                            }
                            SB();
                        }
                    }
                    if (vt) MT(2);
                    if (i == 1) SST(21);
                    fp = (unsigned)jo;
                    fzp = fz_;
                    if (__builtin_expect(jo <= ZEARLY || i + 1 == ni, 0)) {
                        VMWAIT();
                        __builtin_amdgcn_raw_buffer_store_b32(fp, rwin, 0u, fz_, 16);
                    } else asm volatile("s_waitcnt vmcnt(7)" ::: "memory");
                    if (jo < 8) SST(8 + jo);
                    if (vt && (!BSPARSE || jo % 6 == 2)) { const int s = jo; RT(0); RTS(); }
                    if (vt) { MT(3); const int s = i & 63; MTRT(); MTS(); }
                }
            }
            __builtin_amdgcn_s_setprio(0);
            VMWAIT();
            BAR();
        }
    };
    auto qt_role = [&](const u32x8 (&wt)[2][10], int hh, auto qq) INL {
        constexpr bool qt = decltype(qq)::v;
        const unsigned long long mq = bid < NQT ? MXQ : ~MXQ;
        const int jl = 63 - __builtin_clzll(mq);
        auto nextq = [&](int j) { return min(seg_after(mq, j), jl); };
        const int jq0 = qt ? nextq(0) : 0, jq1 = qt ? nextq(jq0) : 0;
        if (qt && jq0 + layer == 1 && w == 7) near_init();
        stage2(jq0, jq1, !qt);
        if (qt && jq0 == 1 && w == 7) __builtin_amdgcn_raw_buffer_store_b32((4u * (unsigned)__builtin_amdgcn_s_memrealtime() + (layer ? DQA : DQA0)) & 0xFFFFFFu, rwin, two(), W_T + (unsigned)hh * 128u + 8u, 16);
        auto check2 = [&]() INL { VMWAIT(); check_h<5>(rws, rwin, hseg(jq1) + w * 1024u, L_H + 40960u + w * 1024u, 8192u); };
        {
            LANES();
            const unsigned to0 = (unsigned)((w ^ 4) >> 2) * 8192u + (unsigned)((w & 3) * 64 + (lane ^ ((fq & 1) * (w < 4 ? 8 : 5)))) * 16u, to1 = to0 + 4096u, hv = lane * 16u;
            const LAS char* c0 = (const LAS char*)(hL + hoff + ((fq ^ (fr & 7)) << 4));
            const LAS char* c1 = (const LAS char*)(hL + hoff + (((4 + fq) ^ (fr & 7)) << 4));
            u32x4 bb[8], g0 = SENT, g1 = SENT;
            f32x4 tc[2][2];
            auto pack = [&]() {
                g0 = (u32x4){pack2(tc[0][0][0], tc[0][0][1]), pack2(tc[0][0][2], tc[0][0][3]), pack2(tc[1][0][0], tc[1][0][1]), pack2(tc[1][0][2], tc[1][0][3])};
                g1 = (u32x4){pack2(tc[0][1][0], tc[0][1][1]), pack2(tc[0][1][2], tc[0][1][3]), pack2(tc[1][1][0], tc[1][1][1]), pack2(tc[1][1][2], tc[1][1][3])};
            };
            const LAS char* n0 = c0 + 40960; const LAS char* n1 = c1 + 40960;
            if (!qt) {
                project0(bb, c0, c1);
                project<0>(wt, tc, bb, c0, c1, c0, c1, []() {}, [](auto) {});
                pack();
                *(u32x4*)(tokL + to0) = g0; *(u32x4*)(tokL + to1) = g1;
                project_end(bb);
            } else {
                project0(bb, c0, c1);
                unsigned hs = hseg(jq1) + (unsigned)(w * 1024), hd = (unsigned)(L_H + 40960 + w * 1024), fv = lane == 0 ? 0u : 0x40000000u, f8 = lane < 8 ? lane * 4u : 0x40000000u, fj = 0;
                const unsigned fq_ = W_FQ + fo + (unsigned)(hh * 128 + (jq0 > 1 ? 4096 : 0) + w * 4);
                for (int j = jq0, jn = jq1;; j = jn, jn = nextq(jn)) {
                    MT(0);
                    unsigned fqe = j > JFW ? 0x40000000u : fq_;
                    asm volatile("" : "+s"(fqe));
                    project<0>(wt, tc, bb, c0, c1, n0, n1,
                        [&]() {
                            if (__builtin_expect(j == jq0, 0)) check2();
                            wait_h(rwin, fo, nextq(jn), hrdy);
                            hs = hseg(nextq(jn)) + (unsigned)(w * 1024); hd = (unsigned)(2 * L_H + 40960 + 2 * w * 1024) - hd;
                            asm volatile("" : "+s"(hs), "+s"(hd));
                            MT(1);
                            VMWAIT();
                            BAR();
                            MT(2);
                            if (w == 0 && j > JFW) __builtin_amdgcn_raw_buffer_store_b32(fj, rwin, f8, fq_, 16);
                        },
                        [&](auto uu) {
                            constexpr int u = decltype(uu)::v;
                            if (u == 34 || u == 38 || u == 2 || u == 6 || u == 10) { constexpr int t = ((u + 6) % 40) / 4; dma1k(rws, hv, hs + t * 8192u, hd + t * 8192u); }
                            if (u == UFQ) { asm volatile("s_waitcnt vmcnt(3)" ::: "memory"); __builtin_amdgcn_raw_buffer_store_b32(fj, rwin, fv, fqe, 16); asm volatile("" ::: "memory"); }
                        });
                    pack();
                    const unsigned qs = O_QK + (unsigned)(j * H_ + hh) * 16384u;
                    st128(g0, rws, to0, qs, 16);
                    st128(g1, rws, to1, qs, 16);
                    { const LAS char* x = c0; c0 = n0; n0 = x; x = c1; c1 = n1; n1 = x; }
                    fj = (unsigned)j;
                    PIN1(fj);
                    if (__builtin_expect(j <= QEARLY || j == jl, 0)) {
                        VMWAIT();
                        __builtin_amdgcn_raw_buffer_store_b32(fj, rwin, fv, fq_, 16);
                        if (j == jl) break;
                    }
                    MT(3);
                    if (j < 8) SST(8 + j);
                    if (!BSPARSE || j % 6 == 2) { const int s = j; RT(0); RTS(); MTRT(); MTS(); }
                }
                project_end(bb);
                VMWAIT();
                BAR0();
            }
        }
        SST(4);
    };
    auto qk_role = [&](const u32x8 (&wt)[2][10]) INL {
        const int hh = bid;
        int qrdy = 0;
        auto steps = [&](auto cc) INL {
        constexpr int C = decltype(cc)::v >> 1, R = decltype(cc)::v & 1;
        LANES();
        const LAS char* ka = (const LAS char*)(smem + L_K + (fr & 1) * TQS + (4 * fq + (fr >> 2)) * 8);
        const LAS char* qa = (const LAS char*)(smem + L_Q + (fr & 1) * 544 + ((fr >> 1) & 1) * 1152 + (16 * fq + 2 * (fr >> 2)) * 4);
        LAS char* qw = (LAS char*)(C < 2 ? smem + L_Q + (fq & 1) * 544 + (fq >> 1) * 1152 + (32 * w + 8 * (fr >> 2) + bs2(fr & 3)) * 4
                                         : smem + L_K + (fq >> 1) * TQS + (32 * (w & 3) + fr) * 8 + (fq & 1) * 4);
        const LAS char* mp0 = (const LAS char*)(memL + ma0);
        const int z = 16 * (fr >> 3) + 4 * (fr & 3) + 2 * ((fr >> 2) & 1);
        const int ko = ((z >> 4) * 256 + fq * 16 + ((z & 15) ^ ((fq & 1) * 5))) * 16;
        unsigned pa0 = 2u * (hh >> 3) * PAIR + (unsigned)(C * 4096 + (hh & 7) * 512 + R * 256 + lane * 4);
        unsigned pa2 = ((8u + (fr >> 3)) * PAIR + C * 4096u + (unsigned)((hh >> 1) * 256 + R * 128 + (hh & 1) * 64 + fq * 16 + (fr & 6) * 2)) | (fr & 1) << 30;
        PIN2(ka, qw); PIN1(mp0);
        constexpr bool TK = C == 2 && R == 1;
        Tk tk = {0u, 0u, 0x40000000u, nullptr};
        if (TK) { tk = tk_init(); __builtin_amdgcn_s_setprio(TKP); }
        if (C < 2) { PIN2(qa, pa0); PIN1(pa2); }
        int qf_ = 0;
        int tbo = 0;
        for (int s = 0; s < NSEG; ++s) {
            RT(0);
            const char* tb = tokL + tbo;
            unsigned pb = __builtin_amdgcn_readfirstlane(s + 1 < NSEG ? p_slot(s) + nh : 0x40000000u);
            asm volatile("" : "+s"(pb));
            bool redo = false;
            unsigned sf = 0;
            if (s <= 1 + LA1) BAR();
            u32x8 kx[2][2];
            if (C < 2) {
#pragma unroll
                for (int u = 0; u < 4; ++u) kx[u >> 1][u & 1] = cat8(*(const u32x4*)(tb + (ko ^ ((u >> 1) * 16)) + (u & 1) * 2048), *(const u32x4*)(tb + (ko ^ ((u >> 1) * 16)) + (u & 1) * 2048 + 1024));
                PIN2(kx[0][0], kx[0][1]); PIN2(kx[1][0], kx[1][1]);
            }
            do {
                ++it;
                f32x4 a0 = zero4(), a1 = zero4();
                PIN2(a0, a1);
                u32x4 m[10];
                if (C < 2) {
                    __builtin_amdgcn_s_setprio(2);
                    asm volatile(ALN ::: "memory");
                    BAR();
                    MT(0);
#pragma unroll
                    for (int t = 0; t < NB1; ++t) { m[t] = *(const LAS u32x4*)(mp0 + t * 1024); SB(); }
                    BAR0();
#pragma unroll
                    for (int t = 0; t < 10; ++t) {
                        if (t == QB2) { BAR0(); MT(1); }
                        if (t + NB1 < 10) m[t + NB1] = *(const LAS u32x4*)(mp0 + (t + NB1) * 1024);
                        SB();
                        a0 = smfmac(m[t], wt[0][t], a0, sidx); a1 = smfmac(m[t], wt[1][t], a1, sidx);
                        PIN2(a0, a1);
                    }
                } else {
                    unsigned mx = 0;
                    asm volatile("" : "+{v[200:203]}"(a0), "+{v[204:207]}"(a1), "+{v208}"(mx));
                    unsigned lim = s > 0 ? poll_lim(budget) : ~0u, sr = tro + (unsigned)(s & 3) * 2u * PAIR;
                    asm volatile("" : "+s"(lim), "+s"(sr));
                    if (TK) fetch(tk, s, !redo, lim, DQC, (unsigned)hh * 128u, true);
                    else asm volatile(ALN "s_waitcnt lgkmcnt(0)\n\ts_barrier\n\ts_barrier\n\ts_barrier" ::: "memory");
                    asm volatile(
                        "ds_read_b128 v[212:215], %[mp] offset:0\n\t"
                        "ds_read_b128 v[216:219], %[mp] offset:1024\n\t"
                        "ds_read_b128 v[220:223], %[mp] offset:2048\n\t"
                        "ds_read_b128 v[224:227], %[mp] offset:3072\n\t"
                        "ds_read_b128 v[228:231], %[mp] offset:4096\n\t"
                        "ds_read_b128 v[232:235], %[mp] offset:5120\n\t"
                        "ds_read_b128 v[236:239], %[mp] offset:6144\n\t"
                        "ds_read_b128 v[240:243], %[mp] offset:7168\n\t"
                        "ds_read_b128 v[244:247], %[mp] offset:8192\n\t"
                        "ds_read_b128 v[248:251], %[mp] offset:9216\n\t"
                        MTA(4)
                        "s_waitcnt lgkmcnt(9)\n\t"
                        "v_smfmac_f32_16x16x64_bf16 v[200:203], v[212:215], %[w00], %[ix]\n\t"
                        "v_max3_u32 v208, v208, v212, v213\n\t"
                        "v_smfmac_f32_16x16x64_bf16 v[204:207], v[212:215], %[w10], %[ix]\n\t"
                        "s_waitcnt lgkmcnt(8)\n\t"
                        "v_max3_u32 v208, v208, v214, v215\n\t"
                        "v_smfmac_f32_16x16x64_bf16 v[200:203], v[216:219], %[w01], %[ix]\n\t"
                        "v_max3_u32 v208, v208, v216, v217\n\t"
                        "v_smfmac_f32_16x16x64_bf16 v[204:207], v[216:219], %[w11], %[ix]\n\t"
                        "s_waitcnt lgkmcnt(7)\n\t"
                        "v_max3_u32 v208, v208, v218, v219\n\t"
                        "v_smfmac_f32_16x16x64_bf16 v[200:203], v[220:223], %[w02], %[ix]\n\t"
                        "v_max3_u32 v208, v208, v220, v221\n\t"
                        "v_smfmac_f32_16x16x64_bf16 v[204:207], v[220:223], %[w12], %[ix]\n\t"
                        "s_waitcnt lgkmcnt(6)\n\t"
                        "v_max3_u32 v208, v208, v222, v223\n\t"
                        "v_smfmac_f32_16x16x64_bf16 v[200:203], v[224:227], %[w03], %[ix]\n\t"
                        "v_max3_u32 v208, v208, v224, v225\n\t"
                        "v_smfmac_f32_16x16x64_bf16 v[204:207], v[224:227], %[w13], %[ix]\n\t"
                        "s_waitcnt lgkmcnt(0)\n\t"
                        "v_max3_u32 v208, v208, v226, v227\n\t"
                        "v_smfmac_f32_16x16x64_bf16 v[200:203], v[228:231], %[w04], %[ix]\n\t"
                        "v_max3_u32 v208, v208, v228, v229\n\t"
                        "v_smfmac_f32_16x16x64_bf16 v[204:207], v[228:231], %[w14], %[ix]\n\t"
                        "v_max3_u32 v208, v208, v230, v231\n\t"
                        "v_smfmac_f32_16x16x64_bf16 v[200:203], v[232:235], %[w05], %[ix]\n\t"
                        "v_max3_u32 v208, v208, v232, v233\n\t"
                        "v_smfmac_f32_16x16x64_bf16 v[204:207], v[232:235], %[w15], %[ix]\n\t"
                        "v_max3_u32 v208, v208, v234, v235\n\t"
                        "v_smfmac_f32_16x16x64_bf16 v[200:203], v[236:239], %[w06], %[ix]\n\t"
                        "v_max3_u32 v208, v208, v236, v237\n\t"
                        "v_smfmac_f32_16x16x64_bf16 v[204:207], v[236:239], %[w16], %[ix]\n\t"
                        "v_max3_u32 v208, v208, v238, v239\n\t"
                        "v_smfmac_f32_16x16x64_bf16 v[200:203], v[240:243], %[w07], %[ix]\n\t"
                        "v_max3_u32 v208, v208, v240, v241\n\t"
                        "v_smfmac_f32_16x16x64_bf16 v[204:207], v[240:243], %[w17], %[ix]\n\t"
                        "v_max3_u32 v208, v208, v242, v243\n\t"
                        "v_smfmac_f32_16x16x64_bf16 v[200:203], v[244:247], %[w08], %[ix]\n\t"
                        "v_max3_u32 v208, v208, v244, v245\n\t"
                        "v_smfmac_f32_16x16x64_bf16 v[204:207], v[244:247], %[w18], %[ix]\n\t"
                        "v_max3_u32 v208, v208, v246, v247\n\t"
                        "v_smfmac_f32_16x16x64_bf16 v[200:203], v[248:251], %[w09], %[ix]\n\t"
                        "v_max3_u32 v208, v208, v248, v249\n\t"
                        "v_smfmac_f32_16x16x64_bf16 v[204:207], v[248:251], %[w19], %[ix]\n\t"
                        "v_max3_u32 v208, v208, v250, v251\n\t"
                        "buffer_load_dword %[sf], %[sl], %[rw], %[sr] offen sc1\n\t"
                        "v_cmp_lt_u32 vcc, %[lim], v208\n\t"
                        "s_nop 3\n\t"
                        "v_pk_add_f32 v[200:201], v[200:201], v[202:203]\n\t"
                        "v_pk_add_f32 v[204:205], v[204:205], v[206:207]\n\t"
                        "v_cvt_pk_bf16_f32 v200, v200, v201\n\t"
                        "v_cvt_pk_bf16_f32 v204, v204, v205\n\t"
                        "ds_write2_b32 %[qw], v200, v204 offset1:32\n\t"
                        MTA(2)
                        "s_cbranch_vccz 1f\n\t"
                        "v_mov_b32 v208, %[it]\n\t"
                        "v_mov_b32 v209, 0\n\t"
                        "ds_write_b32 v209, v208 offset:%[ls]\n\t"
                        "1:\n\t"
                        "s_waitcnt vmcnt(%[nv]) lgkmcnt(0)\n\t"
                        "s_barrier\n\t"
                        "s_waitcnt vmcnt(0)\n\t"
                        : [sf] "=&v"(sf), "+v"(qf_), "+{v[200:203]}"(a0), "+{v[204:207]}"(a1), "+{v208}"(mx)
                        : [mp] "v"(mp0), [qw] "v"(qw), [ix] "v"(sidx), [lim] "s"(lim), [it] "s"(it), [ls] "n"(L_S), [sl] "v"(tk.sl), [sr] "s"(sr), [rw] "s"(rwin), [nv] "n"(TK ? 2 : 1),
                          [w00] "v"(wt[0][0]), [w01] "v"(wt[0][1]), [w02] "v"(wt[0][2]), [w03] "v"(wt[0][3]), [w04] "v"(wt[0][4]), [w05] "v"(wt[0][5]), [w06] "v"(wt[0][6]), [w07] "v"(wt[0][7]), [w08] "v"(wt[0][8]), [w09] "v"(wt[0][9]), [w10] "v"(wt[1][0]), [w11] "v"(wt[1][1]), [w12] "v"(wt[1][2]), [w13] "v"(wt[1][3]), [w14] "v"(wt[1][4]), [w15] "v"(wt[1][5]), [w16] "v"(wt[1][6]), [w17] "v"(wt[1][7]), [w18] "v"(wt[1][8]), [w19] "v"(wt[1][9])
                        : "v209", "v212", "v213", "v214", "v215", "v216", "v217", "v218", "v219", "v220", "v221", "v222", "v223", "v224", "v225", "v226", "v227", "v228", "v229", "v230", "v231", "v232", "v233", "v234", "v235", "v236", "v237", "v238", "v239", "v240", "v241", "v242", "v243", "v244", "v245", "v246", "v247", "v248", "v249", "v250", "v251", "vcc", "memory");
                }
                if (C < 2) {
                    *(LAS unsigned*)qw = hpack(a0); *(LAS unsigned*)(qw + 16) = hpack(a1);
                    SB();
                    MT(2);
                    unsigned vd;
#define SC0 "v[212:215]"
#define SC1 "v[216:219]"
#define SC2 "v[220:223]"
#define DPP(op, d, ctl) op " " d ", " d ", " d " " ctl " row_mask:0xf bank_mask:0xf\n\t"
#define TAIL(A0, A2, B0, B2, C0, C2) \
                        "v_mov_b64 v[212:213], 0\n\tv_mov_b64 v[214:215], 0\n\tv_mov_b64 v[216:217], 0\n\tv_mov_b64 v[218:219], 0\n\tv_mov_b64 v[220:221], 0\n\tv_mov_b64 v[222:223], 0\n\t" \
                        "s_setprio 3\n\t" \
                        "s_waitcnt lgkmcnt(0)\n\t" \
                        "s_barrier\n\t" MTA(3) \
                        "ds_read_b64_tr_b16 v[224:225], %[qa]\n\t" \
                        "ds_read_b64_tr_b16 v[226:227], %[qa] offset:32\n\t" \
                        "ds_read_b64_tr_b16 v[228:229], %[qa] offset:256\n\t" \
                        "ds_read_b64_tr_b16 v[230:231], %[qa] offset:288\n\t" \
                        "ds_read_b64_tr_b16 v[232:233], %[ka]\n\t" \
                        "ds_read_b64_tr_b16 v[234:235], %[ka] offset:128\n\t" \
                        "ds_read_b64_tr_b16 v[236:237], %[ka] offset:256\n\t" \
                        "ds_read_b64_tr_b16 v[238:239], %[ka] offset:384\n\t" \
                        "ds_read_b64_tr_b16 v[240:241], %[ka] offset:512\n\t" \
                        "ds_read_b64_tr_b16 v[242:243], %[ka] offset:640\n\t" \
                        "s_waitcnt lgkmcnt(8)\n\t" \
                        "v_smfmac_f32_16x16x64_bf16 " SC0 ", v[224:227], %[k00], %[ix]\n\t" \
                        "ds_read_b64_tr_b16 v[244:245], %[ka] offset:768\n\t" \
                        "v_smfmac_f32_16x16x64_bf16 " SC1 ", v[224:227], %[k10], %[ix]\n\t" \
                        "ds_read_b64_tr_b16 v[246:247], %[ka] offset:896\n\t" \
                        "s_waitcnt lgkmcnt(8)\n\t" \
                        "v_smfmac_f32_16x16x64_bf16 " SC0 ", v[228:231], %[k01], %[ix]\n\t" \
                        "ds_read_b32 v248, %[vz] offset:%[ls]\n\t" \
                        "v_smfmac_f32_16x16x64_bf16 " SC1 ", v[228:231], %[k11], %[ix]\n\t" \
                        "s_waitcnt lgkmcnt(5)\n\t" \
                        "v_smfmac_f32_16x16x64_bf16 " SC2 ", v[224:227], v[232:239], %[ix]\n\t" \
                        "s_waitcnt lgkmcnt(1)\n\t" \
                        "v_smfmac_f32_16x16x64_bf16 " SC2 ", v[228:231], v[240:247], %[ix]\n\t" \
                        "s_waitcnt lgkmcnt(0)\n\t" \
                        MTA(4) "v_readfirstlane_b32 %[vd], v248\n\t" \
                        "v_add_f32 v232, " A0 ", " A2 "\n\t" \
                        "s_nop 0\n\t" \
                        "v_add_f32 v233, " B0 ", " B2 "\n\t" \
                        "v_max_f32 v235, v232, v233\n\t" \
                        "s_cmp_eq_u32 %[vd], %[it]\n\t" \
                        "s_cselect_b32 %[p0], 2.0, %[p0]\n\t" \
                        DPP("v_max_f32_dpp", "v235", "row_ror:8") \
                        "v_add_f32 v234, " C0 ", " C2 "\n\t" \
                        "v_max_f32 v235, v235, v234\n\t" \
                        "s_add_u32 %[p1], %[p0], 0x4000\n\t" \
                        "s_nop 0\n\t" DPP("v_max_f32_dpp", "v235", "row_ror:4") \
                        "s_nop 1\n\t" DPP("v_max_f32_dpp", "v235", "row_ror:2") \
                        "s_nop 1\n\t" DPP("v_max_f32_dpp", "v235", "row_ror:1") \
                        "v_mul_f32 v235, 0xbe0293ee, v235\n\t" \
                        "v_fmamk_f32 v232, v232, 0x3e0293ee, v235\n\t" \
                        "v_fmamk_f32 v233, v233, 0x3e0293ee, v235\n\t" \
                        "v_exp_f32 v232, v232\n\t" \
                        "v_exp_f32 v233, v233\n\t" \
                        "v_fmamk_f32 v234, v234, 0x3e0293ee, v235\n\t" \
                        "v_add_f32 v235, v232, v233\n\t" \
                        "v_exp_f32 v234, v234\n\t" \
                        "s_nop 0\n\t" DPP("v_add_f32_dpp", "v235", "row_ror:8") \
                        "v_add_f32 v235, v235, v234\n\t" \
                        "v_mov_b32_dpp v236, v234 quad_perm:[1,1,3,3] row_mask:0xf bank_mask:0xf\n\t" \
                        "s_nop 0\n\t" DPP("v_add_f32_dpp", "v235", "row_ror:4") \
                        "s_nop 1\n\t" DPP("v_add_f32_dpp", "v235", "row_ror:2") \
                        "s_nop 1\n\t" DPP("v_add_f32_dpp", "v235", "row_ror:1") \
                        "v_rcp_f32 v235, v235\n\t" \
                        "s_nop 0\n\t" \
                        "v_mul_f32_e64 v232, v232, v235 clamp\n\t" \
                        "v_mul_f32_e64 v233, v233, v235 clamp\n\t" \
                        "v_cvt_pk_bf16_f32 v232, v232, v233\n\t" \
                        MTA(5) "buffer_store_dword v232, %[pa], %[rw], %[p0] offen sc1\n\t" \
                        "v_mul_f32_e64 v234, v234, v235 clamp\n\t" \
                        "v_mul_f32_e64 v236, v236, v235 clamp\n\t" \
                        "buffer_store_dword v232, %[pa], %[rw], %[p1] offen sc1\n\t" \
                        "v_cvt_pk_bf16_f32 v234, v234, v236\n\t" \
                        "buffer_store_dword v234, %[pm], %[rw], %[p0] offen sc1\n\t" \
                        "s_setprio 0"
#define TAILIO : [vd] "=&s"(vd), [p0] "+s"(ps), [p1] "=&s"(ps1) \
                        : [qa] "v"(qa), [ka] "v"(ka), [vz] "v"(0u), [ls] "n"(L_S), [ix] "v"(sidx), [it] "s"(it), [pa] "v"(pa0), [pm] "v"(pa2), [rw] "s"(rwin), \
                          [k00] "v"(kx[0][0]), [k01] "v"(kx[0][1]), [k10] "v"(kx[1][0]), [k11] "v"(kx[1][1]) \
                        : "v212", "v213", "v214", "v215", "v216", "v217", "v218", "v219", "v220", "v221", "v222", "v223", "v224", "v225", "v226", "v227", "v228", "v229", "v230", "v231", "v232", \
                          "v233", "v234", "v235", "v236", "v237", "v238", "v239", "v240", "v241", "v242", "v243", "v244", "v245", "v246", "v247", "v248", "scc", "memory"
                    unsigned ps = pb, ps1;
                    if (R == 0) asm volatile(TAIL("v212", "v214", "v216", "v218", "v220", "v222") TAILIO);
                    else asm volatile(TAIL("v213", "v215", "v217", "v219", "v221", "v223") TAILIO);
                    redo = vd == it;
                    RT(4);
                    MT(6);
                } else {
                    MT(3);
                    if (TK) { __builtin_amdgcn_s_setprio(TKP); if (TKS) __builtin_amdgcn_s_sleep(TKS); }
                    redo = __builtin_amdgcn_readfirstlane(*(volatile LAS unsigned*)(smem + L_S)) == it;
                    if (redo) FC(fc0);
                }
            } while (__builtin_expect(redo, 0));
            if (s < 8) SST(8 + s);
            if (s >= 56) SST(s - 40);
            if (C + R == 1) {
                if (s + layer > 0) {
                    __builtin_amdgcn_s_sleep(DDMA);
                    unsigned l2 = fresh_lane(), s1;
                    asm volatile("v_mov_b32 %0, -1" : "=v"(s1));
                    const unsigned base = p_slot(s - 1) + nh;
                    asm volatile("s_waitcnt vmcnt(3)" ::: "memory");
                    st128(((u32x4){s1, s1, s1, s1}), rwin, (l2 >> 5) * PAIR + (l2 & 31u) * 16u, base + (unsigned)(2 * (hh >> 3)) * PAIR + (unsigned)(R * 4096 + (hh & 7) * 512), 16);
                    if (R) __builtin_amdgcn_raw_buffer_store_b64((u32x2){s1, s1}, rwin, (l2 >> 5) * PAIR + (l2 & 16u) * 256u + (l2 & 8u) * 16u + (l2 & 7u) * 8u, base + 8u * PAIR + (unsigned)((hh >> 1) * 256 + (hh & 1) * 64), 16);
                    else st128(((u32x4){s1, s1, s1, s1}), rwin, two(), W_T + (unsigned)((s + 2) & 3) * 2u * PAIR + (unsigned)hh * 128u, 16);
                }
            } else if (TK) {
                if (s + 1 < NSEG) sched2(tk, s, sf);
            } else if (C == 2 && w == 4) {
                if (s + 2 < NSEG) {
                    auto done = [&](int f) INL {
                        f = min(f, __builtin_amdgcn_update_dpp(f, f, 0xB1, 0xF, 0xF, false)); f = min(f, __builtin_amdgcn_update_dpp(f, f, 0x4E, 0xF, 0xF, false));
                        f = min(f, __builtin_amdgcn_update_dpp(f, f, 0x141, 0xF, 0xF, false));
                        return seg_done(MXQ, __builtin_amdgcn_readlane(f, 0), __builtin_amdgcn_readlane(f, 8));
                    };
                    qrdy = max(qrdy, done(qf_));
                    const int sq = s || !LA1 ? s + 2 : 1;
                    if (__builtin_expect(s == 0, 0)) __builtin_amdgcn_s_sleep(DQ0);
                    if (__builtin_expect(qrdy < sq, 0)) for (unsigned spins = 0;;) {
                        asm volatile("" ::: "memory");
                        const int f = (int)__builtin_amdgcn_raw_buffer_load_b32(rwin, (unsigned)((lane & 8) * 512 + (lane & 7) * 4), W_FQ + fo + (unsigned)hh * 128u, 16);
                        qrdy = done(f);
                        if (qrdy >= sq) break;
                        FC(fc2);
                        if (spin_fail(spins, rwin)) break;
                        __builtin_amdgcn_s_sleep(QSL);
                    }
                    const unsigned so = O_QK + (unsigned)(((s + 2) * H_ + hh) * 16384), lo = (unsigned)(L_TOK + (tbo ? tbo - 16384 : 32768)), l2 = fresh_lane();
                    if (__builtin_expect(s <= LA1, 0)) {
                        for (int i = 0; i < 4; ++i) dma4k(rws, l2 * 16u, so - H_ * 16384u + i * 4096u, L_TOK + (s + 1) * 16384u + i * 4096u);
                    }
                    if (__builtin_expect(s >= LA1, 1)) {
#pragma unroll
                        for (int i = 0; i < 4; ++i) dma4k(rws, l2 * 16u, so + i * 4096u, lo + i * 4096u);
                        if (__builtin_expect(s == LA1, 0)) asm volatile("s_waitcnt vmcnt(24)" ::: "memory");
                    } else asm volatile("s_waitcnt vmcnt(8)" ::: "memory");
                    if (qrdy < min(s + 5, NSEG - 1))
                        asm volatile("s_nop 4\n\tbuffer_load_dword %0, %1, %2, %3 offen sc1" : "+v"(qf_) : "v"((l2 & 8u) * 512u + (l2 & 7u) * 4u), "s"(rwin), "s"(W_FQ + fo + (unsigned)hh * 128u) : "memory");
                }
            } else {
                asm volatile("s_waitcnt vmcnt(3)" ::: "memory");
                if (s > TAILS) __builtin_amdgcn_raw_buffer_store_b32((unsigned)s - 1u, rwin, 0u, W_FPT + fo + (unsigned)(hh * 128 + (w & 3) * 4), 16);
                if (C == 2 && s + 1 < NSEG) __builtin_amdgcn_s_sleep(DPT);
                const float scale = 0.08838834764831845f, l2e = 1.44269504088896340736f * (NORND ? scale : 1.f);
                const int tq = (w >> 1) & 1, qh = w & 1;
                f32x4 sc[3] = {z4, z4, z4};
                u32x4 kt[2][4], qf[4], kf[4];
                const unsigned l2 = fresh_lane(), kto = ((l2 & 8u) * 512u + (l2 & 51u) * 16u + (l2 & 4u) * 32u) ^ ((l2 & 16u) * 5u), qto = ((l2 & 55u) * 16u + qh * 128u) ^ ((l2 & 16u) * 8u);
#pragma unroll
                for (int ks = 0; ks < 4; ++ks) {
                    qf[ks] = *(const u32x4*)(tb + 8192 + (tq * 4 + ks) * 1024 + qto);
                    kt[0][ks] = *(const u32x4*)(tb + ks * 1024 + kto);
                    kt[1][ks] = *(const u32x4*)(tb + ks * 1024 + (kto ^ 64u));
                    SB();
                }
#pragma unroll
                for (int ks = 0; ks < 4; ++ks) {
                    sc[0] = mfma32(kt[0][ks], qf[ks], sc[0]); PIN1(sc[0]);
                    kf[ks] = ldtr2(ka + ks * 256, ka + ks * 256 + 128);
                    SB();
                    sc[1] = mfma32(kt[1][ks], qf[ks], sc[1]); PIN1(sc[1]);
                }
#pragma unroll
                for (int ks = 0; ks < 4; ++ks) sc[2] = mfma32(kf[ks], qf[ks], sc[2]);
                const bool hi = (l2 & 8u) != 0u;
                float v[6];
#pragma unroll
                for (int r = 0; r < 4; ++r) v[r] = hi ? sc[1][r] : sc[0][r];
                v[4] = hi ? sc[2][2] : sc[2][0]; v[5] = hi ? sc[2][3] : sc[2][1];
#pragma unroll
                for (int r = 0; r < 6; r += 2) {
                    if (NORND) break;
                    const unsigned b0 = pack2(v[r], v[r + 1]);
                    const unsigned b1 = pack2(lo2f(b0) * scale, hi2f(b0) * scale);
                    v[r] = lo2f(b1); v[r + 1] = hi2f(b1);
                }
                const int lim = 8 * (w & 3) + (int)(l2 & 7u) - 8 * (int)(l2 >> 4) - (int)((l2 & 8u) >> 1);
#pragma unroll
                for (int r = 0; r < 4; ++r) if (r > lim) v[r] = NORND ? -1000000000.0f / scale : bfr(-1000000000.0f);
                float mx = fmx(fmx(fmx(fmx(fmx(v[0], v[1]), v[2]), v[3]), v[4]), v[5]);
                mx = xmax3216(fmx(mx, ror8f(mx)));
                const float mb = -mx * l2e;
#pragma unroll
                for (int r = 0; r < 6; ++r) v[r] = __builtin_amdgcn_exp2f(fmaf(v[r], l2e, mb));
                float sum = fmaf(v[4] + v[5], 0.5f, (v[0] + v[1]) + (v[2] + v[3]));
                sum = xsum3216(sum + ror8f(sum));
                const float inv = __builtin_amdgcn_rcpf(sum);
                const unsigned p0 = pack2(v[0] * inv, v[1] * inv), p1 = nosent1(pack2(v[2] * inv, v[3] * inv)), p2 = nosent1(pack2(v[4] * inv, v[5] * inv));
                const unsigned base = (s + 2 < NSEG ? O_PT : W_PL + fo - (NSEG - 2) * PTSEG * 2u) + (unsigned)(s * PTSEG) * 2u;
                const __amdgpu_buffer_rsrc_t rp = s + 2 < NSEG ? rws : rwin;
                const unsigned oob = (l2 & 8u) << 27;
                st128(((u32x4){p0, p1, dppu<0x128>(p0), dppu<0x128>(p1)}), rp, (l2 * 16u) | oob, base + (unsigned)((hh * 2 + tq) * 1024 + qh * 128), 16);
                __builtin_amdgcn_raw_buffer_store_b64((u32x2){p2, dppu<0x128>(p2)}, rp, ((l2 & 7u) * 16u + (l2 >> 4) * 8u) | oob | (l2 & 32u) << 25, base + 65536u + (unsigned)((tq * 32 + hh) * 256 + qh * 128), 16);
            }
            tbo = tbo == 32768 ? 0 : tbo + 16384;
            if (C == 2 && __builtin_expect(budget <= 0, 0)) set_tmo(rwin);
            RT(5);
            RTS();
            MT(7);
            MTS();
        }
        };
        if (w) gather_mem0(memL, mytid() - 64, 448);
        else *(unsigned*)(smem + L_S) = 0u;
        if (layer == 0) near_init();
        qt_role(wt, hh, IC<0>());
        if (w == 7) { ck.ta = 4u * (unsigned)__builtin_amdgcn_s_memrealtime() + 8u; ck.ma = (unsigned)__builtin_amdgcn_s_memtime(); }
        if (w == 0) steps(IC<0>()); else if (w == 1) steps(IC<1>()); else if (w == 2) steps(IC<2>()); else if (w == 3) steps(IC<3>()); else if (w < 7) steps(IC<4>()); else steps(IC<5>());
    };
    auto own_role = [&]() INL {
        const unsigned long long tstart = __builtin_amdgcn_s_memtime();
        if (layer == 0) near_init();
        LANES();
        const int o = bid - 96, hg = (w + ROT * o) & 3, ot = tile_of(o);
        const unsigned ztoff = (unsigned)((o * H_ + 8 * hg) * 64 + lane) * 16u;
        const unsigned zoff = zpair(o / 10, o % 10) + (unsigned)(lane * 16);
        const unsigned poff = (2u * hg + (mc & 1u)) * PAIR + nh + (mc >> 1) * 4096u + (unsigned)((fq >> 1) * 512 + (fr >> 2) * 128 + (fr & 1) * 64 + (fq & 1) * 32 + (fr & 2) * 8);
        const unsigned pmoff = (8u + (mc & 1u)) * PAIR + nh + (mc >> 1) * 4096u + (unsigned)(fq * 256 + (fr >> 3) * 128 + (fr & 2) * 32 + (fr & 4) * 8 + (fr & 1) * 16);
        const unsigned moff = (unsigned)(ot >> 4) * 2u * PAIR + nh + (unsigned)((ot & 15) * 256 + lane * 4);
        constexpr unsigned mc2 = 4096u;
        char* op = smem + 8192;
        if (w == 4 && lane < 2) *(volatile LAS int*)(smem + 4096 + lane * 4) = -1;
        BAR();
        SST(2);
        if (w == 7) clk_init(ck);
        wait_until(tstart + DO0);
        if (w < 4) {
            u32x8 zf[4];
            int zr = -1;
            unsigned zb = W_ZM + (unsigned)layer * (NOWN * PAIR), zso = O_Z - (unsigned)layer * (unsigned)(ZTSEG * 2);
            const unsigned ra = (unsigned)((ot >> 4) * 2 + (fq & 1)) * PAIR + nh + (unsigned)((fq >> 1) * 4096 + (ot & 15) * 256 + fr * 16);
            int np = 0;
            auto piece = [&]() INL {
                asm volatile("s_mov_b32 m0, %3\n\ts_nop 3\n\tbuffer_load_dwordx4 %0, %1, %2 offen sc1 lds\n\tbuffer_load_dwordx4 %0, %1, %2 offen offset:1024 sc1 lds"
                             :: "v"(ztoff), "s"(rws), "s"(O_Z + (unsigned)((NSEG - 1 - layer) * (int)ZTSEG) * 2u + np * 2048u), "s"(32768 + hg * 8192 + np * 2048) : "m0", "memory");
                ++np;
            };
            for (int s = 0; s + 1 < NSEG; ++s) {
                const unsigned pb = p_slot(s);
                const int sz = s;
                while (__builtin_expect(zr < sz, 0)) {
                    __builtin_amdgcn_s_sleep(1);
                    zr = __builtin_amdgcn_readfirstlane(*(volatile LAS int*)(smem + 4096));
                }
                if (s == 0) SST(1);
                asm volatile(
                    "1:\n\t"
                    "buffer_load_dwordx4 v[160:163], %[zt], %[rs], %[so] offen sc1\n\t"
                    "buffer_load_dwordx4 v[164:167], %[zt], %[rs], %[so] offen offset:1024 sc1\n\t"
                    "buffer_load_dwordx4 v[168:171], %[zt], %[rs], %[so] offen offset:2048 sc1\n\t"
                    "buffer_load_dwordx4 v[172:175], %[zt], %[rs], %[so] offen offset:3072 sc1\n\t"
                    "buffer_load_dwordx4 v[176:179], %[zt], %[rs], %[s4] offen sc1\n\t"
                    "buffer_load_dwordx4 v[180:183], %[zt], %[rs], %[s4] offen offset:1024 sc1\n\t"
                    "buffer_load_dwordx4 v[184:187], %[zt], %[rs], %[s4] offen offset:2048 sc1\n\t"
                    "buffer_load_dwordx4 v[188:191], %[zt], %[rs], %[s4] offen offset:3072 sc1\n\t"
                    "s_cmp_lg_u32 %[s], 0\n\t"
                    "s_cbranch_scc1 2f\n\t"
                    "s_waitcnt vmcnt(0)\n\t"
                    "v_max3_u32 v100, v161, v163, v165\n\t"
                    "v_max3_u32 v100, v100, v167, v169\n\t"
                    "v_max3_u32 v100, v100, v171, v173\n\t"
                    "v_max3_u32 v100, v100, v175, v177\n\t"
                    "v_max3_u32 v100, v100, v179, v181\n\t"
                    "v_max3_u32 v100, v100, v183, v185\n\t"
                    "v_max3_u32 v100, v100, v187, v189\n\t"
                    "v_max_u32 v100, v100, v191\n\t"
                    "v_cmp_eq_u32 vcc, -1, v100\n\t"
                    "s_sub_i32 %[tr], %[tr], 1\n\t"
                    "s_cbranch_vccz 2f\n\t"
                    "s_cmp_gt_i32 %[tr], 0\n\t"
                    "s_cbranch_scc1 1b\n\t"
                    "2:"
                    : "=&{v[160:167]}"(zf[0]), "=&{v[168:175]}"(zf[1]), "=&{v[176:183]}"(zf[2]), "=&{v[184:191]}"(zf[3]), [tr] "+s"(budget)
                    : [zt] "v"(ztoff), [rs] "s"(rws), [so] "s"(zso), [s4] "s"(zso + 4096u), [s] "s"(s) : "v100", "vcc", "scc", "memory");
                if (s == 0) SST(3);
                zso += (unsigned)(ZTSEG * 2);
                if (zr == NSEG - 1 && np < 4) piece();
                if (__builtin_expect(budget <= 0, 0)) set_tmo(rwin);
                RT(6);
                const unsigned spa = (unsigned)((s & 1) * 2048 + (w < 3 ? w * 512 : 0) + lane * 8);
#define OST(r)
                unsigned long long t0_, t1_, t2_, t3_, t4_ = 0, bad;
                int tries = budget;
                const unsigned sw = (unsigned)(((s & 3) + 1) & 4) * 1024u;
                unsigned po = poff ^ sw, pm = pmoff ^ sw, x0 = 4096u, x1 = 4096u;
                const unsigned mb = W_MEM + (unsigned)((s + 1) & 3) * 6u * PAIR;
                if (w < 3) asm volatile(
                    ALN
                    "s_barrier\n\t"
                    OST("%[t0]")
                    "v_mov_b64 v[96:97], 0\n\t"
                    "v_mov_b64 v[98:99], 0\n\t"
                    "v_mov_b32 v100, 0\n\t"
                    "s_barrier\n\t"
                    "1:\n\t"
                    "buffer_load_dwordx4 v[208:211], %[po], %[rw], %[pb] offen sc1\n\t"
                    "buffer_load_dwordx4 v[212:215], %[po], %[rw], %[pb] offen offset:1024 sc1\n\t"
                    "buffer_load_dwordx4 v[216:219], %[po], %[rw], %[pb] offen offset:2048 sc1\n\t"
                    "buffer_load_dwordx4 v[220:223], %[po], %[rw], %[pb] offen offset:3072 sc1\n\t"
                    OST("%[t1]")
                    "s_waitcnt vmcnt(3)\n\t"
                    OST("%[t3]")
                    "v_smfmac_f32_16x16x64_bf16 v[96:99], v[208:211], v[160:167], %[ix]\n\t"
                    "v_max3_u32 v100, v100, v208, v209\n\t"
                    "v_max3_u32 v100, v100, v210, v211\n\t"
                    "s_waitcnt vmcnt(2)\n\t"
                    "v_smfmac_f32_16x16x64_bf16 v[96:99], v[212:215], v[168:175], %[ix]\n\t"
                    "v_max3_u32 v100, v100, v212, v213\n\t"
                    "v_max3_u32 v100, v100, v214, v215\n\t"
                    "s_waitcnt vmcnt(1)\n\t"
                    "v_smfmac_f32_16x16x64_bf16 v[96:99], v[216:219], v[176:183], %[ix]\n\t"
                    "v_max3_u32 v100, v100, v216, v217\n\t"
                    "v_max3_u32 v100, v100, v218, v219\n\t"
                    "s_waitcnt vmcnt(0)\n\t"
                    "v_smfmac_f32_16x16x64_bf16 v[96:99], v[220:223], v[184:191], %[ix]\n\t"
                    "v_max3_u32 v100, v100, v220, v221\n\t"
                    "v_max3_u32 v100, v100, v222, v223\n\t"
                    "v_cmp_eq_u32 vcc, -1, v100\n\t"
                    OST("%[t2]")
                    "s_nop 4\n\t"
                    "v_pk_add_f32 v[96:97], v[96:97], v[98:99]\n\t"
                    "ds_write_b64 %[sp], v[96:97]\n\t"
                    "s_cbranch_vccz 2f\n\t"
                    "s_sub_i32 %[tr], %[tr], 1\n\t"
                    "s_cmp_lt_i32 %[tr], 1\n\t"
                    "s_cbranch_scc1 2f\n\t"
                    "v_xor_b32 %[po], %[x0], %[po]\n\t"
                    "s_xor_b32 %[x0], %[x0], 0x5000\n\t"
                    "s_waitcnt lgkmcnt(0)\n\t"
                    "v_mov_b64 v[96:97], 0\n\t"
                    "v_mov_b64 v[98:99], 0\n\t"
                    "v_mov_b32 v100, 0\n\t"
                    "s_branch 1b\n\t"
                    "2:\n\t"
                    "s_waitcnt lgkmcnt(0)\n\t"
                    "s_barrier\n\t"
                    "s_sleep 3\n\t"
                    : [tr] "+s"(tries), [po] "+v"(po), [x0] "+s"(x0), [t0] "=&s"(t0_), [t1] "=&s"(t1_), [t2] "=&s"(t2_), [t3] "=&s"(t3_)
                    : [sp] "v"(spa), [ix] "v"(sidx), [rw] "s"(rwin), [pb] "s"(pb), "{v[160:167]}"(zf[0]), "{v[168:175]}"(zf[1]), "{v[176:183]}"(zf[2]), "{v[184:191]}"(zf[3])
                    : "v96", "v97", "v98", "v99", "v100", "v208", "v209", "v210", "v211", "v212", "v213", "v214", "v215", "v216", "v217", "v218", "v219", "v220", "v221", "v222", "v223", "vcc", "scc", "memory");
                else asm volatile(
                    ALN
                    "s_barrier\n\t"
                    "buffer_load_dwordx4 v[128:131], %[zo], %[rw], %[zb] offen sc1\n\t"
                    "buffer_load_dwordx4 v[132:135], %[zo], %[rw], %[zb] offen offset:1024 sc1\n\t"
                    "buffer_load_dwordx4 v[136:139], %[zo], %[rw], %[zb] offen offset:2048 sc1\n\t"
                    "buffer_load_dwordx4 v[140:143], %[zo], %[rw], %[zb] offen offset:3072 sc1\n\t"
                    "buffer_load_dwordx4 v[144:147], %[zo], %[rw], %[z4] offen sc1\n\t"
                    "buffer_load_dwordx4 v[148:151], %[zo], %[rw], %[z4] offen offset:1024 sc1\n\t"
                    "buffer_load_dwordx4 v[152:155], %[zo], %[rw], %[z4] offen offset:2048 sc1\n\t"
                    "buffer_load_dwordx4 v[156:159], %[zo], %[rw], %[z4] offen offset:3072 sc1\n\t"
                    OST("%[t0]")
                    "v_mov_b64 v[96:97], 0\n\t"
                    "v_mov_b64 v[98:99], 0\n\t"
                    "v_mov_b32 v100, 0\n\t"
                    "s_barrier\n\t"
                    "s_sleep %[fd]\n\t"
                    "buffer_load_dwordx4 v[208:211], %[po], %[rw], %[pb] offen sc1\n\t"
                    "buffer_load_dwordx4 v[212:215], %[po], %[rw], %[pb] offen offset:1024 sc1\n\t"
                    "buffer_load_dwordx4 v[216:219], %[po], %[rw], %[pb] offen offset:2048 sc1\n\t"
                    "buffer_load_dwordx4 v[220:223], %[po], %[rw], %[pb] offen offset:3072 sc1\n\t"
                    "buffer_load_dwordx4 v[224:227], %[pm], %[rw], %[pb] offen sc1\n\t"
                    "buffer_load_dwordx4 v[228:231], %[pm], %[rw], %[pb] offen offset:1024 sc1\n\t"
                    "buffer_load_dwordx4 v[232:235], %[pm], %[rw], %[pb] offen offset:2048 sc1\n\t"
                    "buffer_load_dwordx4 v[236:239], %[pm], %[rw], %[pb] offen offset:3072 sc1\n\t"
                    OST("%[t1]")
                    "s_waitcnt vmcnt(8)\n\t"
                    "v_max3_u32 v101, v129, v131, v133\n\t"
                    "v_max3_u32 v101, v101, v135, v137\n\t"
                    "v_max3_u32 v101, v101, v139, v141\n\t"
                    "v_max3_u32 v101, v101, v143, v145\n\t"
                    "v_max3_u32 v101, v101, v147, v149\n\t"
                    "v_max3_u32 v101, v101, v151, v153\n\t"
                    "v_max3_u32 v101, v101, v155, v157\n\t"
                    "v_max_u32 v101, v101, v159\n\t"
                    "v_cmp_eq_u32 vcc, -1, v101\n\t"
                    "s_cbranch_vccz 3f\n\t"
                    "buffer_load_dwordx4 v[128:131], %[zo], %[rw], %[zb] offen sc1\n\t"
                    "buffer_load_dwordx4 v[132:135], %[zo], %[rw], %[zb] offen offset:1024 sc1\n\t"
                    "buffer_load_dwordx4 v[136:139], %[zo], %[rw], %[zb] offen offset:2048 sc1\n\t"
                    "buffer_load_dwordx4 v[140:143], %[zo], %[rw], %[zb] offen offset:3072 sc1\n\t"
                    "buffer_load_dwordx4 v[144:147], %[zo], %[rw], %[z4] offen sc1\n\t"
                    "buffer_load_dwordx4 v[148:151], %[zo], %[rw], %[z4] offen offset:1024 sc1\n\t"
                    "buffer_load_dwordx4 v[152:155], %[zo], %[rw], %[z4] offen offset:2048 sc1\n\t"
                    "buffer_load_dwordx4 v[156:159], %[zo], %[rw], %[z4] offen offset:3072 sc1\n\t"
                    "s_waitcnt vmcnt(0)\n\t"
                    "v_max3_u32 v101, v129, v131, v133\n\t"
                    "v_max3_u32 v101, v101, v135, v137\n\t"
                    "v_max3_u32 v101, v101, v139, v141\n\t"
                    "v_max3_u32 v101, v101, v143, v145\n\t"
                    "v_max3_u32 v101, v101, v147, v149\n\t"
                    "v_max3_u32 v101, v101, v151, v153\n\t"
                    "v_max3_u32 v101, v101, v155, v157\n\t"
                    "v_max_u32 v101, v101, v159\n\t"
                    "v_mov_b32 v100, v101\n\t"
                    "3:\n\t"
                    "v_permlane16_swap_b32 v128, v129\n\t"
                    "v_permlane16_swap_b32 v130, v131\n\t"
                    "v_permlane16_swap_b32 v132, v133\n\t"
                    "v_permlane16_swap_b32 v134, v135\n\t"
                    "v_permlane16_swap_b32 v136, v137\n\t"
                    "v_permlane16_swap_b32 v138, v139\n\t"
                    "v_permlane16_swap_b32 v140, v141\n\t"
                    "v_permlane16_swap_b32 v142, v143\n\t"
                    "v_permlane16_swap_b32 v144, v145\n\t"
                    "v_permlane16_swap_b32 v146, v147\n\t"
                    "v_permlane16_swap_b32 v148, v149\n\t"
                    "v_permlane16_swap_b32 v150, v151\n\t"
                    "v_permlane16_swap_b32 v152, v153\n\t"
                    "v_permlane16_swap_b32 v154, v155\n\t"
                    "v_permlane16_swap_b32 v156, v157\n\t"
                    "v_permlane16_swap_b32 v158, v159\n\t"
                    "s_mov_b32 vcc_lo, 0x00ff00ff\n\t"
                    "s_mov_b32 vcc_hi, 0x00ff00ff\n\t"
                    "s_waitcnt vmcnt(7)\n\t"
                    OST("%[t3]")
                    "v_smfmac_f32_16x16x64_bf16 v[96:99], v[208:211], v[160:167], %[ix]\n\t"
                    "v_max3_u32 v100, v100, v208, v209\n\t"
                    "v_max3_u32 v100, v100, v210, v211\n\t"
                    "s_waitcnt vmcnt(6)\n\t"
                    "v_smfmac_f32_16x16x64_bf16 v[96:99], v[212:215], v[168:175], %[ix]\n\t"
                    "v_max3_u32 v100, v100, v212, v213\n\t"
                    "v_max3_u32 v100, v100, v214, v215\n\t"
                    "s_waitcnt vmcnt(5)\n\t"
                    "v_smfmac_f32_16x16x64_bf16 v[96:99], v[216:219], v[176:183], %[ix]\n\t"
                    "v_max3_u32 v100, v100, v216, v217\n\t"
                    "v_max3_u32 v100, v100, v218, v219\n\t"
                    "s_waitcnt vmcnt(4)\n\t"
                    "v_smfmac_f32_16x16x64_bf16 v[96:99], v[220:223], v[184:191], %[ix]\n\t"
                    "v_max3_u32 v100, v100, v220, v221\n\t"
                    "v_max3_u32 v100, v100, v222, v223\n\t"
                    "s_waitcnt vmcnt(3)\n\t"
                    "v_smfmac_f32_16x16x64_bf16 v[96:99], v[224:227], v[128:135], %[ix]\n\t"
                    "v_max3_u32 v100, v100, v224, v225\n\t"
                    "v_max3_u32 v100, v100, v226, v227\n\t"
                    "s_waitcnt vmcnt(2)\n\t"
                    "v_smfmac_f32_16x16x64_bf16 v[96:99], v[228:231], v[136:143], %[ix]\n\t"
                    "s_barrier\n\t"
                    "ds_read_b64 v[102:103], %[sp]\n\t"
                    "ds_read_b64 v[104:105], %[sp] offset:512\n\t"
                    "ds_read_b64 v[106:107], %[sp] offset:1024\n\t"
                    "v_max3_u32 v100, v100, v228, v229\n\t"
                    "v_max3_u32 v100, v100, v230, v231\n\t"
                    "s_waitcnt vmcnt(1)\n\t"
                    "v_smfmac_f32_16x16x64_bf16 v[96:99], v[232:235], v[144:151], %[ix]\n\t"
                    "v_max3_u32 v100, v100, v232, v233\n\t"
                    "v_max3_u32 v100, v100, v234, v235\n\t"
                    "s_waitcnt vmcnt(0)\n\t"
                    "v_smfmac_f32_16x16x64_bf16 v[96:99], v[236:239], v[152:159], %[ix]\n\t"
                    "v_max3_u32 v100, v100, v236, v237\n\t"
                    "v_max3_u32 v100, v100, v238, v239\n\t"
                    OST("%[t2]")
                    "v_cmp_eq_u32 %[bad], -1, v100\n\t"
                    "s_waitcnt lgkmcnt(0)\n\t"
                    "v_add_f32 v102, v102, v104\n\t"
                    "v_add_f32 v103, v103, v105\n\t"
                    "v_add_f32 v102, v102, v106\n\t"
                    "v_add_f32 v103, v103, v107\n\t"
                    "v_add_f32 v96, v96, v102\n\t"
                    "v_add_f32 v97, v97, v103\n\t"
                    "v_add_f32 v96, v96, v98\n\t"
                    "v_add_f32 v97, v97, v99\n\t"
                    "s_cmp_lg_u64 %[bad], 0\n\t"
                    "s_cbranch_scc1 4f\n\t"
                    "2:\n\t"
                    "v_cndmask_b32_dpp v98, v97, v96, vcc row_ror:8 row_mask:0xf bank_mask:0xf\n\t"
                    "s_not_b64 vcc, vcc\n\t"
                    "v_cndmask_b32_dpp v99, v96, v97, vcc row_ror:8 row_mask:0xf bank_mask:0xf\n\t"
                    "v_cvt_pk_bf16_f32 v98, v98, v99\n\t"
                    "v_min_u32 v98, -2, v98\n\t"
                    "buffer_store_dword v98, %[mo], %[rw], %[m0] offen sc1\n\t"
                    "buffer_store_dword v98, %[mo], %[rw], %[m1] offen sc1\n\t"
                    "buffer_store_dword v98, %[mo], %[rw], %[m2] offen sc1\n\t"
                    "buffer_store_dword v98, %[mo], %[rw], %[m3] offen sc1\n\t"
                    OST("%[t4]")
                    "s_branch 5f\n\t"
                    "4:\n\t"
                    "s_sub_i32 %[tr], %[tr], 1\n\t"
                    "s_cmp_lt_i32 %[tr], 1\n\t"
                    "s_cbranch_scc1 2b\n\t"
                    "v_xor_b32 %[po], %[x0], %[po]\n\t"
                    "s_xor_b32 %[x0], %[x0], 0x5000\n\t"
                    "v_xor_b32 %[pm], %[x1], %[pm]\n\t"
                    "s_xor_b32 %[x1], %[x1], 0x5000\n\t"
                    "buffer_load_dwordx4 v[128:131], %[zo], %[rw], %[zb] offen sc1\n\t"
                    "buffer_load_dwordx4 v[132:135], %[zo], %[rw], %[zb] offen offset:1024 sc1\n\t"
                    "buffer_load_dwordx4 v[136:139], %[zo], %[rw], %[zb] offen offset:2048 sc1\n\t"
                    "buffer_load_dwordx4 v[140:143], %[zo], %[rw], %[zb] offen offset:3072 sc1\n\t"
                    "buffer_load_dwordx4 v[144:147], %[zo], %[rw], %[z4] offen sc1\n\t"
                    "buffer_load_dwordx4 v[148:151], %[zo], %[rw], %[z4] offen offset:1024 sc1\n\t"
                    "buffer_load_dwordx4 v[152:155], %[zo], %[rw], %[z4] offen offset:2048 sc1\n\t"
                    "buffer_load_dwordx4 v[156:159], %[zo], %[rw], %[z4] offen offset:3072 sc1\n\t"
                    "buffer_load_dwordx4 v[208:211], %[po], %[rw], %[pb] offen sc1\n\t"
                    "buffer_load_dwordx4 v[212:215], %[po], %[rw], %[pb] offen offset:1024 sc1\n\t"
                    "buffer_load_dwordx4 v[216:219], %[po], %[rw], %[pb] offen offset:2048 sc1\n\t"
                    "buffer_load_dwordx4 v[220:223], %[po], %[rw], %[pb] offen offset:3072 sc1\n\t"
                    "buffer_load_dwordx4 v[224:227], %[pm], %[rw], %[pb] offen sc1\n\t"
                    "buffer_load_dwordx4 v[228:231], %[pm], %[rw], %[pb] offen offset:1024 sc1\n\t"
                    "buffer_load_dwordx4 v[232:235], %[pm], %[rw], %[pb] offen offset:2048 sc1\n\t"
                    "buffer_load_dwordx4 v[236:239], %[pm], %[rw], %[pb] offen offset:3072 sc1\n\t"
                    "v_mov_b64 v[96:97], 0\n\t"
                    "v_mov_b64 v[98:99], 0\n\t"
                    "v_mov_b32 v100, 0\n\t"
                    "s_waitcnt vmcnt(0)\n\t"
                    "v_max3_u32 v101, v129, v131, v133\n\t"
                    "v_max3_u32 v101, v101, v135, v137\n\t"
                    "v_max3_u32 v101, v101, v139, v141\n\t"
                    "v_max3_u32 v101, v101, v143, v145\n\t"
                    "v_max3_u32 v101, v101, v147, v149\n\t"
                    "v_max3_u32 v101, v101, v151, v153\n\t"
                    "v_max3_u32 v101, v101, v155, v157\n\t"
                    "v_max_u32 v101, v101, v159\n\t"
                    "v_mov_b32 v100, v101\n\t"
                    "v_permlane16_swap_b32 v128, v129\n\t"
                    "v_permlane16_swap_b32 v130, v131\n\t"
                    "v_permlane16_swap_b32 v132, v133\n\t"
                    "v_permlane16_swap_b32 v134, v135\n\t"
                    "v_permlane16_swap_b32 v136, v137\n\t"
                    "v_permlane16_swap_b32 v138, v139\n\t"
                    "v_permlane16_swap_b32 v140, v141\n\t"
                    "v_permlane16_swap_b32 v142, v143\n\t"
                    "v_permlane16_swap_b32 v144, v145\n\t"
                    "v_permlane16_swap_b32 v146, v147\n\t"
                    "v_permlane16_swap_b32 v148, v149\n\t"
                    "v_permlane16_swap_b32 v150, v151\n\t"
                    "v_permlane16_swap_b32 v152, v153\n\t"
                    "v_permlane16_swap_b32 v154, v155\n\t"
                    "v_permlane16_swap_b32 v156, v157\n\t"
                    "v_permlane16_swap_b32 v158, v159\n\t"
                    "s_nop 1\n\t"
                    "v_smfmac_f32_16x16x64_bf16 v[96:99], v[208:211], v[160:167], %[ix]\n\t"
                    "v_max3_u32 v100, v100, v208, v209\n\t"
                    "v_max3_u32 v100, v100, v210, v211\n\t"
                    "v_smfmac_f32_16x16x64_bf16 v[96:99], v[212:215], v[168:175], %[ix]\n\t"
                    "v_max3_u32 v100, v100, v212, v213\n\t"
                    "v_max3_u32 v100, v100, v214, v215\n\t"
                    "v_smfmac_f32_16x16x64_bf16 v[96:99], v[216:219], v[176:183], %[ix]\n\t"
                    "v_max3_u32 v100, v100, v216, v217\n\t"
                    "v_max3_u32 v100, v100, v218, v219\n\t"
                    "v_smfmac_f32_16x16x64_bf16 v[96:99], v[220:223], v[184:191], %[ix]\n\t"
                    "v_max3_u32 v100, v100, v220, v221\n\t"
                    "v_max3_u32 v100, v100, v222, v223\n\t"
                    "v_smfmac_f32_16x16x64_bf16 v[96:99], v[224:227], v[128:135], %[ix]\n\t"
                    "v_max3_u32 v100, v100, v224, v225\n\t"
                    "v_max3_u32 v100, v100, v226, v227\n\t"
                    "v_smfmac_f32_16x16x64_bf16 v[96:99], v[228:231], v[136:143], %[ix]\n\t"
                    "v_max3_u32 v100, v100, v228, v229\n\t"
                    "v_max3_u32 v100, v100, v230, v231\n\t"
                    "v_smfmac_f32_16x16x64_bf16 v[96:99], v[232:235], v[144:151], %[ix]\n\t"
                    "v_max3_u32 v100, v100, v232, v233\n\t"
                    "v_max3_u32 v100, v100, v234, v235\n\t"
                    "v_smfmac_f32_16x16x64_bf16 v[96:99], v[236:239], v[152:159], %[ix]\n\t"
                    "v_max3_u32 v100, v100, v236, v237\n\t"
                    "v_max3_u32 v100, v100, v238, v239\n\t"
                    "v_cmp_eq_u32 %[bad], -1, v100\n\t"
                    "s_nop 5\n\t"
                    "v_add_f32 v96, v96, v102\n\t"
                    "v_add_f32 v97, v97, v103\n\t"
                    "v_add_f32 v96, v96, v98\n\t"
                    "v_add_f32 v97, v97, v99\n\t"
                    "s_cmp_lg_u64 %[bad], 0\n\t"
                    "s_cbranch_scc1 4b\n\t"
                    "s_branch 2b\n\t"
                    "5:\n\t"
                    : [tr] "+s"(tries), [po] "+v"(po), [pm] "+v"(pm), [x0] "+s"(x0), [x1] "+s"(x1), [bad] "=&s"(bad), [t0] "=&s"(t0_), [t1] "=&s"(t1_), [t2] "=&s"(t2_), [t3] "=&s"(t3_), [t4] "=&s"(t4_)
                    : [zo] "v"(zfix(zoff, zb)), [sp] "v"(spa), [ix] "v"(sidx), [rw] "s"(rwin), [zb] "s"(zb), [z4] "s"(zb + 4096u), [pb] "s"(pb), [mo] "v"(moff), [fd] "n"(FDL),
                      [m0] "s"(mb), [m1] "s"(mb + PAIR), [m2] "s"(mb + mc2), [m3] "s"(mb + mc2 + PAIR), "{v[160:167]}"(zf[0]), "{v[168:175]}"(zf[1]), "{v[176:183]}"(zf[2]), "{v[184:191]}"(zf[3])
                    : "v96", "v97", "v98", "v99", "v100", "v101", "v102", "v103", "v104", "v105", "v106", "v107", "v128", "v129", "v130", "v131", "v132", "v133", "v134", "v135", "v136", "v137", "v138", "v139", "v140", "v141", "v142", "v143", "v144", "v145", "v146", "v147", "v148", "v149", "v150", "v151", "v152", "v153", "v154", "v155", "v156", "v157", "v158", "v159", "v208", "v209", "v210", "v211", "v212", "v213", "v214", "v215", "v216", "v217", "v218", "v219", "v220", "v221", "v222", "v223", "v224", "v225", "v226", "v227", "v228", "v229", "v230", "v231", "v232", "v233", "v234", "v235", "v236", "v237", "v238", "v239", "vcc", "scc", "memory");
                budget = tries;
                if (w == 3) *(volatile LAS int*)(smem + 4100) = s;
                __builtin_amdgcn_s_sleep(DPS - 4);
                if (w == 3) {
                    st128(SENT, rwin, ra, W_MEM + (unsigned)(s & 3) * 6u * PAIR, 16);
                    if (lane == 0 && s > 1) __builtin_amdgcn_raw_buffer_store_b32((unsigned)s - 2u, rwin, 0u, W_GO + fo + (unsigned)o * 128u, 16);
                }
                for (;;) {
                    const u32x2 x = *(volatile LAS u32x2*)(smem + 4096);
                    zr = __builtin_amdgcn_readfirstlane(x[0]);
                    if (__builtin_amdgcn_readfirstlane(x[1]) == s) break;
                    __builtin_amdgcn_s_sleep(2);
                }
                zb = zb == W_ZM + 2u * NOWN * PAIR ? W_ZM : zb + NOWN * PAIR;
            }
            const unsigned zq = zb == W_ZM ? W_ZM + 2u * NOWN * PAIR : zb - NOWN * PAIR;
            while (np < 4) {
                while (zr < NSEG - 1) { __builtin_amdgcn_s_sleep(1); zr = __builtin_amdgcn_readfirstlane(*(volatile LAS int*)(smem + 4096)); }
                piece();
            }
            __builtin_amdgcn_s_sleep(DTK);
            for (int e = 0; e < 2; ++e) {
                f32x4 c[2];
                SST(16 + 3 * e);
                for (unsigned spins = 0;;) {
                    asm volatile("" ::: "memory");
                    SST(17 + 3 * e);
                    u32x4 pt[16], ptm[2][2], zm[2];
                    const unsigned pl = W_PL + fo + (unsigned)e * (PTSEG * 2u);
#pragma unroll
                    for (int q = 0; q < 2; ++q) zm[q] = __builtin_amdgcn_raw_buffer_load_b128(rwin, zfix(zoff, e ? zb : zq) + hg * 2048u + q * 1024u, e ? zb : zq, 16);
#pragma unroll
                    for (int q = 0; q < 16; ++q) pt[q] = __builtin_amdgcn_raw_buffer_load_b128(rwin, ((unsigned)(16 * hg) * 1024u + lane * 16u) | (q & 1 ? 0u : (lane & 32u) << 25), pl + q * 1024u, 16);
#pragma unroll
                    for (int q = 0; q < 4; ++q) ptm[q >> 1][q & 1] = __builtin_amdgcn_raw_buffer_load_b128(rwin, (unsigned)(hg * 2048 + lane * 16), pl + 65536u + (q >> 1) * 8192u + (q & 1) * 1024u, 16);
                    unsigned m = max(smax(zm[0]), smax(zm[1]));
#pragma unroll
                    for (int t = 0; t < 2; ++t) {
                        c[t] = z4;
#pragma unroll
                        for (int q = 0; q < 8; ++q) { c[t] = mfma32(half8(zf[q >> 1], q & 1), pt[2 * q + t], c[t]); m = max(m, smax(pt[2 * q + t])); }
#pragma unroll
                        for (int q = 0; q < 2; ++q) { c[t] = mfma32((u32x4){zm[q][0], zm[q][2], zm[q][1], zm[q][3]}, ptm[t][q], c[t]); m = max(m, smax(ptm[t][q])); }
                    }
                    if (!__any(m == 0xFFFFFFFFu)) break;
                    if (spin_fail(spins, rwin)) break;
                }
                SST(18 + 3 * e);
#pragma unroll
                for (int t = 0; t < 2; ++t) *(f32x4*)(op + e * 8192 + hg * 2048 + (16 * t + fr) * 64 + fq * 16) = c[t];
                BAR();
                VMWAIT();
#pragma unroll
                for (int q = 0; q < 4; ++q) zf[q] = cat8(*(const u32x4*)(smem + 32768 + hg * 8192 + q * 2048 + lane * 16), *(const u32x4*)(smem + 33792 + hg * 8192 + q * 2048 + lane * 16));
                if (e == 0) BAR0();
            }
            if (layer == 0) {
#pragma unroll
                for (int q = 0; q < 4; ++q) st128(SENT, rwin, zfix(zoff, q < 2 ? zb : zq) + hg * 2048u + (q & 1) * 1024u, q < 2 ? zb : zq, 16);
                if (w == 3) st128(SENT, rwin, ra, W_MEM + 3u * 6u * PAIR, 16);
            }
        } else if (w == 4) {
            const unsigned fl = fz_at(lane & 31, o / 10) + (lane & 32) * 128u;
            int zk = -1;
            auto note = [&](int f) INL {
                f = min(f, __builtin_amdgcn_update_dpp(f, f, 0xB1, 0xF, 0xF, false)); f = min(f, __builtin_amdgcn_update_dpp(f, f, 0x4E, 0xF, 0xF, false));
                f = min(f, __builtin_amdgcn_update_dpp(f, f, 0x141, 0xF, 0xF, false)); f = min(f, __builtin_amdgcn_update_dpp(f, f, 0x140, 0xF, 0xF, false));
                zk = seg_done(MXV, min(__builtin_amdgcn_readlane(f, 0), __builtin_amdgcn_readlane(f, 16)), min(__builtin_amdgcn_readlane(f, 32), __builtin_amdgcn_readlane(f, 48)), MXA);
                *(volatile LAS int*)(smem + 4096) = zk;
            };
            for (int s = 0; s < NSEG; ++s) {
                for (unsigned spins = 0; __builtin_expect(zk < s && s > 0, 0);) {
                    if (spins == 0) __builtin_amdgcn_s_sleep(DW4);
                    asm volatile("" ::: "memory");
                    note((int)__builtin_amdgcn_raw_buffer_load_b32(rwin, fl, W_FZ + fo, 16));
                    FC(fc2);
                    if (spin_fail(spins, rwin)) note(NSEG);
                    if (zk < s) __builtin_amdgcn_s_sleep(ZPS);
                }
                if (LKE && s > 0 && zk < (s + 8 >= NSEG ? NSEG - 1 : s + ZLA)) { __builtin_amdgcn_s_sleep(LKE); note((int)__builtin_amdgcn_raw_buffer_load_b32(rwin, fl, W_FZ + fo, 16)); }
                asm volatile("s_waitcnt lgkmcnt(0)\n\ts_barrier\n\ts_barrier" ::: "memory");
                int f = 0;
                const bool look = !LKE && zk < (s + 8 >= NSEG ? NSEG - 1 : s + ZLA);
                if (look) { __builtin_amdgcn_s_sleep(6); f = (int)__builtin_amdgcn_raw_buffer_load_b32(rwin, fl, W_FZ + fo, 16); }
                SB();
                BAR0();
                if (look) note(f);
            }
        } else if (w < 7) {
            const unsigned ooff = (unsigned)((S_ - (7 - w) * SEG + (lane >> 1)) * D_ + ot * 16 + (lane & 1) * 8) * 2u;
            const __amdgpu_buffer_rsrc_t ro = rout();
            for (int s = 0; s < 3 * NSEG - 3 + (w - 5); ++s) BAR0();
            if (w == 5) __builtin_amdgcn_s_sleep(DPS);
            const u32x4 xr = __builtin_amdgcn_raw_buffer_load_b128(rws, ooff, O_X1, 16);
            BAR0();
            BAR0();
            const char* src = op + (w - 5) * 8192 + lane * 32;
            f32x4 x = *(const f32x4*)src, y = *(const f32x4*)(src + 16);
#pragma unroll
            for (int ww = 1; ww < 4; ++ww) { x += *(const f32x4*)(src + ww * 2048); y += *(const f32x4*)(src + ww * 2048 + 16); }
            const u32x4 r = {pack2(lo2f(xr[0]) + bfr(x[0]), hi2f(xr[0]) + bfr(x[1])), pack2(lo2f(xr[1]) + bfr(x[2]), hi2f(xr[1]) + bfr(x[3])),
                             pack2(lo2f(xr[2]) + bfr(y[0]), hi2f(xr[2]) + bfr(y[1])), pack2(lo2f(xr[3]) + bfr(y[2]), hi2f(xr[3]) + bfr(y[3]))};
            st128(nosent4(r), ro, ooff, 0u, 16);
            RST(19 + w, layer, w, w & 3);
            if (w == 5) BAR0();
        } else {
            Tk tk = tk_init();
            __builtin_amdgcn_s_setprio(3);
            for (int s = 0; s < NSEG; ++s) {
                const unsigned ta0 = ck.ta, mm0 = ck.ma;
                unsigned tp = tpp + lt + xs + M1;
                if (s < SAD) {
                    if (s) wait_fine(tpp + CF, ck);
                    tp = M2 + xs + (s ? 0 : PEN1);
                    sched(tk, s);
                    tp = tpp - tp;
                    if (s == 0) { wait_fine(tp - DZ0, ck); *(volatile LAS int*)(smem + 4096) = 0; }
                }
                wait_fine_bar(tp - DZP, ck);
                wait_cyc_bar(cyc_at(ck, sx24(tp + (s + 1 < NSEG ? 0 : ML) - ck.ta)));
                clk_rate(ck, ta0, mm0);
                asm volatile("s_barrier ; 7" ::: "memory");
                if (s >= SAD && s + 1 < NSEG) sched(tk, s);
                RTS();
            }
        }
        __builtin_amdgcn_s_setprio(0);
        SST(23);
    };
    static_assert(NSEG % 3 == 1, "ring");
    auto a1_units = [&](auto late, int& u, int u1) INL {
        constexpr bool LATE = decltype(late)::v;
        int tid = mytid();
        RowTw tw;
        if (LATE) row_tw(tw, tid);
        for (; u < u1; u += !LATE ? 24 : u >= 232 && u < 240 ? 8 : 256) {
            if (!LATE) tid = mytid();
            if (w == 4) {
                for (unsigned spins = 0;;) {
                    asm volatile("" ::: "memory");
                    if (LATE) {
                        const int go = __builtin_amdgcn_readfirstlane(__builtin_amdgcn_raw_buffer_load_b32(rwin, 0u, W_GO + (unsigned)(u & 31) * 128u, 16)) - min((u >> 2) - 2, NSEG - 4);
                        if (go >= 0) break;
                        if (go < -3) __builtin_amdgcn_s_sleep(127);
                    } else if (!__any(__builtin_amdgcn_raw_buffer_load_b32(rwin, min(fresh_lane(), (unsigned)NOWN - 1u) * 4u, W_OUT + (unsigned)(u >> 2) * 256u, 16) != 0u)) break;
                    if (spin_fail(spins, rwin)) break;
                    if (u < 200) __builtin_amdgcn_s_sleep(WSLP); else __builtin_amdgcn_s_sleep(16);
                }
            }
            BAR();
            SST(24);
            a1_unit<4, LATE>(mkrsrc(ws + OFF_X2, S_ * D_ * 2), u * 8, rws, rwin, tw, tid, fs);
            if (!LATE) VMWAIT();
            FST(8);
            BAR0();
            FST(9);
            if (!LATE && w == 0) __builtin_amdgcn_raw_buffer_store_b64(__builtin_bit_cast(u32x2, tag + 9ull), rwin, LANE0, W_A1 + (unsigned)u * 128u, 16);
            SST(25);
            FSW(7);
            if (!LATE && u < 176) __builtin_amdgcn_s_sleep(A1S);
        }
    };
    auto out_role = [&](auto nt, int t0, int s0, auto between) INL {
        constexpr int NT = decltype(nt)::v, W0 = 8 - NT;
        const int o = s0 * 10 + (t0 >> 2), send = NSEG - 2;
        int rdy = -1, go = -1, zrdy = -1;
        for (int i = 0; i < s0; ++i) __builtin_amdgcn_s_sleep(OST0);
        for (int s = s0; s < send; s += 4) {
        LANES();
        const unsigned ztoff = (unsigned)((t0 * H_ + 4 * w) * 64 + lane) * 16u;
        const unsigned ptoff = (unsigned)(8 * w * 64 + lane) * 16u, ptmoff = 65536u + (unsigned)(4 * w * 16 + lane) * 16u;
        const unsigned ptoff0 = ptoff | (lane & 32u) << 25;
        const unsigned ooff = (unsigned)((lane >> 1) * D_ + tile_of(t0 + w - W0) * 16 + (lane & 1) * 8) * 2u;
        const unsigned fg = fo + (unsigned)((4 * w + (lane & 3)) * 128), gw = (unsigned)(t0 / 10) * 4u;
        const unsigned tfoff = fg + (lane < 16 ? W_FPT + (lane >> 2) * 4u : W_FZ2 + gw), zfoff = fo + W_FZ + fz_at(4 * w + (lane & 3), gw / 4u) + (lane & 32u) * 128u;
        auto wait_rdy = [&](int s) INL {
            for (unsigned spins = 0; rdy < s;) {
                asm volatile("" ::: "memory");
                if (s < TAILS || go < s - 2) {
                    go = (int)__builtin_amdgcn_readfirstlane(__builtin_amdgcn_raw_buffer_load_b32(rwin, 0u, W_GO + fo + (unsigned)o * 128u, 16));
                    rdy = max(rdy, go);
                    if (rdy < s) { if (s < TAILS || go < s - 3) __builtin_amdgcn_s_sleep(OSLP); else __builtin_amdgcn_s_sleep(OSLP3); }
                } else {
                    if (!__any((int)__builtin_amdgcn_raw_buffer_load_b32(rwin, tfoff, 0u, 16) < s)) rdy = s;
                    else __builtin_amdgcn_s_sleep(OSLP2);
                }
                if (rdy < s && spin_fail(spins, rwin)) break;
            }
        };
        const __amdgpu_buffer_rsrc_t ro = rout();
        u32x4 zf[NT][4];
        auto fetch_z = [&](int s) INL {
            for (unsigned spins = 0; zrdy < s;) {
                asm volatile("" ::: "memory");
                int f = (int)__builtin_amdgcn_raw_buffer_load_b32(rwin, zfoff, 0u, 16);
                f = min(f, __builtin_amdgcn_update_dpp(f, f, 0xB1, 0xF, 0xF, false)); f = min(f, __builtin_amdgcn_update_dpp(f, f, 0x4E, 0xF, 0xF, false));
                f = min(f, __builtin_amdgcn_update_dpp(f, f, 0x141, 0xF, 0xF, false));
                zrdy = seg_done(MXV, __builtin_amdgcn_readlane(f, 0), __builtin_amdgcn_readlane(f, 32));
                if (zrdy >= s || spin_fail(spins, rwin)) break;
                __builtin_amdgcn_s_sleep(OSLP);
            }
#pragma unroll
            for (int q = 0; q < 4 * NT; ++q) zf[q >> 2][q & 3] = __builtin_amdgcn_raw_buffer_load_b128(rws, ztoff + (q & 3) * 1024u, O_Z + (unsigned)((s - layer) * (int)ZTSEG) * 2u + (q >> 2) * (H_ * 1024u), 16);
        };
            u32x4 xr, zx[NT], pt[8], ptm[2];
            RT(0);
            fetch_z(s);
            if (NT > 1 && w >= W0 && layer == 0 && s >= 4 && s < 60) {
                asm volatile("s_waitcnt vmcnt(%0)" :: "n"(4 * NT) : "memory");
                __builtin_amdgcn_raw_buffer_store_b32(0u, rwin, LANE0, W_OUT + (unsigned)((s - 4) * 64 + t0 + w - W0) * 4u, 16);
            }
            if (w >= W0) xr = __builtin_amdgcn_raw_buffer_load_b128(rws, ooff, O_X1 + (unsigned)(s * SEG * D_ * 2), 16);
            if (s == 61) SST(27);
            wait_rdy(s);
            if (s == 61) SST(28);
            RT(1);
            const unsigned ptb = O_PT + (unsigned)(s * PTSEG) * 2u;
#pragma unroll
            for (int q = 0; q < 8; ++q) pt[q] = __builtin_amdgcn_raw_buffer_load_b128(rws, (q & 1 ? ptoff : ptoff0) + q * 1024u, ptb, 16);
#pragma unroll
            for (int q = 0; q < 2; ++q) ptm[q] = __builtin_amdgcn_raw_buffer_load_b128(rws, ptmoff + q * 8192u, ptb, 16);
#pragma unroll
            for (int q = 0; q < NT; ++q) zx[q] = __builtin_amdgcn_raw_buffer_load_b128(rws, tid * 16u, O_Z2 + (unsigned)(s * NOWN + t0 + q) * 8192u, 16);
            BAR0();
            f32x4 c[2 * NT];
#pragma unroll
            for (int q = 0; q < 4; ++q) {
#pragma unroll
                for (int u = 0; u < 2 * NT; ++u) c[u] = mfma32(zf[u >> 1][q], pt[2 * q + (u & 1)], q ? c[u] : z4);
                SB();
            }
#pragma unroll
            for (int u = 0; u < 2 * NT; ++u) {
                const int tq = u & 1, t = u >> 1;
                c[u] = mfma32((u32x4){zx[t][0], zx[t][2], zx[t][1], zx[t][3]}, ptm[tq], c[u]);
                *(f32x4*)(smem + w * (NT * 2048) + t * 2048 + (16 * tq + fr) * 64 + ((fq ^ ((fr >> 1) & 3)) << 4)) = c[u];
            }
            BAR();
            if (s == 61) SST(30);
            if (w >= W0) {
                const char* p0 = smem + (w - W0) * 2048 + (lane >> 1) * 64 + (((2 * (lane & 1)) ^ ((lane >> 2) & 3)) << 4);
                const char* p1 = smem + (w - W0) * 2048 + (lane >> 1) * 64 + (((2 * (lane & 1) + 1) ^ ((lane >> 2) & 3)) << 4);
                f32x4 x = *(const f32x4*)p0, y = *(const f32x4*)p1;
#pragma unroll
                for (int ww = 1; ww < 8; ++ww) { x += *(const f32x4*)(p0 + ww * (NT * 2048)); y += *(const f32x4*)(p1 + ww * (NT * 2048)); }
                const u32x4 r = {pack2(lo2f(xr[0]) + bfr(x[0]), hi2f(xr[0]) + bfr(x[1])), pack2(lo2f(xr[1]) + bfr(x[2]), hi2f(xr[1]) + bfr(x[3])),
                                 pack2(lo2f(xr[2]) + bfr(y[0]), hi2f(xr[2]) + bfr(y[1])), pack2(lo2f(xr[3]) + bfr(y[2]), hi2f(xr[3]) + bfr(y[3]))};
                if (layer) st128(r, ro, ooff, (unsigned)(s * SEG * D_ * 2), OAUX);
                else {
                    st128(nosent4(r), ro, ooff, (unsigned)(s * SEG * D_ * 2), 16);
                    if (NT == 1 && s < 56) { VMWAIT(); __builtin_amdgcn_raw_buffer_store_b32(0u, rwin, LANE0, W_OUT + (unsigned)(s * 64 + t0) * 4u, 16); }
                }
            } else __builtin_amdgcn_s_sleep(OSL4);
            if (s < 8 && NT > 1) SST(8 + s);
            if (s >= 56) SST(s - 40);
            RT(2);
            RTS();
            between(s);
        }
    };
    u32x8 wt[2][10];
#define PINW() asm volatile("" : "+{v[0:7]}"(wt[0][0]), "+{v[8:15]}"(wt[1][0]), "+{v[16:23]}"(wt[0][1]), "+{v[24:31]}"(wt[1][1]), "+{v[32:39]}"(wt[0][2]), "+{v[40:47]}"(wt[1][2]), "+{v[48:55]}"(wt[0][3]), "+{v[56:63]}"(wt[1][3]), "+{v[64:71]}"(wt[0][4]), "+{v[72:79]}"(wt[1][4]), "+{v[80:87]}"(wt[0][5]), "+{v[88:95]}"(wt[1][5]), "+{v[96:103]}"(wt[0][6]), "+{v[104:111]}"(wt[1][6]), "+{v[112:119]}"(wt[0][7]), "+{v[120:127]}"(wt[1][7]), "+{v[128:135]}"(wt[0][8]), "+{v[136:143]}"(wt[1][8]), "+{v[144:151]}"(wt[0][9]), "+{v[152:159]}"(wt[1][9]))
#define KILLW() asm volatile("" : "={v[0:7]}"(wt[0][0]), "={v[8:15]}"(wt[1][0]), "={v[16:23]}"(wt[0][1]), "={v[24:31]}"(wt[1][1]), "={v[32:39]}"(wt[0][2]), "={v[40:47]}"(wt[1][2]), "={v[48:55]}"(wt[0][3]), "={v[56:63]}"(wt[1][3]), "={v[64:71]}"(wt[0][4]), "={v[72:79]}"(wt[1][4]), "={v[80:87]}"(wt[0][5]), "={v[88:95]}"(wt[1][5]), "={v[96:103]}"(wt[0][6]), "={v[104:111]}"(wt[1][6]), "={v[112:119]}"(wt[0][7]), "={v[120:127]}"(wt[1][7]), "={v[128:135]}"(wt[0][8]), "={v[136:143]}"(wt[1][8]), "={v[144:151]}"(wt[0][9]), "={v[152:159]}"(wt[1][9]))
    if (blockIdx.x < 64) {
        for (;;) {
            load_qv(wt, bid & 31, bid >= 32, [&]() INL {
                rot0 = __builtin_amdgcn_raw_buffer_load_b64(rws, 0u, 128u, 16);
                if (w == 0) wait_tags(rwin, 0, 1, 64u, tag + 8ull, true);
                if (w == 7) clk_init(ck);
                if (w == 1) { const unsigned x = __builtin_amdgcn_raw_buffer_load_b32(mkrsrc(arg(A_MEM0), NM * D_ * 2), fresh_lane() * 160u, 0, 0); asm volatile("" :: "v"(x)); }
            });
            const unsigned x = get_rot();
            if (__builtin_expect(x == vx, 1)) break;
            set_role(x);
        }
        SST(1);
        if (TLBW) {
            const unsigned x = __builtin_amdgcn_raw_buffer_load_b32(rws, fresh_lane() << TLBW, ((blockIdx.x >> 3) + w * 8u) << (TLBW + 6) & 0x7FFFFFFFu, 0);
            asm volatile("" :: "v"(x));
        }
        PINW();
    } else {
        if (bid >= NWRK) probe();
        KILLW();
    }
    for (; layer < 2; ++layer) {
        new_layer();
        PINW();
        BAR();
        if (qx || (layer ? bid < 96 : (bid >= 32 && bid < 64) || (bid >= 104 && bid < NREC) || xq)) {
            const int ci = qx ? bid - 80 : bid < 96 ? bid : bid < NREC ? bid - 104 : 64 + (bid - NREC) / 10 * 8 + (bid - NREC) % 10;
            SST(26);
            a2_unit(ci >= 100 ? ci - 100 : ci + 60, layer, rws, rwin, mytid(), [&](int k) INL {
                if (k == 2) SST(28);
                else if (w == 0) {
                    if (layer && bid >= 64) for (unsigned spins = 0;;) {
                        asm volatile("" ::: "memory");
                        if (__all(__builtin_bit_cast(u64, __builtin_amdgcn_raw_buffer_load_b64(rwin, 0u, W_A1 + (unsigned)(200 + (bid & 15)) * 128u, 16)) == tag + 9ull) || spin_fail(spins, rwin)) break;
                        __builtin_amdgcn_s_sleep(100);
                    }
                    if (layer && bid >= 64) rec_end(GATE);
                    wait_tags(rwin, 0, 4, layer ? 32u : 64u, tag + 8ull + layer, !layer);
                    SST(27);
                }
            }, fs);
            SST(29);
            VMWAIT();
            FST(8);
            BAR0();
            FST(9);
            if (w == 0) {
                __builtin_amdgcn_raw_buffer_store_b32(0u, rwin, LANE0, W_COL + fo + (unsigned)ci * 128u, 16);
                if (bid == NQT && layer == 1) __builtin_amdgcn_raw_buffer_store_b64(__builtin_bit_cast(u32x2, tag), rws, LANE0, 0u, 16);
            }
            SST(30);
            FSW(1 + layer);
        }
#define NEXTB() do { PINW(); asm volatile("" : "+s"(bid), "+s"(w)); } while (0)
        NEXTB();
        if (bid < 32) { fresh_rsrc(); qk_role(wt); PINW(); }
        NEXTB();
        const int hh = bid < 96 ? bid & 31 : bid < NQT ? (bid - NREC) / 10 * 8 + (bid - NREC) % 10 : (bid - NQT) & 31;
        if (((qx || (bid >= 32 && bid < 64)) && layer == 0) || xq) { if (layer) { if (w == 0) rec_end(GATX); BAR0(); } load_qv(wt, hh, xt || bid < 64, []() {}); SST(1); PINW(); }
        NEXTB();
        if (bid >= 32 && bid < 64) { if (layer == 0) near_init(); fresh_rsrc(); vo_role(wt); PINW(); }
        NEXTB();
        if ((bid >= 64 && bid < 96) || xt) {
            fresh_rsrc();
            vt_role(wt, hh, xt ? MXV : ~MXV);
            if (xt) {
                out_role(IC<1>(), 5 * (hh & 7) + (hh & 1 ? 0 : 4), hh >> 3, [&](int s) INL {
                    if (s < 28) {
                        if (layer == 0) for (unsigned i = (s >> 2) * 2; i < (s >> 2) * 2 + 2; ++i)
                            st128(SENT, i < 7 ? mkrsrc(ws + O_ARM, O_ARM1 - O_ARM) : i < 12 ? mkrsrc(ws + O_Z - ZTSEG * 2, ZTSEG * 2) : mkrsrc(arg(1) + (W_ARM - W_LAYER) / 2, W_LAYER),
                                  (unsigned)(hh * 512 + mytid()) * 16u, (i < 7 ? i : i < 12 ? i - 7 : i - 12) * 262144u, 16);
                    } else if (s < 32) {
                        VMWAIT();
                        BAR0();
                        if (w >= 4) __builtin_amdgcn_raw_buffer_store_b32(NSEG - 1, rwin, LANE0, W_FZ + fo + fz_at(hh, w - 4) + 4096u, 16);
                    }
                });
                static_assert(O_ARM + 7u * 262144u >= O_ARM1 && ZTSEG * 2 <= 5u * 262144u && W_LAYER <= 262144u && !(MXV >> 40), "arming");
            }
            PINW();
        }
        NEXTB();
        if ((qx && !xt) || xq) { fresh_rsrc(); qt_role(wt, hh, IC<1>()); PINW(); }
        NEXTB();
        if (bid >= 96 && !qx) {
            const int fi = xq ? 256 : bid >= NWRK ? 255 - bid : bid >= NREC ? 8 + (bid - NREC) / 10 * 2 + (bid - NREC) % 10 : bid - 72, tid = mytid();
            RowTw tw;
            row_tw(tw, tid);
            if (layer == 0 && fi >= 16 && fi < 32) { if (w == 0) wait_tags(rwin, 0, 1, 64u, tag + 8ull, true); BAR0(); }
            if (layer && fi < 64) {
                for (unsigned spins = 0;;) {
                    asm volatile("" ::: "memory");
                    if (__all(__builtin_bit_cast(u64, __builtin_amdgcn_raw_buffer_load_b64(rwin, 0u, W_A1 + (unsigned)fi * 128u, 16)) == tag + 9ull) || spin_fail(spins, rwin)) break;
                    __builtin_amdgcn_s_sleep(8);
                }
            }
            bool cols = false;
            for (int u = fi; u < 256; u += 24) {
                a3_unit(mkrsrc(layer ? (const u16*)(ws + OFF_X2) : arg(A_X), S_ * D_ * 2), rout(), u * 8, layer, rws, rwin, tw, tid, [&]() INL {
                    if (cols) return;
                    cols = true;
                    if (w == 7) {
                        const unsigned l = fresh_lane();
                        unsigned o[3] = {l * 128u, l * 128u + 8192u, l < 32u ? l * 128u + 16384u : 0x40000000u};
                        for (unsigned spins = 0;;) {
                            asm volatile("" ::: "memory");
                            unsigned v[3];
#pragma unroll
                            for (int g = 0; g < 3; ++g) v[g] = __builtin_amdgcn_raw_buffer_load_b32(rwin, o[g], W_COL + fo, 16);
#pragma unroll
                            for (int g = 0; g < 3; ++g) if (v[g] == 0u) o[g] = 0x40000000u;
                            if (!__any((v[0] | v[1] | v[2]) != 0u) || spin_fail(spins, rwin)) break;
                            __builtin_amdgcn_s_sleep(CSLP);
                        }
                    }
                    BAR0();
                    SST(6);
                }, fs);
                VMWAIT();
                FST(8);
                BAR0();
                FST(9);
                if (w == 0) __builtin_amdgcn_raw_buffer_store_b32(0u, rwin, LANE0, W_H + fo + (unsigned)((u >> 2) * 128 + (u & 3) * 4), 16);
                if (u < 64) SST(7);
                FSW(u < 64 ? 3 + layer : 5);
                if (fi >= 24) break;
                if (u < 64) u += 40;
            }
            if (bid < NREC) { fresh_rsrc(); own_role(); }
            else if (xq) out_role(IC<4>(), 5 * (hh & 7) + (hh & 1), hh >> 3, [](int) {});
            else if (layer == 0) { int u = fi; a1_units(IC<0>(), u, 224); u = bid < NQT ? 192 + (bid - NREC) / 10 + 4 * ((bid - NREC) % 10) : bid < 248 ? bid - 8 : bid; a1_units(IC<1>(), u, 256); }
            KILLW();
        }
        NEXTB();
        SST(31);
    }
}

constexpr int L_DBG = 0;
extern "C" size_t dbg_offset(int i) {
    const size_t o[] = {OFF_WIN, W_TMO, W_NEAR, OFF_F, OFF_X1, OFF_X2, OFF_H, OFF_WFT};
    return o[i];
}

extern "C" void kernel_launch(void* const* d_in, const int* in_sizes, int n_in, void* d_out, int out_size, void* d_ws, size_t ws_size,
                              hipStream_t stream) {
    char* ws = (char*)d_ws;
    hipFuncSetAttribute((const void*)mega_kernel, hipFuncAttributeMaxDynamicSharedMemorySize, L_END + L_DBG);
    char* win = (char*)(((uintptr_t)(ws + OFF_WIN) + 0x1FFFFFu) & ~(uintptr_t)0x1FFFFFu);
    Args a{ws, win, (const u16*)d_in[0], (const u16*)d_in[1], (const u16*)d_in[2], (const u16*)d_in[4], (const u16*)d_in[5], (const u16*)d_in[6], (const u16*)d_in[7], (const u16*)d_in[8], (u16*)d_out};
    mega_kernel<<<256, 512, L_END + L_DBG, stream>>>(a);
}
```
